```python
import torch

def _softmax(x, axis=-1):
    return torch.softmax(x, dim=axis)
def _gelu(x):
    return torch.nn.functional.gelu(x, approximate="tanh")
def _silu(x):
    return torch.nn.functional.silu(x)
def _sigmoid(x):
    return torch.sigmoid(x)
def _layernorm(x, axis=-1, eps=1e-5):
    m = x.mean(dim=axis, keepdim=True)
    v = x.var(dim=axis, keepdim=True, unbiased=False)
    return (x - m) / torch.sqrt(v + eps)
def _scatter_add(tgt, idx, src, axis=0):
    return tgt.clone().index_add_(axis, idx, src)
def _segment_sum(src, seg, n):
    out = torch.zeros((n,) + src.shape[1:], dtype=src.dtype)
    return out.index_add_(0, seg, src)
def _pad(x, pw):
    flat = tuple(v for pair in reversed(pw) for v in pair)
    return torch.nn.functional.pad(x, flat)

kernel_name = 'interleave_layers__sliding_local_dilated__mixer_channel_mlp__prompt'

def setup_inputs():

    _w = (lambda a: (lambda t: t.to(torch.bfloat16) if t.is_floating_point() else t)(torch.as_tensor(a)))
    def _A_setup():
        B, S, D = 1, 2048, 576
        return {
            "x": _w((lambda *s: torch.randn(*s, dtype=torch.bfloat16) * 0.05)(B, S, D)),
            "wqkv": _w((lambda *s: torch.randn(*s, dtype=torch.bfloat16) * 0.05)(D, 3 * D)),
            "wo": _w((lambda *s: torch.randn(*s, dtype=torch.bfloat16) * 0.05)(D, D)),
        }
    _A_in = _A_setup()

    def _B_setup():
        B, S, D, F = 1, 2048, 576, 4608
        return {
            "x": _w((lambda *s: torch.randn(*s, dtype=torch.bfloat16) * 0.05)(B, S, D)),
            "w1": _w((lambda *s: torch.randn(*s, dtype=torch.bfloat16) * 3.2)(D, F) * (D ** -0.5)),
            "b1": _w((lambda *s: torch.randn(*s, dtype=torch.bfloat16) * 0.8)(F)),
            "w2": _w((lambda *s: torch.randn(*s, dtype=torch.bfloat16) * 0.2)(F, D) * (F ** -0.5)),
            "b2": _w((lambda *s: torch.randn(*s, dtype=torch.bfloat16) * 0.2)(D)),
        }
    _B_in = _B_setup()

    return {
        "A_x": _A_in["x"],
        "A_wqkv": _A_in["wqkv"],
        "A_wo": _A_in["wo"],
        "B_x": _B_in["x"],
        "B_w1": _B_in["w1"],
        "B_b1": _B_in["b1"],
        "B_w2": _B_in["w2"],
        "B_b2": _B_in["b2"],
    }

def reference(A_x, A_wqkv, A_wo, B_x, B_w1, B_b1, B_w2, B_b2):
    def _A_ref(x, wqkv, wo):
        B, S, D = x.shape
        H = 32; Dh = D // H
        W, dil = 8, 2
        qkv = (lambda x, *d: x.permute(*d))((x @ wqkv).reshape(B, S, 3, H, Dh), 2, 0, 3, 1, 4)
        q, k, v = qkv[0], qkv[1], qkv[2]
        scale = Dh ** -0.5
        pos = torch.arange(S)
        off = torch.arange(W) * dil
        idx = pos[:, None] - off[None, :]
        mask = idx >= 0
        idx_c = torch.where(mask, idx, 0).reshape(-1)
        kg = k[:, :, idx_c, :].reshape(B, H, S, W, Dh)
        vg = v[:, :, idx_c, :].reshape(B, H, S, W, Dh)
        logits = torch.einsum("bhsd,bhswd->bhsw", q, kg) * scale
        logits = torch.where(mask[None, None, :, :], logits, -1e9)
        attn = _softmax(logits, axis=-1)
        o = torch.einsum("bhsw,bhswd->bhsd", attn, vg)
        return (lambda x, *d: x.permute(*d))(o, 0, 2, 1, 3).reshape(B, S, D) @ wo

    def _B_ref(x, w1, b1, w2, b2):
        y = _layernorm(x, axis=-1)
        h = _gelu(torch.einsum("bsd,df->bsf", y, w1) + b1)
        return torch.einsum("bsf,fd->bsd", h, w2) + b2

    x = A_x
    x = x + _A_ref(_layernorm(x, axis=-1), A_wqkv, A_wo)
    x = x + _B_ref(_layernorm(x, axis=-1), B_w1, B_b1, B_w2, B_b2)
    x = x + _A_ref(_layernorm(x, axis=-1), A_wqkv, A_wo)
    x = x + _B_ref(_layernorm(x, axis=-1), B_w1, B_b1, B_w2, B_b2)
    return x
```

```cpp
#include <hip/hip_runtime.h>
#include <hip/hip_bf16.h>
#include <stdint.h>

namespace {
#define LAS __attribute__((address_space(3)))
#define GAS __attribute__((address_space(1)))
typedef unsigned v4u __attribute__((ext_vector_type(4)));
typedef unsigned v2u __attribute__((ext_vector_type(2)));
typedef float f32x4 __attribute__((ext_vector_type(4)));
typedef float f32x2 __attribute__((ext_vector_type(2)));
typedef __bf16 bf16x8 __attribute__((ext_vector_type(8)));
typedef __bf16 bf16x2 __attribute__((ext_vector_type(2)));
typedef short s16x4 __attribute__((ext_vector_type(4)));
typedef unsigned long long u64;
typedef __amdgpu_buffer_rsrc_t rsrc_t;

constexpr int KB = 1024;
constexpr int NTHR = 768;
constexpr int WIN = 18;
#define XB 2
constexpr float EPS = 1e-5f;
constexpr int SC1 = 16;
constexpr float GC0 = -2.0f * 0.7978845608028654f * 1.4426950408889634f;
constexpr int FLAGS = 4096;
constexpr int WFLAGS = 64 * KB;
constexpr int WBASE = 256 * KB;
constexpr int WSL = (252 + 1296) * KB;
constexpr int WOB = WBASE + 8 * WSL;
constexpr int ABASE = 14 * 1024 * KB;
constexpr int IMG = 72 * KB;
constexpr int A_Y = 0, A_XR = 2 * IMG, A_O = 3 * IMG, A_X1 = 4 * IMG, A_PT = 5 * IMG, ASL = 1024 * KB;
constexpr int WS_BYTES = ABASE + 32 * ASL;
static_assert(WOB + 648 * KB <= ABASE && 13 * IMG <= ASL, "workspace map");
constexpr int L_H = IMG;
constexpr int L_QK = 90 * KB;
constexpr int L_ST = 144 * KB;
constexpr int L_HC = 148 * KB;
constexpr int LDS_BYTES = 149 * KB;

struct Params {
  const unsigned short *x, *wqkv, *wo, *w1, *b1, *w2, *b2;
  unsigned short *out;
  unsigned char *ws;
};

#define STAMP(i)
#define SB() __builtin_amdgcn_sched_barrier(0)
#define BAR() do { asm volatile("s_waitcnt lgkmcnt(0)" ::: "memory"); __builtin_amdgcn_s_barrier(); asm volatile("" ::: "memory"); } while (0)
#define DRAIN() asm volatile("s_waitcnt vmcnt(0)" ::: "memory")
#define OPAQUE(x) asm("" : "+v"(x))

__device__ __forceinline__ f32x4 mfma(v4u a, v4u b, f32x4 c) {
  return __builtin_amdgcn_mfma_f32_16x16x32_bf16(__builtin_bit_cast(bf16x8, a), __builtin_bit_cast(bf16x8, b), c, 0, 0, 0);
}
#define MF(acc, a, b) asm volatile("v_mfma_f32_16x16x32_bf16 %0, %1, %2, %0" : "+v"(acc) : "v"(a), "v"(b))
template <int NT, int MT> __device__ __forceinline__ void acc_fence(f32x4 (&acc)[NT][MT]) {
#pragma unroll
  for (int t = 0; t < NT; ++t)
#pragma unroll
    for (int m = 0; m < MT; ++m) asm volatile("" : "+v"(acc[t][m]));
}
#define ACC_IN(acc) do { acc_fence(acc); asm volatile("s_nop 1"); SB(); } while (0)
#define ACC_OUT(acc) do { SB(); asm volatile("s_nop 7\n\ts_nop 1"); acc_fence(acc); } while (0)
__device__ __forceinline__ unsigned pk(float a, float b) {
  f32x2 v = {a, b};
  return __builtin_bit_cast(unsigned, __builtin_convertvector(v, bf16x2));
}
__device__ __forceinline__ float blo(unsigned u) { return __uint_as_float(u << 16); }
__device__ __forceinline__ float bhi(unsigned u) { return __uint_as_float(u & 0xffff0000u); }
__device__ __forceinline__ v4u ldg(rsrc_t rs, int voff, int soff) { return __builtin_amdgcn_raw_buffer_load_b128(rs, voff, soff, SC1); }
__device__ __forceinline__ void stg(v4u v, rsrc_t rs, int voff, int soff) { __builtin_amdgcn_raw_buffer_store_b128(v, rs, voff + soff, 0, SC1); }
__device__ __forceinline__ v4u ldf(rsrc_t rs, int lane16, int base, int f) { return ldg(rs, lane16 + (f & 3) * KB, base + (f >> 2) * 4 * KB); }
__device__ __forceinline__ int opq_v(int x) { asm volatile("" : "+v"(x)); return x; }
__device__ __forceinline__ int opq_s(int x) { asm volatile("" : "+s"(x)); return x; }
#define LOCALS() const int lane = opq_v(lane0), w = opq_s(w0), j = opq_s(j0), g = opq_s(g0); \
  const int lane16 = lane * 16, quad = lane >> 4, l15 = lane & 15, ab = ABASE + j * ASL, wb = WBASE + g * WSL; \
  (void)lane16; (void)quad; (void)l15; (void)ab; (void)wb
#define OWNROWS() const int c8 = 6 * w + (lane >> 3); \
  const int piece = ((g >> 1) * 18 + (c8 >> 2)) * KB + ((c8 & 3) * 16 + 8 * (g & 1) + (lane & 7)) * 16; \
  const int mytok = 64 * j + 2 * (16 * ((g >> 1) & 1) + 8 * (g & 1) + (lane & 7)) + (g >> 2)
#define QBASE() (wb + (w < 2 ? 180 + w * 36 : (w - 2) * 18) * KB)
#define MBASE() (wb + (252 + w * 108) * KB)
__device__ __forceinline__ float rows_sum(float x) {
  auto s = __builtin_amdgcn_permlane16_swap(__float_as_uint(x), __float_as_uint(x), false, false);
  x = __uint_as_float(s[0]) + __uint_as_float(s[1]);
  auto t = __builtin_amdgcn_permlane32_swap(__float_as_uint(x), __float_as_uint(x), false, false);
  return __uint_as_float(t[0]) + __uint_as_float(t[1]);
}
__device__ __forceinline__ float rows_max(float x) {
  auto s = __builtin_amdgcn_permlane16_swap(__float_as_uint(x), __float_as_uint(x), false, false);
  x = fmaxf(__uint_as_float(s[0]), __uint_as_float(s[1]));
  auto t = __builtin_amdgcn_permlane32_swap(__float_as_uint(x), __float_as_uint(x), false, false);
  return fmaxf(__uint_as_float(t[0]), __uint_as_float(t[1]));
}
__device__ __forceinline__ float ror8(float x) {
  return __int_as_float(__builtin_amdgcn_mov_dpp(__float_as_int(x), 0x128, 0xf, 0xf, true));
}
__device__ __forceinline__ v4u widen(v2u a, v2u b) {
  auto s0 = __builtin_amdgcn_permlane16_swap(a.x, b.x, false, false);
  auto s1 = __builtin_amdgcn_permlane16_swap(a.y, b.y, false, false);
  return v4u{s0[0], s1[0], s0[1], s1[1]};
}
__device__ __forceinline__ float gelu_tanh(float x, float c0) {
  const float c1 = GC0 * 0.044715f;
  float t = x * __builtin_fmaf(x * x, c1, c0);
  float y = x * __builtin_amdgcn_rcpf(1.0f + __builtin_amdgcn_exp2f(t));
  OPAQUE(y);
  return y;
}
__device__ __forceinline__ void wait_flags(const unsigned char* ws, int off, bool active, u64 tag) {
  const GAS u64* f = (const GAS u64*)(ws + off);
  asm volatile("" : "+s"(tag));
#pragma unroll 1
  for (int spins = 0; spins < (1 << 16); ++spins) {
    const u64 v = active ? __hip_atomic_load(f, __ATOMIC_RELAXED, __HIP_MEMORY_SCOPE_AGENT) : tag;
    if (__all(v == tag)) break;
    __builtin_amdgcn_s_sleep(1);
  }
  asm volatile("" ::: "memory");
}
__device__ __forceinline__ void set_flag(unsigned char* ws, int off, u64 tag) {
  asm volatile("" : "+s"(tag));
  __hip_atomic_store((GAS u64*)(ws + off), tag, __ATOMIC_RELAXED, __HIP_MEMORY_SCOPE_AGENT);
}
__device__ __forceinline__ void wait_all256(const unsigned char* ws, int phase, int lane, u64 tag) {
  const GAS u64* f = (const GAS u64*)(ws + FLAGS + phase * 4096 + (lane >> 3) * 128 + (lane & 7) * 8);
#pragma unroll 1
  for (int spins = 0; spins < (1 << 16); ++spins) {
    bool ok = true;
#pragma unroll
    for (int i = 0; i < 4; ++i) ok &= __hip_atomic_load(f + i * 128, __ATOMIC_RELAXED, __HIP_MEMORY_SCOPE_AGENT) == tag;
    if (__all(ok)) break;
    __builtin_amdgcn_s_sleep(1);
  }
  asm volatile("" ::: "memory");
}
__device__ __forceinline__ void issue_window(v4u (&win)[WIN], rsrc_t rs, int lane16, int base) {
#pragma unroll
  for (int i = 0; i < WIN; ++i) win[i] = ldf(rs, lane16, base, i);
  SB();
}

template <int NT, int MT, bool SWAP>
__device__ __forceinline__ void gemm18(f32x4 (&acc)[NT][MT], v4u (&win)[WIN], LAS unsigned char* img, rsrc_t rs, int lane16, int wbase) {
  constexpr bool DB = NT < 2;
  v4u act[DB ? 2 : 1][MT];
#pragma unroll
  for (int m = 0; m < MT; ++m) act[0][m] = *(LAS v4u*)(img + m * 18 * KB);
  ACC_IN(acc);
#pragma unroll
  for (int s = 0; s < 18; ++s) {
#pragma unroll
    for (int m = 0; m < MT; ++m) {
      if (DB && s + 1 < 18) { act[(s + 1) & 1][m] = *(LAS v4u*)(img + (m * 18 + s + 1) * KB); SB(); }
#pragma unroll
      for (int t = 0; t < NT; ++t) {
        if (SWAP) MF(acc[t][m], act[DB ? (s & 1) : 0][m], win[(s * NT + t) % WIN]);
        else MF(acc[t][m], win[(s * NT + t) % WIN], act[DB ? (s & 1) : 0][m]);
      }
      SB();
      if (!DB && s + 1 < 18) { act[0][m] = *(LAS v4u*)(img + (m * 18 + s + 1) * KB); SB(); }
    }
#pragma unroll
    for (int t = 0; t < NT; ++t) {
      const int f = s * NT + t;
      if (f + WIN < 18 * NT) win[f % WIN] = ldf(rs, lane16, wbase, f + WIN);
    }
    SB();
  }
  ACC_OUT(acc);
}
template <bool HWAIT>
__device__ __forceinline__ void gemm_mlp(f32x4 (&acc)[3][4], v4u (&win)[WIN], LAS unsigned char* img, rsrc_t rs, int lane16, int rb, int vlast,
                                         int rlast, volatile LAS int* hc) {
  v4u act[4];
  if (HWAIT) { while (__builtin_amdgcn_readfirstlane(hc[0]) < 4) __builtin_amdgcn_s_sleep(1); asm volatile("" ::: "memory"); }
#pragma unroll
  for (int m = 0; m < 4; ++m) act[m] = *(LAS v4u*)(img + m * 18 * KB);
  ACC_IN(acc);
#pragma unroll 1
  for (int c = 0; c < 3; ++c) {
    const int vo = c == 2 ? vlast : lane16, so = c == 2 ? rlast : rb + c * 18 * KB;
#pragma unroll
    for (int s = 0; s < 6; ++s) {
      if (HWAIT && s == 5) {
        while (__builtin_amdgcn_readfirstlane(hc[c + 1]) < 4) __builtin_amdgcn_s_sleep(1);
        asm volatile("" ::: "memory");
        SB();
      }
#pragma unroll
      for (int m = 0; m < 4; ++m) {
#pragma unroll
        for (int t = 0; t < 3; ++t) MF(acc[t][m], win[s * 3 + t], act[m]);
        SB();
        act[m] = *(LAS v4u*)(img + (m * 18 + s + 1) * KB);
        SB();
      }
#pragma unroll
      for (int t = 0; t < 3; ++t) win[s * 3 + t] = ldf(rs, vo, so, s * 3 + t);
      SB();
    }
    img += 6 * KB;
  }
  ACC_OUT(acc);
}

__device__ __forceinline__ void publish_rows(const float (&v)[8], LAS unsigned char* L, rsrc_t rs, int w, int lane, int piece, int xr_off, int y_off) {
  const bool act = lane < 48;
  const v4u xb = {pk(v[0], v[1]), pk(v[2], v[3]), pk(v[4], v[5]), pk(v[6], v[7])};
  float r[8] = {blo(xb.x), bhi(xb.x), blo(xb.y), bhi(xb.y), blo(xb.z), bhi(xb.z), blo(xb.w), bhi(xb.w)};
  float s1 = 0.f, s2 = 0.f;
#pragma unroll
  for (int i = 0; i < 8; ++i) { s1 += r[i]; s2 = __builtin_fmaf(r[i], r[i], s2); OPAQUE(s1); }
  if (!act) { s1 = 0.f; s2 = 0.f; }
  s1 += ror8(s1); s2 += ror8(s2); OPAQUE(s1);
  s1 = rows_sum(s1); s2 = rows_sum(s2);
  if (act) stg(xb, rs, piece, xr_off);
  if (lane < 8) *(LAS f32x2*)(L + L_ST + (w * 8 + lane) * 8) = f32x2{s1, s2};
  BAR();
  float t1 = 0.f, t2 = 0.f;
#pragma unroll
  for (int i = 0; i < 12; ++i) { const f32x2 q = *(LAS f32x2*)(L + L_ST + (i * 8 + (lane & 7)) * 8); t1 += q.x; t2 += q.y; }
  const float mean = t1 * (1.0f / 576.0f);
  const float var = fmaxf(__builtin_fmaf(-mean, mean, t2 * (1.0f / 576.0f)), 0.f);
  const float rstd = __builtin_amdgcn_rsqf(var + EPS);
  const v4u yb = {pk((r[0] - mean) * rstd, (r[1] - mean) * rstd), pk((r[2] - mean) * rstd, (r[3] - mean) * rstd),
                  pk((r[4] - mean) * rstd, (r[5] - mean) * rstd), pk((r[6] - mean) * rstd, (r[7] - mean) * rstd)};
  if (act) stg(yb, rs, piece, y_off);
}

__global__ void __launch_bounds__(NTHR) fused_fwd(Params p) {
  extern __shared__ __attribute__((aligned(16))) unsigned char smem[];
  LAS unsigned char* L = (LAS unsigned char*)smem;
  const int lane0 = threadIdx.x & 63, w0 = __builtin_amdgcn_readfirstlane(threadIdx.x >> 6);
  const int bid = blockIdx.x;
  constexpr int MPX = 8 / XB;
  const int j0 = ((bid & 7) / XB) * (32 / MPX) + (bid >> 3) / MPX;
  const int g0 = ((bid & 7) % XB) * MPX + (bid >> 3) % MPX;
  const rsrc_t rs = __builtin_amdgcn_make_buffer_rsrc((void*)p.ws, 0, WS_BYTES, 0x00020000);
  STAMP(0);
  const u64 tag0 = __hip_atomic_load((const GAS u64*)p.ws, __ATOMIC_RELAXED, __HIP_MEMORY_SCOPE_AGENT);
  u64 tag;
  v4u win[WIN];
  {
    LOCALS();
    OWNROWS();
    v4u xin = {0u, 0u, 0u, 0u};
    if (lane < 48) xin = *(const GAS v4u*)(p.x + (size_t)mytok * 576 + c8 * 8);
    const int qf = j * 12 + w;
    unsigned short qv[8];
    int qdst = 0;
    if (qf < 252) {
      const int tile = qf / 18, s = qf % 18, hb = 4 * g;
      int col;
      bool ok = true;
      if (tile < 4) col = 576 + (hb + tile) * 18 + l15;
      else if (tile < 8) col = 1152 + (hb + tile - 4) * 18 + l15;
      else if (tile == 8) col = ((l15 & 2) ? 576 : 0) + (hb + (l15 >> 2)) * 18 + 16 + (l15 & 1);
      else if (tile == 9) { col = 1152 + (hb + ((l15 >> 1) & 3)) * 18 + 16 + (l15 & 1); ok = l15 < 8; }
      else col = (hb + tile - 10) * 18 + l15;
      const GAS unsigned short* src = (const GAS unsigned short*)p.wqkv + (size_t)(32 * s + 8 * quad) * 1728 + col;
#pragma unroll
      for (int i = 0; i < 8; ++i) { qv[i] = src[i * 1728]; }
      if (!ok) {
#pragma unroll
        for (int i = 0; i < 8; ++i) qv[i] = 0;
      }
      qdst = wb + (tile < 10 ? tile * 18 + s : 180 + ((tile - 10) >> 1) * 36 + s * 2 + (tile & 1)) * KB;
    }
    const int item = bid * 12 + w;
    v4u wv[4];
    int dst0 = 0, kind = 3, cb = 0;
    if (item < 2754) {
      kind = item / 1296;
      const int r = item - kind * 1296, gg = r / 162, s = (r % 162) / 9;
      cb = r % 9;
#pragma unroll
      for (int n = 0; n < 4; ++n) {
        const int kap = n * 8 + (lane >> 3), jj = kap & 7;
        const GAS unsigned short* src;
        if (kind == 0) src = (const GAS unsigned short*)p.w1 + (size_t)(32 * s + kap) * 4608 + 576 * gg;
        else if (kind == 1) {
          const int o = s % 3, f = 96 * (s / 3) + (o < 2 ? 48 * o + 16 * (jj >> 2) : 48 * (jj >> 2) + 32) + 4 * n + (jj & 3);
          src = (const GAS unsigned short*)p.w2 + (size_t)(576 * gg + f) * 576;
        } else {
          const int row = s < 16 ? (2 * s + (jj >> 2)) * 18 + 4 * n + (jj & 3) : (16 * (s - 16) + 4 * n + (jj >> 1)) * 18 + 16 + (jj & 1);
          src = (const GAS unsigned short*)p.wo + (size_t)row * 576;
        }
        wv[n] = *(const GAS v4u*)(src + 64 * cb + (lane & 7) * 8);
      }
      dst0 = kind == 2 ? WOB + s * KB : WBASE + gg * WSL + (252 + 54 * kind + 3 * s) * KB;
    }
    tag = (u64)(unsigned)__builtin_amdgcn_readfirstlane((int)(unsigned)tag0) | ((u64)(unsigned)__builtin_amdgcn_readfirstlane((int)(unsigned)(tag0 >> 32)) << 32);
    tag += 1;
    if (qf < 252) {
      const v4u o = {qv[0] | ((unsigned)qv[1] << 16), qv[2] | ((unsigned)qv[3] << 16), qv[4] | ((unsigned)qv[5] << 16), qv[6] | ((unsigned)qv[7] << 16)};
      stg(o, rs, lane16, qdst);
    }
    {
      const float v[8] = {blo(xin.x), bhi(xin.x), blo(xin.y), bhi(xin.y), blo(xin.z), bhi(xin.z), blo(xin.w), bhi(xin.w)};
      publish_rows(v, L, rs, w, lane, piece, ab + A_XR, ab + A_Y);
    }
    DRAIN();
    BAR();
    if (w == 0 && lane == 0) set_flag(p.ws, FLAGS + 0 * 4096 + j * 128 + g * 8, tag);
    STAMP(1);
    if (item < 2754) {
      LAS unsigned char* T = L + w * 4608;
#pragma unroll
      for (int n = 0; n < 4; ++n) *(LAS v4u*)(T + (n * 8 + (lane >> 3)) * 144 + (lane & 7) * 16) = wv[n];
      LAS unsigned char* R = T + (8 * quad + (l15 >> 2)) * 144 + (lane & 3) * 8;
#pragma unroll
      for (int tt = 0; tt < 4; ++tt) {
        const s16x4 lo = __builtin_amdgcn_ds_read_tr16_b64_v4i16((LAS s16x4*)(R + tt * 32));
        const s16x4 hi = __builtin_amdgcn_ds_read_tr16_b64_v4i16((LAS s16x4*)(R + tt * 32 + 4 * 144));
        const v2u a = __builtin_bit_cast(v2u, lo), b = __builtin_bit_cast(v2u, hi);
        const int ft = 4 * cb + tt;
        const int d = kind == 2 ? dst0 + ft * 18 * KB : dst0 + ((ft / 3) * 108 + ft % 3) * KB;
        stg(v4u{a.x, a.y, b.x, b.y}, rs, lane16, d);
      }
    }
    DRAIN();
    BAR();
    if (w == 0 && lane == 0) set_flag(p.ws, FLAGS + 1 * 4096 + j * 128 + g * 8, tag);
    STAMP(2);
    if (w == 11) wait_all256(p.ws, 0, lane, tag);
    BAR();
    STAMP(3);
    issue_window(win, rs, lane16, QBASE());
  }

#pragma unroll 1
  for (int layer = 0; layer < 2; ++layer) {
    {
      LOCALS();
      const int qbase = QBASE();
      const int yb = ab + A_Y + layer * IMG;
      v4u yr[8];
#pragma unroll
      for (int i = 0; i < 6; ++i) yr[i] = ldg(rs, lane16, yb + (w * 6 + i) * KB);
      const int hoff = (1 + 2 * (l15 >> 3)) * 18 * KB + (quad * 16 + 8 + (lane & 7)) * 16;
      yr[6] = v4u{0u, 0u, 0u, 0u};
      yr[7] = v4u{0u, 0u, 0u, 0u};
      if (j > 0) {
        yr[6] = ldg(rs, hoff, yb - ASL + w * KB);
        if (w < 6) yr[7] = ldg(rs, hoff, yb - ASL + (12 + w) * KB);
      }
#pragma unroll
      for (int i = 0; i < 6; ++i) *(LAS v4u*)(L + (w * 6 + i) * KB + lane16) = yr[i];
      *(LAS v4u*)(L + (72 + w) * KB + lane16) = yr[6];
      if (w < 6) *(LAS v4u*)(L + (84 + w) * KB + lane16) = yr[7];
      BAR();
      STAMP(4 + layer * 16);
      LAS unsigned char* X = L + L_QK + lane * 8;
      if (w < 2) {
        f32x4 acc[2][4];
#pragma unroll
        for (int t = 0; t < 2; ++t)
#pragma unroll
          for (int m = 0; m < 4; ++m) acc[t][m] = f32x4{0.f, 0.f, 0.f, 0.f};
        gemm18<2, 4, false>(acc, win, L + lane16, rs, lane16, qbase);
#pragma unroll
        for (int t = 0; t < 2; ++t)
#pragma unroll
          for (int m = 0; m < 4; ++m)
            *(LAS v2u*)(X + ((10 + 2 * w + t) * 5 + m) * 512) = v2u{pk(acc[t][m][0], acc[t][m][1]), pk(acc[t][m][2], acc[t][m][3])};
      } else {
        f32x4 acc[1][5];
#pragma unroll
        for (int m = 0; m < 5; ++m) acc[0][m] = f32x4{0.f, 0.f, 0.f, 0.f};
        const int tile = w - 2;
        if ((tile >= 4 && tile < 8) || tile == 9) gemm18<1, 5, true>(acc, win, L + lane16, rs, lane16, qbase);
        else gemm18<1, 5, false>(acc, win, L + lane16, rs, lane16, qbase);
#pragma unroll
        for (int m = 0; m < 5; ++m)
          *(LAS v2u*)(X + (tile * 5 + m) * 512) = v2u{pk(acc[0][m][0], acc[0][m][1]), pk(acc[0][m][2], acc[0][m][3])};
      }
      STAMP(5 + layer * 16);
      if (layer == 0 && w == 11) wait_all256(p.ws, 1, lane, tag);
      BAR();
      issue_window(win, rs, lane16, w < 9 ? WOB + ((g & 3) * 9 + w) * 18 * KB : MBASE());
      if (w < 8) {
        const int T = w & 3, hp = w >> 2, Tp = (T & 1) ? T - 1 : 4;
        const v2u kxs = *(LAS v2u*)(X + (40 + T) * 512), kxp = *(LAS v2u*)(X + (40 + Tp) * 512);
        const v2u vxs = *(LAS v2u*)(X + (45 + T) * 512), vxp = *(LAS v2u*)(X + (45 + Tp) * 512);
        bool okp[4], oks[4];
#pragma unroll
        for (int r = 0; r < 4; ++r) {
          const int jj = 4 * quad + r, jh = jj - 8 * (T >> 1);
          okp[r] = (T & 1) ? (jj >= l15 + 9) : (j > 0 && jh < 8 && jh >= l15 + 1);
          oks[r] = jj <= l15 && l15 - jj <= 7;
        }
        const unsigned zr = opq_v(0);
        f32x4 o[2], ox = {0.f, 0.f, 0.f, 0.f};
        const f32x4 z = {0.f, 0.f, 0.f, 0.f};
#pragma unroll
        for (int hh = 0; hh < 2; ++hh) {
          const int h = 2 * hp + hh;
          const v2u q = *(LAS v2u*)(X + ((10 + h) * 5 + T) * 512);
          const v2u ks = *(LAS v2u*)(X + (h * 5 + T) * 512), kp = *(LAS v2u*)(X + (h * 5 + Tp) * 512);
          const v2u vs = *(LAS v2u*)(X + ((4 + h) * 5 + T) * 512), vp = *(LAS v2u*)(X + ((4 + h) * 5 + Tp) * 512);
          const v4u qf = {q.x, q.y, quad == h ? kxs.x : 0u, zr};
          const f32x4 sp = mfma(v4u{kp.x, kp.y, kxp.y, zr}, qf, z), ss = mfma(v4u{ks.x, ks.y, kxs.y, zr}, qf, z);
          float e[8], mx = -3.0e38f;
#pragma unroll
          for (int r = 0; r < 4; ++r) {
            e[r] = okp[r] ? sp[r] : -3.0e38f;
            e[4 + r] = oks[r] ? ss[r] : -3.0e38f;
            mx = fmaxf(mx, fmaxf(e[r], e[4 + r]));
          }
          mx = rows_max(mx);
          const float c = 0.23570226039551584f * 1.4426950408889634f;
          float sum = 0.f;
#pragma unroll
          for (int r = 0; r < 8; ++r) {
            const bool ok = r < 4 ? okp[r] : oks[r - 4];
            e[r] = ok ? __builtin_amdgcn_exp2f((e[r] - mx) * c) : 0.f;
            sum += e[r];
          }
          const float inv = __builtin_amdgcn_rcpf(rows_sum(sum));
          const v4u pf = {pk(e[0] * inv, e[1] * inv), pk(e[2] * inv, e[3] * inv), pk(e[4] * inv, e[5] * inv), pk(e[6] * inv, e[7] * inv)};
          o[hh] = mfma(v4u{vp.x, vp.y, vs.x, vs.y}, pf, z);
          const bool mine = (l15 >> 1) == h;
          ox = mfma(v4u{mine ? vxp.x : 0u, mine ? vxp.y : 0u, mine ? vxs.x : 0u, mine ? vxs.y : 0u}, pf, ox);
        }
        stg(v4u{pk(o[0][0], o[0][1]), pk(o[0][2], o[0][3]), pk(o[1][0], o[1][1]), pk(o[1][2], o[1][3])}, rs, lane16,
            ab + A_O + (T * 18 + 2 * g + hp) * KB);
        if (quad == hp)
          __builtin_amdgcn_raw_buffer_store_b64(v2u{pk(ox[0], ox[1]), pk(ox[2], ox[3])}, rs, l15 * 16,
                                                ab + A_O + (T * 18 + 16 + (g >> 2)) * KB + (g & 3) * 256 + hp * 8, SC1);
        DRAIN();
      }
      STAMP(6 + layer * 16);
      BAR();
      if (w == 11) {
        if (lane == 0) set_flag(p.ws, FLAGS + (2 + 3 * layer) * 4096 + j * 128 + g * 8, tag);
        wait_flags(p.ws, FLAGS + (2 + 3 * layer) * 4096 + j * 128 + (lane & 7) * 8, lane < 8, tag);
      }
      BAR();
      STAMP(7 + layer * 16);
    }
    {
      LOCALS();
      const int th = g >> 2, ft = (g & 3) * 9 + w;
      v4u orr[3];
#pragma unroll
      for (int i = 0; i < 3; ++i) orr[i] = ldg(rs, lane16, ab + A_O + (th * 36 + w * 3 + i) * KB);
      const int xoff = ((quad & 1) * 18 + (ft >> 1)) * KB + ((2 * (ft & 1) + (quad >> 1)) * 16 + l15) * 16;
      v4u xr = {0u, 0u, 0u, 0u};
      if (w < 9) xr = ldg(rs, xoff, ab + A_XR + th * 36 * KB);
#pragma unroll
      for (int i = 0; i < 3; ++i) *(LAS v4u*)(L + (w * 3 + i) * KB + lane16) = orr[i];
      BAR();
      STAMP(8 + layer * 16);
      if (w < 9) {
        f32x4 acc[1][2] = {{{0.f, 0.f, 0.f, 0.f}, {0.f, 0.f, 0.f, 0.f}}};
        gemm18<1, 2, false>(acc, win, L + lane16, rs, lane16, 0);
        float a[8];
#pragma unroll
        for (int r = 0; r < 4; ++r) {
          auto s = __builtin_amdgcn_permlane16_swap(__float_as_uint(acc[0][0][r]), __float_as_uint(acc[0][1][r]), false, false);
          a[r] = __uint_as_float(s[0]);
          a[4 + r] = __uint_as_float(s[1]);
        }
        const v4u o = {pk(a[0] + blo(xr.x), a[1] + bhi(xr.x)), pk(a[2] + blo(xr.y), a[3] + bhi(xr.y)),
                       pk(a[4] + blo(xr.z), a[5] + bhi(xr.z)), pk(a[6] + blo(xr.w), a[7] + bhi(xr.w))};
        stg(o, rs, xoff, ab + A_X1 + th * 36 * KB);
        SB();
        issue_window(win, rs, lane16, MBASE());
        asm volatile("s_waitcnt vmcnt(%0)" :: "i"(WIN) : "memory");
      }
      STAMP(9 + layer * 16);
      BAR();
      if (w == 11) {
        if (lane == 0) set_flag(p.ws, FLAGS + (3 + 3 * layer) * 4096 + j * 128 + g * 8, tag);
        wait_flags(p.ws, FLAGS + (3 + 3 * layer) * 4096 + j * 128 + (lane & 7) * 8, lane < 8, tag);
      }
      BAR();
      STAMP(10 + layer * 16);
    }
    {
      LOCALS();
      const int mbase = MBASE();
      const int T = w & 3, cls = w >> 2;
      volatile LAS int* hc = (volatile LAS int*)(L + L_HC);
      {
        v4u xr[6];
#pragma unroll
        for (int i = 0; i < 6; ++i) xr[i] = ldg(rs, lane16, ab + A_X1 + (T * 18 + 6 * cls + i) * KB);
        if (w == 0 && lane < 4) hc[lane] = lane == 3 ? 4 : 0;
        float s1 = 0.f, s2 = 0.f;
#pragma unroll
        for (int i = 0; i < 6; ++i)
#pragma unroll
          for (int e = 0; e < 4; ++e) {
            const float lo = blo(xr[i][e]), hi = bhi(xr[i][e]);
            s1 += lo + hi;
            s2 = __builtin_fmaf(lo, lo, __builtin_fmaf(hi, hi, s2));
          }
        s1 = rows_sum(s1); s2 = rows_sum(s2);
        if (lane < 16) *(LAS f32x2*)(L + L_ST + (cls * 64 + T * 16 + lane) * 8) = f32x2{s1, s2};
        BAR();
        float t1 = 0.f, t2 = 0.f;
#pragma unroll
        for (int i = 0; i < 3; ++i) { const f32x2 q = *(LAS f32x2*)(L + L_ST + (i * 64 + T * 16 + l15) * 8); t1 += q.x; t2 += q.y; }
        const float mean = t1 * (1.0f / 576.0f);
        const float var = fmaxf(__builtin_fmaf(-mean, mean, t2 * (1.0f / 576.0f)), 0.f);
        const float r1 = __builtin_amdgcn_rsqf(var + EPS);
        const float r2 = __builtin_amdgcn_rsqf(var * r1 * r1 + EPS);
        const float sc = r1 * r2, of = -mean * sc;
#pragma unroll
        for (int i = 0; i < 6; ++i) {
          v4u o;
#pragma unroll
          for (int e = 0; e < 4; ++e) o[e] = pk(__builtin_fmaf(blo(xr[i][e]), sc, of), __builtin_fmaf(bhi(xr[i][e]), sc, of));
          *(LAS v4u*)(L + (T * 18 + 6 * cls + i) * KB + lane16) = o;
        }
      }
      f32x4 acc[3][4];
#pragma unroll
      for (int t = 0; t < 3; ++t) {
        const v2u b = *(const GAS v2u*)(p.b1 + 576 * g + 48 * w + 16 * t + 4 * quad);
#pragma unroll
        for (int m = 0; m < 4; ++m) acc[t][m] = f32x4{blo(b.x), bhi(b.x), blo(b.y), bhi(b.y)};
      }
      BAR();
      STAMP(11 + layer * 16);
      __builtin_amdgcn_s_setprio(3);
      gemm_mlp<false>(acc, win, L + lane16, rs, lane16, mbase + 18 * KB, lane16, mbase + 54 * KB, nullptr);
      STAMP(12 + layer * 16);
      {
        LAS unsigned char* H = L + L_H + lane16 + 3 * (w >> 1) * KB;
        const float c0 = __int_as_float(opq_v(__float_as_int(GC0)));
#pragma unroll
        for (int m = 0; m < 4; ++m) {
          unsigned u[6];
#pragma unroll
          for (int t = 0; t < 3; ++t) {
            u[2 * t] = pk(gelu_tanh(acc[t][m][0], c0), gelu_tanh(acc[t][m][1], c0));
            u[2 * t + 1] = pk(gelu_tanh(acc[t][m][2], c0), gelu_tanh(acc[t][m][3], c0));
          }
          *(LAS v4u*)(H + (m * 18 + (w & 1)) * KB) = v4u{u[0], u[1], u[2], u[3]};
          *(LAS v2u*)(H + (m * 18 + 2) * KB + (w & 1) * 8) = v2u{u[4], u[5]};
        }
        asm volatile("s_waitcnt lgkmcnt(0)" ::: "memory");
        if (lane == 0) __hip_atomic_fetch_add((LAS int*)(L + L_HC) + cls, 1, __ATOMIC_RELAXED, __HIP_MEMORY_SCOPE_WORKGROUP);
      }
      STAMP(13 + layer * 16);
      if (cls == 0) __builtin_amdgcn_s_setprio(2);
      else if (cls == 1) __builtin_amdgcn_s_setprio(1);
      else __builtin_amdgcn_s_setprio(0);
#pragma unroll
      for (int t = 0; t < 3; ++t)
#pragma unroll
        for (int m = 0; m < 4; ++m) acc[t][m] = f32x4{0.f, 0.f, 0.f, 0.f};
      gemm_mlp<true>(acc, win, L + L_H + lane16, rs, lane16, mbase + 72 * KB, lane16, QBASE(), hc);
      __builtin_amdgcn_s_setprio(0);
      STAMP(14 + layer * 16);
      const int pw = ab + A_PT + (g * 12 + w) * 6144;
      const int poff = (quad & 1) * 1536 + (quad >> 1) * 256 + l15 * 16;
#pragma unroll
      for (int t = 0; t < 3; ++t)
#pragma unroll
        for (int m = 0; m < 4; m += 2) {
          const v2u a = {pk(acc[t][m][0], acc[t][m][1]), pk(acc[t][m][2], acc[t][m][3])};
          const v2u b = {pk(acc[t][m + 1][0], acc[t][m + 1][1]), pk(acc[t][m + 1][2], acc[t][m + 1][3])};
          stg(widen(a, b), rs, poff + m * 1536 + t * 512, pw);
        }
      DRAIN();
      if (lane == 0) set_flag(p.ws, WFLAGS + ((layer * 32 + j) * 12 + w) * 128 + g * 8, tag);
      STAMP(15 + layer * 16);
    }
    {
      LOCALS();
      OWNROWS();
      const bool act = lane < 48;
      v4u xr = {0u, 0u, 0u, 0u}, bb = {0u, 0u, 0u, 0u};
      if (act) {
        xr = ldg(rs, piece, ab + A_X1);
        bb = *(const GAS v4u*)(p.b2 + c8 * 8);
      }
      wait_flags(p.ws, WFLAGS + ((layer * 32 + j) * 12 + w) * 128 + (lane & 7) * 8, lane < 8, tag);
      STAMP(16 + layer * 16);
      float v[8];
      {
        v4u pr[8];
        const int roff = (g >> 1) * 1536 + (lane >> 3) * 256 + (8 * (g & 1) + (lane & 7)) * 16;
#pragma unroll
        for (int i = 0; i < 8; ++i) pr[i] = act ? ldg(rs, roff, ab + A_PT + (i * 12 + w) * 6144) : v4u{0u, 0u, 0u, 0u};
        SB();
#pragma unroll
        for (int e = 0; e < 4; ++e) {
          float lo = blo(bb[e]), hi = bhi(bb[e]);
#pragma unroll
          for (int i = 0; i < 8; ++i) { lo += blo(pr[i][e]); hi += bhi(pr[i][e]); OPAQUE(lo); }
          v[2 * e] = lo + blo(xr[e]);
          v[2 * e + 1] = hi + bhi(xr[e]);
          OPAQUE(v[2 * e]);
        }
      }
      STAMP(17 + layer * 16);
      if (layer == 0) {
        publish_rows(v, L, rs, w, lane, piece, ab + A_XR, ab + A_Y + IMG);
        DRAIN();
        BAR();
        if (w == 0) {
          if (lane == 0) set_flag(p.ws, FLAGS + 4 * 4096 + j * 128 + g * 8, tag);
          wait_flags(p.ws, FLAGS + 4 * 4096 + (j - (lane >> 3)) * 128 + (lane & 7) * 8, lane < (j > 0 ? 16 : 8), tag);
        }
        BAR();
        STAMP(18);
      } else {
        if (act) *(GAS v4u*)(p.out + (size_t)mytok * 576 + c8 * 8) = v4u{pk(v[0], v[1]), pk(v[2], v[3]), pk(v[4], v[5]), pk(v[6], v[7])};
      }
    }
  }
  if (bid == 0 && w0 == 0 && lane0 == 0) set_flag(p.ws, 0, tag);
  STAMP(40);
}
}

extern "C" void kernel_launch(void* const* d_in, const int* in_sizes, int n_in, void* d_out, int out_size, void* d_ws, size_t ws_size,
                              hipStream_t stream) {
  (void)in_sizes; (void)n_in; (void)out_size; (void)ws_size;
  Params p;
  p.x = (const unsigned short*)d_in[0];
  p.wqkv = (const unsigned short*)d_in[1];
  p.wo = (const unsigned short*)d_in[2];
  p.w1 = (const unsigned short*)d_in[4];
  p.b1 = (const unsigned short*)d_in[5];
  p.w2 = (const unsigned short*)d_in[6];
  p.b2 = (const unsigned short*)d_in[7];
  p.out = (unsigned short*)d_out;
  p.ws = (unsigned char*)d_ws;
  (void)hipFuncSetAttribute((const void*)fused_fwd, hipFuncAttributeMaxDynamicSharedMemorySize, LDS_BYTES);
  hipLaunchKernelGGL(fused_fwd, dim3(256), dim3(NTHR), LDS_BYTES, stream, p);
}
```

```cpp
#define XB 4
#define TAILMODE 2
#define TMAJOR
#define NTW 1
#define WSEND 1
#define P0LATE
#include <hip/hip_runtime.h>
#include <hip/hip_bf16.h>
#include <stdint.h>

namespace {
#define LAS __attribute__((address_space(3)))
#define GAS __attribute__((address_space(1)))
typedef unsigned v4u __attribute__((ext_vector_type(4)));
typedef unsigned v2u __attribute__((ext_vector_type(2)));
typedef float f32x4 __attribute__((ext_vector_type(4)));
typedef float f32x2 __attribute__((ext_vector_type(2)));
typedef __bf16 bf16x8 __attribute__((ext_vector_type(8)));
typedef __bf16 bf16x2 __attribute__((ext_vector_type(2)));
typedef short s16x4 __attribute__((ext_vector_type(4)));
typedef unsigned long long u64;
typedef __amdgpu_buffer_rsrc_t rsrc_t;

constexpr int KB = 1024;
constexpr int NTHR = 768;
constexpr int WIN = 18;
#define P2N 9
#define POLLSLEEP 1
#define QLATE 0
#define PRIO_G1 3
#define PRIO_GELU 3
constexpr float EPS = 1e-5f;
constexpr int SC1 = 16;
constexpr float GC0 = -2.0f * 0.7978845608028654f * 1.4426950408889634f;
constexpr int FLAGS = 4096;
constexpr int WFLAGS = 64 * KB;
constexpr int WBASE = 256 * KB;
constexpr int WSL = (252 + 1296) * KB;
constexpr int WOB = WBASE + 8 * WSL;
constexpr int ABASE = 14 * 1024 * KB;
constexpr int IMG = 72 * KB;
constexpr int A_Y = 0, A_XR = IMG, A_O = 2 * IMG, A_X1 = 3 * IMG, A_PT = 4 * IMG, ALY = 1024 * KB, ASL = 2 * ALY;
constexpr int WS_BYTES = ABASE + 32 * ASL;
static_assert(WOB + 648 * KB <= ABASE && 12 * IMG <= ALY, "workspace map");
constexpr int L_H = IMG;
constexpr int L_QK = 90 * KB;
constexpr int L_ST = 144 * KB;
constexpr int L_HC = 148 * KB;
constexpr int LDS_BYTES = 149 * KB;

struct Params {
  const unsigned short *x, *wqkv, *wo, *w1, *b1, *w2, *b2;
  unsigned short *out;
  unsigned char *ws;
};

#define STAMP(i)
#define STAMP_INIT()
#define STAMP_DUMP()
#define LDS_EXTRA 0
#define SB() __builtin_amdgcn_sched_barrier(0)
#define BAR() do { asm volatile("s_waitcnt lgkmcnt(0)" ::: "memory"); __builtin_amdgcn_s_barrier(); asm volatile("" ::: "memory"); } while (0)
#define DRAIN() asm volatile("s_waitcnt vmcnt(0)" ::: "memory")
#define OPAQUE(x) asm("" : "+v"(x))
#define KEEP(x) asm volatile("" :: "v"(x))

__device__ __forceinline__ f32x4 mfma(v4u a, v4u b, f32x4 c) {
  return __builtin_amdgcn_mfma_f32_16x16x32_bf16(__builtin_bit_cast(bf16x8, a), __builtin_bit_cast(bf16x8, b), c, 0, 0, 0);
}
#define MF(acc, a, b) asm volatile("v_mfma_f32_16x16x32_bf16 %0, %1, %2, %0" : "+v"(acc) : "v"(a), "v"(b))
template <int NT, int MT> __device__ __forceinline__ void acc_fence(f32x4 (&acc)[NT][MT]) {
#pragma unroll
  for (int t = 0; t < NT; ++t)
#pragma unroll
    for (int m = 0; m < MT; ++m) asm volatile("" : "+v"(acc[t][m]));
}
#define ACC_IN(acc) do { acc_fence(acc); asm volatile("s_nop 1"); SB(); } while (0)
#define ACC_OUT(acc) do { SB(); asm volatile("s_nop 7\n\ts_nop 1"); acc_fence(acc); } while (0)
__device__ __forceinline__ unsigned pk(float a, float b) {
  f32x2 v = {a, b};
  return __builtin_bit_cast(unsigned, __builtin_convertvector(v, bf16x2));
}
__device__ __forceinline__ float blo(unsigned u) { return __uint_as_float(u << 16); }
__device__ __forceinline__ float bhi(unsigned u) { return __uint_as_float(u & 0xffff0000u); }
__device__ __forceinline__ v4u ldg(rsrc_t rs, int voff, int soff) { return __builtin_amdgcn_raw_buffer_load_b128(rs, voff, soff, SC1); }
__device__ __forceinline__ void stg(v4u v, rsrc_t rs, int voff, int soff) { __builtin_amdgcn_raw_buffer_store_b128(v, rs, voff + soff, 0, SC1); }
__device__ __forceinline__ v4u ldf(rsrc_t rs, int lane16, int base, int f) { return ldg(rs, lane16 + (f & 3) * KB, base + (f >> 2) * 4 * KB); }
__device__ __forceinline__ int opq_v(int x) { asm volatile("" : "+v"(x)); return x; }
__device__ __forceinline__ int opq_s(int x) { asm volatile("" : "+s"(x)); return x; }
#define LOCALS() const int lane = opq_v(lane0), w = opq_s(w0), j = opq_s(j0), g = opq_s(g0); \
  const int lane16 = lane * 16, quad = lane >> 4, l15 = lane & 15, ab = ABASE + j * ASL + layer * ALY, wb = WBASE + g * WSL; \
  (void)lane16; (void)quad; (void)l15; (void)ab; (void)wb
#define OWNROWS(CPW) const int c8 = CPW * w + (lane >> 3); \
  const int piece = ((g >> 1) * 18 + (c8 >> 2)) * KB + ((c8 & 3) * 16 + 8 * (g & 1) + (lane & 7)) * 16; \
  const int mytok = 64 * j + 2 * (16 * ((g >> 1) & 1) + 8 * (g & 1) + (lane & 7)) + (g >> 2)
#define GET_TAG() tag = 1 + ((u64)(unsigned)__builtin_amdgcn_readfirstlane((int)(unsigned)tag0) | \
                             ((u64)(unsigned)__builtin_amdgcn_readfirstlane((int)(unsigned)(tag0 >> 32)) << 32))
#define QBASE() (wb + (w < 2 ? 180 + w * 36 : (w - 2) * 18) * KB)
#define MBASE() (wb + (252 + w * 108) * KB)
__device__ __forceinline__ float rows_sum(float x) {
  auto s = __builtin_amdgcn_permlane16_swap(__float_as_uint(x), __float_as_uint(x), false, false);
  x = __uint_as_float(s[0]) + __uint_as_float(s[1]);
  auto t = __builtin_amdgcn_permlane32_swap(__float_as_uint(x), __float_as_uint(x), false, false);
  return __uint_as_float(t[0]) + __uint_as_float(t[1]);
}
__device__ __forceinline__ float rows_max(float x) {
  auto s = __builtin_amdgcn_permlane16_swap(__float_as_uint(x), __float_as_uint(x), false, false);
  x = fmaxf(__uint_as_float(s[0]), __uint_as_float(s[1]));
  auto t = __builtin_amdgcn_permlane32_swap(__float_as_uint(x), __float_as_uint(x), false, false);
  return fmaxf(__uint_as_float(t[0]), __uint_as_float(t[1]));
}
__device__ __forceinline__ float ror8(float x) {
  return __int_as_float(__builtin_amdgcn_mov_dpp(__float_as_int(x), 0x128, 0xf, 0xf, true));
}
__device__ __forceinline__ v4u widen(v2u a, v2u b) {
  auto s0 = __builtin_amdgcn_permlane16_swap(a.x, b.x, false, false);
  auto s1 = __builtin_amdgcn_permlane16_swap(a.y, b.y, false, false);
  return v4u{s0[0], s1[0], s0[1], s1[1]};
}
__device__ __forceinline__ float gelu_tanh(float x, float c0) {
  const float c1 = GC0 * 0.044715f;
  float t = x * __builtin_fmaf(x * x, c1, c0);
  float y = x * __builtin_amdgcn_rcpf(1.0f + __builtin_amdgcn_exp2f(t));
  OPAQUE(y);
  return y;
}
template <int SLEEP = 1> __device__ __forceinline__ void wait_flags(const unsigned char* ws, int off, bool active, u64 tag) {
  const GAS u64* f = (const GAS u64*)(ws + off);
  asm volatile("" : "+s"(tag));
#pragma unroll 1
  for (int spins = 0; spins < (1 << 16); ++spins) {
    const u64 v = active ? __hip_atomic_load(f, __ATOMIC_RELAXED, __HIP_MEMORY_SCOPE_AGENT) : tag;
    if (__all(v == tag)) break;
    __builtin_amdgcn_s_sleep(SLEEP);
  }
  asm volatile("" ::: "memory");
}
__device__ __forceinline__ void set_flag(unsigned char* ws, int off, u64 tag) {
  asm volatile("" : "+s"(tag));
  __hip_atomic_store((GAS u64*)(ws + off), tag, __ATOMIC_RELAXED, __HIP_MEMORY_SCOPE_AGENT);
}
__device__ __forceinline__ void wait_all256(const unsigned char* ws, int phase, int lane, u64 tag) {
  const GAS u64* f = (const GAS u64*)(ws + FLAGS + phase * 4096 + (lane >> 3) * 128 + (lane & 7) * 8);
#pragma unroll 1
  for (int spins = 0; spins < (1 << 16); ++spins) {
    bool ok = true;
#pragma unroll
    for (int i = 0; i < 4; ++i) ok &= __hip_atomic_load(f + i * 128, __ATOMIC_RELAXED, __HIP_MEMORY_SCOPE_AGENT) == tag;
    if (__all(ok)) break;
    __builtin_amdgcn_s_sleep(1);
  }
  asm volatile("" ::: "memory");
}
template <int LO, int HI> __device__ __forceinline__ void issue_window(v4u (&win)[WIN], rsrc_t rs, int lane16, int base) {
#pragma unroll
  for (int i = LO; i < HI; ++i) win[i] = ldf(rs, lane16, base, i);
  SB();
}

template <int NT, int MT, bool SWAP, int NNEXT, int R>
__device__ __forceinline__ void gemm18(f32x4 (&acc)[NT][MT], v4u (&win)[WIN], LAS unsigned char* img, rsrc_t rs, int lane16, int wbase, int nbase) {
  v4u act[R];
#pragma unroll
  for (int n = 0; n < R - 1; ++n) { act[n] = *(LAS v4u*)(img + ((n % MT) * 18 + n / MT) * KB); SB(); }
  ACC_IN(acc);
#pragma unroll
  for (int n = 0; n <= 18 * MT; ++n) {
    const int s = n / MT, m = n % MT, nn = n + R - 1;
    if (n < 18 * MT) {
#pragma unroll
      for (int t = 0; t < NT; ++t) {
        if (SWAP) MF(acc[t][m], act[n % R], win[(s * NT + t) % WIN]);
        else MF(acc[t][m], win[(s * NT + t) % WIN], act[n % R]);
      }
      SB();
    }
    if (nn < 18 * MT) act[nn % R] = *(LAS v4u*)(img + ((nn % MT) * 18 + nn / MT) * KB);
    if (m == 0 && s > 0) {
#pragma unroll
      for (int t = 0; t < NT; ++t) {
        const int f = (s - 1) * NT + t;
        if (f + WIN < 18 * NT) win[f % WIN] = ldf(rs, lane16, wbase, f + WIN);
        else if (f % WIN < NNEXT) win[f % WIN] = ldf(rs, lane16, nbase, f % WIN);
      }
    }
    SB();
    if (n < 18 * MT) {
      KEEP(act[n % R]);
#pragma unroll
      for (int t = 0; t < NT; ++t) KEEP(win[(s * NT + t) % WIN]);
      SB();
    }
  }
  ACC_OUT(acc);
}
template <bool HWAIT>
__device__ __forceinline__ void gemm_mlp(f32x4 (&acc)[3][4], v4u (&win)[WIN], LAS unsigned char* img, rsrc_t rs, int lane16, int rb, int vlast,
                                         int rlast, volatile LAS int* hc) {
  constexpr int R = 8;
  v4u act[R];
  if (HWAIT) { while (__builtin_amdgcn_readfirstlane(hc[0]) < 4) __builtin_amdgcn_s_sleep(1); asm volatile("" ::: "memory"); }
#pragma unroll
  for (int n = 0; n < (R == 8 ? 4 : R - 1); ++n) { act[n] = *(LAS v4u*)(img + ((n & 3) * 18 + (n >> 2)) * KB); SB(); }
  ACC_IN(acc);
#pragma unroll 1
  for (int c = 0; c < 3; ++c) {
    const int vo = c == 2 ? vlast : lane16, so = c == 2 ? rlast : rb + 18 * KB;
    int hv = 4;
#pragma unroll
    for (int s = 0; s < 6; ++s) {
      if (HWAIT && s == 5) {
        if (__builtin_amdgcn_readfirstlane(hv) < 4) { while (__builtin_amdgcn_readfirstlane(hc[c + 1]) < 4) __builtin_amdgcn_s_sleep(1); }
        asm volatile("" ::: "memory");
        SB();
      }
#pragma unroll
      for (int t = 0; t < 3; ++t)
#pragma unroll
        for (int m = 0; m < 4; ++m) {
          MF(acc[t][m], win[s * 3 + t], act[(s & 1) * 4 + m]);
          SB();
          if (t == 0) act[((s + 1) & 1) * 4 + m] = *(LAS v4u*)(img + (m * 18 + s + 1) * KB);
          if (HWAIT && s == 3 && t == 2 && m == 0) hv = hc[c + 1];
          if (t == 1 && m < 3) {
            if (s == 0) win[15 + m] = ldf(rs, lane16, rb, 15 + m);
            else win[(s - 1) * 3 + m] = ldf(rs, vo, so, (s - 1) * 3 + m);
          }
          SB();
        }
    }
    img += 6 * KB;
    rb += 18 * KB;
  }
  ACC_OUT(acc);
}

template <int NW>
__device__ __forceinline__ void publish_rows(const float (&v)[8], bool act, LAS unsigned char* L, rsrc_t rs, int w, int lane, int piece, int xr_off, int y_off) {
  const v4u xb = {pk(v[0], v[1]), pk(v[2], v[3]), pk(v[4], v[5]), pk(v[6], v[7])};
  float r[8] = {blo(xb.x), bhi(xb.x), blo(xb.y), bhi(xb.y), blo(xb.z), bhi(xb.z), blo(xb.w), bhi(xb.w)};
  float s1 = 0.f, s2 = 0.f;
#pragma unroll
  for (int i = 0; i < 8; ++i) { s1 += r[i]; s2 = __builtin_fmaf(r[i], r[i], s2); OPAQUE(s1); }
  if (!act) { s1 = 0.f; s2 = 0.f; }
  s1 += ror8(s1); s2 += ror8(s2); OPAQUE(s1);
  s1 = rows_sum(s1); s2 = rows_sum(s2);
  if (act) stg(xb, rs, piece, xr_off);
  if (lane < 8) *(LAS f32x2*)(L + L_ST + (w * 8 + lane) * 8) = f32x2{s1, s2};
  BAR();
  float t1 = 0.f, t2 = 0.f;
#pragma unroll
  for (int i = 0; i < NW; ++i) { const f32x2 q = *(LAS f32x2*)(L + L_ST + (i * 8 + (lane & 7)) * 8); t1 += q.x; t2 += q.y; }
  const float mean = t1 * (1.0f / 576.0f);
  const float var = fmaxf(__builtin_fmaf(-mean, mean, t2 * (1.0f / 576.0f)), 0.f);
  const float rstd = __builtin_amdgcn_rsqf(var + EPS);
  const v4u yb = {pk((r[0] - mean) * rstd, (r[1] - mean) * rstd), pk((r[2] - mean) * rstd, (r[3] - mean) * rstd),
                  pk((r[4] - mean) * rstd, (r[5] - mean) * rstd), pk((r[6] - mean) * rstd, (r[7] - mean) * rstd)};
  if (act) stg(yb, rs, piece, y_off);
}

__device__ __forceinline__ void load_y(v4u (&yr)[8], rsrc_t rs, int w, int lane, int j, int yb) {
#pragma unroll
  for (int i = 0; i < 6; ++i) yr[i] = ldf(rs, lane * 16, yb + w * 6 * KB, i);
  const int hoff = (1 + ((lane >> 2) & 2)) * 18 * KB + ((lane >> 4) * 16 + 8 + (lane & 7)) * 16;
  const int hb = j > 0 ? yb - ASL : yb;
  yr[6] = ldg(rs, hoff, hb + w * KB);
  yr[7] = ldg(rs, hoff, hb + (12 + (w < 6 ? w : 0)) * KB);
  SB();
}
__device__ __forceinline__ void store_y(const v4u (&yr)[8], LAS unsigned char* L, int w, int lane) {
#pragma unroll
  for (int i = 0; i < 6; ++i) *(LAS v4u*)(L + (w * 6 + i) * KB + lane * 16) = yr[i];
  *(LAS v4u*)(L + (72 + w) * KB + lane * 16) = yr[6];
  if (w < 6) *(LAS v4u*)(L + (84 + w) * KB + lane * 16) = yr[7];
}

__global__ void __launch_bounds__(NTHR) fused_fwd(Params p) {
  extern __shared__ __attribute__((aligned(16))) unsigned char smem[];
  LAS unsigned char* L = (LAS unsigned char*)smem;
  const int lane0 = threadIdx.x & 63, w0 = __builtin_amdgcn_readfirstlane(threadIdx.x >> 6);
  const int bid = blockIdx.x;
  constexpr int MPX = 8 / XB;
  const int j0 = ((bid & 7) / XB) * (32 / MPX) + (bid >> 3) / MPX;
  const int g0 = ((bid & 7) % XB) * MPX + (bid >> 3) % MPX;
  const rsrc_t rs = __builtin_amdgcn_make_buffer_rsrc((void*)p.ws, 0, WS_BYTES, 0x00020000);
  STAMP_INIT();
  STAMP(0);
  const u64 tag0 = __hip_atomic_load((const GAS u64*)p.ws, __ATOMIC_RELAXED, __HIP_MEMORY_SCOPE_AGENT);
  u64 tag = 0;
  v4u win[WIN];
  {
    constexpr int layer = 0;
    LOCALS();
    OWNROWS(6);
    LAS unsigned char* T = L + w * 4608;
    const v4u xin = *(const GAS v4u*)(p.x + (size_t)mytok * 576 + (lane < 48 ? c8 : 0) * 8);
    const int qf = 32 * w + j, qc = qf < 252 ? qf : 0;
    const int tile = qc / 18, qs = qc - tile * 18;
    const bool ex = (tile & 14) == 8, vx = tile == 9;
    v4u qv;
    if (w < 8) {
      const int c0 = 72 * g + (ex ? 16 : ((tile + (tile >= 10 ? 2 : 0)) & 3) * 18) + (tile < 4 ? 576 : (tile < 8 || vx) ? 1152 : 0);
      const int cA = ex ? (vx ? 18 : 576) : 2, cB = ex ? (vx ? 36 : 18) : 4, cC = ex ? (vx ? 0 : 36) : 8;
      const GAS unsigned short* src = (const GAS unsigned short*)p.wqkv + (32 * qs + (lane >> 1)) * 1728 + c0 + (lane & 1) * cC;
      qv = v4u{*(const GAS unsigned*)(src), *(const GAS unsigned*)(src + cA), *(const GAS unsigned*)(src + cB), *(const GAS unsigned*)(src + cA + cB)};
    }
    SB();
    const int item = w < 11 ? bid * 11 + w : 9999, itc = item < 2754 ? item : 0;
    const int rbk = itc / 9, cb = itc - rbk * 9, kind = rbk / 144, ir = rbk - kind * 144, gg = ir / 18, is = ir - gg * 18, o = is % 3;
    v4u wv[4];
    {
      const float v[8] = {blo(xin.x), bhi(xin.x), blo(xin.y), bhi(xin.y), blo(xin.z), bhi(xin.z), blo(xin.w), bhi(xin.w)};
      publish_rows<12>(v, lane < 48, L, rs, w, lane, piece, ab + A_XR, ab + A_Y);
    }
    if (qf < 252) {
      *(LAS v4u*)(T + (lane >> 1) * 48 + (lane & 1) * 16) = (vx && (lane & 1)) ? v4u{0u, 0u, 0u, 0u} : qv;
      LAS unsigned char* R = T + (8 * quad + (l15 >> 2)) * 48 + (lane & 3) * 8;
      const v2u a = __builtin_bit_cast(v2u, __builtin_amdgcn_ds_read_tr16_b64_v4i16((LAS s16x4*)(R)));
      const v2u b = __builtin_bit_cast(v2u, __builtin_amdgcn_ds_read_tr16_b64_v4i16((LAS s16x4*)(R + 4 * 48)));
      stg(v4u{a.x, a.y, b.x, b.y}, rs, lane16, wb + (tile < 10 ? tile * 18 + qs : 180 + ((tile - 10) >> 1) * 36 + qs * 2 + (tile & 1)) * KB);
    }
    STAMP(45);
    DRAIN();
    STAMP(46);
    BAR();
    GET_TAG();
    if (w == 11 && lane == 0) set_flag(p.ws, FLAGS + 0 * 4096 + j * 128 + g * 8, tag);
    STAMP(1);
    if (item < 2754) {
      const bool ox = kind == 2 && is >= 16;
      const int r0 = kind == 0 ? 32 * is : kind == 1 ? 576 * gg + 32 * (is - o) + (o < 2 ? 48 * o : 32) : ox ? 288 * (is - 16) + 16 : 36 * is;
      const int rn = kind == 0 ? 8 : ox ? 72 : 4, r2 = ox ? 18 : 2;
      const int r4 = kind == 0 ? 4 : kind == 1 ? (o < 2 ? 16 : 48) : ox ? 36 : 18;
      const int ld = kind == 0 ? 4608 : 576;
      const unsigned short* mat = kind == 0 ? p.w1 + 576 * gg : kind == 1 ? p.w2 : p.wo;
      const int jj = lane >> 3;
      const GAS unsigned short* src = (const GAS unsigned short*)mat + (r0 + (jj & 1) + ((jj >> 1) & 1) * r2 + (jj >> 2) * r4) * ld + 64 * cb + (lane & 7) * 8;
#pragma unroll
      for (int n = 0; n < 4; ++n) wv[n] = NTW ? __builtin_nontemporal_load((const GAS v4u*)(src + n * rn * ld)) : *(const GAS v4u*)(src + n * rn * ld);
    }
    SB();
    STAMP(41);
    if (item < 2754) {
      const int dst0 = kind == 2 ? WOB + is * KB : WBASE + gg * WSL + (252 + 54 * kind + 3 * is) * KB;
#pragma unroll
      for (int n = 0; n < 4; ++n) *(LAS v4u*)(T + (n * 8 + (lane >> 3)) * 144 + (lane & 7) * 16) = wv[n];
      LAS unsigned char* R = T + (8 * quad + (l15 >> 2)) * 144 + (lane & 3) * 8;
#pragma unroll
      for (int tt = 0; tt < 4; ++tt) {
        const v2u a = __builtin_bit_cast(v2u, __builtin_amdgcn_ds_read_tr16_b64_v4i16((LAS s16x4*)(R + tt * 32)));
        const v2u b = __builtin_bit_cast(v2u, __builtin_amdgcn_ds_read_tr16_b64_v4i16((LAS s16x4*)(R + tt * 32 + 4 * 144)));
        const int ft = 4 * cb + tt;
        stg(v4u{a.x, a.y, b.x, b.y}, rs, lane16, dst0 + (kind == 2 ? ft * 18 : (ft / 3) * 105 + ft) * KB);
      }
    }
    STAMP(47);
    DRAIN();
    if (w == 11) wait_all256(p.ws, 0, lane, tag);
    STAMP(2);
    BAR();
    if (w == 0 && lane == 0) set_flag(p.ws, FLAGS + 1 * 4096 + j * 128 + g * 8, tag);
    STAMP(3);
    v4u yr[8];
    load_y(yr, rs, w, lane, j, ab + A_Y);
    issue_window<0, WIN>(win, rs, lane16, QBASE());
    store_y(yr, L, w, lane);
    if (w == 11) wait_all256(p.ws, 1, lane, tag);
  }

#pragma unroll 1
  for (int layer = 0; layer < 2; ++layer) {
    {
      LOCALS();
      const int qbase = QBASE();
      const int nbase = w < 9 ? WOB + ((g & 3) * 9 + w) * 18 * KB : MBASE();
      BAR();
      STAMP(4 + layer * 16);
      LAS unsigned char* X = L + L_QK + lane * 8;
      const int tile = w - 2, role = w < 2 ? 0 : ((tile >= 4 && tile < 8) || tile == 9) ? 1 : 2;
      if (opq_s(role) == 0) {
        f32x4 acc[2][4];
#pragma unroll
        for (int t = 0; t < 2; ++t)
#pragma unroll
          for (int m = 0; m < 4; ++m) acc[t][m] = f32x4{0.f, 0.f, 0.f, 0.f};
        gemm18<2, 4, false, WIN, 6>(acc, win, L + lane16, rs, lane16, qbase, nbase);
#pragma unroll
        for (int t = 0; t < 2; ++t)
#pragma unroll
          for (int m = 0; m < 4; ++m)
            *(LAS v2u*)(X + ((10 + 2 * w + t) * 5 + m) * 512) = v2u{pk(acc[t][m][0], acc[t][m][1]), pk(acc[t][m][2], acc[t][m][3])};
      }
      if (opq_s(role) == 1) {
        f32x4 acc[1][5];
#pragma unroll
        for (int m = 0; m < 5; ++m) acc[0][m] = f32x4{0.f, 0.f, 0.f, 0.f};
        gemm18<1, 5, true, WIN, 10>(acc, win, L + lane16, rs, lane16, qbase, nbase);
#pragma unroll
        for (int m = 0; m < 5; ++m)
          *(LAS v2u*)(X + (tile * 5 + m) * 512) = v2u{pk(acc[0][m][0], acc[0][m][1]), pk(acc[0][m][2], acc[0][m][3])};
      }
      if (opq_s(role) == 2) {
        f32x4 acc[1][5];
#pragma unroll
        for (int m = 0; m < 5; ++m) acc[0][m] = f32x4{0.f, 0.f, 0.f, 0.f};
        gemm18<1, 5, false, WIN, 10>(acc, win, L + lane16, rs, lane16, qbase, nbase);
#pragma unroll
        for (int m = 0; m < 5; ++m)
          *(LAS v2u*)(X + (tile * 5 + m) * 512) = v2u{pk(acc[0][m][0], acc[0][m][1]), pk(acc[0][m][2], acc[0][m][3])};
      }
      STAMP(5 + layer * 16);
      BAR();
      if (w < 8) {
        const int T = w & 3, hp = w >> 2, Tp = (T & 1) ? T - 1 : 4;
        const v2u kxs = *(LAS v2u*)(X + (40 + T) * 512), kxp = *(LAS v2u*)(X + (40 + Tp) * 512);
        const v2u vxs = *(LAS v2u*)(X + (45 + T) * 512), vxp = *(LAS v2u*)(X + (45 + Tp) * 512);
        bool okp[4], oks[4];
#pragma unroll
        for (int r = 0; r < 4; ++r) {
          const int jj = 4 * quad + r, jh = jj - 8 * (T >> 1);
          okp[r] = (T & 1) ? (jj >= l15 + 9) : (j > 0 && jh < 8 && jh >= l15 + 1);
          oks[r] = jj <= l15 && l15 - jj <= 7;
        }
        const unsigned zr = opq_v(0);
        f32x4 o[2], ox = {0.f, 0.f, 0.f, 0.f};
        const f32x4 z = {0.f, 0.f, 0.f, 0.f};
#pragma unroll
        for (int hh = 0; hh < 2; ++hh) {
          const int h = 2 * hp + hh;
          const v2u q = *(LAS v2u*)(X + ((10 + h) * 5 + T) * 512);
          const v2u ks = *(LAS v2u*)(X + (h * 5 + T) * 512), kp = *(LAS v2u*)(X + (h * 5 + Tp) * 512);
          const v2u vs = *(LAS v2u*)(X + ((4 + h) * 5 + T) * 512), vp = *(LAS v2u*)(X + ((4 + h) * 5 + Tp) * 512);
          const v4u qf = {q.x, q.y, quad == h ? kxs.x : 0u, zr};
          const f32x4 sp = mfma(v4u{kp.x, kp.y, kxp.y, zr}, qf, z), ss = mfma(v4u{ks.x, ks.y, kxs.y, zr}, qf, z);
          float e[8], mx = -3.0e38f;
#pragma unroll
          for (int r = 0; r < 4; ++r) {
            e[r] = okp[r] ? sp[r] : -3.0e38f;
            e[4 + r] = oks[r] ? ss[r] : -3.0e38f;
            mx = fmaxf(mx, fmaxf(e[r], e[4 + r]));
          }
          mx = rows_max(mx);
          const float c = 0.23570226039551584f * 1.4426950408889634f;
          float sum = 0.f;
#pragma unroll
          for (int r = 0; r < 8; ++r) {
            const bool ok = r < 4 ? okp[r] : oks[r - 4];
            e[r] = ok ? __builtin_amdgcn_exp2f((e[r] - mx) * c) : 0.f;
            sum += e[r];
          }
          const float inv = __builtin_amdgcn_rcpf(rows_sum(sum));
          const v4u pf = {pk(e[0] * inv, e[1] * inv), pk(e[2] * inv, e[3] * inv), pk(e[4] * inv, e[5] * inv), pk(e[6] * inv, e[7] * inv)};
          o[hh] = mfma(v4u{vp.x, vp.y, vs.x, vs.y}, pf, z);
          const bool mine = (l15 >> 1) == h;
          ox = mfma(v4u{mine ? vxp.x : 0u, mine ? vxp.y : 0u, mine ? vxs.x : 0u, mine ? vxs.y : 0u}, pf, ox);
        }
        stg(v4u{pk(o[0][0], o[0][1]), pk(o[0][2], o[0][3]), pk(o[1][0], o[1][1]), pk(o[1][2], o[1][3])}, rs, lane16,
            ab + A_O + (T * 18 + 2 * g + hp) * KB);
        if (quad == hp)
          __builtin_amdgcn_raw_buffer_store_b64(v2u{pk(ox[0], ox[1]), pk(ox[2], ox[3])}, rs, l15 * 16,
                                                ab + A_O + (T * 18 + 16 + (g >> 2)) * KB + (g & 3) * 256 + hp * 8, SC1);
        STAMP(50);
        DRAIN();
      }
      STAMP(6 + layer * 16);
      BAR();
      if (w == 11) {
        if (lane == 0) set_flag(p.ws, FLAGS + (2 + 3 * layer) * 4096 + j * 128 + g * 8, tag);
        wait_flags(p.ws, FLAGS + (2 + 3 * layer) * 4096 + j * 128 + (lane & 7) * 8, lane < 8, tag);
      }
      BAR();
      STAMP(7 + layer * 16);
    }
    {
      LOCALS();
      const int th = g >> 2, ft = (g & 3) * 9 + w;
      v4u orr[3];
#pragma unroll
      for (int i = 0; i < 3; ++i) orr[i] = ldg(rs, lane16, ab + A_O + (th * 36 + w * 3 + i) * KB);
      const int xoff = ((quad & 1) * 18 + (ft >> 1)) * KB + ((2 * (ft & 1) + (quad >> 1)) * 16 + l15) * 16;
      v4u xr = {0u, 0u, 0u, 0u};
      if (w < 9) xr = ldg(rs, xoff, ab + A_XR + th * 36 * KB);
#pragma unroll
      for (int i = 0; i < 3; ++i) *(LAS v4u*)(L + (w * 3 + i) * KB + lane16) = orr[i];
      BAR();
      STAMP(8 + layer * 16);
      if (w < 9) {
        f32x4 acc[1][2] = {{{0.f, 0.f, 0.f, 0.f}, {0.f, 0.f, 0.f, 0.f}}};
        gemm18<1, 2, false, P2N, 10>(acc, win, L + lane16, rs, lane16, 0, MBASE());
        float a[8];
#pragma unroll
        for (int r = 0; r < 4; ++r) {
          auto s = __builtin_amdgcn_permlane16_swap(__float_as_uint(acc[0][0][r]), __float_as_uint(acc[0][1][r]), false, false);
          a[r] = __uint_as_float(s[0]);
          a[4 + r] = __uint_as_float(s[1]);
        }
        const v4u o = {pk(a[0] + blo(xr.x), a[1] + bhi(xr.x)), pk(a[2] + blo(xr.y), a[3] + bhi(xr.y)),
                       pk(a[4] + blo(xr.z), a[5] + bhi(xr.z)), pk(a[6] + blo(xr.w), a[7] + bhi(xr.w))};
        stg(o, rs, xoff, ab + A_X1 + th * 36 * KB);
        STAMP(51);
        DRAIN();
      }
      STAMP(9 + layer * 16);
      BAR();
      if (w < 9) issue_window<P2N, 15>(win, rs, lane16, MBASE());
      if (w == 11) {
        if (lane == 0) set_flag(p.ws, FLAGS + (3 + 3 * layer) * 4096 + j * 128 + g * 8, tag);
        wait_flags(p.ws, FLAGS + (3 + 3 * layer) * 4096 + j * 128 + (lane & 7) * 8, lane < 8, tag);
      }
      BAR();
      STAMP(10 + layer * 16);
    }
    {
      LOCALS();
      const int mbase = MBASE();
      const int T = w & 3, cls = w >> 2;
      volatile LAS int* hc = (volatile LAS int*)(L + L_HC);
      {
        v4u xr[6];
#pragma unroll
        for (int i = 0; i < 6; ++i) xr[i] = ldg(rs, lane16, ab + A_X1 + (T * 18 + 6 * cls + i) * KB);
        if (w == 0 && lane < 4) hc[lane] = lane == 3 ? 4 : 0;
        float s1 = 0.f, s2 = 0.f;
#pragma unroll
        for (int i = 0; i < 6; ++i)
#pragma unroll
          for (int e = 0; e < 4; ++e) {
            const float lo = blo(xr[i][e]), hi = bhi(xr[i][e]);
            s1 += lo + hi;
            s2 = __builtin_fmaf(lo, lo, __builtin_fmaf(hi, hi, s2));
          }
        s1 = rows_sum(s1); s2 = rows_sum(s2);
        STAMP(55);
        if (lane < 16) *(LAS f32x2*)(L + L_ST + (cls * 64 + T * 16 + lane) * 8) = f32x2{s1, s2};
        BAR();
        float t1 = 0.f, t2 = 0.f;
#pragma unroll
        for (int i = 0; i < 3; ++i) { const f32x2 q = *(LAS f32x2*)(L + L_ST + (i * 64 + T * 16 + l15) * 8); t1 += q.x; t2 += q.y; }
        const float mean = t1 * (1.0f / 576.0f);
        const float var = fmaxf(__builtin_fmaf(-mean, mean, t2 * (1.0f / 576.0f)), 0.f);
        const float r1 = __builtin_amdgcn_rsqf(var + EPS);
        const float r2 = __builtin_amdgcn_rsqf(var * r1 * r1 + EPS);
        const float sc = r1 * r2, of = -mean * sc;
#pragma unroll
        for (int i = 0; i < 6; ++i) {
          v4u o;
#pragma unroll
          for (int e = 0; e < 4; ++e) o[e] = pk(__builtin_fmaf(blo(xr[i][e]), sc, of), __builtin_fmaf(bhi(xr[i][e]), sc, of));
          *(LAS v4u*)(L + (T * 18 + 6 * cls + i) * KB + lane16) = o;
        }
      }
      f32x4 acc[3][4];
#pragma unroll
      for (int t = 0; t < 3; ++t) {
        const v2u b = *(const GAS v2u*)(p.b1 + 576 * g + 48 * w + 16 * t + 4 * quad);
#pragma unroll
        for (int m = 0; m < 4; ++m) acc[t][m] = f32x4{blo(b.x), bhi(b.x), blo(b.y), bhi(b.y)};
      }
      BAR();
      STAMP(11 + layer * 16);
      __builtin_amdgcn_s_setprio(PRIO_G1);
      gemm_mlp<false>(acc, win, L + lane16, rs, lane16, mbase, lane16, mbase + 54 * KB, nullptr);
      STAMP(12 + layer * 16);
      __builtin_amdgcn_s_setprio(PRIO_GELU);
      {
        LAS unsigned char* H = L + L_H + lane16 + 3 * (w >> 1) * KB;
        const float c0 = __int_as_float(opq_v(__float_as_int(GC0)));
#pragma unroll
        for (int m = 0; m < 4; ++m) {
          unsigned u[6];
#pragma unroll
          for (int t = 0; t < 3; ++t) {
            u[2 * t] = pk(gelu_tanh(acc[t][m][0], c0), gelu_tanh(acc[t][m][1], c0));
            u[2 * t + 1] = pk(gelu_tanh(acc[t][m][2], c0), gelu_tanh(acc[t][m][3], c0));
          }
          *(LAS v4u*)(H + (m * 18 + (w & 1)) * KB) = v4u{u[0], u[1], u[2], u[3]};
          *(LAS v2u*)(H + (m * 18 + 2) * KB + (w & 1) * 8) = v2u{u[4], u[5]};
        }
        asm volatile("s_waitcnt lgkmcnt(0)" ::: "memory");
        if (lane == 0) __hip_atomic_fetch_add((LAS int*)(L + L_HC) + cls, 1, __ATOMIC_RELAXED, __HIP_MEMORY_SCOPE_WORKGROUP);
      }
      STAMP(13 + layer * 16);
      if (cls == 0) __builtin_amdgcn_s_setprio(2);
      else if (cls == 1) __builtin_amdgcn_s_setprio(1);
      else __builtin_amdgcn_s_setprio(0);
#pragma unroll
      for (int t = 0; t < 3; ++t)
#pragma unroll
        for (int m = 0; m < 4; ++m) {
          acc[t][m] = f32x4{0.f, 0.f, 0.f, 0.f};
        }
      gemm_mlp<true>(acc, win, L + L_H + lane16, rs, lane16, mbase + 54 * KB, QLATE || layer ? lane16 + WS_BYTES : lane16, QBASE(), hc);
      __builtin_amdgcn_s_setprio(0);
      STAMP(14 + layer * 16);
      const int pw = ab + A_PT + (g * 12 + w) * 6144;
      const int poff = (quad & 1) * 1536 + (quad >> 1) * 256 + l15 * 16;
#pragma unroll
      for (int t = 0; t < 3; ++t)
#pragma unroll
        for (int m = 0; m < 4; m += 2) {
          const v2u a = {pk(acc[t][m][0], acc[t][m][1]), pk(acc[t][m][2], acc[t][m][3])};
          const v2u b = {pk(acc[t][m + 1][0], acc[t][m + 1][1]), pk(acc[t][m + 1][2], acc[t][m + 1][3])};
          stg(widen(a, b), rs, poff + m * 1536 + t * 512, pw);
        }
      DRAIN();
      if (lane == 0) set_flag(p.ws, WFLAGS + ((layer * 32 + j) * 12 + w) * 128 + g * 8, tag);
      STAMP(15 + layer * 16);
      BAR();
    }
    {
      LOCALS();
      OWNROWS(6);
      const bool act = lane < 48;
      v4u xr = {0u, 0u, 0u, 0u}, bb = {0u, 0u, 0u, 0u};
      if (act) {
        xr = ldg(rs, piece, ab + A_X1);
        bb = *(const GAS v4u*)(p.b2 + c8 * 8);
      }
      wait_flags<POLLSLEEP>(p.ws, WFLAGS + ((layer * 32 + j) * 12 + w) * 128 + (lane & 7) * 8, lane < 8, tag);
      STAMP(16 + layer * 16);
      float v[8];
      {
        v4u pr[8];
        const int roff = (g >> 1) * 1536 + (lane >> 3) * 256 + (8 * (g & 1) + (lane & 7)) * 16;
#pragma unroll
        for (int i = 0; i < 8; ++i) pr[i] = act ? ldg(rs, roff, ab + A_PT + (i * 12 + w) * 6144) : v4u{0u, 0u, 0u, 0u};
        SB();
#pragma unroll
        for (int e = 0; e < 4; ++e) {
          float lo = blo(bb[e]), hi = bhi(bb[e]);
#pragma unroll
          for (int i = 0; i < 8; ++i) { lo += blo(pr[i][e]); hi += bhi(pr[i][e]); OPAQUE(lo); }
          v[2 * e] = lo + blo(xr[e]);
          v[2 * e + 1] = hi + bhi(xr[e]);
          OPAQUE(v[2 * e]);
        }
      }
      STAMP(17 + layer * 16);
      if (layer == 0) {
        publish_rows<12>(v, act, L, rs, w, lane, piece, ab + ALY + A_XR, ab + ALY + A_Y);
        STAMP(52);
        DRAIN();
        STAMP(53);
        BAR();
        if (QLATE && w != 0) issue_window<0, QLATE>(win, rs, lane16, QBASE());
        if (w == 0) {
          if (lane == 0) set_flag(p.ws, FLAGS + 4 * 4096 + j * 128 + g * 8, tag);
          wait_flags(p.ws, FLAGS + 4 * 4096 + (j - (lane >> 3)) * 128 + (lane & 7) * 8, lane < (j > 0 ? 16 : 8), tag);
        }
        BAR();
        STAMP(18);
        v4u yr[8];
        load_y(yr, rs, w, lane, j, ab + ALY + A_Y);
        if (QLATE && w == 0) issue_window<0, QLATE>(win, rs, lane16, QBASE());
        issue_window<QLATE ? QLATE : 15, WIN>(win, rs, lane16, QBASE());
        store_y(yr, L, w, lane);
      } else {
        if (act) *(GAS v4u*)(p.out + (size_t)mytok * 576 + c8 * 8) = v4u{pk(v[0], v[1]), pk(v[2], v[3]), pk(v[4], v[5]), pk(v[6], v[7])};
      }
    }
  }
  if (bid == 0 && w0 == 0 && lane0 == 0) set_flag(p.ws, 0, tag);
  STAMP(40);
  STAMP_DUMP();
}
}

extern "C" void kernel_launch(void* const* d_in, const int* in_sizes, int n_in, void* d_out, int out_size, void* d_ws, size_t ws_size,
                              hipStream_t stream) {
  (void)in_sizes; (void)n_in; (void)out_size; (void)ws_size;
  Params p;
  p.x = (const unsigned short*)d_in[0];
  p.wqkv = (const unsigned short*)d_in[1];
  p.wo = (const unsigned short*)d_in[2];
  p.w1 = (const unsigned short*)d_in[4];
  p.b1 = (const unsigned short*)d_in[5];
  p.w2 = (const unsigned short*)d_in[6];
  p.b2 = (const unsigned short*)d_in[7];
  p.out = (unsigned short*)d_out;
  p.ws = (unsigned char*)d_ws + (WSEND ? (ws_size - WS_BYTES - (2u << 20)) / (2u << 20) * (2u << 20) : 0);
  (void)hipFuncSetAttribute((const void*)fused_fwd, hipFuncAttributeMaxDynamicSharedMemorySize, LDS_BYTES + LDS_EXTRA);
  hipLaunchKernelGGL(fused_fwd, dim3(256), dim3(NTHR), LDS_BYTES + LDS_EXTRA, stream, p);
}
```

```cpp
#include <hip/hip_runtime.h>
#include <hip/hip_bf16.h>
#include <stdint.h>

namespace {
#define LAS __attribute__((address_space(3)))
#define GAS __attribute__((address_space(1)))
typedef unsigned v4u __attribute__((ext_vector_type(4)));
typedef unsigned v4u_a4 __attribute__((ext_vector_type(4), aligned(4)));
typedef unsigned v2u __attribute__((ext_vector_type(2)));
typedef float f32x4 __attribute__((ext_vector_type(4)));
typedef float f32x2 __attribute__((ext_vector_type(2)));
typedef __bf16 bf16x8 __attribute__((ext_vector_type(8)));
typedef __bf16 bf16x2 __attribute__((ext_vector_type(2)));
typedef short s16x4 __attribute__((ext_vector_type(4)));
typedef unsigned long long u64;
typedef __amdgpu_buffer_rsrc_t rsrc_t;

constexpr int KB = 1024;
constexpr int NTHR = 768;
constexpr int WIN = 18;
#define XB 2
#define P2N 9
#define TAIL0 2
#define TAIL1 2
#define R1 10
#define R2 7
#define NTW 1
constexpr float EPS = 1e-5f;
constexpr int SC1 = 16;
constexpr float GC0 = -2.0f * 0.7978845608028654f * 1.4426950408889634f;
constexpr int FLAGS = 4096;
constexpr int SFLAGS = 32 * KB;
constexpr int WFLAGS = 48 * KB;
constexpr int CS1 = 192 * KB;
constexpr int CSQ = 520 * KB;
constexpr int WBASE = 704 * KB;
constexpr int WSL = (252 + 1296) * KB;
constexpr int WOB = WBASE + 8 * WSL;
constexpr int ABASE = 14 * 1024 * KB;
constexpr int IMG = 72 * KB;
constexpr int A_X2 = 0, A_O = 96 * KB, A_X1 = A_O + IMG, A_PT = A_X1 + IMG, ALY = 1024 * KB, ASL = 2 * ALY;
constexpr int WS_BYTES = ABASE + 32 * ASL;
static_assert(WOB + 648 * KB <= ABASE && A_PT + 8 * IMG <= ALY && CSQ + 8 * 14 * 1152 + 256 <= WBASE, "workspace map");
constexpr int L_H = IMG;
constexpr int L_QK = 90 * KB;
constexpr int L_ST = 145 * KB;
constexpr int L_T1 = L_ST + 7680;
constexpr int L_TQ = L_T1 + 4608;
constexpr int L_RT = L_TQ + 896;
constexpr int L_HC = L_RT + 640;
constexpr int LDS_BYTES = L_HC + 64;
enum { C_H0 = 0, C_ST = 4, C_RT = 5, C_GD = 6, C_LC = 7, C_XE = 8, C_XL = 9, C_R1 = 10 };

struct Params {
  const unsigned short *x, *wqkv, *wo, *w1, *b1, *w2, *b2;
  unsigned short *out;
  unsigned char *ws;
};

#define STAMP(i)
#define STAMP_INIT()
#define STAMP_ARG
#define STAMP_PASS
#define STAMP_DUMP()
#define SB() __builtin_amdgcn_sched_barrier(0)
#define BAR() do { asm volatile("s_waitcnt lgkmcnt(0)" ::: "memory"); __builtin_amdgcn_s_barrier(); asm volatile("" ::: "memory"); } while (0)
#define DRAIN() asm volatile("s_waitcnt vmcnt(0)" ::: "memory")
#define OPAQUE(x) asm("" : "+v"(x))
#define KEEP(x) asm volatile("" :: "v"(x))

__device__ __forceinline__ f32x4 mfma(v4u a, v4u b, f32x4 c) {
  return __builtin_amdgcn_mfma_f32_16x16x32_bf16(__builtin_bit_cast(bf16x8, a), __builtin_bit_cast(bf16x8, b), c, 0, 0, 0);
}
#define MF(acc, a, b) asm volatile("v_mfma_f32_16x16x32_bf16 %0, %1, %2, %0" : "+v"(acc) : "v"(a), "v"(b))
#define MF0(acc, a, b) asm volatile("v_mfma_f32_16x16x32_bf16 %0, %1, %2, 0" : "=&v"(acc) : "v"(a), "v"(b))
template <int NT, int MT> __device__ __forceinline__ void acc_fence(f32x4 (&acc)[NT][MT]) {
#pragma unroll
  for (int t = 0; t < NT; ++t)
#pragma unroll
    for (int m = 0; m < MT; ++m) asm volatile("" : "+v"(acc[t][m]));
}
#define ACC_IN(acc) do { acc_fence(acc); asm volatile("s_nop 1"); SB(); } while (0)
#define ACC_OUT(acc) do { SB(); asm volatile("s_nop 7\n\ts_nop 1"); acc_fence(acc); } while (0)
__device__ __forceinline__ unsigned pk(float a, float b) {
  f32x2 v = {a, b};
  return __builtin_bit_cast(unsigned, __builtin_convertvector(v, bf16x2));
}
__device__ __forceinline__ float dot2(unsigned a, unsigned b, float c) {
  return __builtin_amdgcn_fdot2_f32_bf16(__builtin_bit_cast(bf16x2, a), __builtin_bit_cast(bf16x2, b), c, false);
}
constexpr unsigned ONES = 0x3f803f80u;
__device__ __forceinline__ float blo(unsigned u) { return __uint_as_float(u << 16); }
__device__ __forceinline__ float bhi(unsigned u) { return __uint_as_float(u & 0xffff0000u); }
__device__ __forceinline__ v4u ldg(rsrc_t rs, int voff, int soff) { return __builtin_amdgcn_raw_buffer_load_b128(rs, voff, soff, SC1); }
__device__ __forceinline__ void stg(v4u v, rsrc_t rs, int voff, int soff) { __builtin_amdgcn_raw_buffer_store_b128(v, rs, voff + soff, 0, SC1); }
__device__ __forceinline__ v4u ldf(rsrc_t rs, int lane16, int base, int f) { return ldg(rs, lane16 + (f & 3) * KB, base + (f >> 2) * 4 * KB); }
__device__ __forceinline__ int opq_v(int x) { asm volatile("" : "+v"(x)); return x; }
__device__ __forceinline__ int lane_id() { int x; asm volatile("v_mbcnt_lo_u32_b32 %0, -1, 0\n\tv_mbcnt_hi_u32_b32 %0, -1, %0" : "=v"(x)); return x; }
__device__ __forceinline__ int opq_s(int x) { asm volatile("" : "+s"(x)); return x; }
#define LOCALS() const int lane = lane_id(), w = opq_s(w0), j = opq_s(j0), g = opq_s(g0); \
  const int lane16 = lane * 16, quad = lane >> 4, l15 = lane & 15, ab = ABASE + j * ASL + layer * ALY, wb = WBASE + g * WSL; \
  (void)lane16; (void)quad; (void)l15; (void)ab; (void)wb
#define OWNROWS(CPW) const int c8 = CPW * w + (lane >> 3); \
  const int piece = ((g >> 1) * 18 + (c8 >> 2)) * KB + ((c8 & 3) * 16 + 8 * (g & 1) + (lane & 7)) * 16; \
  const int mytok = 64 * j + 2 * (16 * ((g >> 1) & 1) + 8 * (g & 1) + (lane & 7)) + (g >> 2)
#define GET_TAG() tag = 1 + ((u64)(unsigned)__builtin_amdgcn_readfirstlane((int)(unsigned)tag0) | \
                             ((u64)(unsigned)__builtin_amdgcn_readfirstlane((int)(unsigned)(tag0 >> 32)) << 32))
#define NBASE() opq_s(nb0)
#define QBASE() opq_s(qb0)
#define MBASE() opq_s(mb0)
__device__ __forceinline__ float rows_sum(float x) {
  auto s = __builtin_amdgcn_permlane16_swap(__float_as_uint(x), __float_as_uint(x), false, false);
  x = __uint_as_float(s[0]) + __uint_as_float(s[1]);
  auto t = __builtin_amdgcn_permlane32_swap(__float_as_uint(x), __float_as_uint(x), false, false);
  return __uint_as_float(t[0]) + __uint_as_float(t[1]);
}
__device__ __forceinline__ float vmax(unsigned a, unsigned b) { float r; asm("v_max_f32 %0, %1, %2\n\ts_nop 1" : "=v"(r) : "v"(a), "v"(b)); return r; }
__device__ __forceinline__ void rows_max2(float& a, float& b) {
  auto s = __builtin_amdgcn_permlane16_swap(__float_as_uint(a), __float_as_uint(b), false, false);
  float x = vmax(s[0], s[1]);
  auto t = __builtin_amdgcn_permlane32_swap(__float_as_uint(x), __float_as_uint(x), false, false);
  x = vmax(t[0], t[1]);
  auto u = __builtin_amdgcn_permlane16_swap(__float_as_uint(x), __float_as_uint(x), false, false);
  a = __uint_as_float(u[0]);
  b = __uint_as_float(u[1]);
}
template <int CTRL> __device__ __forceinline__ float dpp(float x) {
  return __int_as_float(__builtin_amdgcn_mov_dpp(__float_as_int(x), CTRL, 0xf, 0xf, true));
}
__device__ __forceinline__ float ror8(float x) { return dpp<0x128>(x); }
__device__ __forceinline__ void add4(f32x4& a, v4u b) {
  float a0 = a[0], a1 = a[1], a2 = a[2], a3 = a[3];
  asm("v_add_f32 %0, %0, %4\n\tv_add_f32 %1, %1, %5\n\tv_add_f32 %2, %2, %6\n\tv_add_f32 %3, %3, %7"
      : "+v"(a0), "+v"(a1), "+v"(a2), "+v"(a3) : "v"(b.x), "v"(b.y), "v"(b.z), "v"(b.w));
  a = f32x4{a0, a1, a2, a3};
}
__device__ __forceinline__ v4u widen(v2u a, v2u b) {
  auto s0 = __builtin_amdgcn_permlane16_swap(a.x, b.x, false, false);
  auto s1 = __builtin_amdgcn_permlane16_swap(a.y, b.y, false, false);
  return v4u{s0[0], s1[0], s0[1], s1[1]};
}
__device__ __forceinline__ v2u gelu4(f32x4 a, float k0, float k1, float is) {
  float s0, s1, s2, s3;
  unsigned u0, u1;
  asm("v_mul_f32 %2, %6, %6\n\tv_mul_f32 %3, %7, %7\n\tv_fma_f32 %2, %2, %11, %10\n\tv_fma_f32 %3, %3, %11, %10\n\t"
      "v_mul_f32 %2, %2, %6\n\tv_mul_f32 %3, %3, %7\n\tv_exp_f32 %2, %2\n\tv_mul_f32 %4, %8, %8\n\t"
      "v_exp_f32 %3, %3\n\tv_mul_f32 %5, %9, %9\n\tv_fma_f32 %4, %4, %11, %10\n\tv_fma_f32 %5, %5, %11, %10\n\t"
      "v_mul_f32 %4, %4, %8\n\tv_mul_f32 %5, %5, %9\n\tv_exp_f32 %4, %4\n\tv_fma_f32 %2, %2, %12, %12\n\t"
      "v_exp_f32 %5, %5\n\tv_fma_f32 %3, %3, %12, %12\n\tv_rcp_f32 %2, %2\n\tv_fma_f32 %4, %4, %12, %12\n\t"
      "v_rcp_f32 %3, %3\n\tv_fma_f32 %5, %5, %12, %12\n\tv_rcp_f32 %4, %4\n\tv_mul_f32 %2, %2, %6\n\t"
      "v_rcp_f32 %5, %5\n\tv_mul_f32 %3, %3, %7\n\tv_mul_f32 %4, %4, %8\n\tv_cvt_pk_bf16_f32 %0, %2, %3\n\t"
      "v_mul_f32 %5, %5, %9\n\tv_cvt_pk_bf16_f32 %1, %4, %5"
      : "=&v"(u0), "=&v"(u1), "=&v"(s0), "=&v"(s1), "=&v"(s2), "=&v"(s3)
      : "v"(a[0]), "v"(a[1]), "v"(a[2]), "v"(a[3]), "v"(k0), "v"(k1), "v"(is));
  return v2u{u0, u1};
}
__device__ __forceinline__ void shift4(f32x4& a, f32x4 cs, f32x4 b, float nmean, float is) {
  float a0 = a[0], a1 = a[1], a2 = a[2], a3 = a[3];
  asm("v_fmac_f32 %0, %12, %4\n\tv_fmac_f32 %1, %12, %5\n\tv_fmac_f32 %2, %12, %6\n\tv_fmac_f32 %3, %12, %7\n\t"
      "v_fmac_f32 %0, %13, %8\n\tv_fmac_f32 %1, %13, %9\n\tv_fmac_f32 %2, %13, %10\n\tv_fmac_f32 %3, %13, %11"
      : "+v"(a0), "+v"(a1), "+v"(a2), "+v"(a3)
      : "v"(cs[0]), "v"(cs[1]), "v"(cs[2]), "v"(cs[3]), "v"(b[0]), "v"(b[1]), "v"(b[2]), "v"(b[3]), "v"(nmean), "v"(is));
  a = f32x4{a0, a1, a2, a3};
}
__device__ __forceinline__ int cnt_get(LAS unsigned char* L, int i) { return ((volatile LAS int*)(L + L_HC))[i]; }
__device__ __forceinline__ void cnt_wait(LAS unsigned char* L, int i, int n) {
  while (__builtin_amdgcn_readfirstlane(cnt_get(L, i)) < n) __builtin_amdgcn_s_sleep(1);
  asm volatile("" ::: "memory");
}
__device__ __forceinline__ void cnt_add(LAS unsigned char* L, int i, int lane) {
  asm volatile("s_waitcnt lgkmcnt(0)" ::: "memory");
  if (lane == 0) __hip_atomic_fetch_add((LAS int*)(L + L_HC) + i, 1, __ATOMIC_RELAXED, __HIP_MEMORY_SCOPE_WORKGROUP);
}
template <int NP> __device__ __forceinline__ void row_table1(LAS unsigned char* L, int t5, int lane) {
  const int r = t5 * 16 + (lane & 15), q = lane >> 4;
  float t1 = 0.f, t2 = 0.f;
#pragma unroll
  for (int i = 0; i < (NP + 3) / 4; ++i)
    if (NP % 4 == 0 || q < NP) { const f32x2 v = *(LAS f32x2*)(L + L_ST + ((i * 4 + q) * 80 + r) * 8); t1 += v.x; OPAQUE(t1); t2 += v.y; }
  t1 = rows_sum(t1); OPAQUE(t1); t2 = rows_sum(t2);
  const float nmean = t1 * (-1.0f / 576.0f);
  const float var = fmaxf(__builtin_fmaf(-nmean, nmean, t2 * (1.0f / 576.0f)), 0.f);
  const float rstd = __builtin_amdgcn_rsqf(var + EPS);
  if (q < 2) *(LAS float*)(L + L_RT + q * 320 + r * 4) = q ? nmean * rstd : rstd;
  cnt_add(L, C_R1, lane);
}
__device__ __forceinline__ v2u ln_pack(f32x4 a, f32x4 r, f32x4 c, f32x4 nm) {
  float t0, t1, t2, t3;
  unsigned u0, u1;
  asm("v_mul_f32 %2, %14, %18\n\tv_mul_f32 %3, %15, %19\n\tv_mul_f32 %4, %16, %20\n\tv_mul_f32 %5, %17, %21\n\t"
      "v_fmac_f32 %2, %6, %10\n\tv_fmac_f32 %3, %7, %11\n\tv_fmac_f32 %4, %8, %12\n\tv_fmac_f32 %5, %9, %13\n\t"
      "v_cvt_pk_bf16_f32 %0, %2, %3\n\tv_cvt_pk_bf16_f32 %1, %4, %5"
      : "=&v"(u0), "=&v"(u1), "=&v"(t0), "=&v"(t1), "=&v"(t2), "=&v"(t3)
      : "v"(a[0]), "v"(a[1]), "v"(a[2]), "v"(a[3]), "v"(r[0]), "v"(r[1]), "v"(r[2]), "v"(r[3]), "v"(c[0]), "v"(c[1]), "v"(c[2]), "v"(c[3]),
        "v"(nm[0]), "v"(nm[1]), "v"(nm[2]), "v"(nm[3]));
  return v2u{u0, u1};
}
__device__ __forceinline__ f32x4 splat(float x) { return f32x4{x, x, x, x}; }
__device__ __forceinline__ void row_table(LAS unsigned char* L, int lane) {
  cnt_wait(L, C_ST, 8);
  const f32x4 q = *(LAS f32x4*)(L + L_ST + lane * 16);
  float t1 = q[0] + q[2];
  OPAQUE(t1);
  float nmean = t1 * (-1.0f / 576.0f);
  const float var = fmaxf(__builtin_fmaf(-nmean, nmean, (q[1] + q[3]) * (1.0f / 576.0f)), 0.f);
  const float r1 = __builtin_amdgcn_rsqf(var + EPS);
  const float sc = r1 * __builtin_amdgcn_rsqf(var * r1 * r1 + EPS);
  float k0 = GC0 * sc;
  OPAQUE(k0);
  *(LAS f32x4*)(L + L_ST + KB + lane * 16) = f32x4{nmean, __builtin_amdgcn_rcpf(sc), k0, k0 * (0.044715f * sc * sc)};
  cnt_add(L, C_RT, lane);
}
__device__ __forceinline__ void shift_acc(f32x4 (&acc)[3][4], LAS unsigned char* L, int w, int quad, int l15) {
  f32x2 rt[4];
#pragma unroll
  for (int m = 0; m < 4; ++m) rt[m] = *(LAS f32x2*)(L + L_ST + KB + (m * 16 + l15) * 16);
#pragma unroll
  for (int t = 0; t < 3; ++t) {
    const f32x4 cs = *(LAS f32x4*)(L + L_T1 + (48 * w + 16 * t + 4 * quad) * 4), bb = *(LAS f32x4*)(L + L_T1 + 2304 + (48 * w + 16 * t + 4 * quad) * 4);
#pragma unroll
    for (int m = 0; m < 4; ++m) shift4(acc[t][m], cs, bb, rt[m][0], rt[m][1]);
  }
}
template <int SLEEP = 1> __device__ __forceinline__ void wait_flags(const unsigned char* ws, int off, bool active, u64 tag) {
  const GAS u64* f = (const GAS u64*)(ws + off);
  asm volatile("" : "+s"(tag));
#pragma unroll 1
  for (int spins = 0; spins < (1 << 16); ++spins) {
    const u64 v = active ? __hip_atomic_load(f, __ATOMIC_RELAXED, __HIP_MEMORY_SCOPE_AGENT) : tag;
    if (__all(v == tag)) break;
    __builtin_amdgcn_s_sleep(SLEEP);
  }
  asm volatile("" ::: "memory");
}
__device__ __forceinline__ void set_flag(unsigned char* ws, int off, u64 tag) {
  asm volatile("" : "+s"(tag));
  __hip_atomic_store((GAS u64*)(ws + off), tag, __ATOMIC_RELAXED, __HIP_MEMORY_SCOPE_AGENT);
}
template <int LO, int HI> __device__ __forceinline__ void issue_window(v4u (&win)[WIN], rsrc_t rs, int lane16, int base) {
#pragma unroll
  for (int i = LO; i < HI; ++i) win[i] = ldf(rs, lane16, base, i);
  SB();
}

template <int NT, int MT, int SWAP, int NNEXT, int R, int LO, int HI>
__device__ __forceinline__ void gemm18_steps(f32x4 (&acc)[NT][MT], v4u (&win)[WIN], v4u (&act)[R], LAS unsigned char* img, LAS unsigned char* img2, rsrc_t rs,
                                             int lane16, int wbase, int nvo, int nbase) {
#pragma unroll
  for (int n = LO; n < HI; ++n) {
    const int s = n / MT, m = n % MT, nn = n + R - 1;
    if (n < 18 * MT) {
#pragma unroll
      for (int t = 0; t < NT; ++t) {
        if (s == 0) {
          if ((SWAP >> t) & 1) MF0(acc[t][m], act[n % R], win[t]);
          else MF0(acc[t][m], win[t], act[n % R]);
        } else if ((SWAP >> t) & 1) MF(acc[t][m], act[n % R], win[(s * NT + t) % WIN]);
        else MF(acc[t][m], win[(s * NT + t) % WIN], act[n % R]);
      }
      SB();
    }
    if (nn < 18 * MT) act[nn % R] = *(LAS v4u*)(nn % MT < 3 ? img + ((nn % MT) * 18 + nn / MT) * KB : img2 + ((nn % MT - 3) * 18 + nn / MT) * KB);
    if (m == 0 && s > 0) {
#pragma unroll
      for (int t = 0; t < NT; ++t) {
        const int f = (s - 1) * NT + t;
        if (f + WIN < 18 * NT) win[f % WIN] = ldf(rs, lane16, wbase, f + WIN);
        else if (f % WIN < NNEXT) win[f % WIN] = ldf(rs, nvo, nbase, f % WIN);
      }
    }
    SB();
    if (n < 18 * MT) {
      KEEP(act[n % R]);
#pragma unroll
      for (int t = 0; t < NT; ++t) KEEP(win[(s * NT + t) % WIN]);
      SB();
    }
  }
}
template <int NT, int MT, int SWAP, int NNEXT, int R>
__device__ __forceinline__ void gemm18(f32x4 (&acc)[NT][MT], v4u (&win)[WIN], LAS unsigned char* img, rsrc_t rs, int lane16, int wbase, int nbase) {
  v4u act[R];
  LAS unsigned char* img2 = img + 54 * KB;
  if (MT > 3) asm volatile("" : "+v"(img2));
#pragma unroll
  for (int n = 0; n < R - 1; ++n) { act[n] = *(LAS v4u*)(n % MT < 3 ? img + ((n % MT) * 18 + n / MT) * KB : img2 + ((n % MT - 3) * 18 + n / MT) * KB); SB(); }
  gemm18_steps<NT, MT, SWAP, NNEXT, R, 0, 6 * MT>(acc, win, act, img, img2, rs, lane16, wbase, lane16, nbase);
  gemm18_steps<NT, MT, SWAP, NNEXT, R, 6 * MT, 12 * MT>(acc, win, act, img, img2, rs, lane16, wbase, lane16, nbase);
  gemm18_steps<NT, MT, SWAP, NNEXT, R, 12 * MT, 18 * MT + 1>(acc, win, act, img, img2, rs, lane16, wbase, lane16, nbase);
  ACC_OUT(acc);
}
template <int NT, int MT, int SWAP, int R, int tstep>
__device__ __forceinline__ void qkv_tiles(v4u (&win)[WIN], LAS unsigned char* L, rsrc_t rs, int lane, int wbase, int nbase, int tile0, int rwait STAMP_ARG) {
  f32x4 acc[NT][MT];
  gemm18<NT, MT, SWAP, WIN, R>(acc, win, L + lane * 16, rs, lane * 16, wbase, nbase);
  STAMP(38);
  LAS unsigned char *Rq = L + L_RT + (lane & 48), *Rl = L + L_RT + (lane & 15) * 4, *Tq = L + L_TQ + tile0 * 64 + (lane & 48),
                    *Tl = L + L_TQ + tile0 * 64 + (lane & 15) * 4, *X = L + L_QK + lane * 8 + tile0 * 2560;
  asm volatile("" : "+v"(Rq), "+v"(Rl), "+v"(Tq), "+v"(Tl), "+v"(X));
  cnt_wait(L, C_R1, rwait);
  v2u u[NT][MT];
#pragma unroll
  for (int t = 0; t < NT; ++t) {
    if ((SWAP >> t) & 1) {
      const f32x4 cs = splat(*(LAS float*)(Tl + t * tstep * 64));
#pragma unroll
      for (int m = 0; m < MT; ++m) u[t][m] = ln_pack(acc[t][m], *(LAS f32x4*)(Rq + m * 64), cs, *(LAS f32x4*)(Rq + 320 + m * 64));
    } else {
      const f32x4 cs = *(LAS f32x4*)(Tq + t * tstep * 64);
#pragma unroll
      for (int m = 0; m < MT; ++m) u[t][m] = ln_pack(acc[t][m], splat(*(LAS float*)(Rl + m * 64)), cs, splat(*(LAS float*)(Rl + 320 + m * 64)));
    }
  }
#pragma unroll
  for (int t = 0; t < NT; ++t)
#pragma unroll
    for (int m = 0; m < MT; ++m) *(LAS v2u*)(X + (t * tstep * 5 + m) * 512) = u[t][m];
}
template <bool HWAIT>
__device__ __forceinline__ void gemm_mlp(f32x4 (&acc)[3][4], v4u (&win)[WIN], LAS unsigned char* img, rsrc_t rs, int lane16, int rb, int vlast,
                                         int rlast, LAS unsigned char* L) {
  v4u act[8];
  if (HWAIT) cnt_wait(L, C_H0, 4);
#pragma unroll
  for (int n = 0; n < 4; ++n) { act[n] = *(LAS v4u*)(img + n * 18 * KB); SB(); }
  ACC_IN(acc);
#pragma unroll 1
  for (int c = 0; c < 3; ++c) {
    const int vo = c == 2 ? vlast : lane16, so = c == 2 ? rlast : rb + 18 * KB;
    int hv = 4;
#pragma unroll
    for (int s = 0; s < 6; ++s) {
      if (HWAIT && s == 5) {
        if (__builtin_amdgcn_readfirstlane(hv) < 4) cnt_wait(L, C_H0 + c + 1, 4);
        asm volatile("" ::: "memory");
        SB();
      }
#pragma unroll
      for (int t = 0; t < 3; ++t)
#pragma unroll
        for (int m = 0; m < 4; ++m) {
          MF(acc[t][m], win[s * 3 + t], act[(s & 1) * 4 + m]);
          SB();
          if (t == 0) act[((s + 1) & 1) * 4 + m] = *(LAS v4u*)(img + (m * 18 + s + 1) * KB);
          if (HWAIT && s == 3 && t == 2 && m == 0) hv = cnt_get(L, C_H0 + c + 1);
          if (HWAIT && (TAIL0 == 1 || TAIL1 == 1) && s == 1 && t == 2 && m == 0 && lane16 == 0)
            __hip_atomic_fetch_add((LAS int*)(L + L_HC) + C_LC, c == 2, __ATOMIC_RELAXED, __HIP_MEMORY_SCOPE_WORKGROUP);
          if (t == 1 && m < 3) {
            if (s == 0) win[15 + m] = ldf(rs, lane16, rb, 15 + m);
            else win[(s - 1) * 3 + m] = ldf(rs, vo, so, (s - 1) * 3 + m);
          }
          SB();
        }
    }
    img += 6 * KB;
    rb += 18 * KB;
  }
  ACC_OUT(acc);
}

__global__ void __launch_bounds__(NTHR) fused_fwd(Params p) {
  extern __shared__ __attribute__((aligned(16))) unsigned char smem[];
  LAS unsigned char* L = (LAS unsigned char*)smem;
  const int w0 = __builtin_amdgcn_readfirstlane(threadIdx.x >> 6);
  const int bid = blockIdx.x;
  constexpr int MPX = 8 / XB;
  const int j0 = ((bid & 7) / XB) * (32 / MPX) + (bid >> 3) / MPX;
  const int g0 = ((bid & 7) % XB) * MPX + (bid >> 3) % MPX;
  const rsrc_t rs = __builtin_amdgcn_make_buffer_rsrc((void*)p.ws, 0, WS_BYTES, 0x00020000);
  const int mb0 = WBASE + g0 * WSL + (252 + w0 * 108) * KB;
  const int nb0 = w0 < 9 ? WOB + ((g0 & 3) * 9 + w0) * 18 * KB : mb0;
  const int qb0 = w0 < 8 ? WBASE + g0 * WSL + (w0 < 6 ? w0 * 36 : 108 + w0 * 18) * KB : nb0;
  STAMP_INIT();
  STAMP(0);
  const u64 tag0 = __hip_atomic_load((const GAS u64*)p.ws, __ATOMIC_RELAXED, __HIP_MEMORY_SCOPE_AGENT);
  u64 tag = 0;
  v4u win[WIN];
  {
    constexpr int layer = 0;
    LOCALS();
    const rsrc_t rx = __builtin_amdgcn_make_buffer_rsrc((void*)p.x, 0, 2048 * 576 * 2, 0x00020000);
    const int xrow = lane >> 2, t5 = w >> 1;
    const int xtok = t5 < 4 ? 64 * j + 32 * (t5 & 1) + 2 * xrow + (t5 >> 1) : (j > 0 ? 64 * j - 16 : 0) + 2 * (xrow & 7) + (xrow >> 3);
    const int xoff = w < 10 ? xtok * 1152 + (w & 1) * 576 + (lane & 3) * 16 : 0x10000000;
#define LOAD_X() v4u xr[9]; _Pragma("unroll") for (int i = 0; i < 9; ++i) xr[i] = __builtin_amdgcn_raw_buffer_load_b128(rx, xoff + i * 64, 0, 0); SB()
#define STAGE_X() do { float s1 = 0.f, s2 = 0.f; \
      _Pragma("unroll") for (int i = 0; i < 9; ++i) { \
        if (w < 10) *(LAS v4u*)(L + (w * 9 + i) * KB + (lane & 3) * 256 + xrow * 16) = xr[i]; \
        _Pragma("unroll") for (int e = 0; e < 4; ++e) { s1 = dot2(xr[i][e], ONES, s1); s2 = dot2(xr[i][e], xr[i][e], s2); } } \
      s1 += dpp<0xB1>(s1); s2 += dpp<0xB1>(s2); s1 += dpp<0x4E>(s1); s2 += dpp<0x4E>(s2); \
      if (w < 10 && (lane & 3) == 0) *(LAS f32x2*)(L + L_ST + ((w & 1) * 80 + (w >> 1) * 16 + xrow) * 8) = f32x2{s1, s2}; } while (0)
    if (opq_s(w) < 8) {
      LAS unsigned char* T = L + L_QK + w * 3072;
      const int uf = 32 * w + j, uc = uf < 252 ? uf : 0, tile = uc / 18, us = uc - tile * 18;
      const bool vx = tile == 9, ex = (tile & 14) == 8;
      const int c0 = 72 * g + (ex ? 16 : ((tile + (tile >= 10 ? 2 : 0)) & 3) * 18) + (tile < 4 ? 576 : (tile < 8 || vx) ? 1152 : 0);
      const int cA = vx ? 18 : 576, cB = vx ? 36 : 18, cC = ex ? (vx ? 0 : 36) : 8;
      const GAS unsigned short* src = (const GAS unsigned short*)p.wqkv + (32 * us + (lane >> 1)) * 1728 + c0 + (lane & 1) * cC;
      v4u qv;
      if (ex) qv = v4u{*(const GAS unsigned*)(src), *(const GAS unsigned*)(src + cA), *(const GAS unsigned*)(src + cB), *(const GAS unsigned*)(src + cA + cB)};
      else qv = *(const GAS v4u_a4*)src;
      SB();
      const int oc = (g >> 2) * 81 + (uf < 81 ? uf : 0), ot = oc / 18, os = oc - ot * 18;
      const bool ox = os >= 16;
      const int orow = (ox ? 288 * (os - 16) + 16 : 36 * os) + quad * (ox ? 72 : 4) + ((lane >> 1) & 1) + ((lane >> 2) & 1) * (ox ? 18 : 2) + ((lane >> 3) & 1) * (ox ? 36 : 18);
      const rsrc_t ro = __builtin_amdgcn_make_buffer_rsrc((void*)p.wo, 0, 576 * 576 * 2, 0x00020000);
      const v4u ov = __builtin_amdgcn_raw_buffer_load_b128(ro, uf < 81 ? orow * 1152 + 32 * ((g & 3) * 9 + ot) + (lane & 1) * 16 : 0x10000000, 0, 0);
      SB();
      __builtin_amdgcn_s_barrier();
      LOAD_X();
      STAMP(41);
      *(LAS v4u*)(T + (lane >> 1) * 48 + (lane & 1) * 16) = (vx && (lane & 1)) ? v4u{0u, 0u, 0u, 0u} : qv;
      *(LAS v4u*)(T + 1536 + (lane >> 1) * 48 + (lane & 1) * 16) = ov;
      LAS unsigned char* R = T + (8 * quad + (l15 >> 2)) * 48 + (lane & 3) * 8;
      const v2u a = __builtin_bit_cast(v2u, __builtin_amdgcn_ds_read_tr16_b64_v4i16((LAS s16x4*)(R)));
      const v2u b = __builtin_bit_cast(v2u, __builtin_amdgcn_ds_read_tr16_b64_v4i16((LAS s16x4*)(R + 4 * 48)));
      const v2u c = __builtin_bit_cast(v2u, __builtin_amdgcn_ds_read_tr16_b64_v4i16((LAS s16x4*)(R + 1536)));
      const v2u d = __builtin_bit_cast(v2u, __builtin_amdgcn_ds_read_tr16_b64_v4i16((LAS s16x4*)(R + 1536 + 4 * 48)));
      stg(v4u{a.x, a.y, b.x, b.y}, rs, lane16,
          uf >= 252 ? WS_BYTES : wb + (tile < 8 ? (tile & 3) * 36 + us * 2 + (tile >> 2) : tile < 10 ? 72 + uc : (tile >> 1) * 36 - 36 + us * 2 + (tile & 1)) * KB);
      stg(v4u{c.x, c.y, d.x, d.y}, rs, lane16, uf < 81 ? WOB + ((g & 3) * 162 + oc) * KB : WS_BYTES);
      const float cq = rows_sum(dot2(a.x, ONES, dot2(a.y, ONES, dot2(b.x, ONES, dot2(b.y, ONES, 0.f)))));
      __builtin_amdgcn_raw_buffer_store_b32(__float_as_uint(cq), rs, uf < 252 && lane < 16 ? CSQ + ((g * 18 + us) * 14 + tile) * 64 + lane * 4 : WS_BYTES, 0, SC1);
      SB();
      STAMP(43);
      DRAIN();
      STAMP(44);
      BAR();
      GET_TAG();
      if (w == 0 && lane == 0) set_flag(p.ws, SFLAGS + g * 256 + j * 8, tag);
      STAGE_X();
      STAMP(2);
      BAR();
      STAMP(3);
      issue_window<0, WIN>(win, rs, lane16, QBASE());
    }
    if (opq_s(w) >= 8) {
      LAS unsigned char* T = L + 125 * KB + (w - 8) * 4608;
      const int item = 4 * j + w - 8, itc = item < 108 ? item : 0;
      const int rbk = itc / 3, cb = 3 * (itc - rbk * 3), kind = rbk >= 18, is = rbk - kind * 18, o = is % 3;
      const int r0 = kind == 0 ? 32 * is : 576 * g + 32 * (is - o) + (o < 2 ? 48 * o : 32);
      const int rn = kind == 0 ? 8 : 4, r4 = kind == 0 ? 4 : o < 2 ? 16 : 48, ld = kind == 0 ? 9216 : 1152;
      const rsrc_t rw = __builtin_amdgcn_make_buffer_rsrc((void*)(kind == 0 ? p.w1 : p.w2), 0, 576 * 4608 * 2, 0x00020000);
      const int jj = lane >> 3;
      const int woff = item < 108 ? (r0 + (jj & 3) + (jj >> 2) * r4) * ld + (kind == 0 ? 1152 * g : 0) + 128 * cb + (lane & 7) * 16 : 0x10000000;
      if (w == 11 && lane < 16) ((volatile LAS int*)(L + L_HC))[lane] = 0;
      __builtin_amdgcn_s_barrier();
      LOAD_X();
      v4u wv[3][4];
#pragma unroll
      for (int n = 0; n < 4; ++n)
#pragma unroll
        for (int c = 0; c < 3; ++c) wv[c][n] = __builtin_amdgcn_raw_buffer_load_b128(rw, woff + c * 128, n * rn * ld, NTW ? 2 : 0);
      SB();
      STAMP(41);
      BAR();
      GET_TAG();
      STAGE_X();
      if (opq_s(w) == 11) {
        wait_flags<4>(p.ws, SFLAGS + (g ^ ((lane >> 3) & 4)) * 256 + (lane & 31) * 8, true, tag);
#pragma unroll
        for (int i = 0; i < 18; ++i) win[i] = ldg(rs, lane16, CSQ + (g * 18 + i) * 896);
      }
      STAMP(2);
      BAR();
      STAMP(3);
      row_table1<2>(L, w - 8, lane);
      if (opq_s(w) == 11) {
        f32x4 cs = __builtin_bit_cast(f32x4, win[0]);
#pragma unroll
        for (int i = 1; i < 18; ++i) add4(cs, win[i]);
        if (lane < 56) *(LAS f32x4*)(L + L_TQ + lane16) = cs;
        row_table1<2>(L, 4, lane);
      }
      float csum[3];
      LAS unsigned char* R = T + (8 * quad + (l15 >> 2)) * 144 + (lane & 3) * 8;
#pragma unroll
      for (int c = 0; c < 3; ++c) {
#pragma unroll
        for (int n = 0; n < 4; ++n) *(LAS v4u*)(T + (n * 8 + (lane >> 3)) * 144 + (lane & 7) * 16) = wv[c][n];
        float cs[4];
#pragma unroll
        for (int tt = 0; tt < 4; ++tt) {
          const v2u a = __builtin_bit_cast(v2u, __builtin_amdgcn_ds_read_tr16_b64_v4i16((LAS s16x4*)(R + tt * 32)));
          const v2u b = __builtin_bit_cast(v2u, __builtin_amdgcn_ds_read_tr16_b64_v4i16((LAS s16x4*)(R + tt * 32 + 4 * 144)));
          wv[c][tt] = v4u{a.x, a.y, b.x, b.y};
          cs[tt] = rows_sum(dot2(a.x, ONES, dot2(a.y, ONES, dot2(b.x, ONES, dot2(b.y, ONES, 0.f)))));
          OPAQUE(cs[tt]);
        }
        csum[c] = quad == 0 ? cs[0] : quad == 1 ? cs[1] : quad == 2 ? cs[2] : cs[3];
      }
      STAMP(45);
      const int dst0 = item < 108 ? wb + (252 + 54 * kind + 3 * is + (4 * cb / 3) * 108) * KB : WS_BYTES;
#pragma unroll
      for (int c = 0; c < 3; ++c) {
#pragma unroll
        for (int tt = 0; tt < 4; ++tt) stg(wv[c][tt], rs, lane16, dst0 + (((4 * c + tt) / 3) * 105 + 4 * c + tt) * KB);
        __builtin_amdgcn_raw_buffer_store_b32(__float_as_uint(csum[c]), rs,
                                              lane * 4 + (item < 108 && kind == 0 ? CS1 + (is * 4608 + 576 * g + 64 * (cb + c)) * 4 : WS_BYTES), 0, SC1);
      }
      SB();
      if (opq_s(w) == 8) issue_window<0, WIN>(win, rs, lane16, QBASE());
    }
  }

#pragma unroll 1
  for (int layer = 0; layer < 2; ++layer) {
    v4u xres;
    {
      LOCALS();
      STAMP(4 + layer * 16);
      {
        const int qbase = QBASE(), nbase = NBASE(), rw = 5 + 5 * layer;
        if (opq_s(w) < 4) qkv_tiles<2, 5, 2, R2, 4>(win, L, rs, lane, qbase, nbase, w, rw STAMP_PASS);
        if ((opq_s(w) & 14) == 4) qkv_tiles<2, 4, 0, R2, 1>(win, L, rs, lane, qbase, nbase, 2 * w + 2, rw STAMP_PASS);
        if (opq_s(w) == 6) qkv_tiles<1, 5, 0, R1, 0>(win, L, rs, lane, qbase, nbase, 8, rw STAMP_PASS);
        if (opq_s(w) == 7) qkv_tiles<1, 5, 1, R1, 0>(win, L, rs, lane, qbase, nbase, 9, rw STAMP_PASS);
      }
      STAMP(5 + layer * 16);
      if ((opq_s(w) & 12) == 4) {
        BAR();
      }
      if ((opq_s(w) & 12) != 4) {
        const int T = w & 3, hp = w >> 3, Tp = (T & 1) ? T - 1 : 4;
        f32x4 mp, ms;
        const int lo = l15 + ((T & 1) ? 9 : 1 + 4 * T), hi = (T & 1) ? 16 : j > 0 ? 8 + 4 * T : 0;
#pragma unroll
        for (int r = 0; r < 4; ++r) {
          const int jj = 4 * quad + r;
          mp[r] = ((jj >= lo) & (jj < hi)) ? 0.f : -1.0e30f;
          ms[r] = (unsigned)(l15 - jj) <= 7u ? 0.f : -1.0e30f;
        }
        int xs = L_QK + lane * 8 + T * 512, xp = L_QK + lane * 8 + Tp * 512, hs = xs + hp * 5120, hq = xp + hp * 5120;
        int om = ab + A_O + (T * 18 + 2 * g + hp) * KB + lane16;
        int oe = quad == hp ? ab + A_O + (T * 18 + 16 + (g >> 2)) * KB + (g & 3) * 256 + hp * 8 + l15 * 16 : WS_BYTES;
        unsigned qm0 = quad == 2 * hp ? ~0u : 0u, qm1 = quad == 2 * hp + 1 ? ~0u : 0u, zr = 0u;
        v4u one4 = {ONES, ONES, ONES, ONES};
        asm volatile("" : "+v"(mp), "+v"(ms), "+v"(xs), "+v"(xp), "+v"(hs), "+v"(hq), "+v"(om), "+v"(oe), "+v"(qm0), "+v"(qm1), "+v"(zr), "+v"(one4));
        BAR();
        const v2u kxs = *(LAS v2u*)(L + xs + 40 * 512);
        const unsigned kxp = *(LAS unsigned*)(L + xp + 40 * 512 + 4);
        v2u q[2], ks[2], kp[2], vs[2], vp[2];
#pragma unroll
        for (int hh = 0; hh < 2; ++hh) {
          q[hh] = *(LAS v2u*)(L + hs + (50 + 5 * hh) * 512);
          kp[hh] = *(LAS v2u*)(L + hq + 5 * hh * 512);
          ks[hh] = *(LAS v2u*)(L + hs + 5 * hh * 512);
        }
        SB();
#pragma unroll
        for (int hh = 0; hh < 2; ++hh) {
          vp[hh] = *(LAS v2u*)(L + hq + (20 + 5 * hh) * 512);
          vs[hh] = *(LAS v2u*)(L + hs + (20 + 5 * hh) * 512);
        }
        const v2u vxp = *(LAS v2u*)(L + xp + 45 * 512), vxs = *(LAS v2u*)(L + xs + 45 * 512);
        SB();
        const f32x4 z = {0.f, 0.f, 0.f, 0.f};
        f32x4 sp[2], ss[2], o[2], ox[2], sm[2];
        float mx[2];
#pragma unroll
        for (int hh = 0; hh < 2; ++hh) {
          const v4u qf = {q[hh].x, q[hh].y, kxs.x & (hh ? qm1 : qm0), zr};
          sp[hh] = mfma(v4u{kp[hh].x, kp[hh].y, kxp, zr}, qf, mp);
          ss[hh] = mfma(v4u{ks[hh].x, ks[hh].y, kxs.y, zr}, qf, ms);
        }
#pragma unroll
        for (int hh = 0; hh < 2; ++hh) {
          const float m0 = __builtin_fmaxf(__builtin_fmaxf(sp[hh][0], sp[hh][1]), sp[hh][2]), m1 = __builtin_fmaxf(__builtin_fmaxf(ss[hh][0], ss[hh][1]), ss[hh][2]);
          mx[hh] = __builtin_fmaxf(__builtin_fmaxf(__builtin_fmaxf(__builtin_fmaxf(m0, sp[hh][3]), ss[hh][3]), m1), m1);
        }
        rows_max2(mx[0], mx[1]);
#pragma unroll
        for (int hh = 0; hh < 2; ++hh) {
          const float c = 0.23570226039551584f * 1.4426950408889634f, mc = -c * mx[hh];
          float e[8];
#pragma unroll
          for (int r = 0; r < 4; ++r) {
            e[r] = __builtin_amdgcn_exp2f(__builtin_fmaf(sp[hh][r], c, mc));
            e[4 + r] = __builtin_amdgcn_exp2f(__builtin_fmaf(ss[hh][r], c, mc));
          }
          const v4u pf = {pk(e[0], e[1]), pk(e[2], e[3]), pk(e[4], e[5]), pk(e[6], e[7])};
          sm[hh] = mfma(one4, pf, z);
          o[hh] = mfma(v4u{vp[hh].x, vp[hh].y, vs[hh].x, vs[hh].y}, pf, z);
          ox[hh] = mfma(v4u{vxp.x, vxp.y, vxs.x, vxs.y}, pf, z);
        }
        const float i0 = __builtin_amdgcn_rcpf(sm[0][0]), i1 = __builtin_amdgcn_rcpf(sm[1][0]);
        stg(v4u{pk(o[0][0] * i0, o[0][1] * i0), pk(o[0][2] * i0, o[0][3] * i0), pk(o[1][0] * i1, o[1][1] * i1), pk(o[1][2] * i1, o[1][3] * i1)}, rs, om, 0);
        __builtin_amdgcn_raw_buffer_store_b64(v2u{pk(ox[0][0] * i0, ox[0][1] * i0), pk(ox[1][2] * i1, ox[1][3] * i1)}, rs, oe, 0, SC1);
        STAMP(34);
        DRAIN();
      }
      {
        const int ft = (g & 3) * 9 + (w < 9 ? w : 0);
        xres = *(LAS v4u*)(L + ((g >> 2) * 36 + (quad & 1) * 18 + (ft >> 1)) * KB + ((2 * (ft & 1) + (quad >> 1)) * 16 + l15) * 16);
      }
      STAMP(6 + layer * 16);
      BAR();
      if (w == 7) {
        if (lane == 0) set_flag(p.ws, FLAGS + 2 * layer * 4096 + j * 128 + g * 8, tag);
        wait_flags(p.ws, FLAGS + 2 * layer * 4096 + j * 128 + (lane & 7) * 8, lane < 8, tag);
      }
      if (layer == 0 && w == 6 && lane == 0) set_flag(p.ws, SFLAGS + 2048 + g * 256 + j * 8, tag);
      BAR();
      STAMP(7 + layer * 16);
    }
    {
      LOCALS();
      const int th = g >> 2, ft = (g & 3) * 9 + w;
      v4u orr[3];
#pragma unroll
      for (int i = 0; i < 3; ++i) orr[i] = ldg(rs, lane16, ab + A_O + (th * 36 + w * 3 + i) * KB);
      const int xoff = ((quad & 1) * 18 + (ft >> 1)) * KB + ((2 * (ft & 1) + (quad >> 1)) * 16 + l15) * 16;
      const v4u xr = xres;
#pragma unroll
      for (int i = 0; i < 3; ++i) *(LAS v4u*)(L + (w * 3 + i) * KB + lane16) = orr[i];
      if (layer == 0 && w == 0) wait_flags<2>(p.ws, SFLAGS + 2048 + g * 256 + (lane & 31) * 8, lane < 32, tag);
      BAR();
      STAMP(8 + layer * 16);
      if (w < 9) {
        f32x4 acc[1][2];
        gemm18<1, 2, 0, P2N, 10>(acc, win, L + lane16, rs, lane16, 0, MBASE());
        float a[8];
#pragma unroll
        for (int r = 0; r < 4; ++r) {
          auto s = __builtin_amdgcn_permlane16_swap(__float_as_uint(acc[0][0][r]), __float_as_uint(acc[0][1][r]), false, false);
          a[r] = __uint_as_float(s[0]);
          a[4 + r] = __uint_as_float(s[1]);
        }
        const v4u o = {pk(a[0] + blo(xr.x), a[1] + bhi(xr.x)), pk(a[2] + blo(xr.y), a[3] + bhi(xr.y)),
                       pk(a[4] + blo(xr.z), a[5] + bhi(xr.z)), pk(a[6] + blo(xr.w), a[7] + bhi(xr.w))};
        stg(o, rs, xoff, ab + A_X1 + th * 36 * KB);
        STAMP(35);
        DRAIN();
      }
      if (layer == 0 && opq_s(w) >= 9) {
        const int f4 = (w - 9) * 48 + (lane < 48 ? lane : 0);
        f32x4 cs = {0.f, 0.f, 0.f, 0.f};
#pragma unroll
        for (int i = 0; i < 18; ++i) win[i] = ldg(rs, f4 * 16, CS1 + (i * 4608 + 576 * g) * 4);
        const v2u b = *(const GAS v2u*)(p.b1 + 576 * g + 4 * f4);
#pragma unroll
        for (int i = 0; i < 18; ++i) add4(cs, win[i]);
        *(LAS f32x4*)(L + L_T1 + f4 * 16) = cs;
        *(LAS f32x4*)(L + L_T1 + 2304 + f4 * 16) = f32x4{blo(b.x), bhi(b.x), blo(b.y), bhi(b.y)};
        SB();
        issue_window<0, 15>(win, rs, lane16, MBASE());
      }
      if (layer == 1 && bid == 0 && w == 10) {
#pragma unroll 1
        for (int i = 0; i < 4; ++i) wait_flags(p.ws, SFLAGS + (i * 64 + lane) * 8, true, tag);
        if (lane == 0) set_flag(p.ws, 0, tag);
      }
      STAMP(9 + layer * 16);
      BAR();
      if (w < 9) issue_window<P2N, 15>(win, rs, lane16, MBASE());
      if (w == 11) {
        if (lane == 0) set_flag(p.ws, FLAGS + (2 * layer + 1) * 4096 + j * 128 + g * 8, tag);
        wait_flags(p.ws, FLAGS + (2 * layer + 1) * 4096 + j * 128 + (lane & 7) * 8, lane < 8, tag);
      }
      BAR();
      STAMP(10 + layer * 16);
    }
    {
      LOCALS();
      const int mbase = MBASE();
      const int T = w & 3, cls = w >> 2;
      if (opq_s(cls) == 0) {
        if (w == 0 && lane < 8) ((volatile LAS int*)(L + L_HC))[lane] = lane == 3 ? 4 : 0;
        BAR();
      }
      if (opq_s(cls) > 0) {
        v4u xr[9];
#pragma unroll
        for (int i = 0; i < 9; ++i) xr[i] = ldf(rs, lane16, ab + A_X1 + (T * 18 + 9 * (cls - 1)) * KB, i);
#pragma unroll
        for (int i = 0; i < 9; ++i) *(LAS v4u*)(L + (T * 18 + 9 * (cls - 1) + i) * KB + lane16) = xr[i];
        BAR();
        float s1 = 0.f, s2 = 0.f, s3 = 0.f, s4 = 0.f;
#pragma unroll
        for (int i = 0; i < 9; ++i)
#pragma unroll
          for (int e = 0; e < 4; e += 2) {
            s1 = dot2(xr[i][e], ONES, s1); s2 = dot2(xr[i][e], xr[i][e], s2);
            s3 = dot2(xr[i][e + 1], ONES, s3); s4 = dot2(xr[i][e + 1], xr[i][e + 1], s4);
          }
        s1 += s3; OPAQUE(s1); s2 += s4;
        s1 = rows_sum(s1); OPAQUE(s1); s2 = rows_sum(s2);
        if (lane < 16) *(LAS f32x2*)(L + L_ST + ((T * 16 + lane) * 2 + cls - 1) * 8) = f32x2{s1, s2};
        cnt_add(L, C_ST, lane);
      }
      STAMP(11 + layer * 16);
      f32x4 acc[3][4];
#pragma unroll
      for (int t = 0; t < 3; ++t)
#pragma unroll
        for (int m = 0; m < 4; ++m) acc[t][m] = f32x4{0.f, 0.f, 0.f, 0.f};
      if (opq_s(cls) > 0) { row_table(L, lane); shift_acc(acc, L, w, quad, l15); }
      __builtin_amdgcn_s_setprio(3);
      gemm_mlp<false>(acc, win, L + lane16, rs, lane16, mbase, lane16, mbase + 54 * KB, L);
      STAMP(12 + layer * 16);
      {
        LAS unsigned char* H = L + L_H + lane16 + 3 * (w >> 1) * KB;
        if (opq_s(cls) == 0) {
          cnt_wait(L, C_RT, 1);
          shift_acc(acc, L, w, quad, l15);
        }
        f32x4 rt[4];
#pragma unroll
        for (int m = 0; m < 4; ++m) rt[m] = *(LAS f32x4*)(L + L_ST + KB + (m * 16 + l15) * 16);
#pragma unroll
        for (int m = 0; m < 4; ++m) {
          const v2u u0 = gelu4(acc[0][m], rt[m][2], rt[m][3], rt[m][1]), u1 = gelu4(acc[1][m], rt[m][2], rt[m][3], rt[m][1]);
          *(LAS v4u*)(H + (m * 18 + (w & 1)) * KB) = v4u{u0.x, u0.y, u1.x, u1.y};
          *(LAS v2u*)(H + (m * 18 + 2) * KB + (w & 1) * 8) = gelu4(acc[2][m], rt[m][2], rt[m][3], rt[m][1]);
        }
        cnt_add(L, C_H0 + cls, lane);
      }
      STAMP(13 + layer * 16);
      if (cls == 0) __builtin_amdgcn_s_setprio(2);
      else if (cls == 1) __builtin_amdgcn_s_setprio(1);
      else __builtin_amdgcn_s_setprio(0);
#pragma unroll
      for (int t = 0; t < 3; ++t)
#pragma unroll
        for (int m = 0; m < 4; ++m) acc[t][m] = f32x4{0.f, 0.f, 0.f, 0.f};
      gemm_mlp<true>(acc, win, L + L_H + lane16, rs, lane16, mbase + 54 * KB, layer ? lane16 + WS_BYTES : lane16, QBASE(), L);
      __builtin_amdgcn_s_setprio(0);
      cnt_add(L, C_GD, lane);
      STAMP(14 + layer * 16);
      const int pw = ab + A_PT + (g * 12 + w) * 6144;
      const int poff = (quad & 1) * 1536 + (quad >> 1) * 256 + l15 * 16;
#pragma unroll
      for (int t = 0; t < 3; ++t)
#pragma unroll
        for (int m = 0; m < 4; m += 2) {
          const v2u a = {pk(acc[t][m][0], acc[t][m][1]), pk(acc[t][m][2], acc[t][m][3])};
          const v2u b = {pk(acc[t][m + 1][0], acc[t][m + 1][1]), pk(acc[t][m + 1][2], acc[t][m + 1][3])};
          stg(widen(a, b), rs, poff + m * 1536 + t * 512, pw);
        }
      DRAIN();
      if (lane == 0) set_flag(p.ws, WFLAGS + ((layer * 32 + j) * 12 + w) * 128 + g * 8, tag);
      STAMP(15 + layer * 16);
    }
    {
      LOCALS();
      OWNROWS(6);
      const bool act = lane < 48;
      const v4u xr = *(LAS v4u*)(L + (act ? piece : 0));
      const v4u bb = *(const GAS v4u*)(p.b2 + (act ? c8 : 0) * 8);
      const int tail = layer ? TAIL1 : TAIL0;
      if (tail == 1) cnt_wait(L, C_LC, 12);
      if (tail == 2) cnt_wait(L, C_GD, 12);
      wait_flags(p.ws, WFLAGS + ((layer * 32 + j) * 12 + w) * 128 + (lane & 7) * 8, lane < 8, tag);
      STAMP(16 + layer * 16);
      v4u xb;
      {
        v4u pr[8];
        const int roff = (g >> 1) * 1536 + (lane >> 3) * 256 + (8 * (g & 1) + (lane & 7)) * 16 + (act ? 0 : WS_BYTES);
#pragma unroll
        for (int i = 0; i < 8; ++i) pr[i] = ldg(rs, roff, ab + A_PT + (i * 12 + w) * 6144);
        SB();
        const unsigned sel_lo = opq_s(0x00003f80), sel_hi = opq_s(0x3f800000);
#pragma unroll
        for (int e = 0; e < 4; ++e) {
          float lo = blo(bb[e]) + blo(xr[e]), hi = bhi(bb[e]) + bhi(xr[e]);
          OPAQUE(lo);
#pragma unroll
          for (int i = 0; i < 8; ++i) { lo = dot2(pr[i][e], sel_lo, lo); hi = dot2(pr[i][e], sel_hi, hi); }
          xb[e] = pk(lo, hi);
        }
      }
      STAMP(17 + layer * 16);
      if (layer == 0) {
        float s1 = dot2(xb.x, ONES, dot2(xb.y, ONES, dot2(xb.z, ONES, dot2(xb.w, ONES, 0.f))));
        float s2 = dot2(xb.x, xb.x, dot2(xb.y, xb.y, dot2(xb.z, xb.z, dot2(xb.w, xb.w, 0.f))));
        if (!act) { s1 = 0.f; s2 = 0.f; }
        s1 += ror8(s1); OPAQUE(s1); s2 += ror8(s2);
        s1 = rows_sum(s1); OPAQUE(s1); s2 = rows_sum(s2);
        if (!act) { xb.x = __float_as_uint(s1); xb.y = __float_as_uint(s2); }
        if (lane < 56) stg(xb, rs, lane16, ab + ALY + A_X2 + (g * 12 + w) * KB);
        DRAIN();
        STAMP(36);
        BAR();
        if (w == 0) {
          if (lane == 0) set_flag(p.ws, FLAGS + 4 * 4096 + j * 128 + g * 8, tag);
          wait_flags(p.ws, FLAGS + 4 * 4096 + (j - (lane >> 3)) * 128 + (lane < 8 ? lane : 3 + 4 * (lane & 1)) * 8, lane < (j > 0 ? 10 : 8), tag);
        } else issue_window<15, WIN>(win, rs, lane16, QBASE());
        BAR();
        STAMP(37);
        v4u xv[10];
        const int vo = lane < 56 ? lane16 : lane16 + WS_BYTES;
#pragma unroll
        for (int i = 0; i < 10; ++i)
          xv[i] = ldg(rs, vo, ab + ALY + A_X2 + w * KB + (i < 8 ? i * 12 * KB : (3 + 4 * (i - 8)) * 12 * KB - (j > 0 ? ASL : 0)));
        SB();
        if (opq_s(w) == 0) issue_window<15, WIN>(win, rs, lane16, QBASE());
#pragma unroll
        for (int i = 0; i < 10; ++i) KEEP(xv[i]);
        LAS unsigned char* I = L + (c8 >> 2) * KB + ((c8 & 3) * 16 + (lane & 7)) * 16;
        LAS unsigned char* S = L + L_ST + (w * 80 + (lane & 7)) * 8;
#pragma unroll
        for (int i = 0; i < 10; ++i)
          if (act) *(LAS v4u*)(I + (i >> 1) * 18 * KB + (i & 1) * 128) = xv[i];
        if (!act && lane < 56) {
#pragma unroll
          for (int i = 0; i < 10; ++i) *(LAS v2u*)(S + i * 64) = v2u{xv[i].x, xv[i].y};
        }
        BAR();
        STAMP(18);
        if (w == 11) row_table1<12>(L, 4, lane);
        if (w >= 8) row_table1<12>(L, w - 8, lane);
      } else {
        if (act) *(GAS v4u*)(p.out + (size_t)mytok * 576 + c8 * 8) = xb;
      }
    }
  }
  STAMP(40);
  STAMP_DUMP();
}
}

extern "C" void kernel_launch(void* const* d_in, const int* in_sizes, int n_in, void* d_out, int out_size, void* d_ws, size_t ws_size,
                              hipStream_t stream) {
  (void)in_sizes; (void)n_in; (void)out_size; (void)ws_size;
  Params p;
  p.x = (const unsigned short*)d_in[0];
  p.wqkv = (const unsigned short*)d_in[1];
  p.wo = (const unsigned short*)d_in[2];
  p.w1 = (const unsigned short*)d_in[4];
  p.b1 = (const unsigned short*)d_in[5];
  p.w2 = (const unsigned short*)d_in[6];
  p.b2 = (const unsigned short*)d_in[7];
  p.out = (unsigned short*)d_out;
  p.ws = (unsigned char*)d_ws;
  (void)hipFuncSetAttribute((const void*)fused_fwd, hipFuncAttributeMaxDynamicSharedMemorySize, LDS_BYTES);
  hipLaunchKernelGGL(fused_fwd, dim3(256), dim3(NTHR), LDS_BYTES, stream, p);
}
```

```cpp
#define NTW 0
#include <hip/hip_runtime.h>
#include <hip/hip_bf16.h>
#include <stdint.h>

namespace {
#define LAS __attribute__((address_space(3)))
#define GAS __attribute__((address_space(1)))
typedef unsigned v4u __attribute__((ext_vector_type(4)));
typedef unsigned v4u_a4 __attribute__((ext_vector_type(4), aligned(4)));
typedef unsigned v2u __attribute__((ext_vector_type(2)));
typedef float f32x4 __attribute__((ext_vector_type(4)));
typedef float f32x2 __attribute__((ext_vector_type(2)));
typedef __bf16 bf16x8 __attribute__((ext_vector_type(8)));
typedef __bf16 bf16x2 __attribute__((ext_vector_type(2)));
typedef short s16x4 __attribute__((ext_vector_type(4)));
typedef unsigned long long u64;
typedef __amdgpu_buffer_rsrc_t rsrc_t;

constexpr int KB = 1024;
constexpr int NTHR = 768;
constexpr int WIN = 18;
#define XB 2
#define P2N 9
#define TAIL0 2
#define TAIL1 2
#define R1 10
#define R2 7
#define PRET 0
#define REUSE 0
constexpr float EPS = 1e-5f;
constexpr int SC1 = 16;
constexpr float GC0 = -2.0f * 0.7978845608028654f * 1.4426950408889634f;
constexpr int FLAGS = 4096;
constexpr int SFLAGS = 32 * KB;
constexpr int WFLAGS = 48 * KB;
constexpr int CS1 = 192 * KB;
constexpr int CSQ = 520 * KB;
constexpr int WBASE = 704 * KB;
constexpr int WSL = (252 + 1296) * KB;
constexpr int WOB = WBASE + 8 * WSL;
constexpr int ABASE = 14 * 1024 * KB;
constexpr int IMG = 72 * KB;
constexpr int A_X2 = 0, A_O = 96 * KB, A_X1 = A_O + IMG, A_PT = A_X1 + IMG, ALY = REUSE ? 0 : 1024 * KB, ASL = REUSE ? 1024 * KB : 2 * ALY;
constexpr int WS_BYTES = ABASE + 32 * ASL;
static_assert(WOB + 648 * KB <= ABASE && A_PT + 8 * IMG <= 1024 * KB && CSQ + 8 * 14 * 1152 + 256 <= WBASE, "workspace map");
constexpr int L_H = IMG;
constexpr int L_QK = 90 * KB;
constexpr int L_ST = 145 * KB;
constexpr int L_T1 = L_ST + 7680;
constexpr int L_TQ = L_T1 + 4608;
constexpr int L_RT = L_TQ + 896;
constexpr int L_HC = L_RT + 640;
constexpr int LDS_BYTES = L_HC + 64;
enum { C_H0 = 0, C_ST = 4, C_RT = 5, C_GD = 6, C_LC = 7, C_XE = 8, C_XL = 9, C_R1 = 10 };

struct Params {
  const unsigned short *x, *wqkv, *wo, *w1, *b1, *w2, *b2;
  unsigned short *out;
  unsigned char *ws;
};

#define STAMP(i)
#define STAMP_INIT()
#define STAMP_ARG
#define STAMP_PASS
#define STAMP_DUMP()
#define SB() __builtin_amdgcn_sched_barrier(0)
#define BAR() do { asm volatile("s_waitcnt lgkmcnt(0)" ::: "memory"); __builtin_amdgcn_s_barrier(); asm volatile("" ::: "memory"); } while (0)
#define DRAIN() asm volatile("s_waitcnt vmcnt(0)" ::: "memory")
#define OPAQUE(x) asm("" : "+v"(x))
#define KEEP(x) asm volatile("" :: "v"(x))

__device__ __forceinline__ f32x4 mfma(v4u a, v4u b, f32x4 c) {
  return __builtin_amdgcn_mfma_f32_16x16x32_bf16(__builtin_bit_cast(bf16x8, a), __builtin_bit_cast(bf16x8, b), c, 0, 0, 0);
}
#define MF(acc, a, b) asm volatile("v_mfma_f32_16x16x32_bf16 %0, %1, %2, %0" : "+v"(acc) : "v"(a), "v"(b))
#define MF0(acc, a, b) asm volatile("v_mfma_f32_16x16x32_bf16 %0, %1, %2, 0" : "=&v"(acc) : "v"(a), "v"(b))
template <int NT, int MT> __device__ __forceinline__ void acc_fence(f32x4 (&acc)[NT][MT]) {
#pragma unroll
  for (int t = 0; t < NT; ++t)
#pragma unroll
    for (int m = 0; m < MT; ++m) asm volatile("" : "+v"(acc[t][m]));
}
#define ACC_IN(acc) do { acc_fence(acc); asm volatile("s_nop 1"); SB(); } while (0)
#define ACC_OUT(acc) do { SB(); asm volatile("s_nop 7\n\ts_nop 1"); acc_fence(acc); } while (0)
__device__ __forceinline__ unsigned pk(float a, float b) {
  f32x2 v = {a, b};
  return __builtin_bit_cast(unsigned, __builtin_convertvector(v, bf16x2));
}
__device__ __forceinline__ float dot2(unsigned a, unsigned b, float c) {
  return __builtin_amdgcn_fdot2_f32_bf16(__builtin_bit_cast(bf16x2, a), __builtin_bit_cast(bf16x2, b), c, false);
}
constexpr unsigned ONES = 0x3f803f80u;
__device__ __forceinline__ float blo(unsigned u) { return __uint_as_float(u << 16); }
__device__ __forceinline__ float bhi(unsigned u) { return __uint_as_float(u & 0xffff0000u); }
__device__ __forceinline__ v4u ldg(rsrc_t rs, int voff, int soff) { return __builtin_amdgcn_raw_buffer_load_b128(rs, voff, soff, SC1); }
__device__ __forceinline__ void stg(v4u v, rsrc_t rs, int voff, int soff) { __builtin_amdgcn_raw_buffer_store_b128(v, rs, voff + soff, 0, SC1); }
__device__ __forceinline__ v4u ldf(rsrc_t rs, int lane16, int base, int f) { return ldg(rs, lane16 + (f & 3) * KB, base + (f >> 2) * 4 * KB); }
__device__ __forceinline__ int opq_v(int x) { asm volatile("" : "+v"(x)); return x; }
__device__ __forceinline__ int lane_id() { int x; asm volatile("v_mbcnt_lo_u32_b32 %0, -1, 0\n\tv_mbcnt_hi_u32_b32 %0, -1, %0" : "=v"(x)); return x; }
__device__ __forceinline__ int opq_s(int x) { asm volatile("" : "+s"(x)); return x; }
#define LOCALS() const int lane = lane_id(), w = opq_s(w0), j = opq_s(j0), g = opq_s(g0); \
  const int lane16 = lane * 16, quad = lane >> 4, l15 = lane & 15, ab = ABASE + j * ASL + layer * ALY, wb = WBASE + g * WSL; \
  (void)lane16; (void)quad; (void)l15; (void)ab; (void)wb
#define OWNROWS(CPW) const int c8 = CPW * w + (lane >> 3); \
  const int piece = ((g >> 1) * 18 + (c8 >> 2)) * KB + ((c8 & 3) * 16 + 8 * (g & 1) + (lane & 7)) * 16; \
  const int mytok = 64 * j + 2 * (16 * ((g >> 1) & 1) + 8 * (g & 1) + (lane & 7)) + (g >> 2)
#define GET_TAG() tag = 1 + ((u64)(unsigned)__builtin_amdgcn_readfirstlane((int)(unsigned)tag0) | \
                             ((u64)(unsigned)__builtin_amdgcn_readfirstlane((int)(unsigned)(tag0 >> 32)) << 32))
#define NBASE() opq_s(nb0)
#define QBASE() opq_s(qb0)
#define MBASE() opq_s(mb0)
__device__ __forceinline__ float rows_sum(float x) {
  auto s = __builtin_amdgcn_permlane16_swap(__float_as_uint(x), __float_as_uint(x), false, false);
  x = __uint_as_float(s[0]) + __uint_as_float(s[1]);
  auto t = __builtin_amdgcn_permlane32_swap(__float_as_uint(x), __float_as_uint(x), false, false);
  return __uint_as_float(t[0]) + __uint_as_float(t[1]);
}
__device__ __forceinline__ float vmax(unsigned a, unsigned b) { float r; asm("v_max_f32 %0, %1, %2\n\ts_nop 1" : "=v"(r) : "v"(a), "v"(b)); return r; }
__device__ __forceinline__ void rows_max2(float& a, float& b) {
  auto s = __builtin_amdgcn_permlane16_swap(__float_as_uint(a), __float_as_uint(b), false, false);
  float x = vmax(s[0], s[1]);
  auto t = __builtin_amdgcn_permlane32_swap(__float_as_uint(x), __float_as_uint(x), false, false);
  x = vmax(t[0], t[1]);
  auto u = __builtin_amdgcn_permlane16_swap(__float_as_uint(x), __float_as_uint(x), false, false);
  a = __uint_as_float(u[0]);
  b = __uint_as_float(u[1]);
}
template <int CTRL> __device__ __forceinline__ float dpp(float x) {
  return __int_as_float(__builtin_amdgcn_mov_dpp(__float_as_int(x), CTRL, 0xf, 0xf, true));
}
__device__ __forceinline__ float ror8(float x) { return dpp<0x128>(x); }
__device__ __forceinline__ void add4(f32x4& a, v4u b) {
  float a0 = a[0], a1 = a[1], a2 = a[2], a3 = a[3];
  asm("v_add_f32 %0, %0, %4\n\tv_add_f32 %1, %1, %5\n\tv_add_f32 %2, %2, %6\n\tv_add_f32 %3, %3, %7"
      : "+v"(a0), "+v"(a1), "+v"(a2), "+v"(a3) : "v"(b.x), "v"(b.y), "v"(b.z), "v"(b.w));
  a = f32x4{a0, a1, a2, a3};
}
__device__ __forceinline__ v4u widen(v2u a, v2u b) {
  auto s0 = __builtin_amdgcn_permlane16_swap(a.x, b.x, false, false);
  auto s1 = __builtin_amdgcn_permlane16_swap(a.y, b.y, false, false);
  return v4u{s0[0], s1[0], s0[1], s1[1]};
}
__device__ __forceinline__ v2u gelu4(f32x4 a, float k0, float k1, float is) {
  float s0, s1, s2, s3;
  unsigned u0, u1;
  asm("v_mul_f32 %2, %6, %6\n\tv_mul_f32 %3, %7, %7\n\tv_fma_f32 %2, %2, %11, %10\n\tv_fma_f32 %3, %3, %11, %10\n\t"
      "v_mul_f32 %2, %2, %6\n\tv_mul_f32 %3, %3, %7\n\tv_exp_f32 %2, %2\n\tv_mul_f32 %4, %8, %8\n\t"
      "v_exp_f32 %3, %3\n\tv_mul_f32 %5, %9, %9\n\tv_fma_f32 %4, %4, %11, %10\n\tv_fma_f32 %5, %5, %11, %10\n\t"
      "v_mul_f32 %4, %4, %8\n\tv_mul_f32 %5, %5, %9\n\tv_exp_f32 %4, %4\n\tv_fma_f32 %2, %2, %12, %12\n\t"
      "v_exp_f32 %5, %5\n\tv_fma_f32 %3, %3, %12, %12\n\tv_rcp_f32 %2, %2\n\tv_fma_f32 %4, %4, %12, %12\n\t"
      "v_rcp_f32 %3, %3\n\tv_fma_f32 %5, %5, %12, %12\n\tv_rcp_f32 %4, %4\n\tv_mul_f32 %2, %2, %6\n\t"
      "v_rcp_f32 %5, %5\n\tv_mul_f32 %3, %3, %7\n\tv_mul_f32 %4, %4, %8\n\tv_cvt_pk_bf16_f32 %0, %2, %3\n\t"
      "v_mul_f32 %5, %5, %9\n\tv_cvt_pk_bf16_f32 %1, %4, %5"
      : "=&v"(u0), "=&v"(u1), "=&v"(s0), "=&v"(s1), "=&v"(s2), "=&v"(s3)
      : "v"(a[0]), "v"(a[1]), "v"(a[2]), "v"(a[3]), "v"(k0), "v"(k1), "v"(is));
  return v2u{u0, u1};
}
__device__ __forceinline__ void shift4(f32x4& a, f32x4 cs, f32x4 b, float nmean, float is) {
  float a0 = a[0], a1 = a[1], a2 = a[2], a3 = a[3];
  asm("v_fmac_f32 %0, %12, %4\n\tv_fmac_f32 %1, %12, %5\n\tv_fmac_f32 %2, %12, %6\n\tv_fmac_f32 %3, %12, %7\n\t"
      "v_fmac_f32 %0, %13, %8\n\tv_fmac_f32 %1, %13, %9\n\tv_fmac_f32 %2, %13, %10\n\tv_fmac_f32 %3, %13, %11"
      : "+v"(a0), "+v"(a1), "+v"(a2), "+v"(a3)
      : "v"(cs[0]), "v"(cs[1]), "v"(cs[2]), "v"(cs[3]), "v"(b[0]), "v"(b[1]), "v"(b[2]), "v"(b[3]), "v"(nmean), "v"(is));
  a = f32x4{a0, a1, a2, a3};
}
__device__ __forceinline__ int cnt_get(LAS unsigned char* L, int i) { return ((volatile LAS int*)(L + L_HC))[i]; }
__device__ __forceinline__ void cnt_wait(LAS unsigned char* L, int i, int n) {
  while (__builtin_amdgcn_readfirstlane(cnt_get(L, i)) < n) __builtin_amdgcn_s_sleep(1);
  asm volatile("" ::: "memory");
}
__device__ __forceinline__ void cnt_add(LAS unsigned char* L, int i, int lane) {
  asm volatile("s_waitcnt lgkmcnt(0)" ::: "memory");
  if (lane == 0) __hip_atomic_fetch_add((LAS int*)(L + L_HC) + i, 1, __ATOMIC_RELAXED, __HIP_MEMORY_SCOPE_WORKGROUP);
}
template <int NP> __device__ __forceinline__ void row_table1(LAS unsigned char* L, int t5, int lane) {
  const int r = t5 * 16 + (lane & 15), q = lane >> 4;
  float t1 = 0.f, t2 = 0.f;
#pragma unroll
  for (int i = 0; i < (NP + 3) / 4; ++i)
    if (NP % 4 == 0 || q < NP) { const f32x2 v = *(LAS f32x2*)(L + L_ST + ((i * 4 + q) * 80 + r) * 8); t1 += v.x; OPAQUE(t1); t2 += v.y; }
  t1 = rows_sum(t1); OPAQUE(t1); t2 = rows_sum(t2);
  const float nmean = t1 * (-1.0f / 576.0f);
  const float var = fmaxf(__builtin_fmaf(-nmean, nmean, t2 * (1.0f / 576.0f)), 0.f);
  const float rstd = __builtin_amdgcn_rsqf(var + EPS);
  if (q < 2) *(LAS float*)(L + L_RT + q * 320 + r * 4) = q ? nmean * rstd : rstd;
  cnt_add(L, C_R1, lane);
}
__device__ __forceinline__ v2u ln_pack(f32x4 a, f32x4 r, f32x4 c, f32x4 nm) {
  float t0, t1, t2, t3;
  unsigned u0, u1;
  asm("v_mul_f32 %2, %14, %18\n\tv_mul_f32 %3, %15, %19\n\tv_mul_f32 %4, %16, %20\n\tv_mul_f32 %5, %17, %21\n\t"
      "v_fmac_f32 %2, %6, %10\n\tv_fmac_f32 %3, %7, %11\n\tv_fmac_f32 %4, %8, %12\n\tv_fmac_f32 %5, %9, %13\n\t"
      "v_cvt_pk_bf16_f32 %0, %2, %3\n\tv_cvt_pk_bf16_f32 %1, %4, %5"
      : "=&v"(u0), "=&v"(u1), "=&v"(t0), "=&v"(t1), "=&v"(t2), "=&v"(t3)
      : "v"(a[0]), "v"(a[1]), "v"(a[2]), "v"(a[3]), "v"(r[0]), "v"(r[1]), "v"(r[2]), "v"(r[3]), "v"(c[0]), "v"(c[1]), "v"(c[2]), "v"(c[3]),
        "v"(nm[0]), "v"(nm[1]), "v"(nm[2]), "v"(nm[3]));
  return v2u{u0, u1};
}
__device__ __forceinline__ f32x4 splat(float x) { return f32x4{x, x, x, x}; }
__device__ __forceinline__ void row_table(LAS unsigned char* L, int lane) {
  cnt_wait(L, C_ST, 8);
  const f32x4 q = *(LAS f32x4*)(L + L_ST + lane * 16);
  float t1 = q[0] + q[2];
  OPAQUE(t1);
  float nmean = t1 * (-1.0f / 576.0f);
  const float var = fmaxf(__builtin_fmaf(-nmean, nmean, (q[1] + q[3]) * (1.0f / 576.0f)), 0.f);
  const float r1 = __builtin_amdgcn_rsqf(var + EPS);
  const float sc = r1 * __builtin_amdgcn_rsqf(var * r1 * r1 + EPS);
  float k0 = GC0 * sc;
  OPAQUE(k0);
  *(LAS f32x4*)(L + L_ST + KB + lane * 16) = f32x4{nmean, __builtin_amdgcn_rcpf(sc), k0, k0 * (0.044715f * sc * sc)};
  cnt_add(L, C_RT, lane);
}
__device__ __forceinline__ void shift_acc(f32x4 (&acc)[3][4], LAS unsigned char* L, int w, int quad, int l15) {
  f32x2 rt[4];
#pragma unroll
  for (int m = 0; m < 4; ++m) rt[m] = *(LAS f32x2*)(L + L_ST + KB + (m * 16 + l15) * 16);
#pragma unroll
  for (int t = 0; t < 3; ++t) {
    const f32x4 cs = *(LAS f32x4*)(L + L_T1 + (48 * w + 16 * t + 4 * quad) * 4), bb = *(LAS f32x4*)(L + L_T1 + 2304 + (48 * w + 16 * t + 4 * quad) * 4);
#pragma unroll
    for (int m = 0; m < 4; ++m) shift4(acc[t][m], cs, bb, rt[m][0], rt[m][1]);
  }
}
template <int SLEEP = 1> __device__ __forceinline__ void wait_flags(const unsigned char* ws, int off, bool active, u64 tag) {
  const GAS u64* f = (const GAS u64*)(ws + off);
  asm volatile("" : "+s"(tag));
#pragma unroll 1
  for (int spins = 0; spins < (1 << 16); ++spins) {
    const u64 v = active ? __hip_atomic_load(f, __ATOMIC_RELAXED, __HIP_MEMORY_SCOPE_AGENT) : tag;
    if (__all(v == tag)) break;
    __builtin_amdgcn_s_sleep(SLEEP);
  }
  asm volatile("" ::: "memory");
}
__device__ __forceinline__ void set_flag(unsigned char* ws, int off, u64 tag) {
  asm volatile("" : "+s"(tag));
  __hip_atomic_store((GAS u64*)(ws + off), tag, __ATOMIC_RELAXED, __HIP_MEMORY_SCOPE_AGENT);
}
template <int LO, int HI> __device__ __forceinline__ void issue_window(v4u (&win)[WIN], rsrc_t rs, int lane16, int base) {
#pragma unroll
  for (int i = LO; i < HI; ++i) win[i] = ldf(rs, lane16, base, i);
  SB();
}

template <int NT, int MT, int SWAP, int NNEXT, int R, int LO, int HI>
__device__ __forceinline__ void gemm18_steps(f32x4 (&acc)[NT][MT], v4u (&win)[WIN], v4u (&act)[R], LAS unsigned char* img, LAS unsigned char* img2, rsrc_t rs,
                                             int lane16, int wbase, int nvo, int nbase) {
#pragma unroll
  for (int n = LO; n < HI; ++n) {
    const int s = n / MT, m = n % MT, nn = n + R - 1;
    if (n < 18 * MT) {
#pragma unroll
      for (int t = 0; t < NT; ++t) {
        if (s == 0) {
          if ((SWAP >> t) & 1) MF0(acc[t][m], act[n % R], win[t]);
          else MF0(acc[t][m], win[t], act[n % R]);
        } else if ((SWAP >> t) & 1) MF(acc[t][m], act[n % R], win[(s * NT + t) % WIN]);
        else MF(acc[t][m], win[(s * NT + t) % WIN], act[n % R]);
      }
      SB();
    }
    if (nn < 18 * MT) act[nn % R] = *(LAS v4u*)(nn % MT < 3 ? img + ((nn % MT) * 18 + nn / MT) * KB : img2 + ((nn % MT - 3) * 18 + nn / MT) * KB);
    if (m == 0 && s > 0) {
#pragma unroll
      for (int t = 0; t < NT; ++t) {
        const int f = (s - 1) * NT + t;
        if (f + WIN < 18 * NT) win[f % WIN] = ldf(rs, lane16, wbase, f + WIN);
        else if (f % WIN < NNEXT) win[f % WIN] = ldf(rs, nvo, nbase, f % WIN);
      }
    }
    SB();
    if (n < 18 * MT) {
      KEEP(act[n % R]);
#pragma unroll
      for (int t = 0; t < NT; ++t) KEEP(win[(s * NT + t) % WIN]);
      SB();
    }
  }
}
template <int NT, int MT, int SWAP, int NNEXT, int R>
__device__ __forceinline__ void gemm18(f32x4 (&acc)[NT][MT], v4u (&win)[WIN], LAS unsigned char* img, rsrc_t rs, int lane16, int wbase, int nbase) {
  v4u act[R];
  LAS unsigned char* img2 = img + 54 * KB;
  if (MT > 3) asm volatile("" : "+v"(img2));
#pragma unroll
  for (int n = 0; n < R - 1; ++n) { act[n] = *(LAS v4u*)(n % MT < 3 ? img + ((n % MT) * 18 + n / MT) * KB : img2 + ((n % MT - 3) * 18 + n / MT) * KB); SB(); }
  gemm18_steps<NT, MT, SWAP, NNEXT, R, 0, 6 * MT>(acc, win, act, img, img2, rs, lane16, wbase, lane16, nbase);
  gemm18_steps<NT, MT, SWAP, NNEXT, R, 6 * MT, 12 * MT>(acc, win, act, img, img2, rs, lane16, wbase, lane16, nbase);
  gemm18_steps<NT, MT, SWAP, NNEXT, R, 12 * MT, 18 * MT + 1>(acc, win, act, img, img2, rs, lane16, wbase, lane16, nbase);
  ACC_OUT(acc);
}
template <int NT, int MT, int SWAP, int R, int tstep>
__device__ __forceinline__ void qkv_tiles(v4u (&win)[WIN], LAS unsigned char* L, rsrc_t rs, int lane, int wbase, int nbase, int tile0, int rwait STAMP_ARG) {
  f32x4 acc[NT][MT];
  gemm18<NT, MT, SWAP, WIN, R>(acc, win, L + lane * 16, rs, lane * 16, wbase, nbase);
  STAMP(38);
  LAS unsigned char *Rq = L + L_RT + (lane & 48), *Rl = L + L_RT + (lane & 15) * 4, *Tq = L + L_TQ + tile0 * 64 + (lane & 48),
                    *Tl = L + L_TQ + tile0 * 64 + (lane & 15) * 4, *X = L + L_QK + lane * 8 + tile0 * 2560;
  asm volatile("" : "+v"(Rq), "+v"(Rl), "+v"(Tq), "+v"(Tl), "+v"(X));
  cnt_wait(L, C_R1, rwait);
  v2u u[NT][MT];
#pragma unroll
  for (int t = 0; t < NT; ++t) {
    if ((SWAP >> t) & 1) {
      const f32x4 cs = splat(*(LAS float*)(Tl + t * tstep * 64));
#pragma unroll
      for (int m = 0; m < MT; ++m) u[t][m] = ln_pack(acc[t][m], *(LAS f32x4*)(Rq + m * 64), cs, *(LAS f32x4*)(Rq + 320 + m * 64));
    } else {
      const f32x4 cs = *(LAS f32x4*)(Tq + t * tstep * 64);
#pragma unroll
      for (int m = 0; m < MT; ++m) u[t][m] = ln_pack(acc[t][m], splat(*(LAS float*)(Rl + m * 64)), cs, splat(*(LAS float*)(Rl + 320 + m * 64)));
    }
  }
#pragma unroll
  for (int t = 0; t < NT; ++t)
#pragma unroll
    for (int m = 0; m < MT; ++m) *(LAS v2u*)(X + (t * tstep * 5 + m) * 512) = u[t][m];
}
template <bool HWAIT>
__device__ __forceinline__ void gemm_mlp(f32x4 (&acc)[3][4], v4u (&win)[WIN], LAS unsigned char* img, rsrc_t rs, int lane16, int rb, int vlast,
                                         int rlast, LAS unsigned char* L) {
  v4u act[8];
  if (HWAIT) cnt_wait(L, C_H0, 4);
#pragma unroll
  for (int n = 0; n < 4; ++n) { act[n] = *(LAS v4u*)(img + n * 18 * KB); SB(); }
  ACC_IN(acc);
#pragma unroll 1
  for (int c = 0; c < 3; ++c) {
    const int vo = c == 2 ? vlast : lane16, so = c == 2 ? rlast : rb + 18 * KB;
    int hv = 4;
#pragma unroll
    for (int s = 0; s < 6; ++s) {
      if (HWAIT && s == 5) {
        if (__builtin_amdgcn_readfirstlane(hv) < 4) cnt_wait(L, C_H0 + c + 1, 4);
        asm volatile("" ::: "memory");
        SB();
      }
#pragma unroll
      for (int t = 0; t < 3; ++t)
#pragma unroll
        for (int m = 0; m < 4; ++m) {
          MF(acc[t][m], win[s * 3 + t], act[(s & 1) * 4 + m]);
          SB();
          if (t == 0) act[((s + 1) & 1) * 4 + m] = *(LAS v4u*)(img + (m * 18 + s + 1) * KB);
          if (HWAIT && s == 3 && t == 2 && m == 0) hv = cnt_get(L, C_H0 + c + 1);
          if (HWAIT && (TAIL0 == 1 || TAIL1 == 1) && s == 1 && t == 2 && m == 0 && lane16 == 0)
            __hip_atomic_fetch_add((LAS int*)(L + L_HC) + C_LC, c == 2, __ATOMIC_RELAXED, __HIP_MEMORY_SCOPE_WORKGROUP);
          if (t == 1 && m < 3) {
            if (s == 0) win[15 + m] = ldf(rs, lane16, rb, 15 + m);
            else win[(s - 1) * 3 + m] = ldf(rs, vo, so, (s - 1) * 3 + m);
          }
          SB();
        }
    }
    img += 6 * KB;
    rb += 18 * KB;
  }
  ACC_OUT(acc);
}

__global__ void __launch_bounds__(NTHR) fused_fwd(Params p) {
  extern __shared__ __attribute__((aligned(16))) unsigned char smem[];
  LAS unsigned char* L = (LAS unsigned char*)smem;
  const int w0 = __builtin_amdgcn_readfirstlane(threadIdx.x >> 6);
  const int bid = blockIdx.x;
  constexpr int MPX = 8 / XB;
  const int j0 = ((bid & 7) / XB) * (32 / MPX) + (bid >> 3) / MPX;
  const int g0 = ((bid & 7) % XB) * MPX + (bid >> 3) % MPX;
  const rsrc_t rs = __builtin_amdgcn_make_buffer_rsrc((void*)p.ws, 0, WS_BYTES, 0x00020000);
  const int mb0 = WBASE + g0 * WSL + (252 + w0 * 108) * KB;
  const int nb0 = w0 < 9 ? WOB + ((g0 & 3) * 9 + w0) * 18 * KB : mb0;
  const int qb0 = w0 < 8 ? WBASE + g0 * WSL + (w0 < 6 ? w0 * 36 : 108 + w0 * 18) * KB : nb0;
  STAMP_INIT();
  STAMP(0);
  const u64 tag0 = __hip_atomic_load((const GAS u64*)p.ws, __ATOMIC_RELAXED, __HIP_MEMORY_SCOPE_AGENT);
  u64 tag = 0;
  v4u win[WIN];
  {
    constexpr int layer = 0;
    LOCALS();
    const rsrc_t rx = __builtin_amdgcn_make_buffer_rsrc((void*)p.x, 0, 2048 * 576 * 2, 0x00020000);
    const int xrow = lane >> 2, t5 = w >> 1;
    const int xtok = t5 < 4 ? 64 * j + 32 * (t5 & 1) + 2 * xrow + (t5 >> 1) : (j > 0 ? 64 * j - 16 : 0) + 2 * (xrow & 7) + (xrow >> 3);
    const int xoff = w < 10 ? xtok * 1152 + (w & 1) * 576 + (lane & 3) * 16 : 0x10000000;
#define LOAD_X() v4u xr[9]; _Pragma("unroll") for (int i = 0; i < 9; ++i) xr[i] = __builtin_amdgcn_raw_buffer_load_b128(rx, xoff + i * 64, 0, 0); SB()
#define STAGE_X() do { float s1 = 0.f, s2 = 0.f; \
      _Pragma("unroll") for (int i = 0; i < 9; ++i) { \
        if (w < 10) *(LAS v4u*)(L + (w * 9 + i) * KB + (lane & 3) * 256 + xrow * 16) = xr[i]; \
        _Pragma("unroll") for (int e = 0; e < 4; ++e) { s1 = dot2(xr[i][e], ONES, s1); s2 = dot2(xr[i][e], xr[i][e], s2); } } \
      s1 += dpp<0xB1>(s1); s2 += dpp<0xB1>(s2); s1 += dpp<0x4E>(s1); s2 += dpp<0x4E>(s2); \
      if (w < 10 && (lane & 3) == 0) *(LAS f32x2*)(L + L_ST + ((w & 1) * 80 + (w >> 1) * 16 + xrow) * 8) = f32x2{s1, s2}; } while (0)
    if (opq_s(w) < 8) {
      LAS unsigned char* T = L + L_QK + w * 3072;
      const int uf = 32 * w + j, uc = uf < 252 ? uf : 0, tile = uc / 18, us = uc - tile * 18;
      const bool vx = tile == 9, ex = (tile & 14) == 8;
      const int c0 = 72 * g + (ex ? 16 : ((tile + (tile >= 10 ? 2 : 0)) & 3) * 18) + (tile < 4 ? 576 : (tile < 8 || vx) ? 1152 : 0);
      const int cA = vx ? 18 : 576, cB = vx ? 36 : 18, cC = ex ? (vx ? 0 : 36) : 8;
      const GAS unsigned short* src = (const GAS unsigned short*)p.wqkv + (32 * us + (lane >> 1)) * 1728 + c0 + (lane & 1) * cC;
      v4u qv;
      if (ex) qv = v4u{*(const GAS unsigned*)(src), *(const GAS unsigned*)(src + cA), *(const GAS unsigned*)(src + cB), *(const GAS unsigned*)(src + cA + cB)};
      else qv = *(const GAS v4u_a4*)src;
      SB();
      const int oc = (g >> 2) * 81 + (uf < 81 ? uf : 0), ot = oc / 18, os = oc - ot * 18;
      const bool ox = os >= 16;
      const int orow = (ox ? 288 * (os - 16) + 16 : 36 * os) + quad * (ox ? 72 : 4) + ((lane >> 1) & 1) + ((lane >> 2) & 1) * (ox ? 18 : 2) + ((lane >> 3) & 1) * (ox ? 36 : 18);
      const rsrc_t ro = __builtin_amdgcn_make_buffer_rsrc((void*)p.wo, 0, 576 * 576 * 2, 0x00020000);
      const v4u ov = __builtin_amdgcn_raw_buffer_load_b128(ro, uf < 81 ? orow * 1152 + 32 * ((g & 3) * 9 + ot) + (lane & 1) * 16 : 0x10000000, 0, 0);
      SB();
      __builtin_amdgcn_s_barrier();
      LOAD_X();
      STAMP(41);
      *(LAS v4u*)(T + (lane >> 1) * 48 + (lane & 1) * 16) = (vx && (lane & 1)) ? v4u{0u, 0u, 0u, 0u} : qv;
      *(LAS v4u*)(T + 1536 + (lane >> 1) * 48 + (lane & 1) * 16) = ov;
      LAS unsigned char* R = T + (8 * quad + (l15 >> 2)) * 48 + (lane & 3) * 8;
      const v2u a = __builtin_bit_cast(v2u, __builtin_amdgcn_ds_read_tr16_b64_v4i16((LAS s16x4*)(R)));
      const v2u b = __builtin_bit_cast(v2u, __builtin_amdgcn_ds_read_tr16_b64_v4i16((LAS s16x4*)(R + 4 * 48)));
      const v2u c = __builtin_bit_cast(v2u, __builtin_amdgcn_ds_read_tr16_b64_v4i16((LAS s16x4*)(R + 1536)));
      const v2u d = __builtin_bit_cast(v2u, __builtin_amdgcn_ds_read_tr16_b64_v4i16((LAS s16x4*)(R + 1536 + 4 * 48)));
      stg(v4u{a.x, a.y, b.x, b.y}, rs, lane16,
          uf >= 252 ? WS_BYTES : wb + (tile < 8 ? (tile & 3) * 36 + us * 2 + (tile >> 2) : tile < 10 ? 72 + uc : (tile >> 1) * 36 - 36 + us * 2 + (tile & 1)) * KB);
      stg(v4u{c.x, c.y, d.x, d.y}, rs, lane16, uf < 81 ? WOB + ((g & 3) * 162 + oc) * KB : WS_BYTES);
      const float cq = rows_sum(dot2(a.x, ONES, dot2(a.y, ONES, dot2(b.x, ONES, dot2(b.y, ONES, 0.f)))));
      __builtin_amdgcn_raw_buffer_store_b32(__float_as_uint(cq), rs, uf < 252 && lane < 16 ? CSQ + ((g * 18 + us) * 14 + tile) * 64 + lane * 4 : WS_BYTES, 0, SC1);
      SB();
      STAMP(43);
      DRAIN();
      STAMP(44);
      BAR();
      GET_TAG();
      if (w == 0 && lane == 0) set_flag(p.ws, SFLAGS + g * 256 + j * 8, tag);
      STAGE_X();
      STAMP(2);
      BAR();
      STAMP(3);
      issue_window<0, WIN>(win, rs, lane16, QBASE());
    }
    if (opq_s(w) >= 8) {
      LAS unsigned char* T = L + 125 * KB + (w - 8) * 4608;
      const int item = 4 * j + w - 8, itc = item < 108 ? item : 0;
      const int rbk = itc / 3, cb = 3 * (itc - rbk * 3), kind = rbk >= 18, is = rbk - kind * 18, o = is % 3;
      const int r0 = kind == 0 ? 32 * is : 576 * g + 32 * (is - o) + (o < 2 ? 48 * o : 32);
      const int rn = kind == 0 ? 8 : 4, r4 = kind == 0 ? 4 : o < 2 ? 16 : 48, ld = kind == 0 ? 9216 : 1152;
      const rsrc_t rw = __builtin_amdgcn_make_buffer_rsrc((void*)(kind == 0 ? p.w1 : p.w2), 0, 576 * 4608 * 2, 0x00020000);
      const int jj = lane >> 3;
      const int woff = item < 108 ? (r0 + (jj & 3) + (jj >> 2) * r4) * ld + (kind == 0 ? 1152 * g : 0) + 128 * cb + (lane & 7) * 16 : 0x10000000;
      if (w == 11 && lane < 16) ((volatile LAS int*)(L + L_HC))[lane] = 0;
      __builtin_amdgcn_s_barrier();
      LOAD_X();
      v4u wv[3][4];
#pragma unroll
      for (int n = 0; n < 4; ++n)
#pragma unroll
        for (int c = 0; c < 3; ++c) wv[c][n] = __builtin_amdgcn_raw_buffer_load_b128(rw, woff + c * 128, n * rn * ld, NTW ? 2 : 0);
      SB();
      STAMP(41);
      BAR();
      GET_TAG();
      STAGE_X();
      if (opq_s(w) == 11) {
        wait_flags<4>(p.ws, SFLAGS + (g ^ ((lane >> 3) & 4)) * 256 + (lane & 31) * 8, true, tag);
#pragma unroll
        for (int i = 0; i < 18; ++i) win[i] = ldg(rs, lane16, CSQ + (g * 18 + i) * 896);
      }
      STAMP(2);
      BAR();
      STAMP(3);
      row_table1<2>(L, w - 8, lane);
      if (opq_s(w) == 11) {
        f32x4 cs = __builtin_bit_cast(f32x4, win[0]);
#pragma unroll
        for (int i = 1; i < 18; ++i) add4(cs, win[i]);
        if (lane < 56) *(LAS f32x4*)(L + L_TQ + lane16) = cs;
        row_table1<2>(L, 4, lane);
      }
      float csum[3];
      LAS unsigned char* R = T + (8 * quad + (l15 >> 2)) * 144 + (lane & 3) * 8;
#pragma unroll
      for (int c = 0; c < 3; ++c) {
#pragma unroll
        for (int n = 0; n < 4; ++n) *(LAS v4u*)(T + (n * 8 + (lane >> 3)) * 144 + (lane & 7) * 16) = wv[c][n];
        float cs[4];
#pragma unroll
        for (int tt = 0; tt < 4; ++tt) {
          const v2u a = __builtin_bit_cast(v2u, __builtin_amdgcn_ds_read_tr16_b64_v4i16((LAS s16x4*)(R + tt * 32)));
          const v2u b = __builtin_bit_cast(v2u, __builtin_amdgcn_ds_read_tr16_b64_v4i16((LAS s16x4*)(R + tt * 32 + 4 * 144)));
          wv[c][tt] = v4u{a.x, a.y, b.x, b.y};
          cs[tt] = rows_sum(dot2(a.x, ONES, dot2(a.y, ONES, dot2(b.x, ONES, dot2(b.y, ONES, 0.f)))));
          OPAQUE(cs[tt]);
        }
        csum[c] = quad == 0 ? cs[0] : quad == 1 ? cs[1] : quad == 2 ? cs[2] : cs[3];
      }
      STAMP(45);
      const int dst0 = item < 108 ? wb + (252 + 54 * kind + 3 * is + (4 * cb / 3) * 108) * KB : WS_BYTES;
#pragma unroll
      for (int c = 0; c < 3; ++c) {
#pragma unroll
        for (int tt = 0; tt < 4; ++tt) stg(wv[c][tt], rs, lane16, dst0 + (((4 * c + tt) / 3) * 105 + 4 * c + tt) * KB);
        __builtin_amdgcn_raw_buffer_store_b32(__float_as_uint(csum[c]), rs,
                                              lane * 4 + (item < 108 && kind == 0 ? CS1 + (is * 4608 + 576 * g + 64 * (cb + c)) * 4 : WS_BYTES), 0, SC1);
      }
      SB();
      if (opq_s(w) == 8) issue_window<0, WIN>(win, rs, lane16, QBASE());
    }
  }

#pragma unroll 1
  for (int layer = 0; layer < 2; ++layer) {
    v4u xres;
    {
      LOCALS();
      STAMP(4 + layer * 16);
      {
        const int qbase = QBASE(), nbase = NBASE(), rw = 5 + 5 * layer;
        if (opq_s(w) < 4) qkv_tiles<2, 5, 2, R2, 4>(win, L, rs, lane, qbase, nbase, w, rw STAMP_PASS);
        if ((opq_s(w) & 14) == 4) qkv_tiles<2, 4, 0, R2, 1>(win, L, rs, lane, qbase, nbase, 2 * w + 2, rw STAMP_PASS);
        if (opq_s(w) == 6) qkv_tiles<1, 5, 0, R1, 0>(win, L, rs, lane, qbase, nbase, 8, rw STAMP_PASS);
        if (opq_s(w) == 7) qkv_tiles<1, 5, 1, R1, 0>(win, L, rs, lane, qbase, nbase, 9, rw STAMP_PASS);
      }
      STAMP(5 + layer * 16);
      if ((opq_s(w) & 12) == 4) {
        BAR();
      }
      if ((opq_s(w) & 12) != 4) {
        const int T = w & 3, hp = w >> 3, Tp = (T & 1) ? T - 1 : 4;
        f32x4 mp, ms;
        const int lo = l15 + ((T & 1) ? 9 : 1 + 4 * T), hi = (T & 1) ? 16 : j > 0 ? 8 + 4 * T : 0;
#pragma unroll
        for (int r = 0; r < 4; ++r) {
          const int jj = 4 * quad + r;
          mp[r] = ((jj >= lo) & (jj < hi)) ? 0.f : -1.0e30f;
          ms[r] = (unsigned)(l15 - jj) <= 7u ? 0.f : -1.0e30f;
        }
        int xs = L_QK + lane * 8 + T * 512, xp = L_QK + lane * 8 + Tp * 512, hs = xs + hp * 5120, hq = xp + hp * 5120;
        int om = ab + A_O + (T * 18 + 2 * g + hp) * KB + lane16;
        int oe = quad == hp ? ab + A_O + (T * 18 + 16 + (g >> 2)) * KB + (g & 3) * 256 + hp * 8 + l15 * 16 : WS_BYTES;
        unsigned qm0 = quad == 2 * hp ? ~0u : 0u, qm1 = quad == 2 * hp + 1 ? ~0u : 0u, zr = 0u;
        v4u one4 = {ONES, ONES, ONES, ONES};
        asm volatile("" : "+v"(mp), "+v"(ms), "+v"(xs), "+v"(xp), "+v"(hs), "+v"(hq), "+v"(om), "+v"(oe), "+v"(qm0), "+v"(qm1), "+v"(zr), "+v"(one4));
        BAR();
        const v2u kxs = *(LAS v2u*)(L + xs + 40 * 512);
        const unsigned kxp = *(LAS unsigned*)(L + xp + 40 * 512 + 4);
        v2u q[2], ks[2], kp[2], vs[2], vp[2];
#pragma unroll
        for (int hh = 0; hh < 2; ++hh) {
          q[hh] = *(LAS v2u*)(L + hs + (50 + 5 * hh) * 512);
          kp[hh] = *(LAS v2u*)(L + hq + 5 * hh * 512);
          ks[hh] = *(LAS v2u*)(L + hs + 5 * hh * 512);
        }
        SB();
#pragma unroll
        for (int hh = 0; hh < 2; ++hh) {
          vp[hh] = *(LAS v2u*)(L + hq + (20 + 5 * hh) * 512);
          vs[hh] = *(LAS v2u*)(L + hs + (20 + 5 * hh) * 512);
        }
        const v2u vxp = *(LAS v2u*)(L + xp + 45 * 512), vxs = *(LAS v2u*)(L + xs + 45 * 512);
        SB();
        const f32x4 z = {0.f, 0.f, 0.f, 0.f};
        f32x4 sp[2], ss[2], o[2], ox[2], sm[2];
        float mx[2];
#pragma unroll
        for (int hh = 0; hh < 2; ++hh) {
          const v4u qf = {q[hh].x, q[hh].y, kxs.x & (hh ? qm1 : qm0), zr};
          sp[hh] = mfma(v4u{kp[hh].x, kp[hh].y, kxp, zr}, qf, mp);
          ss[hh] = mfma(v4u{ks[hh].x, ks[hh].y, kxs.y, zr}, qf, ms);
        }
#pragma unroll
        for (int hh = 0; hh < 2; ++hh) {
          const float m0 = __builtin_fmaxf(__builtin_fmaxf(sp[hh][0], sp[hh][1]), sp[hh][2]), m1 = __builtin_fmaxf(__builtin_fmaxf(ss[hh][0], ss[hh][1]), ss[hh][2]);
          mx[hh] = __builtin_fmaxf(__builtin_fmaxf(__builtin_fmaxf(__builtin_fmaxf(m0, sp[hh][3]), ss[hh][3]), m1), m1);
        }
        rows_max2(mx[0], mx[1]);
#pragma unroll
        for (int hh = 0; hh < 2; ++hh) {
          const float c = 0.23570226039551584f * 1.4426950408889634f, mc = -c * mx[hh];
          float e[8];
#pragma unroll
          for (int r = 0; r < 4; ++r) {
            e[r] = __builtin_amdgcn_exp2f(__builtin_fmaf(sp[hh][r], c, mc));
            e[4 + r] = __builtin_amdgcn_exp2f(__builtin_fmaf(ss[hh][r], c, mc));
          }
          const v4u pf = {pk(e[0], e[1]), pk(e[2], e[3]), pk(e[4], e[5]), pk(e[6], e[7])};
          sm[hh] = mfma(one4, pf, z);
          o[hh] = mfma(v4u{vp[hh].x, vp[hh].y, vs[hh].x, vs[hh].y}, pf, z);
          ox[hh] = mfma(v4u{vxp.x, vxp.y, vxs.x, vxs.y}, pf, z);
        }
        const float i0 = __builtin_amdgcn_rcpf(sm[0][0]), i1 = __builtin_amdgcn_rcpf(sm[1][0]);
        stg(v4u{pk(o[0][0] * i0, o[0][1] * i0), pk(o[0][2] * i0, o[0][3] * i0), pk(o[1][0] * i1, o[1][1] * i1), pk(o[1][2] * i1, o[1][3] * i1)}, rs, om, 0);
        __builtin_amdgcn_raw_buffer_store_b64(v2u{pk(ox[0][0] * i0, ox[0][1] * i0), pk(ox[1][2] * i1, ox[1][3] * i1)}, rs, oe, 0, SC1);
        STAMP(34);
        DRAIN();
      }
      {
        const int ft = (g & 3) * 9 + (w < 9 ? w : 0);
        xres = *(LAS v4u*)(L + ((g >> 2) * 36 + (quad & 1) * 18 + (ft >> 1)) * KB + ((2 * (ft & 1) + (quad >> 1)) * 16 + l15) * 16);
      }
      STAMP(6 + layer * 16);
      BAR();
      if (w == 7) {
        if (lane == 0) set_flag(p.ws, FLAGS + 2 * layer * 4096 + j * 128 + g * 8, tag);
        wait_flags(p.ws, FLAGS + 2 * layer * 4096 + j * 128 + (lane & 7) * 8, lane < 8, tag);
      }
      if (layer == 0 && w == 6 && lane == 0) set_flag(p.ws, SFLAGS + 2048 + g * 256 + j * 8, tag);
      BAR();
      STAMP(7 + layer * 16);
    }
    {
      LOCALS();
      const int th = g >> 2, ft = (g & 3) * 9 + w;
      v4u orr[3];
#pragma unroll
      for (int i = 0; i < 3; ++i) orr[i] = ldg(rs, lane16, ab + A_O + (th * 36 + w * 3 + i) * KB);
      const int xoff = ((quad & 1) * 18 + (ft >> 1)) * KB + ((2 * (ft & 1) + (quad >> 1)) * 16 + l15) * 16;
      const v4u xr = xres;
#pragma unroll
      for (int i = 0; i < 3; ++i) *(LAS v4u*)(L + (w * 3 + i) * KB + lane16) = orr[i];
      if (layer == 0 && w == 0) wait_flags<2>(p.ws, SFLAGS + 2048 + g * 256 + (lane & 31) * 8, lane < 32, tag);
      BAR();
      STAMP(8 + layer * 16);
      if (w < 9) {
        f32x4 acc[1][2];
        gemm18<1, 2, 0, P2N, 10>(acc, win, L + lane16, rs, lane16, 0, MBASE());
        float a[8];
#pragma unroll
        for (int r = 0; r < 4; ++r) {
          auto s = __builtin_amdgcn_permlane16_swap(__float_as_uint(acc[0][0][r]), __float_as_uint(acc[0][1][r]), false, false);
          a[r] = __uint_as_float(s[0]);
          a[4 + r] = __uint_as_float(s[1]);
        }
        const v4u o = {pk(a[0] + blo(xr.x), a[1] + bhi(xr.x)), pk(a[2] + blo(xr.y), a[3] + bhi(xr.y)),
                       pk(a[4] + blo(xr.z), a[5] + bhi(xr.z)), pk(a[6] + blo(xr.w), a[7] + bhi(xr.w))};
        stg(o, rs, xoff, ab + A_X1 + th * 36 * KB);
        STAMP(35);
        DRAIN();
      }
      if (layer == 0 && opq_s(w) >= 9) {
        const int f4 = (w - 9) * 48 + (lane < 48 ? lane : 0);
        f32x4 cs = {0.f, 0.f, 0.f, 0.f};
#pragma unroll
        for (int i = 0; i < 18; ++i) win[i] = ldg(rs, f4 * 16, CS1 + (i * 4608 + 576 * g) * 4);
        const v2u b = *(const GAS v2u*)(p.b1 + 576 * g + 4 * f4);
#pragma unroll
        for (int i = 0; i < 18; ++i) add4(cs, win[i]);
        *(LAS f32x4*)(L + L_T1 + f4 * 16) = cs;
        *(LAS f32x4*)(L + L_T1 + 2304 + f4 * 16) = f32x4{blo(b.x), bhi(b.x), blo(b.y), bhi(b.y)};
        SB();
        issue_window<0, 15>(win, rs, lane16, MBASE());
      }
      if (layer == 1 && bid == 0 && w == 10) {
#pragma unroll 1
        for (int i = 0; i < 4; ++i) wait_flags(p.ws, SFLAGS + (i * 64 + lane) * 8, true, tag);
        if (lane == 0) set_flag(p.ws, 0, tag);
      }
      STAMP(9 + layer * 16);
      BAR();
      if (w < 9) issue_window<P2N, 15>(win, rs, lane16, MBASE());
      if (w == 11) {
        if (lane == 0) set_flag(p.ws, FLAGS + (2 * layer + 1) * 4096 + j * 128 + g * 8, tag);
        wait_flags(p.ws, FLAGS + (2 * layer + 1) * 4096 + j * 128 + (lane & 7) * 8, lane < 8, tag);
      }
      BAR();
      STAMP(10 + layer * 16);
    }
    {
      LOCALS();
      const int mbase = MBASE();
      const int T = w & 3, cls = w >> 2;
      if (opq_s(cls) == 0) {
        if (w == 0 && lane < 8) ((volatile LAS int*)(L + L_HC))[lane] = lane == 3 ? 4 : 0;
        BAR();
      }
      if (opq_s(cls) > 0) {
        v4u xr[9];
#pragma unroll
        for (int i = 0; i < 9; ++i) xr[i] = ldf(rs, lane16, ab + A_X1 + (T * 18 + 9 * (cls - 1)) * KB, i);
#pragma unroll
        for (int i = 0; i < 9; ++i) *(LAS v4u*)(L + (T * 18 + 9 * (cls - 1) + i) * KB + lane16) = xr[i];
        BAR();
        float s1 = 0.f, s2 = 0.f, s3 = 0.f, s4 = 0.f;
#pragma unroll
        for (int i = 0; i < 9; ++i)
#pragma unroll
          for (int e = 0; e < 4; e += 2) {
            s1 = dot2(xr[i][e], ONES, s1); s2 = dot2(xr[i][e], xr[i][e], s2);
            s3 = dot2(xr[i][e + 1], ONES, s3); s4 = dot2(xr[i][e + 1], xr[i][e + 1], s4);
          }
        s1 += s3; OPAQUE(s1); s2 += s4;
        s1 = rows_sum(s1); OPAQUE(s1); s2 = rows_sum(s2);
        if (lane < 16) *(LAS f32x2*)(L + L_ST + ((T * 16 + lane) * 2 + cls - 1) * 8) = f32x2{s1, s2};
        cnt_add(L, C_ST, lane);
      }
      STAMP(11 + layer * 16);
      f32x4 acc[3][4];
#pragma unroll
      for (int t = 0; t < 3; ++t)
#pragma unroll
        for (int m = 0; m < 4; ++m) acc[t][m] = f32x4{0.f, 0.f, 0.f, 0.f};
      if (opq_s(cls) > 0) { row_table(L, lane); shift_acc(acc, L, w, quad, l15); }
      __builtin_amdgcn_s_setprio(3);
      gemm_mlp<false>(acc, win, L + lane16, rs, lane16, mbase, lane16, mbase + 54 * KB, L);
      STAMP(12 + layer * 16);
      {
        LAS unsigned char* H = L + L_H + lane16 + 3 * (w >> 1) * KB;
        if (opq_s(cls) == 0) {
          cnt_wait(L, C_RT, 1);
          shift_acc(acc, L, w, quad, l15);
        }
        f32x4 rt[4];
#pragma unroll
        for (int m = 0; m < 4; ++m) rt[m] = *(LAS f32x4*)(L + L_ST + KB + (m * 16 + l15) * 16);
#pragma unroll
        for (int m = 0; m < 4; ++m) {
          const v2u u0 = gelu4(acc[0][m], rt[m][2], rt[m][3], rt[m][1]), u1 = gelu4(acc[1][m], rt[m][2], rt[m][3], rt[m][1]);
          *(LAS v4u*)(H + (m * 18 + (w & 1)) * KB) = v4u{u0.x, u0.y, u1.x, u1.y};
          *(LAS v2u*)(H + (m * 18 + 2) * KB + (w & 1) * 8) = gelu4(acc[2][m], rt[m][2], rt[m][3], rt[m][1]);
        }
        cnt_add(L, C_H0 + cls, lane);
      }
      STAMP(13 + layer * 16);
      if (cls == 0) __builtin_amdgcn_s_setprio(2);
      else if (cls == 1) __builtin_amdgcn_s_setprio(1);
      else __builtin_amdgcn_s_setprio(0);
#pragma unroll
      for (int t = 0; t < 3; ++t)
#pragma unroll
        for (int m = 0; m < 4; ++m) acc[t][m] = f32x4{0.f, 0.f, 0.f, 0.f};
      gemm_mlp<true>(acc, win, L + L_H + lane16, rs, lane16, mbase + 54 * KB, layer ? lane16 + WS_BYTES : lane16, QBASE(), L);
      __builtin_amdgcn_s_setprio(0);
      cnt_add(L, C_GD, lane);
      STAMP(14 + layer * 16);
      const int pw = ab + A_PT + (g * 12 + w) * 6144;
      const int poff = (quad & 1) * 1536 + (quad >> 1) * 256 + l15 * 16;
#pragma unroll
      for (int t = 0; t < 3; ++t)
#pragma unroll
        for (int m = 0; m < 4; m += 2) {
          const v2u a = {pk(acc[t][m][0], acc[t][m][1]), pk(acc[t][m][2], acc[t][m][3])};
          const v2u b = {pk(acc[t][m + 1][0], acc[t][m + 1][1]), pk(acc[t][m + 1][2], acc[t][m + 1][3])};
          stg(widen(a, b), rs, poff + m * 1536 + t * 512, pw);
        }
      DRAIN();
      if (lane == 0) set_flag(p.ws, WFLAGS + ((layer * 32 + j) * 12 + w) * 128 + g * 8, tag);
      STAMP(15 + layer * 16);
    }
    {
      LOCALS();
      OWNROWS(6);
      const bool act = lane < 48;
      const v4u xr = *(LAS v4u*)(L + (act ? piece : 0));
      const v4u bb = *(const GAS v4u*)(p.b2 + (act ? c8 : 0) * 8);
      const int tail = layer ? TAIL1 : TAIL0;
      if (tail == 1) cnt_wait(L, C_LC, 12);
      if (tail == 2) cnt_wait(L, C_GD, 12);
      wait_flags(p.ws, WFLAGS + ((layer * 32 + j) * 12 + w) * 128 + (lane & 7) * 8, lane < 8, tag);
      STAMP(16 + layer * 16);
      v4u xb;
      {
        v4u pr[8];
        const int roff = (g >> 1) * 1536 + (lane >> 3) * 256 + (8 * (g & 1) + (lane & 7)) * 16 + (act ? 0 : WS_BYTES);
#pragma unroll
        for (int i = 0; i < 8; ++i) pr[i] = ldg(rs, roff, ab + A_PT + (i * 12 + w) * 6144);
        SB();
        const unsigned sel_lo = opq_s(0x00003f80), sel_hi = opq_s(0x3f800000);
#pragma unroll
        for (int e = 0; e < 4; ++e) {
          float lo = blo(bb[e]) + blo(xr[e]), hi = bhi(bb[e]) + bhi(xr[e]);
          OPAQUE(lo);
#pragma unroll
          for (int i = 0; i < 8; ++i) { lo = dot2(pr[i][e], sel_lo, lo); hi = dot2(pr[i][e], sel_hi, hi); }
          xb[e] = pk(lo, hi);
        }
      }
      STAMP(17 + layer * 16);
      if (layer == 0) {
        float s1 = dot2(xb.x, ONES, dot2(xb.y, ONES, dot2(xb.z, ONES, dot2(xb.w, ONES, 0.f))));
        float s2 = dot2(xb.x, xb.x, dot2(xb.y, xb.y, dot2(xb.z, xb.z, dot2(xb.w, xb.w, 0.f))));
        if (!act) { s1 = 0.f; s2 = 0.f; }
        s1 += ror8(s1); OPAQUE(s1); s2 += ror8(s2);
        s1 = rows_sum(s1); OPAQUE(s1); s2 = rows_sum(s2);
        if (!act) { xb.x = __float_as_uint(s1); xb.y = __float_as_uint(s2); }
        if (lane < 56) stg(xb, rs, lane16, ab + ALY + A_X2 + (g * 12 + w) * KB);
        DRAIN();
        STAMP(36);
        BAR();
        if (w == 0) {
          if (lane == 0) set_flag(p.ws, FLAGS + 4 * 4096 + j * 128 + g * 8, tag);
          wait_flags(p.ws, FLAGS + 4 * 4096 + (j - (lane >> 3)) * 128 + (lane < 8 ? lane : 3 + 4 * (lane & 1)) * 8, lane < (j > 0 ? 10 : 8), tag);
        } else issue_window<15, WIN>(win, rs, lane16, QBASE());
        BAR();
        STAMP(37);
        v4u xv[10];
        const int vo = lane < 56 ? lane16 : lane16 + WS_BYTES;
#pragma unroll
        for (int i = 0; i < 10; ++i)
          xv[i] = ldg(rs, vo, ab + ALY + A_X2 + w * KB + (i < 8 ? i * 12 * KB : (3 + 4 * (i - 8)) * 12 * KB - (j > 0 ? ASL : 0)));
        SB();
        if (opq_s(w) == 0) issue_window<15, WIN>(win, rs, lane16, QBASE());
#pragma unroll
        for (int i = 0; i < 10; ++i) KEEP(xv[i]);
        LAS unsigned char* I = L + (c8 >> 2) * KB + ((c8 & 3) * 16 + (lane & 7)) * 16;
        LAS unsigned char* S = L + L_ST + (w * 80 + (lane & 7)) * 8;
#pragma unroll
        for (int i = 0; i < 10; ++i)
          if (act) *(LAS v4u*)(I + (i >> 1) * 18 * KB + (i & 1) * 128) = xv[i];
        if (!act && lane < 56) {
#pragma unroll
          for (int i = 0; i < 10; ++i) *(LAS v2u*)(S + i * 64) = v2u{xv[i].x, xv[i].y};
        }
        BAR();
        STAMP(18);
        if (w == 11) row_table1<12>(L, 4, lane);
        if (w >= 8) row_table1<12>(L, w - 8, lane);
      } else {
        if (act) *(GAS v4u*)(p.out + (size_t)mytok * 576 + c8 * 8) = xb;
      }
    }
  }
  STAMP(40);
  STAMP_DUMP();
}
}

extern "C" void kernel_launch(void* const* d_in, const int* in_sizes, int n_in, void* d_out, int out_size, void* d_ws, size_t ws_size,
                              hipStream_t stream) {
  (void)in_sizes; (void)n_in; (void)out_size; (void)ws_size;
  Params p;
  p.x = (const unsigned short*)d_in[0];
  p.wqkv = (const unsigned short*)d_in[1];
  p.wo = (const unsigned short*)d_in[2];
  p.w1 = (const unsigned short*)d_in[4];
  p.b1 = (const unsigned short*)d_in[5];
  p.w2 = (const unsigned short*)d_in[6];
  p.b2 = (const unsigned short*)d_in[7];
  p.out = (unsigned short*)d_out;
  p.ws = (unsigned char*)d_ws;
  (void)hipFuncSetAttribute((const void*)fused_fwd, hipFuncAttributeMaxDynamicSharedMemorySize, LDS_BYTES);
  hipLaunchKernelGGL(fused_fwd, dim3(256), dim3(NTHR), LDS_BYTES, stream, p);
}
```

```cpp
#include <hip/hip_runtime.h>
#include <hip/hip_bf16.h>
#include <stdint.h>

namespace {
#define LAS __attribute__((address_space(3)))
#define GAS __attribute__((address_space(1)))
typedef unsigned v4u __attribute__((ext_vector_type(4)));
typedef unsigned v4u_a4 __attribute__((ext_vector_type(4), aligned(4)));
typedef unsigned v2u __attribute__((ext_vector_type(2)));
typedef float f32x4 __attribute__((ext_vector_type(4)));
typedef float f32x2 __attribute__((ext_vector_type(2)));
typedef __bf16 bf16x8 __attribute__((ext_vector_type(8)));
typedef __bf16 bf16x2 __attribute__((ext_vector_type(2)));
typedef short s16x4 __attribute__((ext_vector_type(4)));
typedef unsigned long long u64;
typedef __amdgpu_buffer_rsrc_t rsrc_t;

constexpr int KB = 1024;
constexpr int NTHR = 768;
constexpr int WIN = 18;
#define XB 2
#define P2N 9
#define TAIL0 2
#define TAIL1 2
#define R1 10
#define R2 7
#define CODEPF 0
#define REUSE 0
#define NTW 1
constexpr float EPS = 1e-5f;
constexpr int SC1 = 16;
constexpr float GC0 = -2.0f * 0.7978845608028654f * 1.4426950408889634f;
constexpr int FLAGS = 4096;
constexpr int SFLAGS = 32 * KB;
constexpr int WFLAGS = 48 * KB;
constexpr int CS1 = 192 * KB;
constexpr int CSQ = 520 * KB;
constexpr int WBASE = 704 * KB;
constexpr int WSL = (252 + 1296) * KB;
constexpr int WOB = WBASE + 8 * WSL;
constexpr int ABASE = 14 * 1024 * KB;
constexpr int IMG = 72 * KB;
constexpr int A_X2 = 0, A_O = 96 * KB, A_X1 = A_O + IMG, A_PT = A_X1 + IMG, ALY = REUSE ? 0 : 1024 * KB, ASL = REUSE ? 1024 * KB : 2 * ALY;
constexpr int WS_BYTES = ABASE + 32 * ASL;
static_assert(WOB + 648 * KB <= ABASE && A_PT + 8 * IMG <= 1024 * KB && CSQ + 8 * 14 * 1152 + 256 <= WBASE, "workspace map");
constexpr int L_H = IMG;
constexpr int L_QK = 90 * KB;
constexpr int L_ST = 145 * KB;
constexpr int L_T1 = L_ST + 7680;
constexpr int L_TQ = L_T1 + 4608;
constexpr int L_RT = L_TQ + 896;
constexpr int L_HC = L_RT + 640;
constexpr int LDS_BYTES = L_HC + 64;
enum { C_H0 = 0, C_ST = 4, C_RT = 5, C_GD = 6, C_LC = 7, C_XE = 8, C_XL = 9, C_R1 = 10 };

struct Params {
  const unsigned short *x, *wqkv, *wo, *w1, *b1, *w2, *b2;
  unsigned short *out;
  unsigned char *ws;
};

#define STAMP(i)
#define STAMP_INIT()
#define STAMP_ARG
#define STAMP_PASS
#define STAMP_DUMP()
#define SB() __builtin_amdgcn_sched_barrier(0)
#define BAR() do { asm volatile("s_waitcnt lgkmcnt(0)" ::: "memory"); __builtin_amdgcn_s_barrier(); asm volatile("" ::: "memory"); } while (0)
#define DRAIN() asm volatile("s_waitcnt vmcnt(0)" ::: "memory")
#define OPAQUE(x) asm("" : "+v"(x))
#define KEEP(x) asm volatile("" :: "v"(x))

__device__ __forceinline__ f32x4 mfma(v4u a, v4u b, f32x4 c) {
  return __builtin_amdgcn_mfma_f32_16x16x32_bf16(__builtin_bit_cast(bf16x8, a), __builtin_bit_cast(bf16x8, b), c, 0, 0, 0);
}
#define MF(acc, a, b) asm volatile("v_mfma_f32_16x16x32_bf16 %0, %1, %2, %0" : "+v"(acc) : "v"(a), "v"(b))
#define MF0(acc, a, b) asm volatile("v_mfma_f32_16x16x32_bf16 %0, %1, %2, 0" : "=&v"(acc) : "v"(a), "v"(b))
template <int NT, int MT> __device__ __forceinline__ void acc_fence(f32x4 (&acc)[NT][MT]) {
#pragma unroll
  for (int t = 0; t < NT; ++t)
#pragma unroll
    for (int m = 0; m < MT; ++m) asm volatile("" : "+v"(acc[t][m]));
}
#define ACC_IN(acc) do { acc_fence(acc); asm volatile("s_nop 1"); SB(); } while (0)
#define ACC_OUT(acc) do { SB(); asm volatile("s_nop 7\n\ts_nop 1"); acc_fence(acc); } while (0)
__device__ __forceinline__ unsigned pk(float a, float b) {
  f32x2 v = {a, b};
  return __builtin_bit_cast(unsigned, __builtin_convertvector(v, bf16x2));
}
__device__ __forceinline__ float dot2(unsigned a, unsigned b, float c) {
  return __builtin_amdgcn_fdot2_f32_bf16(__builtin_bit_cast(bf16x2, a), __builtin_bit_cast(bf16x2, b), c, false);
}
constexpr unsigned ONES = 0x3f803f80u;
__device__ __forceinline__ float blo(unsigned u) { return __uint_as_float(u << 16); }
__device__ __forceinline__ float bhi(unsigned u) { return __uint_as_float(u & 0xffff0000u); }
__device__ __forceinline__ v4u ldg(rsrc_t rs, int voff, int soff) { return __builtin_amdgcn_raw_buffer_load_b128(rs, voff, soff, SC1); }
__device__ __forceinline__ void stg(v4u v, rsrc_t rs, int voff, int soff) { __builtin_amdgcn_raw_buffer_store_b128(v, rs, voff + soff, 0, SC1); }
__device__ __forceinline__ v4u ldf(rsrc_t rs, int lane16, int base, int f) { return ldg(rs, lane16 + (f & 3) * KB, base + (f >> 2) * 4 * KB); }
__device__ __forceinline__ int opq_v(int x) { asm volatile("" : "+v"(x)); return x; }
__device__ __forceinline__ int lane_id() { int x; asm volatile("v_mbcnt_lo_u32_b32 %0, -1, 0\n\tv_mbcnt_hi_u32_b32 %0, -1, %0" : "=v"(x)); return x; }
__device__ __forceinline__ int opq_s(int x) { asm volatile("" : "+s"(x)); return x; }
#define LOCALS() const int lane = lane_id(), w = opq_s(w0), j = opq_s(j0), g = opq_s(g0); \
  const int lane16 = lane * 16, quad = lane >> 4, l15 = lane & 15, ab = ABASE + j * ASL + layer * ALY, wb = WBASE + g * WSL; \
  (void)lane16; (void)quad; (void)l15; (void)ab; (void)wb
#define OWNROWS(CPW) const int c8 = CPW * w + (lane >> 3); \
  const int piece = ((g >> 1) * 18 + (c8 >> 2)) * KB + ((c8 & 3) * 16 + 8 * (g & 1) + (lane & 7)) * 16; \
  const int mytok = 64 * j + 2 * (16 * ((g >> 1) & 1) + 8 * (g & 1) + (lane & 7)) + (g >> 2)
#define GET_TAG() tag = 1 + ((u64)(unsigned)__builtin_amdgcn_readfirstlane((int)(unsigned)tag0) | \
                             ((u64)(unsigned)__builtin_amdgcn_readfirstlane((int)(unsigned)(tag0 >> 32)) << 32))
#define NBASE() opq_s(nb0)
#define QBASE() opq_s(qb0)
#define MBASE() opq_s(mb0)
__device__ __forceinline__ float rows_sum(float x) {
  auto s = __builtin_amdgcn_permlane16_swap(__float_as_uint(x), __float_as_uint(x), false, false);
  x = __uint_as_float(s[0]) + __uint_as_float(s[1]);
  auto t = __builtin_amdgcn_permlane32_swap(__float_as_uint(x), __float_as_uint(x), false, false);
  return __uint_as_float(t[0]) + __uint_as_float(t[1]);
}
__device__ __forceinline__ float vmax(unsigned a, unsigned b) { float r; asm("v_max_f32 %0, %1, %2\n\ts_nop 1" : "=v"(r) : "v"(a), "v"(b)); return r; }
__device__ __forceinline__ void rows_max2(float& a, float& b) {
  auto s = __builtin_amdgcn_permlane16_swap(__float_as_uint(a), __float_as_uint(b), false, false);
  float x = vmax(s[0], s[1]);
  auto t = __builtin_amdgcn_permlane32_swap(__float_as_uint(x), __float_as_uint(x), false, false);
  x = vmax(t[0], t[1]);
  auto u = __builtin_amdgcn_permlane16_swap(__float_as_uint(x), __float_as_uint(x), false, false);
  a = __uint_as_float(u[0]);
  b = __uint_as_float(u[1]);
}
template <int CTRL> __device__ __forceinline__ float dpp(float x) {
  return __int_as_float(__builtin_amdgcn_mov_dpp(__float_as_int(x), CTRL, 0xf, 0xf, true));
}
__device__ __forceinline__ float ror8(float x) { return dpp<0x128>(x); }
__device__ __forceinline__ void add4(f32x4& a, v4u b) {
  float a0 = a[0], a1 = a[1], a2 = a[2], a3 = a[3];
  asm("v_add_f32 %0, %0, %4\n\tv_add_f32 %1, %1, %5\n\tv_add_f32 %2, %2, %6\n\tv_add_f32 %3, %3, %7"
      : "+v"(a0), "+v"(a1), "+v"(a2), "+v"(a3) : "v"(b.x), "v"(b.y), "v"(b.z), "v"(b.w));
  a = f32x4{a0, a1, a2, a3};
}
__device__ __forceinline__ v4u widen(v2u a, v2u b) {
  auto s0 = __builtin_amdgcn_permlane16_swap(a.x, b.x, false, false);
  auto s1 = __builtin_amdgcn_permlane16_swap(a.y, b.y, false, false);
  return v4u{s0[0], s1[0], s0[1], s1[1]};
}
__device__ __forceinline__ v2u gelu4(f32x4 a, float k0, float k1, float is) {
  float s0, s1, s2, s3;
  unsigned u0, u1;
  asm("v_mul_f32 %2, %6, %6\n\tv_mul_f32 %3, %7, %7\n\tv_fma_f32 %2, %2, %11, %10\n\tv_fma_f32 %3, %3, %11, %10\n\t"
      "v_mul_f32 %2, %2, %6\n\tv_mul_f32 %3, %3, %7\n\tv_exp_f32 %2, %2\n\tv_mul_f32 %4, %8, %8\n\t"
      "v_exp_f32 %3, %3\n\tv_mul_f32 %5, %9, %9\n\tv_fma_f32 %4, %4, %11, %10\n\tv_fma_f32 %5, %5, %11, %10\n\t"
      "v_mul_f32 %4, %4, %8\n\tv_mul_f32 %5, %5, %9\n\tv_exp_f32 %4, %4\n\tv_fma_f32 %2, %2, %12, %12\n\t"
      "v_exp_f32 %5, %5\n\tv_fma_f32 %3, %3, %12, %12\n\tv_rcp_f32 %2, %2\n\tv_fma_f32 %4, %4, %12, %12\n\t"
      "v_rcp_f32 %3, %3\n\tv_fma_f32 %5, %5, %12, %12\n\tv_rcp_f32 %4, %4\n\tv_mul_f32 %2, %2, %6\n\t"
      "v_rcp_f32 %5, %5\n\tv_mul_f32 %3, %3, %7\n\tv_mul_f32 %4, %4, %8\n\tv_cvt_pk_bf16_f32 %0, %2, %3\n\t"
      "v_mul_f32 %5, %5, %9\n\tv_cvt_pk_bf16_f32 %1, %4, %5"
      : "=&v"(u0), "=&v"(u1), "=&v"(s0), "=&v"(s1), "=&v"(s2), "=&v"(s3)
      : "v"(a[0]), "v"(a[1]), "v"(a[2]), "v"(a[3]), "v"(k0), "v"(k1), "v"(is));
  return v2u{u0, u1};
}
__device__ __forceinline__ void shift4(f32x4& a, f32x4 cs, f32x4 b, float nmean, float is) {
  float a0 = a[0], a1 = a[1], a2 = a[2], a3 = a[3];
  asm("v_fmac_f32 %0, %12, %4\n\tv_fmac_f32 %1, %12, %5\n\tv_fmac_f32 %2, %12, %6\n\tv_fmac_f32 %3, %12, %7\n\t"
      "v_fmac_f32 %0, %13, %8\n\tv_fmac_f32 %1, %13, %9\n\tv_fmac_f32 %2, %13, %10\n\tv_fmac_f32 %3, %13, %11"
      : "+v"(a0), "+v"(a1), "+v"(a2), "+v"(a3)
      : "v"(cs[0]), "v"(cs[1]), "v"(cs[2]), "v"(cs[3]), "v"(b[0]), "v"(b[1]), "v"(b[2]), "v"(b[3]), "v"(nmean), "v"(is));
  a = f32x4{a0, a1, a2, a3};
}
__device__ __forceinline__ int cnt_get(LAS unsigned char* L, int i) { return ((volatile LAS int*)(L + L_HC))[i]; }
__device__ __forceinline__ void cnt_wait(LAS unsigned char* L, int i, int n) {
  while (__builtin_amdgcn_readfirstlane(cnt_get(L, i)) < n) __builtin_amdgcn_s_sleep(1);
  asm volatile("" ::: "memory");
}
__device__ __forceinline__ void cnt_add(LAS unsigned char* L, int i, int lane) {
  asm volatile("s_waitcnt lgkmcnt(0)" ::: "memory");
  if (lane == 0) __hip_atomic_fetch_add((LAS int*)(L + L_HC) + i, 1, __ATOMIC_RELAXED, __HIP_MEMORY_SCOPE_WORKGROUP);
}
template <int NP> __device__ __forceinline__ void row_table1(LAS unsigned char* L, int t5, int lane) {
  const int r = t5 * 16 + (lane & 15), q = lane >> 4;
  float t1 = 0.f, t2 = 0.f;
#pragma unroll
  for (int i = 0; i < (NP + 3) / 4; ++i)
    if (NP % 4 == 0 || q < NP) { const f32x2 v = *(LAS f32x2*)(L + L_ST + ((i * 4 + q) * 80 + r) * 8); t1 += v.x; OPAQUE(t1); t2 += v.y; }
  t1 = rows_sum(t1); OPAQUE(t1); t2 = rows_sum(t2);
  const float nmean = t1 * (-1.0f / 576.0f);
  const float var = fmaxf(__builtin_fmaf(-nmean, nmean, t2 * (1.0f / 576.0f)), 0.f);
  const float rstd = __builtin_amdgcn_rsqf(var + EPS);
  if (q < 2) *(LAS float*)(L + L_RT + q * 320 + r * 4) = q ? nmean * rstd : rstd;
  cnt_add(L, C_R1, lane);
}
__device__ __forceinline__ v2u ln_pack(f32x4 a, f32x4 r, f32x4 c, f32x4 nm) {
  float t0, t1, t2, t3;
  unsigned u0, u1;
  asm("v_mul_f32 %2, %14, %18\n\tv_mul_f32 %3, %15, %19\n\tv_mul_f32 %4, %16, %20\n\tv_mul_f32 %5, %17, %21\n\t"
      "v_fmac_f32 %2, %6, %10\n\tv_fmac_f32 %3, %7, %11\n\tv_fmac_f32 %4, %8, %12\n\tv_fmac_f32 %5, %9, %13\n\t"
      "v_cvt_pk_bf16_f32 %0, %2, %3\n\tv_cvt_pk_bf16_f32 %1, %4, %5"
      : "=&v"(u0), "=&v"(u1), "=&v"(t0), "=&v"(t1), "=&v"(t2), "=&v"(t3)
      : "v"(a[0]), "v"(a[1]), "v"(a[2]), "v"(a[3]), "v"(r[0]), "v"(r[1]), "v"(r[2]), "v"(r[3]), "v"(c[0]), "v"(c[1]), "v"(c[2]), "v"(c[3]),
        "v"(nm[0]), "v"(nm[1]), "v"(nm[2]), "v"(nm[3]));
  return v2u{u0, u1};
}
__device__ __forceinline__ f32x4 splat(float x) { return f32x4{x, x, x, x}; }
__device__ __forceinline__ void row_table(LAS unsigned char* L, int lane) {
  cnt_wait(L, C_ST, 8);
  const f32x4 q = *(LAS f32x4*)(L + L_ST + lane * 16);
  float t1 = q[0] + q[2];
  OPAQUE(t1);
  float nmean = t1 * (-1.0f / 576.0f);
  const float var = fmaxf(__builtin_fmaf(-nmean, nmean, (q[1] + q[3]) * (1.0f / 576.0f)), 0.f);
  const float r1 = __builtin_amdgcn_rsqf(var + EPS);
  const float sc = r1 * __builtin_amdgcn_rsqf(var * r1 * r1 + EPS);
  float k0 = GC0 * sc;
  OPAQUE(k0);
  *(LAS f32x4*)(L + L_ST + KB + lane * 16) = f32x4{nmean, __builtin_amdgcn_rcpf(sc), k0, k0 * (0.044715f * sc * sc)};
  cnt_add(L, C_RT, lane);
}
__device__ __forceinline__ void shift_acc(f32x4 (&acc)[3][4], LAS unsigned char* L, int w, int quad, int l15) {
  f32x2 rt[4];
#pragma unroll
  for (int m = 0; m < 4; ++m) rt[m] = *(LAS f32x2*)(L + L_ST + KB + (m * 16 + l15) * 16);
#pragma unroll
  for (int t = 0; t < 3; ++t) {
    const f32x4 cs = *(LAS f32x4*)(L + L_T1 + (48 * w + 16 * t + 4 * quad) * 4), bb = *(LAS f32x4*)(L + L_T1 + 2304 + (48 * w + 16 * t + 4 * quad) * 4);
#pragma unroll
    for (int m = 0; m < 4; ++m) shift4(acc[t][m], cs, bb, rt[m][0], rt[m][1]);
  }
}
template <int SLEEP = 1> __device__ __forceinline__ void wait_flags(const unsigned char* ws, int off, bool active, u64 tag) {
  const GAS u64* f = (const GAS u64*)(ws + off);
  asm volatile("" : "+s"(tag));
#pragma unroll 1
  for (int spins = 0; spins < (1 << 16); ++spins) {
    const u64 v = active ? __hip_atomic_load(f, __ATOMIC_RELAXED, __HIP_MEMORY_SCOPE_AGENT) : tag;
    if (__all(v == tag)) break;
    __builtin_amdgcn_s_sleep(SLEEP);
  }
  asm volatile("" ::: "memory");
}
__device__ __forceinline__ void set_flag(unsigned char* ws, int off, u64 tag) {
  asm volatile("" : "+s"(tag));
  __hip_atomic_store((GAS u64*)(ws + off), tag, __ATOMIC_RELAXED, __HIP_MEMORY_SCOPE_AGENT);
}
template <int LO, int HI> __device__ __forceinline__ void issue_window(v4u (&win)[WIN], rsrc_t rs, int lane16, int base) {
#pragma unroll
  for (int i = LO; i < HI; ++i) win[i] = ldf(rs, lane16, base, i);
  SB();
}

template <int NT, int MT, int SWAP, int NNEXT, int R, int LO, int HI>
__device__ __forceinline__ void gemm18_steps(f32x4 (&acc)[NT][MT], v4u (&win)[WIN], v4u (&act)[R], LAS unsigned char* img, LAS unsigned char* img2, rsrc_t rs,
                                             int lane16, int wbase, int nvo, int nbase) {
#pragma unroll
  for (int n = LO; n < HI; ++n) {
    const int s = n / MT, m = n % MT, nn = n + R - 1;
    if (n < 18 * MT) {
#pragma unroll
      for (int t = 0; t < NT; ++t) {
        if (s == 0) {
          if ((SWAP >> t) & 1) MF0(acc[t][m], act[n % R], win[t]);
          else MF0(acc[t][m], win[t], act[n % R]);
        } else if ((SWAP >> t) & 1) MF(acc[t][m], act[n % R], win[(s * NT + t) % WIN]);
        else MF(acc[t][m], win[(s * NT + t) % WIN], act[n % R]);
      }
      SB();
    }
    if (nn < 18 * MT) act[nn % R] = *(LAS v4u*)(nn % MT < 3 ? img + ((nn % MT) * 18 + nn / MT) * KB : img2 + ((nn % MT - 3) * 18 + nn / MT) * KB);
    if (m == 0 && s > 0) {
#pragma unroll
      for (int t = 0; t < NT; ++t) {
        const int f = (s - 1) * NT + t;
        if (f + WIN < 18 * NT) win[f % WIN] = ldf(rs, lane16, wbase, f + WIN);
        else if (f % WIN < NNEXT) win[f % WIN] = ldf(rs, nvo, nbase, f % WIN);
      }
    }
    SB();
    if (n < 18 * MT) {
      KEEP(act[n % R]);
#pragma unroll
      for (int t = 0; t < NT; ++t) KEEP(win[(s * NT + t) % WIN]);
      SB();
    }
  }
}
template <int NT, int MT, int SWAP, int NNEXT, int R>
__device__ __forceinline__ void gemm18(f32x4 (&acc)[NT][MT], v4u (&win)[WIN], LAS unsigned char* img, rsrc_t rs, int lane16, int wbase, int nbase) {
  v4u act[R];
  LAS unsigned char* img2 = img + 54 * KB;
  if (MT > 3) asm volatile("" : "+v"(img2));
#pragma unroll
  for (int n = 0; n < R - 1; ++n) { act[n] = *(LAS v4u*)(n % MT < 3 ? img + ((n % MT) * 18 + n / MT) * KB : img2 + ((n % MT - 3) * 18 + n / MT) * KB); SB(); }
  gemm18_steps<NT, MT, SWAP, NNEXT, R, 0, 6 * MT>(acc, win, act, img, img2, rs, lane16, wbase, lane16, nbase);
  gemm18_steps<NT, MT, SWAP, NNEXT, R, 6 * MT, 12 * MT>(acc, win, act, img, img2, rs, lane16, wbase, lane16, nbase);
  gemm18_steps<NT, MT, SWAP, NNEXT, R, 12 * MT, 18 * MT + 1>(acc, win, act, img, img2, rs, lane16, wbase, lane16, nbase);
  ACC_OUT(acc);
}
template <int NT, int MT, int SWAP, int R, int tstep>
__device__ __forceinline__ void qkv_tiles(v4u (&win)[WIN], LAS unsigned char* L, rsrc_t rs, int lane, int wbase, int nbase, int tile0, int rwait STAMP_ARG) {
  f32x4 acc[NT][MT];
  gemm18<NT, MT, SWAP, WIN, R>(acc, win, L + lane * 16, rs, lane * 16, wbase, nbase);
  STAMP(38);
  LAS unsigned char *Rq = L + L_RT + (lane & 48), *Rl = L + L_RT + (lane & 15) * 4, *Tq = L + L_TQ + tile0 * 64 + (lane & 48),
                    *Tl = L + L_TQ + tile0 * 64 + (lane & 15) * 4, *X = L + L_QK + lane * 8 + tile0 * 2560;
  asm volatile("" : "+v"(Rq), "+v"(Rl), "+v"(Tq), "+v"(Tl), "+v"(X));
  cnt_wait(L, C_R1, rwait);
  v2u u[NT][MT];
#pragma unroll
  for (int t = 0; t < NT; ++t) {
    if ((SWAP >> t) & 1) {
      const f32x4 cs = splat(*(LAS float*)(Tl + t * tstep * 64));
#pragma unroll
      for (int m = 0; m < MT; ++m) u[t][m] = ln_pack(acc[t][m], *(LAS f32x4*)(Rq + m * 64), cs, *(LAS f32x4*)(Rq + 320 + m * 64));
    } else {
      const f32x4 cs = *(LAS f32x4*)(Tq + t * tstep * 64);
#pragma unroll
      for (int m = 0; m < MT; ++m) u[t][m] = ln_pack(acc[t][m], splat(*(LAS float*)(Rl + m * 64)), cs, splat(*(LAS float*)(Rl + 320 + m * 64)));
    }
  }
#pragma unroll
  for (int t = 0; t < NT; ++t)
#pragma unroll
    for (int m = 0; m < MT; ++m) *(LAS v2u*)(X + (t * tstep * 5 + m) * 512) = u[t][m];
}
template <bool HWAIT>
__device__ __forceinline__ void gemm_mlp(f32x4 (&acc)[3][4], v4u (&win)[WIN], LAS unsigned char* img, rsrc_t rs, int lane16, int rb, int vlast,
                                         int rlast, LAS unsigned char* L) {
  v4u act[8];
  if (HWAIT) cnt_wait(L, C_H0, 4);
#pragma unroll
  for (int n = 0; n < 4; ++n) { act[n] = *(LAS v4u*)(img + n * 18 * KB); SB(); }
  ACC_IN(acc);
#pragma unroll 1
  for (int c = 0; c < 3; ++c) {
    const int vo = c == 2 ? vlast : lane16, so = c == 2 ? rlast : rb + 18 * KB;
    int hv = 4;
#pragma unroll
    for (int s = 0; s < 6; ++s) {
      if (HWAIT && s == 5) {
        if (__builtin_amdgcn_readfirstlane(hv) < 4) cnt_wait(L, C_H0 + c + 1, 4);
        asm volatile("" ::: "memory");
        SB();
      }
#pragma unroll
      for (int t = 0; t < 3; ++t)
#pragma unroll
        for (int m = 0; m < 4; ++m) {
          MF(acc[t][m], win[s * 3 + t], act[(s & 1) * 4 + m]);
          SB();
          if (t == 0) act[((s + 1) & 1) * 4 + m] = *(LAS v4u*)(img + (m * 18 + s + 1) * KB);
          if (HWAIT && s == 3 && t == 2 && m == 0) hv = cnt_get(L, C_H0 + c + 1);
          if (HWAIT && (TAIL0 == 1 || TAIL1 == 1) && s == 1 && t == 2 && m == 0 && lane16 == 0)
            __hip_atomic_fetch_add((LAS int*)(L + L_HC) + C_LC, c == 2, __ATOMIC_RELAXED, __HIP_MEMORY_SCOPE_WORKGROUP);
          if (t == 1 && m < 3) {
            if (s == 0) win[15 + m] = ldf(rs, lane16, rb, 15 + m);
            else win[(s - 1) * 3 + m] = ldf(rs, vo, so, (s - 1) * 3 + m);
          }
          SB();
        }
    }
    img += 6 * KB;
    rb += 18 * KB;
  }
  ACC_OUT(acc);
}

__global__ void __launch_bounds__(NTHR) fused_fwd(Params p) {
  extern __shared__ __attribute__((aligned(16))) unsigned char smem[];
  LAS unsigned char* L = (LAS unsigned char*)smem;
  const int w0 = __builtin_amdgcn_readfirstlane(threadIdx.x >> 6);
  const int bid = blockIdx.x;
  constexpr int MPX = 8 / XB;
  const int j0 = ((bid & 7) / XB) * (32 / MPX) + (bid >> 3) / MPX;
  const int g0 = ((bid & 7) % XB) * MPX + (bid >> 3) % MPX;
  const rsrc_t rs = __builtin_amdgcn_make_buffer_rsrc((void*)p.ws, 0, WS_BYTES, 0x00020000);
  const int mb0 = WBASE + g0 * WSL + (252 + w0 * 108) * KB;
  const int nb0 = w0 < 9 ? WOB + ((g0 & 3) * 9 + w0) * 18 * KB : mb0;
  const int qb0 = w0 < 8 ? WBASE + g0 * WSL + (w0 < 6 ? w0 * 36 : 108 + w0 * 18) * KB : nb0;
  u64 pc0;
  asm volatile("s_getpc_b64 %0" : "=s"(pc0));
  pc0 += 2048;
  STAMP_INIT();
  STAMP(0);
  const u64 tag0 = __hip_atomic_load((const GAS u64*)p.ws, __ATOMIC_RELAXED, __HIP_MEMORY_SCOPE_AGENT);
  u64 tag = 0;
  v4u win[WIN];
  {
    constexpr int layer = 0;
    LOCALS();
    const rsrc_t rx = __builtin_amdgcn_make_buffer_rsrc((void*)p.x, 0, 2048 * 576 * 2, 0x00020000);
    const int xrow = lane >> 2, t5 = w >> 1;
    const int xtok = t5 < 4 ? 64 * j + 32 * (t5 & 1) + 2 * xrow + (t5 >> 1) : (j > 0 ? 64 * j - 16 : 0) + 2 * (xrow & 7) + (xrow >> 3);
    const int xoff = w < 10 ? xtok * 1152 + (w & 1) * 576 + (lane & 3) * 16 : 0x10000000;
#define LOAD_X() v4u xr[9]; _Pragma("unroll") for (int i = 0; i < 9; ++i) xr[i] = __builtin_amdgcn_raw_buffer_load_b128(rx, xoff + i * 64, 0, 0); SB()
#define STAGE_X() do { float s1 = 0.f, s2 = 0.f; \
      _Pragma("unroll") for (int i = 0; i < 9; ++i) { \
        if (w < 10) *(LAS v4u*)(L + (w * 9 + i) * KB + (lane & 3) * 256 + xrow * 16) = xr[i]; \
        _Pragma("unroll") for (int e = 0; e < 4; ++e) { s1 = dot2(xr[i][e], ONES, s1); s2 = dot2(xr[i][e], xr[i][e], s2); } } \
      s1 += dpp<0xB1>(s1); s2 += dpp<0xB1>(s2); s1 += dpp<0x4E>(s1); s2 += dpp<0x4E>(s2); \
      if (w < 10 && (lane & 3) == 0) *(LAS f32x2*)(L + L_ST + ((w & 1) * 80 + (w >> 1) * 16 + xrow) * 8) = f32x2{s1, s2}; } while (0)
    if (opq_s(w) < 8) {
      LAS unsigned char* T = L + L_QK + w * 3072;
      const int uf = 32 * w + j, uc = uf < 252 ? uf : 0, tile = uc / 18, us = uc - tile * 18;
      const bool vx = tile == 9, ex = (tile & 14) == 8;
      const int c0 = 72 * g + (ex ? 16 : ((tile + (tile >= 10 ? 2 : 0)) & 3) * 18) + (tile < 4 ? 576 : (tile < 8 || vx) ? 1152 : 0);
      const int cA = vx ? 18 : 576, cB = vx ? 36 : 18, cC = ex ? (vx ? 0 : 36) : 8;
      const GAS unsigned short* src = (const GAS unsigned short*)p.wqkv + (32 * us + (lane >> 1)) * 1728 + c0 + (lane & 1) * cC;
      v4u qv;
      if (ex) qv = v4u{*(const GAS unsigned*)(src), *(const GAS unsigned*)(src + cA), *(const GAS unsigned*)(src + cB), *(const GAS unsigned*)(src + cA + cB)};
      else qv = *(const GAS v4u_a4*)src;
      SB();
      const int oc = (g >> 2) * 81 + (uf < 81 ? uf : 0), ot = oc / 18, os = oc - ot * 18;
      const bool ox = os >= 16;
      const int orow = (ox ? 288 * (os - 16) + 16 : 36 * os) + quad * (ox ? 72 : 4) + ((lane >> 1) & 1) + ((lane >> 2) & 1) * (ox ? 18 : 2) + ((lane >> 3) & 1) * (ox ? 36 : 18);
      const rsrc_t ro = __builtin_amdgcn_make_buffer_rsrc((void*)p.wo, 0, 576 * 576 * 2, 0x00020000);
      const v4u ov = __builtin_amdgcn_raw_buffer_load_b128(ro, uf < 81 ? orow * 1152 + 32 * ((g & 3) * 9 + ot) + (lane & 1) * 16 : 0x10000000, 0, 0);
      SB();
      __builtin_amdgcn_s_barrier();
      LOAD_X();
      STAMP(41);
      *(LAS v4u*)(T + (lane >> 1) * 48 + (lane & 1) * 16) = (vx && (lane & 1)) ? v4u{0u, 0u, 0u, 0u} : qv;
      *(LAS v4u*)(T + 1536 + (lane >> 1) * 48 + (lane & 1) * 16) = ov;
      LAS unsigned char* R = T + (8 * quad + (l15 >> 2)) * 48 + (lane & 3) * 8;
      const v2u a = __builtin_bit_cast(v2u, __builtin_amdgcn_ds_read_tr16_b64_v4i16((LAS s16x4*)(R)));
      const v2u b = __builtin_bit_cast(v2u, __builtin_amdgcn_ds_read_tr16_b64_v4i16((LAS s16x4*)(R + 4 * 48)));
      const v2u c = __builtin_bit_cast(v2u, __builtin_amdgcn_ds_read_tr16_b64_v4i16((LAS s16x4*)(R + 1536)));
      const v2u d = __builtin_bit_cast(v2u, __builtin_amdgcn_ds_read_tr16_b64_v4i16((LAS s16x4*)(R + 1536 + 4 * 48)));
      stg(v4u{a.x, a.y, b.x, b.y}, rs, lane16,
          uf >= 252 ? WS_BYTES : wb + (tile < 8 ? (tile & 3) * 36 + us * 2 + (tile >> 2) : tile < 10 ? 72 + uc : (tile >> 1) * 36 - 36 + us * 2 + (tile & 1)) * KB);
      stg(v4u{c.x, c.y, d.x, d.y}, rs, lane16, uf < 81 ? WOB + ((g & 3) * 162 + oc) * KB : WS_BYTES);
      const float cq = rows_sum(dot2(a.x, ONES, dot2(a.y, ONES, dot2(b.x, ONES, dot2(b.y, ONES, 0.f)))));
      __builtin_amdgcn_raw_buffer_store_b32(__float_as_uint(cq), rs, uf < 252 && lane < 16 ? CSQ + ((g * 18 + us) * 14 + tile) * 64 + lane * 4 : WS_BYTES, 0, SC1);
      SB();
      STAMP(43);
      DRAIN();
      STAMP(44);
      BAR();
      GET_TAG();
      if (w == 0 && lane == 0) set_flag(p.ws, SFLAGS + g * 256 + j * 8, tag);
      STAGE_X();
      STAMP(2);
      BAR();
      STAMP(3);
      issue_window<0, WIN>(win, rs, lane16, QBASE());
    }
    if (opq_s(w) >= 8) {
      LAS unsigned char* T = L + 125 * KB + (w - 8) * 4608;
      const int item = 4 * j + w - 8, itc = item < 108 ? item : 0;
      const int rbk = itc / 3, cb = 3 * (itc - rbk * 3), kind = rbk >= 18, is = rbk - kind * 18, o = is % 3;
      const int r0 = kind == 0 ? 32 * is : 576 * g + 32 * (is - o) + (o < 2 ? 48 * o : 32);
      const int rn = kind == 0 ? 8 : 4, r4 = kind == 0 ? 4 : o < 2 ? 16 : 48, ld = kind == 0 ? 9216 : 1152;
      const rsrc_t rw = __builtin_amdgcn_make_buffer_rsrc((void*)(kind == 0 ? p.w1 : p.w2), 0, 576 * 4608 * 2, 0x00020000);
      const int jj = lane >> 3;
      const int woff = item < 108 ? (r0 + (jj & 3) + (jj >> 2) * r4) * ld + (kind == 0 ? 1152 * g : 0) + 128 * cb + (lane & 7) * 16 : 0x10000000;
      if (w == 11 && lane < 16) ((volatile LAS int*)(L + L_HC))[lane] = 0;
      __builtin_amdgcn_s_barrier();
      LOAD_X();
      v4u wv[3][4];
#pragma unroll
      for (int n = 0; n < 4; ++n)
#pragma unroll
        for (int c = 0; c < 3; ++c) wv[c][n] = __builtin_amdgcn_raw_buffer_load_b128(rw, woff + c * 128, n * rn * ld, NTW ? 2 : 0);
      SB();
      STAMP(41);
      BAR();
      GET_TAG();
      STAGE_X();
      if (opq_s(w) == 11) {
        wait_flags<4>(p.ws, SFLAGS + (g ^ ((lane >> 3) & 4)) * 256 + (lane & 31) * 8, true, tag);
#pragma unroll
        for (int i = 0; i < 18; ++i) win[i] = ldg(rs, lane16, CSQ + (g * 18 + i) * 896);
      }
      STAMP(2);
      BAR();
      STAMP(3);
      row_table1<2>(L, w - 8, lane);
      if (opq_s(w) == 11) {
        f32x4 cs = __builtin_bit_cast(f32x4, win[0]);
#pragma unroll
        for (int i = 1; i < 18; ++i) add4(cs, win[i]);
        if (lane < 56) *(LAS f32x4*)(L + L_TQ + lane16) = cs;
        row_table1<2>(L, 4, lane);
      }
      float csum[3];
      LAS unsigned char* R = T + (8 * quad + (l15 >> 2)) * 144 + (lane & 3) * 8;
#pragma unroll
      for (int c = 0; c < 3; ++c) {
#pragma unroll
        for (int n = 0; n < 4; ++n) *(LAS v4u*)(T + (n * 8 + (lane >> 3)) * 144 + (lane & 7) * 16) = wv[c][n];
        float cs[4];
#pragma unroll
        for (int tt = 0; tt < 4; ++tt) {
          const v2u a = __builtin_bit_cast(v2u, __builtin_amdgcn_ds_read_tr16_b64_v4i16((LAS s16x4*)(R + tt * 32)));
          const v2u b = __builtin_bit_cast(v2u, __builtin_amdgcn_ds_read_tr16_b64_v4i16((LAS s16x4*)(R + tt * 32 + 4 * 144)));
          wv[c][tt] = v4u{a.x, a.y, b.x, b.y};
          cs[tt] = rows_sum(dot2(a.x, ONES, dot2(a.y, ONES, dot2(b.x, ONES, dot2(b.y, ONES, 0.f)))));
          OPAQUE(cs[tt]);
        }
        csum[c] = quad == 0 ? cs[0] : quad == 1 ? cs[1] : quad == 2 ? cs[2] : cs[3];
      }
      STAMP(45);
      const int dst0 = item < 108 ? wb + (252 + 54 * kind + 3 * is + (4 * cb / 3) * 108) * KB : WS_BYTES;
#pragma unroll
      for (int c = 0; c < 3; ++c) {
#pragma unroll
        for (int tt = 0; tt < 4; ++tt) stg(wv[c][tt], rs, lane16, dst0 + (((4 * c + tt) / 3) * 105 + 4 * c + tt) * KB);
        __builtin_amdgcn_raw_buffer_store_b32(__float_as_uint(csum[c]), rs,
                                              lane * 4 + (item < 108 && kind == 0 ? CS1 + (is * 4608 + 576 * g + 64 * (cb + c)) * 4 : WS_BYTES), 0, SC1);
      }
      SB();
      if (opq_s(w) == 8) issue_window<0, WIN>(win, rs, lane16, QBASE());
    }
  }

#pragma unroll 1
  for (int layer = 0; layer < 2; ++layer) {
    v4u xres;
    {
      LOCALS();
      STAMP(4 + layer * 16);
      {
        const int qbase = QBASE(), nbase = NBASE(), rw = 5 + 5 * layer;
        if (opq_s(w) < 4) qkv_tiles<2, 5, 2, R2, 4>(win, L, rs, lane, qbase, nbase, w, rw STAMP_PASS);
        if ((opq_s(w) & 14) == 4) qkv_tiles<2, 4, 0, R2, 1>(win, L, rs, lane, qbase, nbase, 2 * w + 2, rw STAMP_PASS);
        if (opq_s(w) == 6) qkv_tiles<1, 5, 0, R1, 0>(win, L, rs, lane, qbase, nbase, 8, rw STAMP_PASS);
        if (opq_s(w) == 7) qkv_tiles<1, 5, 1, R1, 0>(win, L, rs, lane, qbase, nbase, 9, rw STAMP_PASS);
      }
      STAMP(5 + layer * 16);
      if ((opq_s(w) & 12) == 4) {
        BAR();
      }
      if ((opq_s(w) & 12) != 4) {
        const int T = w & 3, hp = w >> 3, Tp = (T & 1) ? T - 1 : 4;
        f32x4 mp, ms;
        const int lo = l15 + ((T & 1) ? 9 : 1 + 4 * T), hi = (T & 1) ? 16 : j > 0 ? 8 + 4 * T : 0;
#pragma unroll
        for (int r = 0; r < 4; ++r) {
          const int jj = 4 * quad + r;
          mp[r] = ((jj >= lo) & (jj < hi)) ? 0.f : -1.0e30f;
          ms[r] = (unsigned)(l15 - jj) <= 7u ? 0.f : -1.0e30f;
        }
        int xs = L_QK + lane * 8 + T * 512, xp = L_QK + lane * 8 + Tp * 512, hs = xs + hp * 5120, hq = xp + hp * 5120;
        int om = ab + A_O + (T * 18 + 2 * g + hp) * KB + lane16;
        int oe = quad == hp ? ab + A_O + (T * 18 + 16 + (g >> 2)) * KB + (g & 3) * 256 + hp * 8 + l15 * 16 : WS_BYTES;
        unsigned qm0 = quad == 2 * hp ? ~0u : 0u, qm1 = quad == 2 * hp + 1 ? ~0u : 0u, zr = 0u;
        v4u one4 = {ONES, ONES, ONES, ONES};
        asm volatile("" : "+v"(mp), "+v"(ms), "+v"(xs), "+v"(xp), "+v"(hs), "+v"(hq), "+v"(om), "+v"(oe), "+v"(qm0), "+v"(qm1), "+v"(zr), "+v"(one4));
        BAR();
        const v2u kxs = *(LAS v2u*)(L + xs + 40 * 512);
        const unsigned kxp = *(LAS unsigned*)(L + xp + 40 * 512 + 4);
        v2u q[2], ks[2], kp[2], vs[2], vp[2];
#pragma unroll
        for (int hh = 0; hh < 2; ++hh) {
          q[hh] = *(LAS v2u*)(L + hs + (50 + 5 * hh) * 512);
          kp[hh] = *(LAS v2u*)(L + hq + 5 * hh * 512);
          ks[hh] = *(LAS v2u*)(L + hs + 5 * hh * 512);
        }
        SB();
#pragma unroll
        for (int hh = 0; hh < 2; ++hh) {
          vp[hh] = *(LAS v2u*)(L + hq + (20 + 5 * hh) * 512);
          vs[hh] = *(LAS v2u*)(L + hs + (20 + 5 * hh) * 512);
        }
        const v2u vxp = *(LAS v2u*)(L + xp + 45 * 512), vxs = *(LAS v2u*)(L + xs + 45 * 512);
        SB();
        const f32x4 z = {0.f, 0.f, 0.f, 0.f};
        f32x4 sp[2], ss[2], o[2], ox[2], sm[2];
        float mx[2];
#pragma unroll
        for (int hh = 0; hh < 2; ++hh) {
          const v4u qf = {q[hh].x, q[hh].y, kxs.x & (hh ? qm1 : qm0), zr};
          sp[hh] = mfma(v4u{kp[hh].x, kp[hh].y, kxp, zr}, qf, mp);
          ss[hh] = mfma(v4u{ks[hh].x, ks[hh].y, kxs.y, zr}, qf, ms);
        }
#pragma unroll
        for (int hh = 0; hh < 2; ++hh) {
          const float m0 = __builtin_fmaxf(__builtin_fmaxf(sp[hh][0], sp[hh][1]), sp[hh][2]), m1 = __builtin_fmaxf(__builtin_fmaxf(ss[hh][0], ss[hh][1]), ss[hh][2]);
          mx[hh] = __builtin_fmaxf(__builtin_fmaxf(__builtin_fmaxf(__builtin_fmaxf(m0, sp[hh][3]), ss[hh][3]), m1), m1);
        }
        rows_max2(mx[0], mx[1]);
#pragma unroll
        for (int hh = 0; hh < 2; ++hh) {
          const float c = 0.23570226039551584f * 1.4426950408889634f, mc = -c * mx[hh];
          float e[8];
#pragma unroll
          for (int r = 0; r < 4; ++r) {
            e[r] = __builtin_amdgcn_exp2f(__builtin_fmaf(sp[hh][r], c, mc));
            e[4 + r] = __builtin_amdgcn_exp2f(__builtin_fmaf(ss[hh][r], c, mc));
          }
          const v4u pf = {pk(e[0], e[1]), pk(e[2], e[3]), pk(e[4], e[5]), pk(e[6], e[7])};
          sm[hh] = mfma(one4, pf, z);
          o[hh] = mfma(v4u{vp[hh].x, vp[hh].y, vs[hh].x, vs[hh].y}, pf, z);
          ox[hh] = mfma(v4u{vxp.x, vxp.y, vxs.x, vxs.y}, pf, z);
        }
        const float i0 = __builtin_amdgcn_rcpf(sm[0][0]), i1 = __builtin_amdgcn_rcpf(sm[1][0]);
        stg(v4u{pk(o[0][0] * i0, o[0][1] * i0), pk(o[0][2] * i0, o[0][3] * i0), pk(o[1][0] * i1, o[1][1] * i1), pk(o[1][2] * i1, o[1][3] * i1)}, rs, om, 0);
        __builtin_amdgcn_raw_buffer_store_b64(v2u{pk(ox[0][0] * i0, ox[0][1] * i0), pk(ox[1][2] * i1, ox[1][3] * i1)}, rs, oe, 0, SC1);
        STAMP(34);
        DRAIN();
      }
      {
        const int ft = (g & 3) * 9 + (w < 9 ? w : 0);
        xres = *(LAS v4u*)(L + ((g >> 2) * 36 + (quad & 1) * 18 + (ft >> 1)) * KB + ((2 * (ft & 1) + (quad >> 1)) * 16 + l15) * 16);
      }
      STAMP(6 + layer * 16);
      BAR();
      if (w == 7) {
        if (lane == 0) set_flag(p.ws, FLAGS + 2 * layer * 4096 + j * 128 + g * 8, tag);
        wait_flags(p.ws, FLAGS + 2 * layer * 4096 + j * 128 + (lane & 7) * 8, lane < 8, tag);
      }
      if (layer == 0 && w == 6 && lane == 0) set_flag(p.ws, SFLAGS + 2048 + g * 256 + j * 8, tag);
      BAR();
      STAMP(7 + layer * 16);
    }
    {
      LOCALS();
      const int th = g >> 2, ft = (g & 3) * 9 + w;
      v4u orr[3];
#pragma unroll
      for (int i = 0; i < 3; ++i) orr[i] = ldg(rs, lane16, ab + A_O + (th * 36 + w * 3 + i) * KB);
      const int xoff = ((quad & 1) * 18 + (ft >> 1)) * KB + ((2 * (ft & 1) + (quad >> 1)) * 16 + l15) * 16;
      const v4u xr = xres;
#pragma unroll
      for (int i = 0; i < 3; ++i) *(LAS v4u*)(L + (w * 3 + i) * KB + lane16) = orr[i];
      if (layer == 0 && w == 0) wait_flags<2>(p.ws, SFLAGS + 2048 + g * 256 + (lane & 31) * 8, lane < 32, tag);
      BAR();
      STAMP(8 + layer * 16);
      if (w < 9) {
        f32x4 acc[1][2];
        gemm18<1, 2, 0, P2N, 10>(acc, win, L + lane16, rs, lane16, 0, MBASE());
        float a[8];
#pragma unroll
        for (int r = 0; r < 4; ++r) {
          auto s = __builtin_amdgcn_permlane16_swap(__float_as_uint(acc[0][0][r]), __float_as_uint(acc[0][1][r]), false, false);
          a[r] = __uint_as_float(s[0]);
          a[4 + r] = __uint_as_float(s[1]);
        }
        const v4u o = {pk(a[0] + blo(xr.x), a[1] + bhi(xr.x)), pk(a[2] + blo(xr.y), a[3] + bhi(xr.y)),
                       pk(a[4] + blo(xr.z), a[5] + bhi(xr.z)), pk(a[6] + blo(xr.w), a[7] + bhi(xr.w))};
        stg(o, rs, xoff, ab + A_X1 + th * 36 * KB);
        STAMP(35);
        DRAIN();
      }
      if (layer == 0 && opq_s(w) >= 9) {
        const int f4 = (w - 9) * 48 + (lane < 48 ? lane : 0);
        f32x4 cs = {0.f, 0.f, 0.f, 0.f};
#pragma unroll
        for (int i = 0; i < 18; ++i) win[i] = ldg(rs, f4 * 16, CS1 + (i * 4608 + 576 * g) * 4);
        const v2u b = *(const GAS v2u*)(p.b1 + 576 * g + 4 * f4);
#pragma unroll
        for (int i = 0; i < 18; ++i) add4(cs, win[i]);
        *(LAS f32x4*)(L + L_T1 + f4 * 16) = cs;
        *(LAS f32x4*)(L + L_T1 + 2304 + f4 * 16) = f32x4{blo(b.x), bhi(b.x), blo(b.y), bhi(b.y)};
        SB();
        issue_window<0, 15>(win, rs, lane16, MBASE());
      }
      if (layer == 1 && bid == 0 && w == 10) {
#pragma unroll 1
        for (int i = 0; i < 4; ++i) wait_flags(p.ws, SFLAGS + (i * 64 + lane) * 8, true, tag);
        if (lane == 0) set_flag(p.ws, 0, tag);
      }
      STAMP(9 + layer * 16);
      BAR();
      if (w < 9) issue_window<P2N, 15>(win, rs, lane16, MBASE());
      if (w == 11) {
        if (lane == 0) set_flag(p.ws, FLAGS + (2 * layer + 1) * 4096 + j * 128 + g * 8, tag);
        wait_flags(p.ws, FLAGS + (2 * layer + 1) * 4096 + j * 128 + (lane & 7) * 8, lane < 8, tag);
      }
      BAR();
      STAMP(10 + layer * 16);
    }
    {
      LOCALS();
      const int mbase = MBASE();
      const int T = w & 3, cls = w >> 2;
      if (opq_s(cls) == 0) {
        if (w == 0 && lane < 8) ((volatile LAS int*)(L + L_HC))[lane] = lane == 3 ? 4 : 0;
        BAR();
      }
      if (opq_s(cls) > 0) {
        v4u xr[9];
#pragma unroll
        for (int i = 0; i < 9; ++i) xr[i] = ldf(rs, lane16, ab + A_X1 + (T * 18 + 9 * (cls - 1)) * KB, i);
#pragma unroll
        for (int i = 0; i < 9; ++i) *(LAS v4u*)(L + (T * 18 + 9 * (cls - 1) + i) * KB + lane16) = xr[i];
        BAR();
        float s1 = 0.f, s2 = 0.f, s3 = 0.f, s4 = 0.f;
#pragma unroll
        for (int i = 0; i < 9; ++i)
#pragma unroll
          for (int e = 0; e < 4; e += 2) {
            s1 = dot2(xr[i][e], ONES, s1); s2 = dot2(xr[i][e], xr[i][e], s2);
            s3 = dot2(xr[i][e + 1], ONES, s3); s4 = dot2(xr[i][e + 1], xr[i][e + 1], s4);
          }
        s1 += s3; OPAQUE(s1); s2 += s4;
        s1 = rows_sum(s1); OPAQUE(s1); s2 = rows_sum(s2);
        if (lane < 16) *(LAS f32x2*)(L + L_ST + ((T * 16 + lane) * 2 + cls - 1) * 8) = f32x2{s1, s2};
        cnt_add(L, C_ST, lane);
      }
      STAMP(11 + layer * 16);
      f32x4 acc[3][4];
#pragma unroll
      for (int t = 0; t < 3; ++t)
#pragma unroll
        for (int m = 0; m < 4; ++m) acc[t][m] = f32x4{0.f, 0.f, 0.f, 0.f};
      if (opq_s(cls) > 0) { row_table(L, lane); shift_acc(acc, L, w, quad, l15); }
      __builtin_amdgcn_s_setprio(3);
      gemm_mlp<false>(acc, win, L + lane16, rs, lane16, mbase, lane16, mbase + 54 * KB, L);
      STAMP(12 + layer * 16);
      {
        LAS unsigned char* H = L + L_H + lane16 + 3 * (w >> 1) * KB;
        if (opq_s(cls) == 0) {
          cnt_wait(L, C_RT, 1);
          shift_acc(acc, L, w, quad, l15);
        }
        f32x4 rt[4];
#pragma unroll
        for (int m = 0; m < 4; ++m) rt[m] = *(LAS f32x4*)(L + L_ST + KB + (m * 16 + l15) * 16);
#pragma unroll
        for (int m = 0; m < 4; ++m) {
          const v2u u0 = gelu4(acc[0][m], rt[m][2], rt[m][3], rt[m][1]), u1 = gelu4(acc[1][m], rt[m][2], rt[m][3], rt[m][1]);
          *(LAS v4u*)(H + (m * 18 + (w & 1)) * KB) = v4u{u0.x, u0.y, u1.x, u1.y};
          *(LAS v2u*)(H + (m * 18 + 2) * KB + (w & 1) * 8) = gelu4(acc[2][m], rt[m][2], rt[m][3], rt[m][1]);
        }
        cnt_add(L, C_H0 + cls, lane);
      }
      STAMP(13 + layer * 16);
      if (cls == 0) __builtin_amdgcn_s_setprio(2);
      else if (cls == 1) __builtin_amdgcn_s_setprio(1);
      else __builtin_amdgcn_s_setprio(0);
#pragma unroll
      for (int t = 0; t < 3; ++t)
#pragma unroll
        for (int m = 0; m < 4; ++m) acc[t][m] = f32x4{0.f, 0.f, 0.f, 0.f};
      gemm_mlp<true>(acc, win, L + L_H + lane16, rs, lane16, mbase + 54 * KB, layer ? lane16 + WS_BYTES : lane16, QBASE(), L);
      __builtin_amdgcn_s_setprio(0);
      cnt_add(L, C_GD, lane);
      STAMP(14 + layer * 16);
      const int pw = ab + A_PT + (g * 12 + w) * 6144;
      const int poff = (quad & 1) * 1536 + (quad >> 1) * 256 + l15 * 16;
#pragma unroll
      for (int t = 0; t < 3; ++t)
#pragma unroll
        for (int m = 0; m < 4; m += 2) {
          const v2u a = {pk(acc[t][m][0], acc[t][m][1]), pk(acc[t][m][2], acc[t][m][3])};
          const v2u b = {pk(acc[t][m + 1][0], acc[t][m + 1][1]), pk(acc[t][m + 1][2], acc[t][m + 1][3])};
          stg(widen(a, b), rs, poff + m * 1536 + t * 512, pw);
        }
      DRAIN();
      if (lane == 0) set_flag(p.ws, WFLAGS + ((layer * 32 + j) * 12 + w) * 128 + g * 8, tag);
      STAMP(15 + layer * 16);
    }
    {
      LOCALS();
      OWNROWS(6);
      const bool act = lane < 48;
      const v4u xr = *(LAS v4u*)(L + (act ? piece : 0));
      const v4u bb = *(const GAS v4u*)(p.b2 + (act ? c8 : 0) * 8);
      const int tail = layer ? TAIL1 : TAIL0;
      if (tail == 1) cnt_wait(L, C_LC, 12);
      if (tail == 2) cnt_wait(L, C_GD, 12);
      wait_flags(p.ws, WFLAGS + ((layer * 32 + j) * 12 + w) * 128 + (lane & 7) * 8, lane < 8, tag);
      STAMP(16 + layer * 16);
      v4u xb;
      {
        v4u pr[8];
        const int roff = (g >> 1) * 1536 + (lane >> 3) * 256 + (8 * (g & 1) + (lane & 7)) * 16 + (act ? 0 : WS_BYTES);
#pragma unroll
        for (int i = 0; i < 8; ++i) pr[i] = ldg(rs, roff, ab + A_PT + (i * 12 + w) * 6144);
        SB();
        const unsigned sel_lo = opq_s(0x00003f80), sel_hi = opq_s(0x3f800000);
#pragma unroll
        for (int e = 0; e < 4; ++e) {
          float lo = blo(bb[e]) + blo(xr[e]), hi = bhi(bb[e]) + bhi(xr[e]);
          OPAQUE(lo);
#pragma unroll
          for (int i = 0; i < 8; ++i) { lo = dot2(pr[i][e], sel_lo, lo); hi = dot2(pr[i][e], sel_hi, hi); }
          xb[e] = pk(lo, hi);
        }
      }
      STAMP(17 + layer * 16);
      if (layer == 0) {
        float s1 = dot2(xb.x, ONES, dot2(xb.y, ONES, dot2(xb.z, ONES, dot2(xb.w, ONES, 0.f))));
        float s2 = dot2(xb.x, xb.x, dot2(xb.y, xb.y, dot2(xb.z, xb.z, dot2(xb.w, xb.w, 0.f))));
        if (!act) { s1 = 0.f; s2 = 0.f; }
        s1 += ror8(s1); OPAQUE(s1); s2 += ror8(s2);
        s1 = rows_sum(s1); OPAQUE(s1); s2 = rows_sum(s2);
        if (!act) { xb.x = __float_as_uint(s1); xb.y = __float_as_uint(s2); }
        if (lane < 56) stg(xb, rs, lane16, ab + ALY + A_X2 + (g * 12 + w) * KB);
        DRAIN();
        STAMP(36);
        BAR();
        if (w == 0) {
          if (lane == 0) set_flag(p.ws, FLAGS + 4 * 4096 + j * 128 + g * 8, tag);
          wait_flags(p.ws, FLAGS + 4 * 4096 + (j - (lane >> 3)) * 128 + (lane < 8 ? lane : 3 + 4 * (lane & 1)) * 8, lane < (j > 0 ? 10 : 8), tag);
        } else issue_window<15, WIN>(win, rs, lane16, QBASE());
        BAR();
        STAMP(37);
        v4u xv[10];
        const int vo = lane < 56 ? lane16 : lane16 + WS_BYTES;
#pragma unroll
        for (int i = 0; i < 10; ++i)
          xv[i] = ldg(rs, vo, ab + ALY + A_X2 + w * KB + (i < 8 ? i * 12 * KB : (3 + 4 * (i - 8)) * 12 * KB - (j > 0 ? ASL : 0)));
        SB();
        if (opq_s(w) == 0) issue_window<15, WIN>(win, rs, lane16, QBASE());
#pragma unroll
        for (int i = 0; i < 10; ++i) KEEP(xv[i]);
        LAS unsigned char* I = L + (c8 >> 2) * KB + ((c8 & 3) * 16 + (lane & 7)) * 16;
        LAS unsigned char* S = L + L_ST + (w * 80 + (lane & 7)) * 8;
#pragma unroll
        for (int i = 0; i < 10; ++i)
          if (act) *(LAS v4u*)(I + (i >> 1) * 18 * KB + (i & 1) * 128) = xv[i];
        if (!act && lane < 56) {
#pragma unroll
          for (int i = 0; i < 10; ++i) *(LAS v2u*)(S + i * 64) = v2u{xv[i].x, xv[i].y};
        }
        BAR();
        STAMP(18);
        if (w == 11) row_table1<12>(L, 4, lane);
        if (w >= 8) row_table1<12>(L, w - 8, lane);
      } else {
        if (act) *(GAS v4u*)(p.out + (size_t)mytok * 576 + c8 * 8) = xb;
      }
    }
  }
  STAMP(40);
  STAMP_DUMP();
}
}

extern "C" void kernel_launch(void* const* d_in, const int* in_sizes, int n_in, void* d_out, int out_size, void* d_ws, size_t ws_size,
                              hipStream_t stream) {
  (void)in_sizes; (void)n_in; (void)out_size; (void)ws_size;
  Params p;
  p.x = (const unsigned short*)d_in[0];
  p.wqkv = (const unsigned short*)d_in[1];
  p.wo = (const unsigned short*)d_in[2];
  p.w1 = (const unsigned short*)d_in[4];
  p.b1 = (const unsigned short*)d_in[5];
  p.w2 = (const unsigned short*)d_in[6];
  p.b2 = (const unsigned short*)d_in[7];
  p.out = (unsigned short*)d_out;
  p.ws = (unsigned char*)d_ws;
  (void)hipFuncSetAttribute((const void*)fused_fwd, hipFuncAttributeMaxDynamicSharedMemorySize, LDS_BYTES);
  hipLaunchKernelGGL(fused_fwd, dim3(256), dim3(NTHR), LDS_BYTES, stream, p);
}
```

```cpp
#define CODEPF 36
#include <hip/hip_runtime.h>
#include <hip/hip_bf16.h>
#include <stdint.h>

namespace {
#define LAS __attribute__((address_space(3)))
#define GAS __attribute__((address_space(1)))
typedef unsigned v4u __attribute__((ext_vector_type(4)));
typedef unsigned v4u_a4 __attribute__((ext_vector_type(4), aligned(4)));
typedef unsigned v2u __attribute__((ext_vector_type(2)));
typedef float f32x4 __attribute__((ext_vector_type(4)));
typedef float f32x2 __attribute__((ext_vector_type(2)));
typedef __bf16 bf16x8 __attribute__((ext_vector_type(8)));
typedef __bf16 bf16x2 __attribute__((ext_vector_type(2)));
typedef short s16x4 __attribute__((ext_vector_type(4)));
typedef unsigned long long u64;
typedef __amdgpu_buffer_rsrc_t rsrc_t;

constexpr int KB = 1024;
constexpr int NTHR = 768;
constexpr int WIN = 18;
#define XB 2
#define P2N 15
#define R1 10
#define R2 7
#define SPF 0
#define SPFH 1
#define SNAKE 1
#define REUSE 0
#define OWN 1
#define DST 4
#define PRE 5
#define ABL_LD 0
#define G1GATE 0
#define PGELU 0
#define PEPI 0
#define N45 0
#define N67 18
#define DLO 0
#define DLX 10
#define PZ0 1
#define PZD 32
#define SETL -1
#define EFL 0
#define OUTAUX 0
#define NTW 1
constexpr float EPS = 1e-5f;
constexpr int SC1 = 16;
constexpr float GC0 = -2.0f * 0.7978845608028654f * 1.4426950408889634f;
constexpr int FLAGS = 4096;
constexpr int SFLAGS = 32 * KB;
constexpr int WFLAGS = 48 * KB;
constexpr int CS1 = 192 * KB;
constexpr int CSQ = 520 * KB;
constexpr int WBASE = 704 * KB;
constexpr int WSL = (252 + 1296) * KB;
constexpr int WOB = WBASE + 8 * WSL;
constexpr int ABASE = 14 * 1024 * KB;
constexpr int IMG = 72 * KB;
constexpr int A_X2 = 0, A_O = 96 * KB, A_X1 = A_O + IMG, A_PT = A_X1 + IMG, ALY = REUSE ? 0 : 1024 * KB, ASL = REUSE ? 1024 * KB : 2 * ALY;
constexpr int WS_BYTES = ABASE + 32 * ASL;
static_assert(WOB + 648 * KB <= ABASE && A_PT + 8 * IMG <= 1024 * KB && CSQ + 8 * 14 * 1152 + 256 <= WBASE, "workspace map");
constexpr int L_H = IMG;
constexpr int L_QK = 90 * KB;
constexpr int L_ST = 145 * KB;
constexpr int L_T1 = L_ST + 7680;
constexpr int L_TQ = L_T1 + 4608;
constexpr int L_RT = L_TQ + 896;
constexpr int L_HC = L_RT + 640;
constexpr int LDS_BYTES = L_HC + 64;
enum { C_H0 = 0, C_ST = 3, C_RT = 4, C_LC = 5, C_G1 = 6, C_R1 = 10 };

struct Params {
  const unsigned short *x, *wqkv, *wo, *w1, *b1, *w2, *b2;
  unsigned short *out;
  unsigned char *ws;
};

#define STAMP(i)
#define STAMP_INIT()
#define RETRY_COUNT()
#define STAMP_ARG
#define STAMP_PASS
#define STAMP_DUMP()
#define SB() __builtin_amdgcn_sched_barrier(0)
#define BAR() do { asm volatile("s_waitcnt lgkmcnt(0)" ::: "memory"); __builtin_amdgcn_s_barrier(); asm volatile("" ::: "memory"); } while (0)
#define DRAIN() asm volatile("s_waitcnt vmcnt(0)" ::: "memory")
#define SETTLE() DRAIN()
#define OPAQUE(x) asm("" : "+v"(x))
#define KEEP(x) asm volatile("" :: "v"(x))

__device__ __forceinline__ f32x4 mfma(v4u a, v4u b, f32x4 c) {
  return __builtin_amdgcn_mfma_f32_16x16x32_bf16(__builtin_bit_cast(bf16x8, a), __builtin_bit_cast(bf16x8, b), c, 0, 0, 0);
}
#define MF(acc, a, b) asm volatile("v_mfma_f32_16x16x32_bf16 %0, %1, %2, %0" : "+v"(acc) : "v"(a), "v"(b))
#define MF0(acc, a, b) asm volatile("v_mfma_f32_16x16x32_bf16 %0, %1, %2, 0" : "=&v"(acc) : "v"(a), "v"(b))
template <int NT, int MT> __device__ __forceinline__ void acc_fence(f32x4 (&acc)[NT][MT]) {
#pragma unroll
  for (int t = 0; t < NT; ++t)
#pragma unroll
    for (int m = 0; m < MT; ++m) asm volatile("" : "+v"(acc[t][m]));
}
#define ACC_IN(acc) do { acc_fence(acc); asm volatile("s_nop 1"); SB(); } while (0)
#define ACC_OUT(acc) do { SB(); asm volatile("s_nop 7\n\ts_nop 1"); acc_fence(acc); } while (0)
__device__ __forceinline__ unsigned pk(float a, float b) {
  f32x2 v = {a, b};
  return __builtin_bit_cast(unsigned, __builtin_convertvector(v, bf16x2));
}
__device__ __forceinline__ float dot2(unsigned a, unsigned b, float c) {
  return __builtin_amdgcn_fdot2_f32_bf16(__builtin_bit_cast(bf16x2, a), __builtin_bit_cast(bf16x2, b), c, false);
}
constexpr unsigned ONES = 0x3f803f80u;
__device__ __forceinline__ float blo(unsigned u) { return __uint_as_float(u << 16); }
__device__ __forceinline__ float bhi(unsigned u) { return __uint_as_float(u & 0xffff0000u); }
__device__ __forceinline__ v4u ldg(rsrc_t rs, int voff, int soff) { return __builtin_amdgcn_raw_buffer_load_b128(rs, voff, soff, SC1); }
__device__ __forceinline__ void stg(v4u v, rsrc_t rs, int voff, int soff) { __builtin_amdgcn_raw_buffer_store_b128(v, rs, voff + soff, 0, SC1); }
__device__ __forceinline__ v4u ldf(rsrc_t rs, int lane16, int base, int f) { return ldg(rs, lane16 + (f & 3) * KB, base + (f >> 2) * 4 * KB); }
__device__ __forceinline__ int opq_v(int x) { asm volatile("" : "+v"(x)); return x; }
__device__ __forceinline__ int lane_id() { int x; asm volatile("v_mbcnt_lo_u32_b32 %0, -1, 0\n\tv_mbcnt_hi_u32_b32 %0, -1, %0" : "=v"(x)); return x; }
template <unsigned C> __device__ __forceinline__ unsigned vconst() { unsigned x; asm volatile("v_mov_b32 %0, %1" : "=v"(x) : "i"(C)); return x; }
__device__ __forceinline__ int opq_s(int x) { asm volatile("" : "+s"(x)); return x; }
#define LOCALS() const int lane = lane_id(), w = opq_s(w0), j = opq_s(j0), g = opq_s(g0); \
  const int lane16 = lane * 16, quad = lane >> 4, l15 = lane & 15, ab = ABASE + j * ASL + layer * ALY, wb = WBASE + g * WSL; \
  (void)lane16; (void)quad; (void)l15; (void)ab; (void)wb
#define OWNROWS(CPW) const int c8 = CPW * w + (lane >> 3); \
  const int piece = ((g >> 1) * 18 + (c8 >> 2)) * KB + ((c8 & 3) * 16 + 8 * (g & 1) + (lane & 7)) * 16; \
  const int mytok = 64 * j + 2 * (16 * ((g >> 1) & 1) + 8 * (g & 1) + (lane & 7)) + (g >> 2)
#define CPF_DONE() KEEP(cpf)
#define GET_TAG() CPF_DONE(); tag = 1 + ((u64)(unsigned)__builtin_amdgcn_readfirstlane((int)(unsigned)tag0) | \
                             ((u64)(unsigned)__builtin_amdgcn_readfirstlane((int)(unsigned)(tag0 >> 32)) << 32))
#define NBASE() opq_s(nb0)
#define QBASE() opq_s(qb0)
#define MBASE() opq_s(mb0)
__device__ __forceinline__ float rows_sum(float x) {
  auto s = __builtin_amdgcn_permlane16_swap(__float_as_uint(x), __float_as_uint(x), false, false);
  x = __uint_as_float(s[0]) + __uint_as_float(s[1]);
  auto t = __builtin_amdgcn_permlane32_swap(__float_as_uint(x), __float_as_uint(x), false, false);
  return __uint_as_float(t[0]) + __uint_as_float(t[1]);
}
__device__ __forceinline__ float vmax(unsigned a, unsigned b) { float r; asm("v_max_f32 %0, %1, %2\n\ts_nop 1" : "=v"(r) : "v"(a), "v"(b)); return r; }
__device__ __forceinline__ void rows_max2(float& a, float& b) {
  auto s = __builtin_amdgcn_permlane16_swap(__float_as_uint(a), __float_as_uint(b), false, false);
  float x = vmax(s[0], s[1]);
  auto t = __builtin_amdgcn_permlane32_swap(__float_as_uint(x), __float_as_uint(x), false, false);
  x = vmax(t[0], t[1]);
  auto u = __builtin_amdgcn_permlane16_swap(__float_as_uint(x), __float_as_uint(x), false, false);
  a = __uint_as_float(u[0]);
  b = __uint_as_float(u[1]);
}
template <int CTRL> __device__ __forceinline__ float dpp(float x) {
  return __int_as_float(__builtin_amdgcn_mov_dpp(__float_as_int(x), CTRL, 0xf, 0xf, true));
}
__device__ __forceinline__ float ror8(float x) { return dpp<0x128>(x); }
__device__ __forceinline__ void add4(f32x4& a, v4u b) {
  float a0 = a[0], a1 = a[1], a2 = a[2], a3 = a[3];
  asm("v_add_f32 %0, %0, %4\n\tv_add_f32 %1, %1, %5\n\tv_add_f32 %2, %2, %6\n\tv_add_f32 %3, %3, %7"
      : "+v"(a0), "+v"(a1), "+v"(a2), "+v"(a3) : "v"(b.x), "v"(b.y), "v"(b.z), "v"(b.w));
  a = f32x4{a0, a1, a2, a3};
}
__device__ __forceinline__ v4u widen(v2u a, v2u b) {
  auto s0 = __builtin_amdgcn_permlane16_swap(a.x, b.x, false, false);
  auto s1 = __builtin_amdgcn_permlane16_swap(a.y, b.y, false, false);
  return v4u{s0[0], s1[0], s0[1], s1[1]};
}
__device__ __forceinline__ v2u gelu4(f32x4 a, float k0, float k1, float is) {
  float s0, s1, s2, s3;
  unsigned u0, u1;
  asm("v_mul_f32 %2, %6, %6\n\tv_mul_f32 %3, %7, %7\n\tv_fma_f32 %2, %2, %11, %10\n\tv_fma_f32 %3, %3, %11, %10\n\t"
      "v_mul_f32 %2, %2, %6\n\tv_mul_f32 %3, %3, %7\n\tv_exp_f32 %2, %2\n\tv_mul_f32 %4, %8, %8\n\t"
      "v_exp_f32 %3, %3\n\tv_mul_f32 %5, %9, %9\n\tv_fma_f32 %4, %4, %11, %10\n\tv_fma_f32 %5, %5, %11, %10\n\t"
      "v_mul_f32 %4, %4, %8\n\tv_mul_f32 %5, %5, %9\n\tv_exp_f32 %4, %4\n\tv_fma_f32 %2, %2, %12, %12\n\t"
      "v_exp_f32 %5, %5\n\tv_fma_f32 %3, %3, %12, %12\n\tv_rcp_f32 %2, %2\n\tv_fma_f32 %4, %4, %12, %12\n\t"
      "v_rcp_f32 %3, %3\n\tv_fma_f32 %5, %5, %12, %12\n\tv_rcp_f32 %4, %4\n\tv_mul_f32 %2, %2, %6\n\t"
      "v_rcp_f32 %5, %5\n\tv_mul_f32 %3, %3, %7\n\tv_mul_f32 %4, %4, %8\n\tv_cvt_pk_bf16_f32 %0, %2, %3\n\t"
      "v_mul_f32 %5, %5, %9\n\tv_cvt_pk_bf16_f32 %1, %4, %5"
      : "=&v"(u0), "=&v"(u1), "=&v"(s0), "=&v"(s1), "=&v"(s2), "=&v"(s3)
      : "v"(a[0]), "v"(a[1]), "v"(a[2]), "v"(a[3]), "v"(k0), "v"(k1), "v"(is));
  return v2u{u0, u1};
}
__device__ __forceinline__ void shift4(f32x4& a, f32x4 cs, f32x4 b, float nmean, float is) {
  float a0 = a[0], a1 = a[1], a2 = a[2], a3 = a[3];
  asm("v_fmac_f32 %0, %12, %4\n\tv_fmac_f32 %1, %12, %5\n\tv_fmac_f32 %2, %12, %6\n\tv_fmac_f32 %3, %12, %7\n\t"
      "v_fmac_f32 %0, %13, %8\n\tv_fmac_f32 %1, %13, %9\n\tv_fmac_f32 %2, %13, %10\n\tv_fmac_f32 %3, %13, %11"
      : "+v"(a0), "+v"(a1), "+v"(a2), "+v"(a3)
      : "v"(cs[0]), "v"(cs[1]), "v"(cs[2]), "v"(cs[3]), "v"(b[0]), "v"(b[1]), "v"(b[2]), "v"(b[3]), "v"(nmean), "v"(is));
  a = f32x4{a0, a1, a2, a3};
}
__device__ __forceinline__ int cnt_get(LAS unsigned char* L, int i) { return ((volatile LAS int*)(L + L_HC))[i]; }
__device__ __forceinline__ void cnt_wait(LAS unsigned char* L, int i, int n) {
  while (__builtin_amdgcn_readfirstlane(cnt_get(L, i)) < n) __builtin_amdgcn_s_sleep(1);
  asm volatile("" ::: "memory");
}
__device__ __forceinline__ void cnt_add(LAS unsigned char* L, int i, int lane) {
  asm volatile("s_waitcnt lgkmcnt(0)" ::: "memory");
  if (lane == 0) __hip_atomic_fetch_add((LAS int*)(L + L_HC) + i, 1, __ATOMIC_RELAXED, __HIP_MEMORY_SCOPE_WORKGROUP);
}
template <int NP> __device__ __forceinline__ void row_table1(LAS unsigned char* L, int t5, int lane) {
  const int r = t5 * 16 + (lane & 15), q = lane >> 4;
  float t1 = 0.f, t2 = 0.f;
#pragma unroll
  for (int i = 0; i < (NP + 3) / 4; ++i)
    if (NP % 4 == 0 || q < NP) { const f32x2 v = *(LAS f32x2*)(L + L_ST + ((i * 4 + q) * 80 + r) * 8); t1 += v.x; OPAQUE(t1); t2 += v.y; }
  t1 = rows_sum(t1); OPAQUE(t1); t2 = rows_sum(t2);
  const float nmean = t1 * (-1.0f / 576.0f);
  const float var = fmaxf(__builtin_fmaf(-nmean, nmean, t2 * (1.0f / 576.0f)), 0.f);
  const float rstd = __builtin_amdgcn_rsqf(var + EPS);
  if (q < 2) *(LAS float*)(L + L_RT + q * 320 + r * 4) = q ? nmean * rstd : rstd;
  cnt_add(L, C_R1, lane);
}
__device__ __forceinline__ v2u ln_pack(f32x4 a, f32x4 r, f32x4 c, f32x4 nm) {
  float t0, t1, t2, t3;
  unsigned u0, u1;
  asm("v_mul_f32 %2, %14, %18\n\tv_mul_f32 %3, %15, %19\n\tv_mul_f32 %4, %16, %20\n\tv_mul_f32 %5, %17, %21\n\t"
      "v_fmac_f32 %2, %6, %10\n\tv_fmac_f32 %3, %7, %11\n\tv_fmac_f32 %4, %8, %12\n\tv_fmac_f32 %5, %9, %13\n\t"
      "v_cvt_pk_bf16_f32 %0, %2, %3\n\tv_cvt_pk_bf16_f32 %1, %4, %5"
      : "=&v"(u0), "=&v"(u1), "=&v"(t0), "=&v"(t1), "=&v"(t2), "=&v"(t3)
      : "v"(a[0]), "v"(a[1]), "v"(a[2]), "v"(a[3]), "v"(r[0]), "v"(r[1]), "v"(r[2]), "v"(r[3]), "v"(c[0]), "v"(c[1]), "v"(c[2]), "v"(c[3]),
        "v"(nm[0]), "v"(nm[1]), "v"(nm[2]), "v"(nm[3]));
  return v2u{u0, u1};
}
__device__ __forceinline__ f32x4 zero4() {
  float a, b, c, d;
  asm volatile("v_mov_b32 %0, 0\n\tv_mov_b32 %1, 0\n\tv_mov_b32 %2, 0\n\tv_mov_b32 %3, 0" : "=v"(a), "=v"(b), "=v"(c), "=v"(d));
  return f32x4{a, b, c, d};
}
__device__ __forceinline__ f32x4 splat(float x) { return f32x4{x, x, x, x}; }
__device__ __forceinline__ void row_table(LAS unsigned char* L, int lane) {
  cnt_wait(L, C_ST, 8);
  const f32x4 q = *(LAS f32x4*)(L + L_ST + lane * 16);
  float t1 = q[0] + q[2];
  OPAQUE(t1);
  float nmean = t1 * (-1.0f / 576.0f);
  const float var = fmaxf(__builtin_fmaf(-nmean, nmean, (q[1] + q[3]) * (1.0f / 576.0f)), 0.f);
  const float r1 = __builtin_amdgcn_rsqf(var + EPS);
  const float sc = r1 * __builtin_amdgcn_rsqf(var * r1 * r1 + EPS);
  float k0 = GC0 * sc;
  OPAQUE(k0);
  *(LAS f32x4*)(L + L_ST + KB + lane * 16) = f32x4{nmean, __builtin_amdgcn_rcpf(sc), k0, k0 * (0.044715f * sc * sc)};
  cnt_add(L, C_RT, lane);
}
__device__ __forceinline__ void shift_acc(f32x4 (&acc)[3][4], LAS unsigned char* L, int w, int quad, int l15) {
  f32x2 rt[4];
#pragma unroll
  for (int m = 0; m < 4; ++m) rt[m] = *(LAS f32x2*)(L + L_ST + KB + (m * 16 + l15) * 16);
#pragma unroll
  for (int t = 0; t < 3; ++t) {
    const f32x4 cs = *(LAS f32x4*)(L + L_T1 + (48 * w + 16 * t + 4 * quad) * 4), bb = *(LAS f32x4*)(L + L_T1 + 2304 + (48 * w + 16 * t + 4 * quad) * 4);
#pragma unroll
    for (int m = 0; m < 4; ++m) shift4(acc[t][m], cs, bb, rt[m][0], rt[m][1]);
  }
}
template <int SLEEP = 1> __device__ __forceinline__ void wait_flags(const unsigned char* ws, int off, bool active, u64 tag) {
  const GAS u64* f = (const GAS u64*)(ws + off);
  asm volatile("" : "+s"(tag));
#pragma unroll 1
  for (int spins = 0; spins < (1 << 16); ++spins) {
    const u64 v = active ? __hip_atomic_load(f, __ATOMIC_RELAXED, __HIP_MEMORY_SCOPE_AGENT) : tag;
    if (__all(v == tag)) break;
    __builtin_amdgcn_s_sleep(SLEEP);
  }
  asm volatile("" ::: "memory");
}
__device__ __forceinline__ void set_flag(unsigned char* ws, int off, u64 tag) {
  asm volatile("" : "+s"(tag));
  __hip_atomic_store((GAS u64*)(ws + off), tag, __ATOMIC_RELAXED, __HIP_MEMORY_SCOPE_AGENT);
}
constexpr unsigned POISON = 0xffffffffu;
__device__ __forceinline__ unsigned umax4(unsigned m, v4u v) { return max(max(max(m, v.x), v.y), max(v.z, v.w)); }
#define RETRY_BEGIN() _Pragma("unroll 1") for (int spins_ = 0; spins_ < (1 << 15); ++spins_) { unsigned seen_ = 0
#define RETRY_END() if (!__any(seen_ == POISON)) break; RETRY_COUNT(); __builtin_amdgcn_s_sleep(2); asm volatile("" ::: "memory"); }
template <int LO, int HI> __device__ __forceinline__ void issue_window(v4u (&win)[WIN], rsrc_t rs, int lane16, int base) {
#pragma unroll
  for (int i = LO; i < HI; ++i) win[i] = ldf(rs, lane16, base, i);
  SB();
}

template <int NT, int MT, int SWAP, int NNEXT, int R, int LO, int HI>
__device__ __forceinline__ void gemm18_steps(f32x4 (&acc)[NT][MT], v4u (&win)[WIN], v4u (&act)[R], LAS unsigned char* img, LAS unsigned char* img2, rsrc_t rs,
                                             int lane16, int wbase, int nvo, int nbase) {
#pragma unroll
  for (int n = LO; n < HI; ++n) {
    const int s = n / MT, m = n % MT, nn = n + R - 1;
    if (n < 18 * MT) {
#pragma unroll
      for (int t = 0; t < NT; ++t) {
        if (s == 0) {
          if ((SWAP >> t) & 1) MF0(acc[t][m], act[n % R], win[t]);
          else MF0(acc[t][m], win[t], act[n % R]);
        } else if ((SWAP >> t) & 1) MF(acc[t][m], act[n % R], win[(s * NT + t) % WIN]);
        else MF(acc[t][m], win[(s * NT + t) % WIN], act[n % R]);
      }
      SB();
    }
    if (nn < 18 * MT) act[nn % R] = *(LAS v4u*)(nn % MT < 3 ? img + ((nn % MT) * 18 + nn / MT) * KB : img2 + ((nn % MT - 3) * 18 + nn / MT) * KB);
    if (m == 0 && s > 0) {
#pragma unroll
      for (int t = 0; t < NT; ++t) {
        const int f = (s - 1) * NT + t;
        if (f + WIN < 18 * NT) win[f % WIN] = ldf(rs, lane16, wbase, f + WIN);
        else if (f % WIN < NNEXT) win[f % WIN] = ldf(rs, nvo, nbase, f % WIN);
      }
    }
    SB();
    if (n < 18 * MT) {
      KEEP(act[n % R]);
#pragma unroll
      for (int t = 0; t < NT; ++t) KEEP(win[(s * NT + t) % WIN]);
      SB();
    }
  }
}
template <int NT, int MT, int SWAP, int NNEXT, int R>
__device__ __forceinline__ void gemm18(f32x4 (&acc)[NT][MT], v4u (&win)[WIN], LAS unsigned char* img, rsrc_t rs, int lane16, int wbase, int nbase) {
  v4u act[R];
  LAS unsigned char* img2 = img + 54 * KB;
  if (MT > 3) asm volatile("" : "+v"(img2));
#pragma unroll
  for (int n = 0; n < R - 1; ++n) { act[n] = *(LAS v4u*)(n % MT < 3 ? img + ((n % MT) * 18 + n / MT) * KB : img2 + ((n % MT - 3) * 18 + n / MT) * KB); SB(); }
  gemm18_steps<NT, MT, SWAP, NNEXT, R, 0, 6 * MT>(acc, win, act, img, img2, rs, lane16, wbase, lane16, nbase);
  gemm18_steps<NT, MT, SWAP, NNEXT, R, 6 * MT, 12 * MT>(acc, win, act, img, img2, rs, lane16, wbase, lane16, nbase);
  gemm18_steps<NT, MT, SWAP, NNEXT, R, 12 * MT, 18 * MT + 1>(acc, win, act, img, img2, rs, lane16, wbase, lane16, nbase);
  ACC_OUT(acc);
}
template <int NT, int MT, int SWAP, int R, int tstep, int NNEXT>
__device__ __forceinline__ void qkv_tiles(v4u (&win)[WIN], LAS unsigned char* L, rsrc_t rs, int lane, int wbase, int nbase, int tile0, int rwait STAMP_ARG) {
  f32x4 acc[NT][MT];
  gemm18<NT, MT, SWAP, NNEXT, R>(acc, win, L + lane * 16, rs, lane * 16, wbase, nbase);
  STAMP(38);
  LAS unsigned char *Rq = L + L_RT + (lane & 48), *Rl = L + L_RT + (lane & 15) * 4, *Tq = L + L_TQ + tile0 * 64 + (lane & 48),
                    *Tl = L + L_TQ + tile0 * 64 + (lane & 15) * 4, *X = L + L_QK + lane * 8 + tile0 * 2560;
  asm volatile("" : "+v"(Rq), "+v"(Rl), "+v"(Tq), "+v"(Tl), "+v"(X));
  cnt_wait(L, C_R1, rwait);
  v2u u[NT][MT];
#pragma unroll
  for (int t = 0; t < NT; ++t) {
    if ((SWAP >> t) & 1) {
      const f32x4 cs = splat(*(LAS float*)(Tl + t * tstep * 64));
#pragma unroll
      for (int m = 0; m < MT; ++m) u[t][m] = ln_pack(acc[t][m], *(LAS f32x4*)(Rq + m * 64), cs, *(LAS f32x4*)(Rq + 320 + m * 64));
    } else {
      const f32x4 cs = *(LAS f32x4*)(Tq + t * tstep * 64);
#pragma unroll
      for (int m = 0; m < MT; ++m) u[t][m] = ln_pack(acc[t][m], splat(*(LAS float*)(Rl + m * 64)), cs, splat(*(LAS float*)(Rl + 320 + m * 64)));
    }
  }
#pragma unroll
  for (int t = 0; t < NT; ++t)
#pragma unroll
    for (int m = 0; m < MT; ++m) *(LAS v2u*)(X + (t * tstep * 5 + m) * 512) = u[t][m];
}
template <int C>
__device__ __forceinline__ void mlp_chunk(f32x4 (&acc)[3][4], v4u (&win)[WIN], v4u (&act)[8], LAS unsigned char* img, rsrc_t rs, int lane16, int rb,
                                          LAS unsigned char* L) {
  int hv = 4;
#pragma unroll
  for (int s = 0; s < 6; ++s) {
    if ((C == 0 || C == 1) && s == 5) {
      if (__builtin_amdgcn_readfirstlane(hv) < 4) cnt_wait(L, C_H0 + C + 1, 4);
      asm volatile("" ::: "memory");
      SB();
    }
    if (C == 2 && s == 6 - DST) {
      if (lane16 == 0) __hip_atomic_fetch_add((LAS int*)(L + L_HC) + C_LC, 1, __ATOMIC_RELAXED, __HIP_MEMORY_SCOPE_WORKGROUP);
      SB();
    }
    asm volatile("" :: "v"(act[(s & 1) * 4]), "v"(act[(s & 1) * 4 + 1]), "v"(act[(s & 1) * 4 + 2]), "v"(act[(s & 1) * 4 + 3]), "v"(win[s * 3]),
                 "v"(win[s * 3 + 1]), "v"(win[s * 3 + 2]));
    SB();
#pragma unroll
    for (int t = 0; t < 3; ++t)
#pragma unroll
      for (int m = 0; m < 4; ++m) {
        const int mm = t == 1 ? 3 - m : m;
        if (C == 0 && s == 0) MF0(acc[t][mm], win[t], act[mm]);
        else MF(acc[t][mm], win[s * 3 + t], act[(s & 1) * 4 + mm]);
        SB();
        if (t == 0 && !(C == 2 && s == 5)) act[((s + 1) & 1) * 4 + m] = *(LAS v4u*)(img + (m * 18 + s + 1) * KB);
        if ((C == 0 || C == 1) && s == 3 && t == 2 && m == 0) hv = cnt_get(L, C_H0 + C + 1);
        if (!ABL_LD && t == 1 && m < 3 && !(C == 2 && s + DST >= 6) && !(C == -2 && s + DST < PRE))
          win[((s + DST) % 6) * 3 + m] = ldf(rs, lane16, rb, (s + DST) * 3 + m);
        SB();
      }
  }
}
__device__ __forceinline__ void gemm_up(f32x4 (&acc)[3][4], v4u (&win)[WIN], LAS unsigned char* img, rsrc_t rs, int lane16, int rb) {
  v4u act[8];
#pragma unroll
  for (int n = 0; n < 4; ++n) { act[n] = *(LAS v4u*)(img + n * 18 * KB); SB(); }
  ACC_IN(acc);
  if (DST < PRE) {
    mlp_chunk<-2>(acc, win, act, img, rs, lane16, rb, nullptr);
    img += 6 * KB;
    asm volatile("" : "+v"(img));
    rb += 18 * KB;
  }
#pragma unroll 1
  for (int c = DST < PRE; c < 3; ++c) {
    mlp_chunk<-1>(acc, win, act, img, rs, lane16, rb, nullptr);
    img += 6 * KB;
    rb += 18 * KB;
  }
  ACC_OUT(acc);
}
__device__ __forceinline__ void gemm_down(f32x4 (&acc)[3][4], v4u (&win)[WIN], LAS unsigned char* img, rsrc_t rs, int lane16, int rb, LAS unsigned char* L STAMP_ARG) {
  v4u act[8];
  cnt_wait(L, C_H0, 4);
  STAMP(46);
#pragma unroll
  for (int n = 0; n < 4; ++n) { act[n] = *(LAS v4u*)(img + n * 18 * KB); SB(); }
  mlp_chunk<0>(acc, win, act, img, rs, lane16, rb, L);
  img += 6 * KB;
  asm volatile("" : "+v"(img));
  STAMP(47);
  mlp_chunk<1>(acc, win, act, img, rs, lane16, rb + 18 * KB, L);
  img += 6 * KB;
  asm volatile("" : "+v"(img));
  STAMP(48);
  mlp_chunk<2>(acc, win, act, img, rs, lane16, rb + 36 * KB, L);
  ACC_OUT(acc);
}

__global__ void __launch_bounds__(NTHR) fused_fwd(Params p) {
  extern __shared__ __attribute__((aligned(16))) unsigned char smem[];
  LAS unsigned char* L = (LAS unsigned char*)smem;
  const int w0 = __builtin_amdgcn_readfirstlane(threadIdx.x >> 6);
  const int bid = blockIdx.x;
  u64 pc0;
  asm volatile("s_getpc_b64 %0" : "=s"(pc0));
  unsigned cpf = 0;
  if (threadIdx.x < CODEPF * 16) cpf = *(const GAS unsigned*)(pc0 + threadIdx.x * 64);
  constexpr int MPX = 8 / XB;
  const int j0 = ((bid & 7) / XB) * (32 / MPX) + (bid >> 3) / MPX;
  const int g0 = ((bid & 7) % XB) * MPX + (bid >> 3) % MPX;
  const rsrc_t rs = __builtin_amdgcn_make_buffer_rsrc((void*)p.ws, 0, WS_BYTES, 0x00020000);
  const int mb0 = WBASE + g0 * WSL + (252 + w0 * 108) * KB;
  const int nb0 = w0 < 9 ? WOB + ((g0 & 3) * 9 + w0) * 18 * KB : mb0;
  const int qb0 = w0 < 8 ? WBASE + g0 * WSL + (w0 < 6 ? w0 * 36 : 108 + w0 * 18) * KB : nb0;
  int os0[3];
  {
    const int T = 2 * (g0 >> 2) + (w0 >= 6), s0 = 3 * (w0 >= 6 ? w0 - 6 : w0);
#pragma unroll
    for (int i = 0; i < 3; ++i) {
      const int s = s0 + i;
      os0[i] = ABASE + j0 * ASL + A_O + (s < 16 ? ((s >> 1) * 9 + T * 2 + (s & 1)) * KB : ((s - 16) * 36 + 8) * KB + T * 256);
    }
  }
  const int oq0 = (w0 == 5 || w0 == 11) ? 9 * KB : 256;
  const int ft0 = (g0 & 3) * 9 + (w0 < 9 ? w0 : 0), xs0 = ((g0 >> 2) * 36 + (ft0 >> 1)) * KB + (ft0 & 1) * 512;
  STAMP_INIT();
  STAMP(0);
  const u64 tag0 = __hip_atomic_load((const GAS u64*)p.ws, __ATOMIC_RELAXED, __HIP_MEMORY_SCOPE_AGENT);
  u64 tag = 0;
  v4u win[WIN];
  {
    constexpr int layer = 0;
    LOCALS();
    const rsrc_t rx = __builtin_amdgcn_make_buffer_rsrc((void*)p.x, 0, 2048 * 576 * 2, 0x00020000);
    const int xrow = lane >> 2, t5 = w >> 1;
    const int xtok = t5 < 4 ? 64 * j + 32 * (t5 & 1) + 2 * xrow + (t5 >> 1) : (j > 0 ? 64 * j - 16 : 0) + 2 * (xrow & 7) + (xrow >> 3);
    const int xoff = w < 10 ? xtok * 1152 + (w & 1) * 576 + (lane & 3) * 16 : 0x10000000;
#define LOAD_X() v4u xr[9]; _Pragma("unroll") for (int i = 0; i < 9; ++i) xr[i] = __builtin_amdgcn_raw_buffer_load_b128(rx, xoff + i * 64, 0, 0); SB()
#define STAGE_X() do { float s1 = 0.f, s2 = 0.f; \
      _Pragma("unroll") for (int i = 0; i < 9; ++i) { \
        if (w < 10) *(LAS v4u*)(L + (w * 9 + i) * KB + (lane & 3) * 256 + xrow * 16) = xr[i]; \
        _Pragma("unroll") for (int e = 0; e < 4; ++e) { s1 = dot2(xr[i][e], ONES, s1); s2 = dot2(xr[i][e], xr[i][e], s2); } } \
      s1 += dpp<0xB1>(s1); s2 += dpp<0xB1>(s2); s1 += dpp<0x4E>(s1); s2 += dpp<0x4E>(s2); \
      if (w < 10 && (lane & 3) == 0) *(LAS f32x2*)(L + L_ST + ((w & 1) * 80 + (w >> 1) * 16 + xrow) * 8) = f32x2{s1, s2}; } while (0)
    if (opq_s(w) < 8) {
      LAS unsigned char* T = L + L_QK + w * 3072;
      const int uf = 32 * w + j, uc = uf < 252 ? uf : 0, tile = uc / 18, us = uc - tile * 18;
      const bool vx = tile == 9, ex = (tile & 14) == 8;
      const int c0 = 72 * g + (ex ? 16 : ((tile + (tile >= 10 ? 2 : 0)) & 3) * 18) + (tile < 4 ? 576 : (tile < 8 || vx) ? 1152 : 0);
      const int cA = vx ? 18 : 576, cB = vx ? 36 : 18, cC = ex ? (vx ? 0 : 36) : 8;
      const GAS unsigned short* src = (const GAS unsigned short*)p.wqkv + (32 * us + (lane >> 1)) * 1728 + c0 + (lane & 1) * cC;
      v4u qv;
      if (ex) qv = v4u{*(const GAS unsigned*)(src), *(const GAS unsigned*)(src + cA), *(const GAS unsigned*)(src + cB), *(const GAS unsigned*)(src + cA + cB)};
      else qv = *(const GAS v4u_a4*)src;
      SB();
      const int oc = (g >> 2) * 81 + (uf < 81 ? uf : 0), ot = oc / 18, os = oc - ot * 18;
      const bool ox = os >= 16;
      const int orow = (ox ? 288 * (os - 16) + 16 : 36 * os) + quad * (ox ? 72 : 4) + ((lane >> 1) & 1) + ((lane >> 2) & 1) * (ox ? 18 : 2) + ((lane >> 3) & 1) * (ox ? 36 : 18);
      const rsrc_t ro = __builtin_amdgcn_make_buffer_rsrc((void*)p.wo, 0, 576 * 576 * 2, 0x00020000);
      const v4u ov = __builtin_amdgcn_raw_buffer_load_b128(ro, uf < 81 ? orow * 1152 + 32 * ((g & 3) * 9 + ot) + (lane & 1) * 16 : 0x10000000, 0, 0);
      SB();
      __builtin_amdgcn_s_barrier();
      LOAD_X();
      STAMP(41);
      *(LAS v4u*)(T + (lane >> 1) * 48 + (lane & 1) * 16) = (vx && (lane & 1)) ? v4u{0u, 0u, 0u, 0u} : qv;
      *(LAS v4u*)(T + 1536 + (lane >> 1) * 48 + (lane & 1) * 16) = ov;
      LAS unsigned char* R = T + (8 * quad + (l15 >> 2)) * 48 + (lane & 3) * 8;
      const v2u a = __builtin_bit_cast(v2u, __builtin_amdgcn_ds_read_tr16_b64_v4i16((LAS s16x4*)(R)));
      const v2u b = __builtin_bit_cast(v2u, __builtin_amdgcn_ds_read_tr16_b64_v4i16((LAS s16x4*)(R + 4 * 48)));
      const v2u c = __builtin_bit_cast(v2u, __builtin_amdgcn_ds_read_tr16_b64_v4i16((LAS s16x4*)(R + 1536)));
      const v2u d = __builtin_bit_cast(v2u, __builtin_amdgcn_ds_read_tr16_b64_v4i16((LAS s16x4*)(R + 1536 + 4 * 48)));
      stg(v4u{a.x, a.y, b.x, b.y}, rs, lane16,
          uf >= 252 ? WS_BYTES : wb + (tile < 8 ? (tile & 3) * 36 + us * 2 + (tile >> 2) : tile < 10 ? 72 + uc : (tile >> 1) * 36 - 36 + us * 2 + (tile & 1)) * KB);
      stg(v4u{c.x, c.y, d.x, d.y}, rs, lane16, uf < 81 ? WOB + ((g & 3) * 162 + oc) * KB : WS_BYTES);
      const float cq = rows_sum(dot2(a.x, ONES, dot2(a.y, ONES, dot2(b.x, ONES, dot2(b.y, ONES, 0.f)))));
      __builtin_amdgcn_raw_buffer_store_b32(__float_as_uint(cq), rs, uf < 252 && lane < 16 ? CSQ + ((g * 18 + us) * 14 + tile) * 64 + lane * 4 : WS_BYTES, 0, SC1);
      SB();
      STAMP(43);
      DRAIN();
      STAMP(44);
      BAR();
      GET_TAG();
      if (w == 0 && lane == 0) set_flag(p.ws, SFLAGS + g * 256 + j * 8, tag);
      STAGE_X();
      STAMP(2);
      BAR();
      STAMP(3);
      issue_window<0, WIN>(win, rs, lane16, QBASE());
    }
    if (opq_s(w) >= 8) {
#define FILL0() _Pragma("unroll") for (int i = 0; i < 5; ++i) { const int n = (w - 8) * 5 + i; \
        stg(v4u{POISON, POISON, POISON, POISON}, rs, lane16, n < 18 ? ab + (n < 9 ? A_O : A_X1 - 9 * KB) + (g * 9 + n) * KB : WS_BYTES); }
      if (PZ0 == 0) { FILL0(); SB(); }
      LAS unsigned char* T = L + 125 * KB + (w - 8) * 4608;
      const int item = 4 * j + w - 8, itc = item < 108 ? item : 0;
      const int rbk = itc / 9, cb = itc - rbk * 9, kind = rbk >= 6, k3 = rbk - kind * 6;
      const int rn = kind == 0 ? 8 : 4, ld = kind == 0 ? 9216 : 1152;
      const rsrc_t rw = __builtin_amdgcn_make_buffer_rsrc((void*)(kind == 0 ? p.w1 : p.w2), 0, 576 * 4608 * 2, 0x00020000);
      const int jj = lane >> 3;
      const int woff = item < 108 ? ((kind == 0 ? 0 : 576 * g) + 96 * k3 + (jj & 3)) * ld + (kind == 0 ? 1152 * g : 0) + 128 * cb + (lane & 7) * 16 : 0x10000000;
      if (w == 11 && lane < 16) ((volatile LAS int*)(L + L_HC))[lane] = 0;
      __builtin_amdgcn_s_barrier();
      LOAD_X();
      v4u wv[3][4];
#pragma unroll
      for (int n = 0; n < 4; ++n)
#pragma unroll
        for (int c = 0; c < 3; ++c)
          wv[c][n] = __builtin_amdgcn_raw_buffer_load_b128(rw, woff + (jj >> 2) * (kind == 0 ? 4 : c < 2 ? 16 : 48) * ld,
                                                           (n * rn + (kind == 0 ? 32 * c : c < 2 ? 48 * c : 32)) * ld, NTW ? 2 : 0);
      SB();
      STAMP(41);
      BAR();
      GET_TAG();
      STAGE_X();
      if (opq_s(w) == 11) {
        wait_flags<4>(p.ws, SFLAGS + (g ^ ((lane >> 3) & 4)) * 256 + (lane & 31) * 8, true, tag);
#pragma unroll
        for (int i = 0; i < 18; ++i) win[i] = ldg(rs, lane16, CSQ + (g * 18 + i) * 896);
      }
      STAMP(2);
      BAR();
      STAMP(3);
      row_table1<2>(L, w - 8, lane);
      if (opq_s(w) == 11) {
        f32x4 cs = __builtin_bit_cast(f32x4, win[0]);
#pragma unroll
        for (int i = 1; i < 18; ++i) add4(cs, win[i]);
        if (lane < 56) *(LAS f32x4*)(L + L_TQ + lane16) = cs;
        row_table1<2>(L, 4, lane);
      }
      float cs[4] = {0.f, 0.f, 0.f, 0.f};
      LAS unsigned char* R = T + (8 * quad + (l15 >> 2)) * 144 + (lane & 3) * 8;
#pragma unroll
      for (int c = 0; c < 3; ++c) {
#pragma unroll
        for (int n = 0; n < 4; ++n) *(LAS v4u*)(T + (n * 8 + (lane >> 3)) * 144 + (lane & 7) * 16) = wv[c][n];
#pragma unroll
        for (int tt = 0; tt < 4; ++tt) {
          const v2u a = __builtin_bit_cast(v2u, __builtin_amdgcn_ds_read_tr16_b64_v4i16((LAS s16x4*)(R + tt * 32)));
          const v2u b = __builtin_bit_cast(v2u, __builtin_amdgcn_ds_read_tr16_b64_v4i16((LAS s16x4*)(R + tt * 32 + 4 * 144)));
          wv[c][tt] = v4u{a.x, a.y, b.x, b.y};
          cs[tt] = dot2(a.x, ONES, dot2(a.y, ONES, dot2(b.x, ONES, dot2(b.y, ONES, cs[tt]))));
        }
      }
#pragma unroll
      for (int tt = 0; tt < 4; ++tt) { cs[tt] = rows_sum(cs[tt]); OPAQUE(cs[tt]); }
      const float csum = quad == 0 ? cs[0] : quad == 1 ? cs[1] : quad == 2 ? cs[2] : cs[3];
      STAMP(45);
      const int dst0 = item < 108 ? wb + (252 + 54 * kind + 9 * k3) * KB : WS_BYTES;
#pragma unroll
      for (int tt = 0; tt < 4; ++tt)
#pragma unroll
        for (int c = 0; c < 3; ++c) stg(wv[c][tt], rs, lane16 + c * 3 * KB, dst0 + (((4 * cb + tt) / 3) * 105 + 4 * cb + tt) * KB);
      __builtin_amdgcn_raw_buffer_store_b32(__float_as_uint(csum), rs,
                                            lane * 4 + (item < 108 && kind == 0 ? CS1 + (k3 * 4608 + 576 * g + 64 * cb) * 4 : WS_BYTES), 0, SC1);
      if (PZ0 == 1) { FILL0(); }
      SB();
      if (opq_s(w) == 8) issue_window<0, WIN>(win, rs, lane16, QBASE());
    }
  }

#pragma unroll 1
  for (int layer = 0; layer < 2; ++layer) {
    v4u xres;
    {
      LOCALS();
      STAMP(4 + layer * 16);
      {
        const int qbase = QBASE(), nbase = NBASE(), rw = 5 + 5 * layer;
        if (opq_s(w) < 4) qkv_tiles<2, 5, 2, R2, 4, WIN>(win, L, rs, lane, qbase, nbase, w, rw STAMP_PASS);
        if ((opq_s(w) & 14) == 4) qkv_tiles<2, 4, 0, R2, 1, N45>(win, L, rs, lane, qbase, nbase, 2 * w + 2, rw STAMP_PASS);
        if (opq_s(w) == 6) qkv_tiles<1, 5, 0, R1, 0, N67>(win, L, rs, lane, qbase, nbase, 8, rw STAMP_PASS);
        if (opq_s(w) == 7) qkv_tiles<1, 5, 1, R1, 0, N67>(win, L, rs, lane, qbase, nbase, 9, rw STAMP_PASS);
      }
      if (layer == 1 && bid == 0 && w == 10) {
#pragma unroll 1
        for (int i = 0; i < 4; ++i) wait_flags(p.ws, SFLAGS + (i * 64 + lane) * 8, true, tag);
        if (lane == 0) set_flag(p.ws, 0, tag);
      }
      STAMP(5 + layer * 16);
      xres = *(LAS v4u*)(L + opq_s(xs0) + (quad & 1) * 18 * KB + (quad >> 1) * 256 + l15 * 16);
      if (layer == 0 && w >= 8) DRAIN();
      if ((opq_s(w) & 12) == 4) {
        BAR();
        if (w == 7 && (EFL || layer == 0)) {
          if (lane == 0) set_flag(p.ws, FLAGS + 2 * layer * 4096 + j * 128 + g * 8, tag);
          wait_flags(p.ws, FLAGS + 2 * layer * 4096 + j * 128 + (lane & 7) * 8, lane < 8, tag);
          __builtin_amdgcn_s_sleep(DLO);
        }
        if (layer == 0 && w == 6) {
          if (lane == 0) set_flag(p.ws, SFLAGS + 2048 + g * 256 + j * 8, tag);
        }
        if (N45 == N67) issue_window<N45, WIN>(win, rs, lane16, NBASE());
        else if (w < 6) issue_window<N45, WIN>(win, rs, lane16, NBASE());
      }
      if ((opq_s(w) & 12) != 4) {
        const int T = w & 3, hp = w >> 3, Tp = (T & 1) ? T - 1 : 4;
        f32x4 mp, ms;
        const float neg = __uint_as_float(vconst<0xf149f2cau>());
        const int lo = l15 + ((T & 1) ? 9 : 1 + 4 * T), hi = (T & 1) ? 16 : j > 0 ? 8 + 4 * T : 0;
#pragma unroll
        for (int r = 0; r < 4; ++r) {
          const int jj = 4 * quad + r;
          mp[r] = ((jj >= lo) & (jj < hi)) ? 0.f : neg;
          ms[r] = (unsigned)(l15 - jj) <= 7u ? 0.f : neg;
        }
        int xs = L_QK + lane * 8 + T * 512, xp = L_QK + lane * 8 + Tp * 512, hs = xs + hp * 5120, hq = xp + hp * 5120;
        int om = ab + A_O + (g * 9 + T * 2 + hp) * KB + lane16;
        int oe = quad == hp ? ab + A_O + (g * 9 + 8) * KB + T * 256 + hp * 8 + l15 * 16 : WS_BYTES;
        unsigned qm0 = quad == 2 * hp ? ~0u : 0u, qm1 = quad == 2 * hp + 1 ? ~0u : 0u, zr = vconst<0>();
        v4u one4 = {vconst<ONES>(), vconst<ONES>(), vconst<ONES>(), vconst<ONES>()};
        asm volatile("" : "+v"(mp), "+v"(ms), "+v"(xs), "+v"(xp), "+v"(hs), "+v"(hq), "+v"(om), "+v"(oe), "+v"(qm0), "+v"(qm1), "+v"(zr), "+v"(one4));
        BAR();
        const v2u kxs = *(LAS v2u*)(L + xs + 40 * 512);
        const unsigned kxp = *(LAS unsigned*)(L + xp + 40 * 512 + 4);
        v2u q[2], ks[2], kp[2], vs[2], vp[2];
#pragma unroll
        for (int hh = 0; hh < 2; ++hh) {
          q[hh] = *(LAS v2u*)(L + hs + (50 + 5 * hh) * 512);
          kp[hh] = *(LAS v2u*)(L + hq + 5 * hh * 512);
          ks[hh] = *(LAS v2u*)(L + hs + 5 * hh * 512);
        }
        SB();
#pragma unroll
        for (int hh = 0; hh < 2; ++hh) {
          vp[hh] = *(LAS v2u*)(L + hq + (20 + 5 * hh) * 512);
          vs[hh] = *(LAS v2u*)(L + hs + (20 + 5 * hh) * 512);
        }
        const v2u vxp = *(LAS v2u*)(L + xp + 45 * 512), vxs = *(LAS v2u*)(L + xs + 45 * 512);
        SB();
        const f32x4 z = {0.f, 0.f, 0.f, 0.f};
        f32x4 sp[2], ss[2], o[2], ox[2], sm[2];
        float mx[2];
#pragma unroll
        for (int hh = 0; hh < 2; ++hh) {
          const v4u qf = {q[hh].x, q[hh].y, kxs.x & (hh ? qm1 : qm0), zr};
          sp[hh] = mfma(v4u{kp[hh].x, kp[hh].y, kxp, zr}, qf, mp);
          ss[hh] = mfma(v4u{ks[hh].x, ks[hh].y, kxs.y, zr}, qf, ms);
        }
#pragma unroll
        for (int hh = 0; hh < 2; ++hh) {
          const float m0 = __builtin_fmaxf(__builtin_fmaxf(sp[hh][0], sp[hh][1]), sp[hh][2]), m1 = __builtin_fmaxf(__builtin_fmaxf(ss[hh][0], ss[hh][1]), ss[hh][2]);
          mx[hh] = __builtin_fmaxf(__builtin_fmaxf(__builtin_fmaxf(__builtin_fmaxf(m0, sp[hh][3]), ss[hh][3]), m1), m1);
        }
        rows_max2(mx[0], mx[1]);
#pragma unroll
        for (int hh = 0; hh < 2; ++hh) {
          const float c = 0.23570226039551584f * 1.4426950408889634f, mc = -c * mx[hh];
          float e[8];
#pragma unroll
          for (int r = 0; r < 4; ++r) {
            e[r] = __builtin_amdgcn_exp2f(__builtin_fmaf(sp[hh][r], c, mc));
            e[4 + r] = __builtin_amdgcn_exp2f(__builtin_fmaf(ss[hh][r], c, mc));
          }
          const v4u pf = {pk(e[0], e[1]), pk(e[2], e[3]), pk(e[4], e[5]), pk(e[6], e[7])};
          sm[hh] = mfma(one4, pf, z);
          o[hh] = mfma(v4u{vp[hh].x, vp[hh].y, vs[hh].x, vs[hh].y}, pf, z);
          ox[hh] = mfma(v4u{vxp.x, vxp.y, vxs.x, vxs.y}, pf, z);
        }
        const float i0 = __builtin_amdgcn_rcpf(sm[0][0]), i1 = __builtin_amdgcn_rcpf(sm[1][0]);
        stg(v4u{pk(o[0][0] * i0, o[0][1] * i0), pk(o[0][2] * i0, o[0][3] * i0), pk(o[1][0] * i1, o[1][1] * i1), pk(o[1][2] * i1, o[1][3] * i1)}, rs, om, 0);
        __builtin_amdgcn_raw_buffer_store_b64(v2u{pk(ox[0][0] * i0, ox[0][1] * i0), pk(ox[1][2] * i1, ox[1][3] * i1)}, rs, oe, 0, SC1);
        STAMP(34);
        SETTLE();
      }
      STAMP(6 + layer * 16);
      BAR();
      STAMP(7 + layer * 16);
    }
    {
      LOCALS();
      const int th = g >> 2, ft = (g & 3) * 9 + w;
      {
        const int ov = quad * opq_s(oq0) + l15 * 16;
        RETRY_BEGIN();
        v4u orr[3];
#pragma unroll
        for (int i = 0; i < 3; ++i) orr[i] = ldg(rs, i ? ov : lane16, os0[i] + layer * ALY);
#pragma unroll
        for (int i = 0; i < 3; ++i) { *(LAS v4u*)(L + (w * 3 + i) * KB + lane16) = orr[i]; seen_ = umax4(seen_, orr[i]); }
        RETRY_END();
      }
      const v4u xr = xres;
      if (layer == 0 && w == 0) wait_flags<2>(p.ws, SFLAGS + 2048 + g * 256 + (lane & 31) * 8, lane < 32, tag);
      BAR();
      STAMP(8 + layer * 16);
      if (EFL && w == 11 && lane == 0) set_flag(p.ws, FLAGS + (2 * layer + 1) * 4096 + j * 128 + g * 8, tag);
      if (w < 9) {
        f32x4 acc[1][2];
        gemm18<1, 2, 0, P2N, 10>(acc, win, L + lane16, rs, lane16, 0, MBASE());
        float a[8];
#pragma unroll
        for (int r = 0; r < 4; ++r) {
          auto s = __builtin_amdgcn_permlane16_swap(__float_as_uint(acc[0][0][r]), __float_as_uint(acc[0][1][r]), false, false);
          a[r] = __uint_as_float(s[0]);
          a[4 + r] = __uint_as_float(s[1]);
        }
        const v4u o = {pk(a[0] + blo(xr.x), a[1] + bhi(xr.x)), pk(a[2] + blo(xr.y), a[3] + bhi(xr.y)),
                       pk(a[4] + blo(xr.z), a[5] + bhi(xr.z)), pk(a[6] + blo(xr.w), a[7] + bhi(xr.w))};
        stg(o, rs, lane16, ab + A_X1 + (th * 36 + ft) * KB);
        STAMP(35);
        SETTLE();
      }
      if (layer == 0 && opq_s(w) >= 9) {
        const int f4 = (w - 9) * 48 + (lane < 48 ? lane : 0);
        v4u cp[6];
#pragma unroll
        for (int i = 0; i < 6; ++i) cp[i] = ldg(rs, f4 * 16, CS1 + (i * 4608 + 576 * g) * 4);
        const v2u b = *(const GAS v2u*)(p.b1 + 576 * g + 4 * f4);
        f32x4 cs = __builtin_bit_cast(f32x4, cp[0]);
#pragma unroll
        for (int i = 1; i < 6; ++i) add4(cs, cp[i]);
        *(LAS f32x4*)(L + L_T1 + f4 * 16) = cs;
        *(LAS f32x4*)(L + L_T1 + 2304 + f4 * 16) = f32x4{blo(b.x), bhi(b.x), blo(b.y), bhi(b.y)};
      }
      if (EFL && w == 11) {
        wait_flags(p.ws, FLAGS + (2 * layer + 1) * 4096 + j * 128 + (lane & 7) * 8, lane < 8, tag);
        __builtin_amdgcn_s_sleep(DLX);
      }
      STAMP(9 + layer * 16);
      BAR();
      STAMP(10 + layer * 16);
    }
    {
      LOCALS();
      const int mbase = MBASE();
      const int T = w & 3, cls = w >> 2;
      if (opq_s(cls) == 0) {
        if (w == 0 && lane < 10) ((volatile LAS int*)(L + L_HC))[lane] = 0;
        if (layer == 0) {
          __builtin_amdgcn_s_sleep(PZD);
          const v4u pz = {vconst<POISON>(), vconst<POISON>(), vconst<POISON>(), vconst<POISON>()};
#pragma unroll
          for (int i = 0; i < 8; ++i) {
            const int n = w * 8 + i;
            stg(pz, rs, lane16,
                n < 30 ? ab + ALY + (n < 12 ? A_X2 + (g * 12 + n) * KB : (n < 21 ? A_O : A_X1 - 9 * KB) + (g * 9 + n - 12) * KB) : WS_BYTES);
          }
        }
        BAR();
      }
      if (opq_s(cls) > 0) {
        v4u xr[9];
        const int xo = (quad >> 1) * KB + ((quad & 1) * 2 + (T & 1)) * 256 + l15 * 16;
        float s1, s2, s3, s4;
        RETRY_BEGIN();
#pragma unroll
        for (int i = 0; i < 9; ++i) xr[i] = ldg(rs, xo + (i & 1) * 2 * KB, ab + A_X1 + ((T >> 1) * 36 + 18 * (cls - 1) + (i >> 1) * 4) * KB);
        s1 = 0.f; s2 = 0.f; s3 = 0.f; s4 = 0.f;
#pragma unroll
        for (int i = 0; i < 9; ++i) {
          *(LAS v4u*)(L + (T * 18 + 9 * (cls - 1) + i) * KB + lane16) = xr[i];
          seen_ = umax4(seen_, xr[i]);
#pragma unroll
          for (int e = 0; e < 4; e += 2) {
            s1 = dot2(xr[i][e], ONES, s1); s2 = dot2(xr[i][e], xr[i][e], s2);
            s3 = dot2(xr[i][e + 1], ONES, s3); s4 = dot2(xr[i][e + 1], xr[i][e + 1], s4);
          }
          SB();
        }
        asm volatile("" : "+v"(s1), "+v"(s2), "+v"(s3), "+v"(s4));
        RETRY_END();
        BAR();
        if (layer == 0 && opq_s(w) >= 9) issue_window<0, 3 * PRE>(win, rs, lane16, mbase);
        s1 += s3; OPAQUE(s1); s2 += s4;
        s1 = rows_sum(s1); OPAQUE(s1); s2 = rows_sum(s2);
        if (lane < 16) *(LAS f32x2*)(L + L_ST + ((T * 16 + lane) * 2 + cls - 1) * 8) = f32x2{s1, s2};
        cnt_add(L, C_ST, lane);
      }
      STAMP(11 + layer * 16);
      f32x4 acc[3][4];
#pragma unroll
      for (int t = 0; t < 3; ++t)
#pragma unroll
        for (int m = 0; m < 4; ++m) acc[t][m] = zero4();
      if (opq_s(cls) > 0) { row_table(L, lane); shift_acc(acc, L, w, quad, l15); }
      STAMP(49);
      __builtin_amdgcn_s_setprio(3);
      gemm_up(acc, win, L + lane16, rs, lane16, mbase);
      if (G1GATE) cnt_add(L, C_G1 + (w & 3), lane);
      __builtin_amdgcn_s_setprio(PGELU);
      STAMP(12 + layer * 16);
      {
        LAS unsigned char* H = L + L_H + lane16 + 3 * (w >> 1) * KB;
        if (opq_s(cls) == 0) {
          cnt_wait(L, C_RT, 1);
          shift_acc(acc, L, w, quad, l15);
        }
        f32x4 rt[4];
#pragma unroll
        for (int m = 0; m < 4; ++m) rt[m] = *(LAS f32x4*)(L + L_ST + KB + (m * 16 + l15) * 16);
#pragma unroll
        for (int m = 0; m < 4; ++m) {
          const v2u u0 = gelu4(acc[0][m], rt[m][2], rt[m][3], rt[m][1]), u1 = gelu4(acc[1][m], rt[m][2], rt[m][3], rt[m][1]);
          *(LAS v4u*)(H + (m * 18 + (w & 1)) * KB) = v4u{u0.x, u0.y, u1.x, u1.y};
          *(LAS v2u*)(H + (m * 18 + 2) * KB + (w & 1) * 8) = gelu4(acc[2][m], rt[m][2], rt[m][3], rt[m][1]);
        }
        cnt_add(L, C_H0 + cls, lane);
      }
      STAMP(13 + layer * 16);
      __builtin_amdgcn_s_setprio(2);
      if (G1GATE) cnt_wait(L, C_G1 + (w & 3), 3);
      gemm_down(acc, win, L + L_H + lane16, rs, lane16, mbase + 54 * KB, L STAMP_PASS);
      __builtin_amdgcn_s_setprio(PEPI);
      STAMP(14 + layer * 16);
      const int pw = ab + A_PT + (g * 12 + w) * 6144;
      const int poff = (quad & 1) * 1536 + (quad >> 1) * 256 + l15 * 16;
#pragma unroll
      for (int t = 0; t < 3; ++t)
#pragma unroll
        for (int m = 0; m < 4; m += 2) {
          const v2u a = {pk(acc[t][m][0], acc[t][m][1]), pk(acc[t][m][2], acc[t][m][3])};
          const v2u b = {pk(acc[t][m + 1][0], acc[t][m + 1][1]), pk(acc[t][m + 1][2], acc[t][m + 1][3])};
          const v4u o = widen(a, b);
          const bool own = OWN && m + (quad & 1) == (g >> 1) && (l15 >> 3) == (g & 1);
          stg(o, rs, own ? WS_BYTES : poff + m * 1536 + t * 512, pw);
          if (own) *(LAS v4u*)(L + (((g >> 1) ^ 1) * 18 + w) * KB + ((2 * t + (quad >> 1)) * 8 + (lane & 7)) * 16) = o;
        }
      __builtin_amdgcn_s_setprio(0);
      DRAIN();
      if (lane == 0) set_flag(p.ws, WFLAGS + ((layer * 32 + j) * 12 + w) * 128 + g * 8, tag);
      STAMP(15 + layer * 16);
      if (layer == 0 && opq_s(w) < 8) {
        cnt_wait(L, C_LC, 12);
        issue_window<0, WIN>(win, rs, lane16, QBASE());
      }
      __builtin_amdgcn_s_barrier();
    }
    {
      LOCALS();
      OWNROWS(6);
      const bool act = lane < 48;
      const v4u xr = *(LAS v4u*)(L + (act ? piece : 0));
      const v4u bb = *(const GAS v4u*)(p.b2 + (act ? c8 : 0) * 8);
      wait_flags(p.ws, WFLAGS + ((layer * 32 + j - (lane >> 3)) * 12 + w) * 128 + (lane < 8 ? lane : 3 + 4 * (lane & 1)) * 8,
                 lane < (layer == 0 && j > 0 ? 10 : 8), tag);
      STAMP(16 + layer * 16);
      v4u xb;
      {
        v4u pr[8];
        const int roff = (g >> 1) * 1536 + (lane >> 3) * 256 + (8 * (g & 1) + (lane & 7)) * 16 + (act ? 0 : WS_BYTES);
#pragma unroll
        for (int i = OWN; i < 8; ++i) pr[i] = ldg(rs, roff, ab + A_PT + (((i + g) & 7) * 12 + w) * 6144);
        if (OWN) pr[0] = *(LAS v4u*)(L + (((g >> 1) ^ 1) * 18 + w) * KB + lane16);
        SB();
        const unsigned sel_lo = opq_s(0x00003f80), sel_hi = opq_s(0x3f800000);
#pragma unroll
        for (int e = 0; e < 4; ++e) {
          float lo = blo(bb[e]) + blo(xr[e]), hi = bhi(bb[e]) + bhi(xr[e]);
          OPAQUE(lo);
#pragma unroll
          for (int i = 0; i < 8; ++i) { lo = dot2(pr[i][e], sel_lo, lo); hi = dot2(pr[i][e], sel_hi, hi); }
          xb[e] = pk(lo, hi);
        }
      }
      STAMP(17 + layer * 16);
      if (layer == 0) {
        float s1 = dot2(xb.x, ONES, dot2(xb.y, ONES, dot2(xb.z, ONES, dot2(xb.w, ONES, 0.f))));
        float s2 = dot2(xb.x, xb.x, dot2(xb.y, xb.y, dot2(xb.z, xb.z, dot2(xb.w, xb.w, 0.f))));
        if (!act) { s1 = 0.f; s2 = 0.f; }
        s1 += ror8(s1); OPAQUE(s1); s2 += ror8(s2);
        s1 = rows_sum(s1); OPAQUE(s1); s2 = rows_sum(s2);
        if (!act) xb = v4u{__float_as_uint(s1), __float_as_uint(s2), 0u, 0u};
        if (lane < 56) stg(xb, rs, lane16, ab + ALY + A_X2 + (g * 12 + w) * KB);
        SETTLE();
        STAMP(36);
        const bool stl = (lane >> 3) == 6;
        LAS unsigned char* Iw = act ? L + (c8 >> 2) * KB + ((c8 & 3) * 16 + (lane & 7)) * 16 : L + 126 * KB + lane16;
        LAS unsigned char* Sw = L + L_ST + (w * 80 + (lane & 7)) * 8;
        BAR();
        STAMP(37);
        v4u xv[10];
        const int vo = lane < 56 ? lane16 : lane16 + WS_BYTES;
        xv[0] = xb;
        RETRY_BEGIN();
#pragma unroll
        for (int i = 1; i < 10; ++i)
          xv[i] = ldg(rs, vo, ab + ALY + A_X2 + w * KB + (i < 8 ? (i ^ g) * 12 * KB : (3 + 4 * (i - 8)) * 12 * KB - (j > 0 ? ASL : 0)));
        SB();
#pragma unroll
        for (int i = 0; i < 10; ++i) {
          const int mi = i < 8 ? i ^ g : i;
          *(LAS v4u*)(Iw + (act ? (mi >> 1) * 18 * KB + (mi & 1) * 128 : 0)) = xv[i];
          if (i) seen_ = umax4(seen_, xv[i]);
        }
        RETRY_END();
        if (stl) {
#pragma unroll
          for (int i = 0; i < 10; ++i) *(LAS v2u*)(Sw + (i < 8 ? i ^ g : i) * 64) = v2u{xv[i].x, xv[i].y};
        }
        BAR();
        STAMP(18);
        if (w == 11) row_table1<12>(L, 4, lane);
        if (w >= 8) row_table1<12>(L, w - 8, lane);
        if (opq_s(w) >= 8) issue_window<0, WIN>(win, rs, lane16, QBASE());
      } else {
        const rsrc_t ro = __builtin_amdgcn_make_buffer_rsrc((void*)p.out, 0, 2048 * 576 * 2, 0x00020000);
        __builtin_amdgcn_raw_buffer_store_b128(xb, ro, act ? mytok * 1152 + c8 * 16 : 0x10000000, 0, OUTAUX);
      }
    }
  }
  STAMP(40);
  STAMP_DUMP();
}
}

extern "C" void kernel_launch(void* const* d_in, const int* in_sizes, int n_in, void* d_out, int out_size, void* d_ws, size_t ws_size,
                              hipStream_t stream) {
  (void)in_sizes; (void)n_in; (void)out_size; (void)ws_size;
  Params p;
  p.x = (const unsigned short*)d_in[0];
  p.wqkv = (const unsigned short*)d_in[1];
  p.wo = (const unsigned short*)d_in[2];
  p.w1 = (const unsigned short*)d_in[4];
  p.b1 = (const unsigned short*)d_in[5];
  p.w2 = (const unsigned short*)d_in[6];
  p.b2 = (const unsigned short*)d_in[7];
  p.out = (unsigned short*)d_out;
  p.ws = (unsigned char*)d_ws;
  (void)hipFuncSetAttribute((const void*)fused_fwd, hipFuncAttributeMaxDynamicSharedMemorySize, LDS_BYTES);
  hipLaunchKernelGGL(fused_fwd, dim3(256), dim3(NTHR), LDS_BYTES, stream, p);
}
```

```cpp
#define DST 4
#define PRE 5
#define P2N 15
#include <hip/hip_runtime.h>
#include <hip/hip_bf16.h>
#include <stdint.h>

namespace {
#define LAS __attribute__((address_space(3)))
#define GAS __attribute__((address_space(1)))
typedef unsigned v4u __attribute__((ext_vector_type(4)));
typedef unsigned v4u_a4 __attribute__((ext_vector_type(4), aligned(4)));
typedef unsigned v2u __attribute__((ext_vector_type(2)));
typedef float f32x4 __attribute__((ext_vector_type(4)));
typedef float f32x2 __attribute__((ext_vector_type(2)));
typedef __bf16 bf16x8 __attribute__((ext_vector_type(8)));
typedef __bf16 bf16x2 __attribute__((ext_vector_type(2)));
typedef short s16x4 __attribute__((ext_vector_type(4)));
typedef unsigned long long u64;
typedef __amdgpu_buffer_rsrc_t rsrc_t;

constexpr int KB = 1024;
constexpr int NTHR = 768;
constexpr int WIN = 18;
#define XB 2
#define R1 10
#define R2 7
#define SPF 0
#define SPFH 1
#define SNAKE 1
#define REUSE 0
#define OWN 1
#define ABL_LD 0
#define PGELU 0
#define PEPI 0
#define N45 0
#define N67 18
#define DLO 0
#define DLX 10
#define PZ0 1
#define PZD 20
#define SETL -1
#define EFL 0
#define CODEPF 35
#define ROLL 0
#define R5 9
#define SFSTR 8
#define PSLP 4
#define BDELAY 50
#define QA 6
#define QB 12
#define OUTAUX 0
#define NTW 1
constexpr float EPS = 1e-5f;
constexpr int SC1 = 16;
constexpr float GC0 = -2.0f * 0.7978845608028654f * 1.4426950408889634f;
constexpr int FLAGS = 4096;
constexpr int SFLAGS = 400 * KB;
constexpr int SFS = SFSTR;
constexpr int WFLAGS = 48 * KB;
constexpr int CS1 = 192 * KB;
constexpr int CSQ = 520 * KB;
constexpr int WBASE = 704 * KB;
constexpr int WSL = (252 + 1296) * KB;
constexpr int WOB = WBASE + 8 * WSL;
constexpr int ABASE = 14 * 1024 * KB;
constexpr int IMG = 72 * KB;
constexpr int A_X2 = 0, A_O = 96 * KB, A_X1 = A_O + IMG, A_PT = A_X1 + IMG, ALY = REUSE ? 0 : 1024 * KB, ASL = REUSE ? 1024 * KB : 2 * ALY;
constexpr int WS_BYTES = ABASE + 32 * ASL;
static_assert(WOB + 648 * KB <= ABASE && A_PT + 8 * IMG <= 1024 * KB && CSQ + 8 * 14 * 1152 + 256 <= WBASE, "workspace map");
constexpr int L_H = IMG;
constexpr int L_QK = 90 * KB;
constexpr int L_ST = 145 * KB;
constexpr int L_T1 = L_ST + 7680;
constexpr int L_TQ = L_T1 + 4608;
constexpr int L_RT = L_TQ + 896;
constexpr int L_HC = L_RT + 640;
constexpr int LDS_BYTES = L_HC + 64;
enum { C_H0 = 0, C_ST = 3, C_RT = 4, C_LC = 5, C_XA = 6, C_R1 = 10, C_P0 = 11 };

struct Params {
  const unsigned short *x, *wqkv, *wo, *w1, *b1, *w2, *b2;
  unsigned short *out;
  unsigned char *ws;
};

#define STAMP(i)
#define STAMP_INIT()
#define RETRY_COUNT()
#define STAMP_ARG
#define STAMP_PASS
#define STAMP_DUMP()
#define SB() __builtin_amdgcn_sched_barrier(0)
#define BAR() do { asm volatile("s_waitcnt lgkmcnt(0)" ::: "memory"); __builtin_amdgcn_s_barrier(); asm volatile("" ::: "memory"); } while (0)
#define DRAIN() asm volatile("s_waitcnt vmcnt(0)" ::: "memory")
#define SETTLE() DRAIN()
#define OPAQUE(x) asm("" : "+v"(x))
#define KEEP(x) asm volatile("" :: "v"(x))

__device__ __forceinline__ f32x4 mfma(v4u a, v4u b, f32x4 c) {
  return __builtin_amdgcn_mfma_f32_16x16x32_bf16(__builtin_bit_cast(bf16x8, a), __builtin_bit_cast(bf16x8, b), c, 0, 0, 0);
}
#define MF(acc, a, b) asm volatile("v_mfma_f32_16x16x32_bf16 %0, %1, %2, %0" : "+v"(acc) : "v"(a), "v"(b))
#define MF0(acc, a, b) asm volatile("v_mfma_f32_16x16x32_bf16 %0, %1, %2, 0" : "=&v"(acc) : "v"(a), "v"(b))
template <int NT, int MT> __device__ __forceinline__ void acc_fence(f32x4 (&acc)[NT][MT]) {
#pragma unroll
  for (int t = 0; t < NT; ++t)
#pragma unroll
    for (int m = 0; m < MT; ++m) asm volatile("" : "+v"(acc[t][m]));
}
#define ACC_IN(acc) do { acc_fence(acc); asm volatile("s_nop 1"); SB(); } while (0)
#define ACC_OUT(acc) do { SB(); asm volatile("s_nop 7\n\ts_nop 1"); acc_fence(acc); } while (0)
__device__ __forceinline__ unsigned pk(float a, float b) {
  f32x2 v = {a, b};
  return __builtin_bit_cast(unsigned, __builtin_convertvector(v, bf16x2));
}
__device__ __forceinline__ float dot2(unsigned a, unsigned b, float c) {
  return __builtin_amdgcn_fdot2_f32_bf16(__builtin_bit_cast(bf16x2, a), __builtin_bit_cast(bf16x2, b), c, false);
}
constexpr unsigned ONES = 0x3f803f80u;
__device__ __forceinline__ float blo(unsigned u) { return __uint_as_float(u << 16); }
__device__ __forceinline__ float bhi(unsigned u) { return __uint_as_float(u & 0xffff0000u); }
__device__ __forceinline__ v4u ldg(rsrc_t rs, int voff, int soff) { return __builtin_amdgcn_raw_buffer_load_b128(rs, voff, soff, SC1); }
__device__ __forceinline__ void stg(v4u v, rsrc_t rs, int voff, int soff) { __builtin_amdgcn_raw_buffer_store_b128(v, rs, voff + soff, 0, SC1); }
__device__ __forceinline__ v4u ldf(rsrc_t rs, int lane16, int base, int f) { return ldg(rs, lane16 + (f & 3) * KB, base + (f >> 2) * 4 * KB); }
__device__ __forceinline__ int opq_v(int x) { asm volatile("" : "+v"(x)); return x; }
__device__ __forceinline__ int lane_id() { int x; asm volatile("v_mbcnt_lo_u32_b32 %0, -1, 0\n\tv_mbcnt_hi_u32_b32 %0, -1, %0" : "=v"(x)); return x; }
template <unsigned C> __device__ __forceinline__ unsigned vconst() { unsigned x; asm volatile("v_mov_b32 %0, %1" : "=v"(x) : "i"(C)); return x; }
__device__ __forceinline__ int sel(bool c, int a, int b) { return c ? a : b; }
constexpr int OOR = 0x10000000;
__device__ __forceinline__ int opq_s(int x) { asm volatile("" : "+s"(x)); return x; }
#define LOCALS() const int lane = lane_id(), w = opq_s(w0), j = opq_s(j0), g = opq_s(g0); \
  const int lane16 = lane * 16, quad = lane >> 4, l15 = lane & 15, ab = ABASE + j * ASL + layer * ALY, wb = WBASE + g * WSL; \
  (void)lane16; (void)quad; (void)l15; (void)ab; (void)wb
#define OWNROWS(CPW) const int c8 = CPW * w + (lane >> 3); \
  const int piece = ((g >> 1) * 18 + (c8 >> 2)) * KB + ((c8 & 3) * 16 + 8 * (g & 1) + (lane & 7)) * 16; \
  const int mytok = 64 * j + 2 * (16 * ((g >> 1) & 1) + 8 * (g & 1) + (lane & 7)) + (g >> 2)
#define CPF_DONE() KEEP(cpf)
#define GET_TAG() CPF_DONE(); tag = 1 + ((u64)(unsigned)__builtin_amdgcn_readfirstlane((int)(unsigned)tag0) | \
                             ((u64)(unsigned)__builtin_amdgcn_readfirstlane((int)(unsigned)(tag0 >> 32)) << 32))
#define NBASE() opq_s(nb0)
#define QBASE() opq_s(qb0)
#define MBASE() opq_s(mb0)
__device__ __forceinline__ float rows_sum(float x) {
  auto s = __builtin_amdgcn_permlane16_swap(__float_as_uint(x), __float_as_uint(x), false, false);
  x = __uint_as_float(s[0]) + __uint_as_float(s[1]);
  auto t = __builtin_amdgcn_permlane32_swap(__float_as_uint(x), __float_as_uint(x), false, false);
  return __uint_as_float(t[0]) + __uint_as_float(t[1]);
}
__device__ __forceinline__ float vmax(unsigned a, unsigned b) { float r; asm("v_max_f32 %0, %1, %2\n\ts_nop 1" : "=v"(r) : "v"(a), "v"(b)); return r; }
__device__ __forceinline__ void rows_max2(float& a, float& b) {
  auto s = __builtin_amdgcn_permlane16_swap(__float_as_uint(a), __float_as_uint(b), false, false);
  float x = vmax(s[0], s[1]);
  auto t = __builtin_amdgcn_permlane32_swap(__float_as_uint(x), __float_as_uint(x), false, false);
  x = vmax(t[0], t[1]);
  auto u = __builtin_amdgcn_permlane16_swap(__float_as_uint(x), __float_as_uint(x), false, false);
  a = __uint_as_float(u[0]);
  b = __uint_as_float(u[1]);
}
template <int CTRL> __device__ __forceinline__ float dpp(float x) {
  return __int_as_float(__builtin_amdgcn_mov_dpp(__float_as_int(x), CTRL, 0xf, 0xf, true));
}
__device__ __forceinline__ float ror8(float x) { return dpp<0x128>(x); }
__device__ __forceinline__ void add4(f32x4& a, v4u b) {
  float a0 = a[0], a1 = a[1], a2 = a[2], a3 = a[3];
  asm("v_add_f32 %0, %0, %4\n\tv_add_f32 %1, %1, %5\n\tv_add_f32 %2, %2, %6\n\tv_add_f32 %3, %3, %7"
      : "+v"(a0), "+v"(a1), "+v"(a2), "+v"(a3) : "v"(b.x), "v"(b.y), "v"(b.z), "v"(b.w));
  a = f32x4{a0, a1, a2, a3};
}
__device__ __forceinline__ v4u widen(v2u a, v2u b) {
  auto s0 = __builtin_amdgcn_permlane16_swap(a.x, b.x, false, false);
  auto s1 = __builtin_amdgcn_permlane16_swap(a.y, b.y, false, false);
  return v4u{s0[0], s1[0], s0[1], s1[1]};
}
__device__ __forceinline__ v2u gelu4(f32x4 a, float k0, float k1, float is) {
  float s0, s1, s2, s3;
  unsigned u0, u1;
  asm("v_mul_f32 %2, %6, %6\n\tv_mul_f32 %3, %7, %7\n\tv_fma_f32 %2, %2, %11, %10\n\tv_fma_f32 %3, %3, %11, %10\n\t"
      "v_mul_f32 %2, %2, %6\n\tv_mul_f32 %3, %3, %7\n\tv_exp_f32 %2, %2\n\tv_mul_f32 %4, %8, %8\n\t"
      "v_exp_f32 %3, %3\n\tv_mul_f32 %5, %9, %9\n\tv_fma_f32 %4, %4, %11, %10\n\tv_fma_f32 %5, %5, %11, %10\n\t"
      "v_mul_f32 %4, %4, %8\n\tv_mul_f32 %5, %5, %9\n\tv_exp_f32 %4, %4\n\tv_fma_f32 %2, %2, %12, %12\n\t"
      "v_exp_f32 %5, %5\n\tv_fma_f32 %3, %3, %12, %12\n\tv_rcp_f32 %2, %2\n\tv_fma_f32 %4, %4, %12, %12\n\t"
      "v_rcp_f32 %3, %3\n\tv_fma_f32 %5, %5, %12, %12\n\tv_rcp_f32 %4, %4\n\tv_mul_f32 %2, %2, %6\n\t"
      "v_rcp_f32 %5, %5\n\tv_mul_f32 %3, %3, %7\n\tv_mul_f32 %4, %4, %8\n\tv_cvt_pk_bf16_f32 %0, %2, %3\n\t"
      "v_mul_f32 %5, %5, %9\n\tv_cvt_pk_bf16_f32 %1, %4, %5"
      : "=&v"(u0), "=&v"(u1), "=&v"(s0), "=&v"(s1), "=&v"(s2), "=&v"(s3)
      : "v"(a[0]), "v"(a[1]), "v"(a[2]), "v"(a[3]), "v"(k0), "v"(k1), "v"(is));
  return v2u{u0, u1};
}
__device__ __forceinline__ void shift4(f32x4& a, f32x4 cs, f32x4 b, float nmean, float is) {
  float a0 = a[0], a1 = a[1], a2 = a[2], a3 = a[3];
  asm("v_fmac_f32 %0, %12, %4\n\tv_fmac_f32 %1, %12, %5\n\tv_fmac_f32 %2, %12, %6\n\tv_fmac_f32 %3, %12, %7\n\t"
      "v_fmac_f32 %0, %13, %8\n\tv_fmac_f32 %1, %13, %9\n\tv_fmac_f32 %2, %13, %10\n\tv_fmac_f32 %3, %13, %11"
      : "+v"(a0), "+v"(a1), "+v"(a2), "+v"(a3)
      : "v"(cs[0]), "v"(cs[1]), "v"(cs[2]), "v"(cs[3]), "v"(b[0]), "v"(b[1]), "v"(b[2]), "v"(b[3]), "v"(nmean), "v"(is));
  a = f32x4{a0, a1, a2, a3};
}
__device__ __forceinline__ int cnt_get(LAS unsigned char* L, int i) { return ((volatile LAS int*)(L + L_HC))[i]; }
__device__ __forceinline__ void cnt_wait(LAS unsigned char* L, int i, int n) {
  while (__builtin_amdgcn_readfirstlane(cnt_get(L, i)) < n) __builtin_amdgcn_s_sleep(1);
  asm volatile("" ::: "memory");
}
__device__ __forceinline__ void cnt_add(LAS unsigned char* L, int i, int lane) {
  asm volatile("s_waitcnt lgkmcnt(0)" ::: "memory");
  if (lane == 0) __hip_atomic_fetch_add((LAS int*)(L + L_HC) + i, 1, __ATOMIC_RELAXED, __HIP_MEMORY_SCOPE_WORKGROUP);
}
template <int NP> __device__ __forceinline__ void row_table1(LAS unsigned char* L, int t5, int lane) {
  const int r = t5 * 16 + (lane & 15), q = lane >> 4;
  float t1 = 0.f, t2 = 0.f;
#pragma unroll
  for (int i = 0; i < (NP + 3) / 4; ++i)
    if (NP % 4 == 0 || q < NP) { const f32x2 v = *(LAS f32x2*)(L + L_ST + ((i * 4 + q) * 80 + r) * 8); t1 += v.x; OPAQUE(t1); t2 += v.y; }
  t1 = rows_sum(t1); OPAQUE(t1); t2 = rows_sum(t2);
  const float nmean = t1 * (-1.0f / 576.0f);
  const float var = fmaxf(__builtin_fmaf(-nmean, nmean, t2 * (1.0f / 576.0f)), 0.f);
  const float rstd = __builtin_amdgcn_rsqf(var + EPS);
  if (q < 2) *(LAS float*)(L + L_RT + q * 320 + r * 4) = q ? nmean * rstd : rstd;
  cnt_add(L, C_R1, lane);
}
__device__ __forceinline__ v2u ln_pack(f32x4 a, f32x4 r, f32x4 c, f32x4 nm) {
  float t0, t1, t2, t3;
  unsigned u0, u1;
  asm("v_mul_f32 %2, %14, %18\n\tv_mul_f32 %3, %15, %19\n\tv_mul_f32 %4, %16, %20\n\tv_mul_f32 %5, %17, %21\n\t"
      "v_fmac_f32 %2, %6, %10\n\tv_fmac_f32 %3, %7, %11\n\tv_fmac_f32 %4, %8, %12\n\tv_fmac_f32 %5, %9, %13\n\t"
      "v_cvt_pk_bf16_f32 %0, %2, %3\n\tv_cvt_pk_bf16_f32 %1, %4, %5"
      : "=&v"(u0), "=&v"(u1), "=&v"(t0), "=&v"(t1), "=&v"(t2), "=&v"(t3)
      : "v"(a[0]), "v"(a[1]), "v"(a[2]), "v"(a[3]), "v"(r[0]), "v"(r[1]), "v"(r[2]), "v"(r[3]), "v"(c[0]), "v"(c[1]), "v"(c[2]), "v"(c[3]),
        "v"(nm[0]), "v"(nm[1]), "v"(nm[2]), "v"(nm[3]));
  return v2u{u0, u1};
}
__device__ __forceinline__ f32x4 zero4() {
  float a, b, c, d;
  asm volatile("v_mov_b32 %0, 0\n\tv_mov_b32 %1, 0\n\tv_mov_b32 %2, 0\n\tv_mov_b32 %3, 0" : "=v"(a), "=v"(b), "=v"(c), "=v"(d));
  return f32x4{a, b, c, d};
}
__device__ __forceinline__ f32x4 splat(float x) { return f32x4{x, x, x, x}; }
__device__ __forceinline__ void row_table(LAS unsigned char* L, int lane) {
  cnt_wait(L, C_ST, 8);
  const f32x4 q = *(LAS f32x4*)(L + L_ST + lane * 16);
  float t1 = q[0] + q[2];
  OPAQUE(t1);
  float nmean = t1 * (-1.0f / 576.0f);
  const float var = fmaxf(__builtin_fmaf(-nmean, nmean, (q[1] + q[3]) * (1.0f / 576.0f)), 0.f);
  const float r1 = __builtin_amdgcn_rsqf(var + EPS);
  const float sc = r1 * __builtin_amdgcn_rsqf(var * r1 * r1 + EPS);
  float k0 = GC0 * sc;
  OPAQUE(k0);
  *(LAS f32x4*)(L + L_ST + KB + lane * 16) = f32x4{nmean, __builtin_amdgcn_rcpf(sc), k0, k0 * (0.044715f * sc * sc)};
  cnt_add(L, C_RT, lane);
}
__device__ __forceinline__ void shift_acc(f32x4 (&acc)[3][4], LAS unsigned char* L, int w, int quad, int l15) {
  f32x2 rt[4];
#pragma unroll
  for (int m = 0; m < 4; ++m) rt[m] = *(LAS f32x2*)(L + L_ST + KB + (m * 16 + l15) * 16);
#pragma unroll
  for (int t = 0; t < 3; ++t) {
    const f32x4 cs = *(LAS f32x4*)(L + L_T1 + (48 * w + 16 * t + 4 * quad) * 4), bb = *(LAS f32x4*)(L + L_T1 + 2304 + (48 * w + 16 * t + 4 * quad) * 4);
#pragma unroll
    for (int m = 0; m < 4; ++m) shift4(acc[t][m], cs, bb, rt[m][0], rt[m][1]);
  }
}
template <int SLEEP = 1> __device__ __forceinline__ void wait_flags(const unsigned char* ws, int off, bool active, u64 tag) {
  const GAS u64* f = (const GAS u64*)(ws + off);
  asm volatile("" : "+s"(tag));
#pragma unroll 1
  for (int spins = 0; spins < (1 << 16); ++spins) {
    const u64 v = active ? __hip_atomic_load(f, __ATOMIC_RELAXED, __HIP_MEMORY_SCOPE_AGENT) : tag;
    if (__all(v == tag)) break;
    __builtin_amdgcn_s_sleep(SLEEP);
  }
  asm volatile("" ::: "memory");
}
__device__ __forceinline__ void set_flag(unsigned char* ws, int off, u64 tag) {
  asm volatile("" : "+s"(tag));
  __hip_atomic_store((GAS u64*)(ws + off), tag, __ATOMIC_RELAXED, __HIP_MEMORY_SCOPE_AGENT);
}
constexpr unsigned POISON = 0xffffffffu;
__device__ __forceinline__ unsigned umax4(unsigned m, v4u v) { return max(max(max(m, v.x), v.y), max(v.z, v.w)); }
#define RETRY_BEGIN() _Pragma("unroll 1") for (int spins_ = 0; spins_ < (1 << 15); ++spins_) { unsigned seen_ = 0
#define RETRY_END() if (!__any(seen_ == POISON)) break; RETRY_COUNT(); __builtin_amdgcn_s_sleep(2); asm volatile("" ::: "memory"); }
template <int LO, int HI> __device__ __forceinline__ void issue_window(v4u (&win)[WIN], rsrc_t rs, int lane16, int base) {
#pragma unroll
  for (int i = LO; i < HI; ++i) win[i] = ldf(rs, lane16, base, i);
  SB();
}

template <int NT, int MT, int SWAP, int NNEXT, int R, int LO, int HI>
__device__ __forceinline__ void gemm18_steps(f32x4 (&acc)[NT][MT], v4u (&win)[WIN], v4u (&act)[R], LAS unsigned char* img, LAS unsigned char* img2, rsrc_t rs,
                                             int lane16, int wbase, int nvo, int nbase) {
#pragma unroll
  for (int n = LO; n < HI; ++n) {
    const int s = n / MT, m = n % MT, nn = n + R - 1;
    if (n < 18 * MT) {
#pragma unroll
      for (int t = 0; t < NT; ++t) {
        if (s == 0) {
          if ((SWAP >> t) & 1) MF0(acc[t][m], act[n % R], win[t]);
          else MF0(acc[t][m], win[t], act[n % R]);
        } else if ((SWAP >> t) & 1) MF(acc[t][m], act[n % R], win[(s * NT + t) % WIN]);
        else MF(acc[t][m], win[(s * NT + t) % WIN], act[n % R]);
      }
      SB();
    }
    if (nn < 18 * MT) act[nn % R] = *(LAS v4u*)(nn % MT < 3 ? img + ((nn % MT) * 18 + nn / MT) * KB : img2 + ((nn % MT - 3) * 18 + nn / MT) * KB);
    if (m == 0 && s > 0) {
#pragma unroll
      for (int t = 0; t < NT; ++t) {
        const int f = (s - 1) * NT + t;
        if (f + WIN < 18 * NT) win[f % WIN] = ldf(rs, lane16, wbase, f + WIN);
        else if (f % WIN < NNEXT) win[f % WIN] = ldf(rs, nvo, nbase, f % WIN);
      }
    }
    SB();
    if (n < 18 * MT) {
      KEEP(act[n % R]);
#pragma unroll
      for (int t = 0; t < NT; ++t) KEEP(win[(s * NT + t) % WIN]);
      SB();
    }
  }
}
template <int NT, int MT, int SWAP, int NNEXT, int R>
__device__ __forceinline__ void gemm18(f32x4 (&acc)[NT][MT], v4u (&win)[WIN], LAS unsigned char* img, rsrc_t rs, int lane16, int wbase, int nbase) {
  v4u act[R];
  LAS unsigned char* img2 = img + 54 * KB;
  if (MT > 3) asm volatile("" : "+v"(img2));
#pragma unroll
  for (int n = 0; n < R - 1; ++n) { act[n] = *(LAS v4u*)(n % MT < 3 ? img + ((n % MT) * 18 + n / MT) * KB : img2 + ((n % MT - 3) * 18 + n / MT) * KB); SB(); }
  gemm18_steps<NT, MT, SWAP, NNEXT, R, 0, 6 * MT>(acc, win, act, img, img2, rs, lane16, wbase, lane16, nbase);
  gemm18_steps<NT, MT, SWAP, NNEXT, R, 6 * MT, 12 * MT>(acc, win, act, img, img2, rs, lane16, wbase, lane16, nbase);
  gemm18_steps<NT, MT, SWAP, NNEXT, R, 12 * MT, 18 * MT + 1>(acc, win, act, img, img2, rs, lane16, wbase, lane16, nbase);
  ACC_OUT(acc);
}
template <int MT, int SWAP, int NNEXT, int R>
__device__ __forceinline__ void gemm18x2(f32x4 (&acc)[2][MT], v4u (&win)[WIN], LAS unsigned char* img, rsrc_t rs, int lane16, int wbase, int nbase) {
  static_assert(9 * MT % R == 0, "the ring closes over one window");
  v4u act[R];
  LAS unsigned char* img2 = img + 54 * KB;
  asm volatile("" : "+v"(img2));
#pragma unroll
  for (int n = 0; n < R - 1; ++n) { act[n] = *(LAS v4u*)(n % MT < 3 ? img + ((n % MT) * 18 + n / MT) * KB : img2 + ((n % MT - 3) * 18 + n / MT) * KB); SB(); }
#pragma unroll
  for (int t = 0; t < 2; ++t)
#pragma unroll
    for (int m = 0; m < MT; ++m) acc[t][m] = zero4();
  ACC_IN(acc);
  int so = wbase + 18 * KB, vo = lane16;
#pragma unroll 1
  for (int h = 0; h < 2; ++h) {
#pragma unroll
    for (int n = 0; n < 9 * MT; ++n) {
      const int s = n / MT, m = n % MT, nn = n + R - 1;
#pragma unroll
      for (int t = 0; t < 2; ++t) {
        if ((SWAP >> t) & 1) MF(acc[t][m], act[n % R], win[s * 2 + t]);
        else MF(acc[t][m], win[s * 2 + t], act[n % R]);
      }
      SB();
      act[nn % R] = *(LAS v4u*)(nn % MT < 3 ? img + ((nn % MT) * 18 + nn / MT) * KB : img2 + ((nn % MT - 3) * 18 + nn / MT) * KB);
      if (m < 2 && s > 0) win[s * 2 - 2 + m] = ldf(rs, vo, so, s * 2 - 2 + m);
      SB();
      KEEP(act[n % R]);
      SB();
    }
    win[16] = ldf(rs, vo, so, 16);
    win[17] = ldf(rs, vo, so, 17);
    img += 9 * KB;
    img2 += 9 * KB;
    so = nbase;
    if (NNEXT == 0) vo = lane16 + OOR;
  }
  ACC_OUT(acc);
}
template <int NT, int MT, int SWAP, int R, int tstep, int NNEXT>
__device__ __forceinline__ void qkv_tiles(v4u (&win)[WIN], LAS unsigned char* L, rsrc_t rs, int lane, int wbase, int nbase, int tile0, int rwait STAMP_ARG) {
  f32x4 acc[NT][MT];
  if constexpr (NT == 2 && ROLL) gemm18x2<MT, SWAP, NNEXT, MT == 5 ? R5 : 6>(acc, win, L + lane * 16, rs, lane * 16, wbase, nbase);
  else gemm18<NT, MT, SWAP, NNEXT, R>(acc, win, L + lane * 16, rs, lane * 16, wbase, nbase);
  STAMP(38);
  LAS unsigned char *Rq = L + L_RT + (lane & 48), *Rl = L + L_RT + (lane & 15) * 4, *Tq = L + L_TQ + tile0 * 64 + (lane & 48),
                    *Tl = L + L_TQ + tile0 * 64 + (lane & 15) * 4, *X = L + L_QK + lane * 8 + tile0 * 2560;
  asm volatile("" : "+v"(Rq), "+v"(Rl), "+v"(Tq), "+v"(Tl), "+v"(X));
  cnt_wait(L, C_R1, rwait);
  v2u u[NT][MT];
#pragma unroll
  for (int t = 0; t < NT; ++t) {
    if ((SWAP >> t) & 1) {
      const f32x4 cs = splat(*(LAS float*)(Tl + t * tstep * 64));
#pragma unroll
      for (int m = 0; m < MT; ++m) u[t][m] = ln_pack(acc[t][m], *(LAS f32x4*)(Rq + m * 64), cs, *(LAS f32x4*)(Rq + 320 + m * 64));
    } else {
      const f32x4 cs = *(LAS f32x4*)(Tq + t * tstep * 64);
#pragma unroll
      for (int m = 0; m < MT; ++m) u[t][m] = ln_pack(acc[t][m], splat(*(LAS float*)(Rl + m * 64)), cs, splat(*(LAS float*)(Rl + 320 + m * 64)));
    }
  }
#pragma unroll
  for (int t = 0; t < NT; ++t)
#pragma unroll
    for (int m = 0; m < MT; ++m) *(LAS v2u*)(X + (t * tstep * 5 + m) * 512) = u[t][m];
}
template <int C>
__device__ __forceinline__ void mlp_chunk(f32x4 (&acc)[3][4], v4u (&win)[WIN], v4u (&act)[8], LAS unsigned char* img, rsrc_t rs, int lane16, int rb,
                                          LAS unsigned char* L) {
  int hv = 4;
#pragma unroll
  for (int s = 0; s < 6; ++s) {
    if (C != 2 && s == 5) {
      if (__builtin_amdgcn_readfirstlane(hv) < 4) cnt_wait(L, C < 0 ? C_XA + 1 : C_H0 + C + 1, 4);
      asm volatile("" ::: "memory");
      SB();
    }
    if (C == 2 && s == 6 - DST) {
      if (lane16 == 0) __hip_atomic_fetch_add((LAS int*)(L + L_HC) + C_LC, 1, __ATOMIC_RELAXED, __HIP_MEMORY_SCOPE_WORKGROUP);
      SB();
    }
    asm volatile("" :: "v"(act[(s & 1) * 4]), "v"(act[(s & 1) * 4 + 1]), "v"(act[(s & 1) * 4 + 2]), "v"(act[(s & 1) * 4 + 3]), "v"(win[s * 3]),
                 "v"(win[s * 3 + 1]), "v"(win[s * 3 + 2]));
    SB();
#pragma unroll
    for (int t = 0; t < 3; ++t)
#pragma unroll
      for (int m = 0; m < 4; ++m) {
        const int mm = t == 1 ? 3 - m : m;
        if (C == 0 && s == 0) MF0(acc[t][mm], win[t], act[mm]);
        else MF(acc[t][mm], win[s * 3 + t], act[(s & 1) * 4 + mm]);
        SB();
        if (t == 0 && !(C == 2 && s == 5)) act[((s + 1) & 1) * 4 + m] = *(LAS v4u*)(img + (m * 18 + s + 1) * KB);
        if (C != 2 && s == 3 && t == 2 && m == 0) hv = cnt_get(L, C < 0 ? C_XA + 1 : C_H0 + C + 1);
        if (!ABL_LD && t == 1 && m < 3 && !(C == 2 && s + DST >= 6) && !(C == -2 && s + DST < PRE))
          win[((s + DST) % 6) * 3 + m] = ldf(rs, lane16, rb, (s + DST) * 3 + m);
        SB();
      }
  }
}
__device__ __forceinline__ void gemm_up(f32x4 (&acc)[3][4], v4u (&win)[WIN], LAS unsigned char* img, rsrc_t rs, int lane16, int rb, LAS unsigned char* L) {
  v4u act[8];
  cnt_wait(L, C_XA, 4);
#pragma unroll
  for (int n = 0; n < 4; ++n) { act[n] = *(LAS v4u*)(img + n * 18 * KB); SB(); }
  ACC_IN(acc);
  if (DST < PRE) {
    mlp_chunk<-2>(acc, win, act, img, rs, lane16, rb, L);
    img += 6 * KB;
    asm volatile("" : "+v"(img));
    rb += 18 * KB;
  }
#pragma unroll 1
  for (int c = DST < PRE; c < 3; ++c) {
    mlp_chunk<-1>(acc, win, act, img, rs, lane16, rb, L);
    img += 6 * KB;
    rb += 18 * KB;
  }
  ACC_OUT(acc);
}
__device__ __forceinline__ void gemm_down(f32x4 (&acc)[3][4], v4u (&win)[WIN], LAS unsigned char* img, rsrc_t rs, int lane16, int rb, LAS unsigned char* L STAMP_ARG) {
  v4u act[8];
  cnt_wait(L, C_H0, 4);
  STAMP(46);
#pragma unroll
  for (int n = 0; n < 4; ++n) { act[n] = *(LAS v4u*)(img + n * 18 * KB); SB(); }
  mlp_chunk<0>(acc, win, act, img, rs, lane16, rb, L);
  img += 6 * KB;
  asm volatile("" : "+v"(img));
  STAMP(47);
  mlp_chunk<1>(acc, win, act, img, rs, lane16, rb + 18 * KB, L);
  img += 6 * KB;
  asm volatile("" : "+v"(img));
  STAMP(48);
  mlp_chunk<2>(acc, win, act, img, rs, lane16, rb + 36 * KB, L);
  ACC_OUT(acc);
}

__global__ void __launch_bounds__(NTHR) fused_fwd(Params p) {
  extern __shared__ __attribute__((aligned(16))) unsigned char smem[];
  LAS unsigned char* L = (LAS unsigned char*)smem;
  const int w0 = __builtin_amdgcn_readfirstlane(threadIdx.x >> 6);
  const int bid = blockIdx.x;
  u64 pc0;
  asm volatile("s_getpc_b64 %0" : "=s"(pc0));
  unsigned cpf = 0;
  if (threadIdx.x < CODEPF * 16) cpf = *(const GAS unsigned*)(pc0 + threadIdx.x * 64);
  constexpr unsigned MPX = 8 / XB;
  const int j0 = ((blockIdx.x & 7) / XB) * (32 / MPX) + (blockIdx.x >> 3) / MPX;
  const int g0 = ((blockIdx.x & 7) % XB) * MPX + (blockIdx.x >> 3) % MPX;
  const rsrc_t rs = __builtin_amdgcn_make_buffer_rsrc((void*)p.ws, 0, WS_BYTES, 0x00020000);
  const int mb0 = WBASE + g0 * WSL + (252 + w0 * 108) * KB;
  const int nb0 = sel(w0 < 9, WOB + ((g0 & 3) * 9 + w0) * 18 * KB, mb0);
  const int qb0 = sel(w0 < 8, WBASE + g0 * WSL + sel(w0 < 6, w0 * 36, 108 + w0 * 18) * KB, nb0);
  STAMP_INIT();
  STAMP(0);
  const u64 tag0 = __hip_atomic_load((const GAS u64*)p.ws, __ATOMIC_RELAXED, __HIP_MEMORY_SCOPE_AGENT);
  u64 tag = 0;
  v4u win[WIN];
  {
    constexpr int layer = 0;
    LOCALS();
    const rsrc_t rx = __builtin_amdgcn_make_buffer_rsrc((void*)p.x, 0, 2048 * 576 * 2, 0x00020000);
    const int xrow = lane >> 2;
#define LOAD_X() v4u xr[9]; _Pragma("unroll") for (int i = 0; i < 9; ++i) xr[i] = __builtin_amdgcn_raw_buffer_load_b128(rx, xoff + i * 64, 0, 0); SB()
#define STAGE_X() do { float s1 = 0.f, s2 = 0.f; \
      _Pragma("unroll") for (int i = 0; i < 9; ++i) { \
        if (w < 10) *(LAS v4u*)(L + (w * 9 + i) * KB + (lane & 3) * 256 + xrow * 16) = xr[i]; \
        _Pragma("unroll") for (int e = 0; e < 4; ++e) { s1 = dot2(xr[i][e], ONES, s1); s2 = dot2(xr[i][e], xr[i][e], s2); } } \
      s1 += dpp<0xB1>(s1); s2 += dpp<0xB1>(s2); s1 += dpp<0x4E>(s1); s2 += dpp<0x4E>(s2); \
      if (w < 10 && (lane & 3) == 0) *(LAS f32x2*)(L + L_ST + ((w & 1) * 80 + (w >> 1) * 16 + xrow) * 8) = f32x2{s1, s2}; } while (0)
    if (opq_s(w) < 8) {
      LAS unsigned char* T = L + L_QK + w * 3072;
      const int uf = 8 * j + w, uc = sel(uf < 252, uf, 0), us = (uc * 293) >> 12, tp = uc - us * 14;
      const int tile = sel(tp < 4, 10 + tp, sel(tp == 4, 8, sel(tp == 13, 9, tp - 5)));
      const bool vx = tile == 9, ex = (tile & 14) == 8;
      const int c0 = 72 * g + sel(ex, 16, ((tile + sel(tile >= 10, 2, 0)) & 3) * 18) + sel(tile < 4, 576, sel(tile < 8 || vx, 1152, 0));
      const rsrc_t rq = __builtin_amdgcn_make_buffer_rsrc((void*)p.wqkv, 0, 576 * 1728 * 2, 0x00020000);
      const int qo = (lane >> 1) * 3456 + (lane & 1) * sel(ex, sel(vx, 0, 72), 16), qs = us * 32 * 3456 + c0 * 2;
      v4u qv;
      if (ex) {
        const int cA = sel(vx, 36, 1152), cB = sel(vx, 72, 36);
        qv = v4u{__builtin_amdgcn_raw_buffer_load_b32(rq, qo, qs, 0), __builtin_amdgcn_raw_buffer_load_b32(rq, qo, qs + cA, 0),
                 __builtin_amdgcn_raw_buffer_load_b32(rq, qo, qs + cB, 0), __builtin_amdgcn_raw_buffer_load_b32(rq, qo, qs + cA + cB, 0)};
      } else qv = __builtin_amdgcn_raw_buffer_load_b128(rq, qo, qs, 0);
      SB();
      const int of = 3 * j + w, oc = (g >> 2) * 81 + of, os = (oc * 57) >> 9, ot = oc - os * 9;
      const bool hasO = w < 3 && of < 81;
      const bool ox = os >= 16;
      const int orow = quad * sel(ox, 72, 4) + ((lane >> 1) & 1) + ((lane >> 2) & 1) * sel(ox, 18, 2) + ((lane >> 3) & 1) * sel(ox, 36, 18);
      const rsrc_t ro = __builtin_amdgcn_make_buffer_rsrc((void*)p.wo, 0, 576 * 576 * 2, 0x00020000);
      const v4u ov = __builtin_amdgcn_raw_buffer_load_b128(ro, orow * 1152 + (lane & 1) * 16 + sel(hasO, 0, OOR),
                                                           sel(ox, 288 * (os - 16) + 16, 36 * os) * 1152 + 32 * ((g & 3) * 9 + ot), 0);
      SB();
      const int xoff = (64 * j + 32 * ((w >> 1) & 1) + (w >> 2) + 2 * xrow) * 1152 + (w & 1) * 576 + (lane & 3) * 16;
      __builtin_amdgcn_s_barrier();
      LOAD_X();
      STAMP(41);
      *(LAS v4u*)(T + (lane >> 1) * 48 + (lane & 1) * 16) = (vx && (lane & 1)) ? v4u{0u, 0u, 0u, 0u} : qv;
      *(LAS v4u*)(T + 1536 + (lane >> 1) * 48 + (lane & 1) * 16) = ov;
      LAS unsigned char* R = T + (8 * quad + (l15 >> 2)) * 48 + (lane & 3) * 8;
      const v2u a = __builtin_bit_cast(v2u, __builtin_amdgcn_ds_read_tr16_b64_v4i16((LAS s16x4*)(R)));
      const v2u b = __builtin_bit_cast(v2u, __builtin_amdgcn_ds_read_tr16_b64_v4i16((LAS s16x4*)(R + 4 * 48)));
      const v2u c = __builtin_bit_cast(v2u, __builtin_amdgcn_ds_read_tr16_b64_v4i16((LAS s16x4*)(R + 1536)));
      const v2u d = __builtin_bit_cast(v2u, __builtin_amdgcn_ds_read_tr16_b64_v4i16((LAS s16x4*)(R + 1536 + 4 * 48)));
      stg(v4u{a.x, a.y, b.x, b.y}, rs, lane16,
          sel(uf >= 252, OOR, wb + sel(tile < 8, (tile & 3) * 36 + us * 2 + (tile >> 2), sel(tile < 10, 72 + tile * 18 + us, (tile >> 1) * 36 - 36 + us * 2 + (tile & 1))) * KB));
      stg(v4u{c.x, c.y, d.x, d.y}, rs, lane16, sel(hasO, WOB + ((g & 3) * 162 + ot * 18 + os) * KB, OOR));
      const float cq = rows_sum(dot2(a.x, ONES, dot2(a.y, ONES, dot2(b.x, ONES, dot2(b.y, ONES, 0.f)))));
      __builtin_amdgcn_raw_buffer_store_b32(__float_as_uint(cq), rs, sel(lane < 16, lane * 4, OOR) + sel(uf < 252, CSQ + ((g * 18 + us) * 14 + tile) * 64, OOR), 0, SC1);
      SB();
      STAMP(43);
      DRAIN();
      STAMP(44);
      GET_TAG();
      if (lane == 0 && __hip_atomic_fetch_add((LAS int*)(L + L_HC) + C_P0, 1, __ATOMIC_RELAXED, __HIP_MEMORY_SCOPE_WORKGROUP) == 7)
        set_flag(p.ws, SFLAGS + (g * 32 + j) * SFS, tag);
      STAGE_X();
      STAMP(2);
      BAR();
      STAMP(3);
      issue_window<0, WIN>(win, rs, lane16, QBASE());
    }
    if (opq_s(w) >= 8) {
#define FILL0() _Pragma("unroll") for (int i = 0; i < 5; ++i) { const int n = (w - 8) * 5 + i; \
        stg(v4u{POISON, POISON, POISON, POISON}, rs, lane16, sel(n < 18, ab + sel(n < 9, A_O, A_X1 - 9 * KB) + (g * 9 + n) * KB, OOR)); }
      if (PZ0 == 0) { FILL0(); SB(); }
      LAS unsigned char* T = L + 125 * KB + (w - 8) * 4608;
      const int item = 4 * j + w - 8, rbk = (item * 57) >> 9, cb = item - rbk * 9, kind = sel(rbk >= 6, 1, 0), k3 = rbk - kind * 6;
      const int rn = sel(kind, 4, 8), ld = sel(kind, 1152, 9216);
      const rsrc_t rw = __builtin_amdgcn_make_buffer_rsrc((void*)(kind == 0 ? p.w1 : p.w2), 0, 576 * 4608 * 2, 0x00020000);
      const int jj = lane >> 3;
      const int woff = (jj & 3) * ld + (lane & 7) * 16 + sel(item < 108, 96 * k3 * ld + g * sel(kind, 576 * 1152, 1152) + 128 * cb, OOR);
      const int xoff = (sel(j > 0, 64 * j - 16, 0) + 2 * (xrow & 7) + (xrow >> 3)) * 1152 + (lane & 3) * 16 + sel(w < 10, (w & 1) * 576, OOR);
      if (w == 11 && lane < 16) ((volatile LAS int*)(L + L_HC))[lane] = 0;
      BAR();
      LOAD_X();
      v4u wv[3][4];
#pragma unroll
      for (int n = 0; n < 4; ++n)
#pragma unroll
        for (int c = 0; c < 3; ++c)
          wv[c][n] = __builtin_amdgcn_raw_buffer_load_b128(rw, woff + (jj >> 2) * (sel(kind, c < 2 ? 16 : 48, 4) * ld),
                                                           (n * rn + sel(kind, c < 2 ? 48 * c : 32, 32 * c)) * ld, NTW ? 2 : 0);
      SB();
      STAMP(41);
      GET_TAG();
      STAGE_X();
      float cs[4] = {0.f, 0.f, 0.f, 0.f};
      LAS unsigned char* R = T + (8 * quad + (l15 >> 2)) * 144 + (lane & 3) * 8;
#pragma unroll
      for (int c = 0; c < 3; ++c) {
#pragma unroll
        for (int n = 0; n < 4; ++n) *(LAS v4u*)(T + (n * 8 + (lane >> 3)) * 144 + (lane & 7) * 16) = wv[c][n];
#pragma unroll
        for (int tt = 0; tt < 4; ++tt) {
          const v2u a = __builtin_bit_cast(v2u, __builtin_amdgcn_ds_read_tr16_b64_v4i16((LAS s16x4*)(R + tt * 32)));
          const v2u b = __builtin_bit_cast(v2u, __builtin_amdgcn_ds_read_tr16_b64_v4i16((LAS s16x4*)(R + tt * 32 + 4 * 144)));
          wv[c][tt] = v4u{a.x, a.y, b.x, b.y};
          cs[tt] = dot2(a.x, ONES, dot2(a.y, ONES, dot2(b.x, ONES, dot2(b.y, ONES, cs[tt]))));
        }
      }
#pragma unroll
      for (int tt = 0; tt < 4; ++tt) { cs[tt] = rows_sum(cs[tt]); OPAQUE(cs[tt]); }
      float csum = quad == 0 ? cs[0] : quad == 1 ? cs[1] : quad == 2 ? cs[2] : cs[3];
      asm volatile("" : "+v"(csum));
      if (opq_s(w) == 11) {
        cnt_wait(L, C_P0, 8);
        wait_flags<PSLP>(p.ws, SFLAGS + ((g ^ ((lane >> 3) & 4)) * 32 + (lane & 31)) * SFS, true, tag);
#pragma unroll
        for (int i = 0; i < 18; ++i) win[i] = ldg(rs, lane16, CSQ + (g * 18 + i) * 896);
      }
      STAMP(2);
      BAR();
      STAMP(3);
      row_table1<2>(L, w - 8, lane);
      if (opq_s(w) == 11) {
        f32x4 cs = __builtin_bit_cast(f32x4, win[0]);
#pragma unroll
        for (int i = 1; i < 18; ++i) add4(cs, win[i]);
        if (lane < 56) *(LAS f32x4*)(L + L_TQ + lane16) = cs;
        row_table1<2>(L, 4, lane);
      }
      __builtin_amdgcn_s_sleep(BDELAY);
      STAMP(45);
      const int dst0 = sel(item < 108, wb + (252 + 54 * kind + 9 * k3) * KB, OOR);
#pragma unroll
      for (int tt = 0; tt < 4; ++tt)
#pragma unroll
        for (int c = 0; c < 3; ++c) stg(wv[c][tt], rs, lane16 + c * 3 * KB, dst0 + ((((4 * cb + tt) * 43) >> 7) * 105 + 4 * cb + tt) * KB);
      __builtin_amdgcn_raw_buffer_store_b32(__float_as_uint(csum), rs,
                                            lane * 4 + sel(kind == 0, CS1 + (k3 * 4608 + 576 * g + 64 * cb) * 4, OOR), 0, SC1);
      if (PZ0 == 1) { FILL0(); }
      SB();
      if (opq_s(w) == 8) issue_window<0, WIN>(win, rs, lane16, QBASE());
    }
  }
  int os0[3];
  const int wq = opq_s(w0);
  {
    const int T = 2 * (g0 >> 2) + sel(wq >= 6, 1, 0), s0 = 3 * sel(wq >= 6, wq - 6, wq);
#pragma unroll
    for (int i = 0; i < 3; ++i) {
      const int s = s0 + i;
      os0[i] = ABASE + j0 * ASL + A_O + sel(s < 16, ((s >> 1) * 9 + T * 2 + (s & 1)) * KB, ((s - 16) * 36 + 8) * KB + T * 256);
    }
  }
  const int oq0 = sel(wq == 5 || wq == 11, 9 * KB, 256);
  const int ft0 = (g0 & 3) * 9 + sel(wq < 9, wq, 0), xs0 = ((g0 >> 2) * 36 + (ft0 >> 1)) * KB + (ft0 & 1) * 512;

#pragma unroll 1
  for (int layer = 0; layer < 2; ++layer) {
    v4u xres;
    {
      LOCALS();
      STAMP(4 + layer * 16);
      {
        const int qbase = QBASE(), nbase = NBASE(), rw = 5 + 5 * layer;
        if (opq_s(w) < 4) qkv_tiles<2, 5, 2, R2, 4, WIN>(win, L, rs, lane, qbase, nbase, w, rw STAMP_PASS);
        if ((opq_s(w) & 14) == 4) qkv_tiles<2, 4, 0, R2, 1, N45>(win, L, rs, lane, qbase, nbase, 2 * w + 2, rw STAMP_PASS);
        if (opq_s(w) == 6) qkv_tiles<1, 5, 0, R1, 0, N67>(win, L, rs, lane, qbase, nbase, 8, rw STAMP_PASS);
        if (opq_s(w) == 7) qkv_tiles<1, 5, 1, R1, 0, N67>(win, L, rs, lane, qbase, nbase, 9, rw STAMP_PASS);
      }
      if (layer == 1 && bid == 0 && w == 10) {
#pragma unroll 1
        for (int i = 0; i < 4; ++i) wait_flags(p.ws, SFLAGS + (i * 64 + lane) * SFS, true, tag);
        if (lane == 0) set_flag(p.ws, 0, tag);
      }
      STAMP(5 + layer * 16);
      xres = *(LAS v4u*)(L + opq_s(xs0) + (quad & 1) * 18 * KB + (quad >> 1) * 256 + l15 * 16);
      if (layer == 0 && w >= 8) DRAIN();
      if ((opq_s(w) & 12) == 4) {
        BAR();
        if (w == 7 && (EFL || layer == 0)) {
          if (lane == 0) set_flag(p.ws, FLAGS + 2 * layer * 4096 + j * 128 + g * 8, tag);
          wait_flags(p.ws, FLAGS + 2 * layer * 4096 + j * 128 + (lane & 7) * 8, lane < 8, tag);
          __builtin_amdgcn_s_sleep(DLO);
        }
        if (layer == 0 && w == 6) {
          if (lane == 0) set_flag(p.ws, SFLAGS + (256 + g * 32 + j) * SFS, tag);
        }
        if (N45 == N67) issue_window<N45, WIN>(win, rs, lane16, NBASE());
        else if (w < 6) issue_window<N45, WIN>(win, rs, lane16, NBASE());
      }
      if ((opq_s(w) & 12) != 4) {
        const int T = w & 3, hp = w >> 3, Tp = (T & 1) ? T - 1 : 4;
        f32x4 mp, ms;
        const float neg = __uint_as_float(vconst<0xf149f2cau>());
        const int lo = l15 + ((T & 1) ? 9 : 1 + 4 * T), hi = (T & 1) ? 16 : j > 0 ? 8 + 4 * T : 0;
#pragma unroll
        for (int r = 0; r < 4; ++r) {
          const int jj = 4 * quad + r;
          mp[r] = ((jj >= lo) & (jj < hi)) ? 0.f : neg;
          ms[r] = (unsigned)(l15 - jj) <= 7u ? 0.f : neg;
        }
        int xs = L_QK + lane * 8 + T * 512, xp = L_QK + lane * 8 + Tp * 512, hs = xs + hp * 5120, hq = xp + hp * 5120;
        int om = ab + A_O + (g * 9 + T * 2 + hp) * KB + lane16;
        int oe = quad == hp ? ab + A_O + (g * 9 + 8) * KB + T * 256 + hp * 8 + l15 * 16 : WS_BYTES;
        unsigned qm0 = quad == 2 * hp ? ~0u : 0u, qm1 = quad == 2 * hp + 1 ? ~0u : 0u, zr = vconst<0>();
        v4u one4 = {vconst<ONES>(), vconst<ONES>(), vconst<ONES>(), vconst<ONES>()};
        asm volatile("" : "+v"(mp), "+v"(ms), "+v"(xs), "+v"(xp), "+v"(hs), "+v"(hq), "+v"(om), "+v"(oe), "+v"(qm0), "+v"(qm1), "+v"(zr), "+v"(one4));
        BAR();
        const v2u kxs = *(LAS v2u*)(L + xs + 40 * 512);
        const unsigned kxp = *(LAS unsigned*)(L + xp + 40 * 512 + 4);
        v2u q[2], ks[2], kp[2], vs[2], vp[2];
#pragma unroll
        for (int hh = 0; hh < 2; ++hh) {
          q[hh] = *(LAS v2u*)(L + hs + (50 + 5 * hh) * 512);
          kp[hh] = *(LAS v2u*)(L + hq + 5 * hh * 512);
          ks[hh] = *(LAS v2u*)(L + hs + 5 * hh * 512);
        }
        SB();
#pragma unroll
        for (int hh = 0; hh < 2; ++hh) {
          vp[hh] = *(LAS v2u*)(L + hq + (20 + 5 * hh) * 512);
          vs[hh] = *(LAS v2u*)(L + hs + (20 + 5 * hh) * 512);
        }
        const v2u vxp = *(LAS v2u*)(L + xp + 45 * 512), vxs = *(LAS v2u*)(L + xs + 45 * 512);
        SB();
        const f32x4 z = {0.f, 0.f, 0.f, 0.f};
        f32x4 sp[2], ss[2], o[2], ox[2], sm[2];
        float mx[2];
#pragma unroll
        for (int hh = 0; hh < 2; ++hh) {
          const v4u qf = {q[hh].x, q[hh].y, kxs.x & (hh ? qm1 : qm0), zr};
          sp[hh] = mfma(v4u{kp[hh].x, kp[hh].y, kxp, zr}, qf, mp);
          ss[hh] = mfma(v4u{ks[hh].x, ks[hh].y, kxs.y, zr}, qf, ms);
        }
#pragma unroll
        for (int hh = 0; hh < 2; ++hh) {
          const float m0 = __builtin_fmaxf(__builtin_fmaxf(sp[hh][0], sp[hh][1]), sp[hh][2]), m1 = __builtin_fmaxf(__builtin_fmaxf(ss[hh][0], ss[hh][1]), ss[hh][2]);
          mx[hh] = __builtin_fmaxf(__builtin_fmaxf(__builtin_fmaxf(__builtin_fmaxf(m0, sp[hh][3]), ss[hh][3]), m1), m1);
        }
        rows_max2(mx[0], mx[1]);
#pragma unroll
        for (int hh = 0; hh < 2; ++hh) {
          const float c = 0.23570226039551584f * 1.4426950408889634f, mc = -c * mx[hh];
          float e[8];
#pragma unroll
          for (int r = 0; r < 4; ++r) {
            e[r] = __builtin_amdgcn_exp2f(__builtin_fmaf(sp[hh][r], c, mc));
            e[4 + r] = __builtin_amdgcn_exp2f(__builtin_fmaf(ss[hh][r], c, mc));
          }
          const v4u pf = {pk(e[0], e[1]), pk(e[2], e[3]), pk(e[4], e[5]), pk(e[6], e[7])};
          sm[hh] = mfma(one4, pf, z);
          o[hh] = mfma(v4u{vp[hh].x, vp[hh].y, vs[hh].x, vs[hh].y}, pf, z);
          ox[hh] = mfma(v4u{vxp.x, vxp.y, vxs.x, vxs.y}, pf, z);
        }
        const float i0 = __builtin_amdgcn_rcpf(sm[0][0]), i1 = __builtin_amdgcn_rcpf(sm[1][0]);
        stg(v4u{pk(o[0][0] * i0, o[0][1] * i0), pk(o[0][2] * i0, o[0][3] * i0), pk(o[1][0] * i1, o[1][1] * i1), pk(o[1][2] * i1, o[1][3] * i1)}, rs, om, 0);
        __builtin_amdgcn_raw_buffer_store_b64(v2u{pk(ox[0][0] * i0, ox[0][1] * i0), pk(ox[1][2] * i1, ox[1][3] * i1)}, rs, oe, 0, SC1);
        STAMP(34);
        SETTLE();
      }
      STAMP(6 + layer * 16);
      BAR();
      STAMP(7 + layer * 16);
    }
    {
      LOCALS();
      const int th = g >> 2, ft = (g & 3) * 9 + w;
      {
        const int ov = quad * opq_s(oq0) + l15 * 16;
        RETRY_BEGIN();
        v4u orr[3];
#pragma unroll
        for (int i = 0; i < 3; ++i) orr[i] = ldg(rs, i ? ov : lane16, os0[i] + layer * ALY);
#pragma unroll
        for (int i = 0; i < 3; ++i) { *(LAS v4u*)(L + (w * 3 + i) * KB + lane16) = orr[i]; seen_ = umax4(seen_, orr[i]); }
        RETRY_END();
      }
      const v4u xr = xres;
      if (layer == 0 && w == 0) wait_flags<2>(p.ws, SFLAGS + (256 + g * 32 + (lane & 31)) * SFS, lane < 32, tag);
      BAR();
      STAMP(8 + layer * 16);
      if (EFL && w == 11 && lane == 0) set_flag(p.ws, FLAGS + (2 * layer + 1) * 4096 + j * 128 + g * 8, tag);
      if (w < 9) {
        f32x4 acc[1][2];
        gemm18<1, 2, 0, P2N, 10>(acc, win, L + lane16, rs, lane16, 0, MBASE());
        float a[8];
#pragma unroll
        for (int r = 0; r < 4; ++r) {
          auto s = __builtin_amdgcn_permlane16_swap(__float_as_uint(acc[0][0][r]), __float_as_uint(acc[0][1][r]), false, false);
          a[r] = __uint_as_float(s[0]);
          a[4 + r] = __uint_as_float(s[1]);
        }
        const v4u o = {pk(a[0] + blo(xr.x), a[1] + bhi(xr.x)), pk(a[2] + blo(xr.y), a[3] + bhi(xr.y)),
                       pk(a[4] + blo(xr.z), a[5] + bhi(xr.z)), pk(a[6] + blo(xr.w), a[7] + bhi(xr.w))};
        stg(o, rs, lane16, ab + A_X1 + (th * 36 + ft) * KB);
        STAMP(35);
        SETTLE();
      }
      if (layer == 0 && opq_s(w) >= 9) {
        const int f4 = (w - 9) * 48 + (lane < 48 ? lane : 0);
        v4u cp[6];
#pragma unroll
        for (int i = 0; i < 6; ++i) cp[i] = ldg(rs, f4 * 16, CS1 + (i * 4608 + 576 * g) * 4);
        const v2u b = *(const GAS v2u*)(p.b1 + 576 * g + 4 * f4);
        f32x4 cs = __builtin_bit_cast(f32x4, cp[0]);
#pragma unroll
        for (int i = 1; i < 6; ++i) add4(cs, cp[i]);
        *(LAS f32x4*)(L + L_T1 + f4 * 16) = cs;
        *(LAS f32x4*)(L + L_T1 + 2304 + f4 * 16) = f32x4{blo(b.x), bhi(b.x), blo(b.y), bhi(b.y)};
      }
      if (EFL && w == 11) {
        wait_flags(p.ws, FLAGS + (2 * layer + 1) * 4096 + j * 128 + (lane & 7) * 8, lane < 8, tag);
        __builtin_amdgcn_s_sleep(DLX);
      }
      if (w == 10 && lane < 10) ((volatile LAS int*)(L + L_HC))[lane] = 0;
      STAMP(9 + layer * 16);
      BAR();
      STAMP(10 + layer * 16);
    }
    {
      LOCALS();
      const int mbase = MBASE();
      const int T = w & 3, cls = w >> 2;
      if (opq_s(cls) == 0) {
        if (layer == 0) {
          __builtin_amdgcn_s_sleep(PZD);
          const v4u pz = {vconst<POISON>(), vconst<POISON>(), vconst<POISON>(), vconst<POISON>()};
#pragma unroll
          for (int i = 0; i < 8; ++i) {
            const int n = w * 8 + i;
            stg(pz, rs, lane16,
                sel(n < 30, ab + ALY + n * KB + sel(n < 12, A_X2 + g * 12 * KB, sel(n < 21, A_O, A_X1 - 9 * KB) + (g * 9 - 12) * KB), OOR));
          }
        }
      }
      if (opq_s(cls) > 0) {
        v4u xr[9];
        const int xo = (quad >> 1) * KB + ((quad & 1) * 2 + (T & 1)) * 256 + l15 * 16;
        RETRY_BEGIN();
#pragma unroll
        for (int i = 0; i < 9; ++i) xr[i] = ldg(rs, xo + (i & 1) * 2 * KB, ab + A_X1 + ((T >> 1) * 36 + 18 * (cls - 1) + (i >> 1) * 4) * KB);
#pragma unroll
        for (int i = 0; i < 9; ++i) { *(LAS v4u*)(L + (T * 18 + 9 * (cls - 1) + i) * KB + lane16) = xr[i]; seen_ = umax4(seen_, xr[i]); }
        RETRY_END();
        cnt_add(L, C_XA + cls - 1, lane);
        __builtin_amdgcn_s_setprio(3);
        const v4u one4 = {vconst<ONES>(), vconst<ONES>(), vconst<ONES>(), vconst<ONES>()};
        f32x4 sa = {0.f, 0.f, 0.f, 0.f}, sq = {0.f, 0.f, 0.f, 0.f};
        asm volatile("s_nop 1");
#pragma unroll
        for (int i = 0; i < 9; ++i) { sa = mfma(one4, xr[i], sa); sq = mfma(xr[i], xr[i], sq); }
        __builtin_amdgcn_s_setprio(0);
        if (layer == 0 && opq_s(w) >= 9) issue_window<0, 3 * PRE>(win, rs, lane16, mbase);
        const float dg = (lane & 2) ? ((lane & 1) ? sq[3] : sq[2]) : ((lane & 1) ? sq[1] : sq[0]);
        if (quad == (l15 >> 2)) *(LAS f32x2*)(L + L_ST + ((T * 16 + l15) * 2 + cls - 1) * 8) = f32x2{sa[0], dg};
        cnt_add(L, C_ST, lane);
      }
      STAMP(11 + layer * 16);
      f32x4 acc[3][4];
#pragma unroll
      for (int t = 0; t < 3; ++t)
#pragma unroll
        for (int m = 0; m < 4; ++m) acc[t][m] = zero4();
      if (opq_s(cls) > 0) { row_table(L, lane); shift_acc(acc, L, w, quad, l15); }
      STAMP(49);
      __builtin_amdgcn_s_setprio(3);
      gemm_up(acc, win, L + lane16, rs, lane16, mbase, L);
      __builtin_amdgcn_s_setprio(PGELU);
      STAMP(12 + layer * 16);
      {
        LAS unsigned char* H = L + L_H + lane16 + 3 * (w >> 1) * KB;
        if (opq_s(cls) == 0) {
          cnt_wait(L, C_RT, 1);
          shift_acc(acc, L, w, quad, l15);
        }
        f32x4 rt[4];
#pragma unroll
        for (int m = 0; m < 4; ++m) rt[m] = *(LAS f32x4*)(L + L_ST + KB + (m * 16 + l15) * 16);
#pragma unroll
        for (int m = 0; m < 4; ++m) {
          const v2u u0 = gelu4(acc[0][m], rt[m][2], rt[m][3], rt[m][1]), u1 = gelu4(acc[1][m], rt[m][2], rt[m][3], rt[m][1]);
          *(LAS v4u*)(H + (m * 18 + (w & 1)) * KB) = v4u{u0.x, u0.y, u1.x, u1.y};
          *(LAS v2u*)(H + (m * 18 + 2) * KB + (w & 1) * 8) = gelu4(acc[2][m], rt[m][2], rt[m][3], rt[m][1]);
        }
        cnt_add(L, C_H0 + cls, lane);
      }
      STAMP(13 + layer * 16);
      __builtin_amdgcn_s_setprio(2);
      gemm_down(acc, win, L + L_H + lane16, rs, lane16, mbase + 54 * KB, L STAMP_PASS);
      __builtin_amdgcn_s_setprio(PEPI);
      STAMP(14 + layer * 16);
      const int pw = ab + A_PT + (g * 12 + w) * 6144;
      const int poff = (quad & 1) * 1536 + (quad >> 1) * 256 + l15 * 16;
#pragma unroll
      for (int t = 0; t < 3; ++t)
#pragma unroll
        for (int m = 0; m < 4; m += 2) {
          const v2u a = {pk(acc[t][m][0], acc[t][m][1]), pk(acc[t][m][2], acc[t][m][3])};
          const v2u b = {pk(acc[t][m + 1][0], acc[t][m + 1][1]), pk(acc[t][m + 1][2], acc[t][m + 1][3])};
          const v4u o = widen(a, b);
          const bool own = OWN && m + (quad & 1) == (g >> 1) && (l15 >> 3) == (g & 1);
          stg(o, rs, own ? WS_BYTES : poff + m * 1536 + t * 512, pw);
          if (own) *(LAS v4u*)(L + (((g >> 1) ^ 1) * 18 + w) * KB + ((2 * t + (quad >> 1)) * 8 + (lane & 7)) * 16) = o;
        }
      __builtin_amdgcn_s_setprio(0);
      DRAIN();
      if (lane == 0) set_flag(p.ws, WFLAGS + ((layer * 32 + j) * 12 + w) * 128 + g * 8, tag);
      STAMP(15 + layer * 16);
      if (layer == 0 && opq_s(w) < 8) {
        if (QA) cnt_wait(L, C_LC, 12);
        issue_window<0, QA>(win, rs, lane16, QBASE());
      }
      __builtin_amdgcn_s_barrier();
    }
    {
      LOCALS();
      OWNROWS(6);
      const bool act = lane < 48;
      const v4u xr = *(LAS v4u*)(L + (act ? piece : 0));
      const v4u bb = *(const GAS v4u*)(p.b2 + (act ? c8 : 0) * 8);
      wait_flags(p.ws, WFLAGS + ((layer * 32 + j - (lane >> 3)) * 12 + w) * 128 + (lane < 8 ? lane : 3 + 4 * (lane & 1)) * 8,
                 lane < (layer == 0 && j > 0 ? 10 : 8), tag);
      STAMP(16 + layer * 16);
      v4u xb;
      {
        v4u pr[8];
        const int roff = (g >> 1) * 1536 + (lane >> 3) * 256 + (8 * (g & 1) + (lane & 7)) * 16 + (act ? 0 : WS_BYTES);
#pragma unroll
        for (int i = OWN; i < 8; ++i) pr[i] = ldg(rs, roff, ab + A_PT + (((i + g) & 7) * 12 + w) * 6144);
        if (OWN) pr[0] = *(LAS v4u*)(L + (((g >> 1) ^ 1) * 18 + w) * KB + lane16);
        SB();
        const unsigned sel_lo = opq_s(0x00003f80), sel_hi = opq_s(0x3f800000);
#pragma unroll
        for (int e = 0; e < 4; ++e) {
          float lo = blo(bb[e]) + blo(xr[e]), hi = bhi(bb[e]) + bhi(xr[e]);
          OPAQUE(lo);
#pragma unroll
          for (int i = 0; i < 8; ++i) { lo = dot2(pr[i][e], sel_lo, lo); hi = dot2(pr[i][e], sel_hi, hi); }
          xb[e] = pk(lo, hi);
        }
      }
      STAMP(17 + layer * 16);
      if (layer == 0) {
        float s1 = dot2(xb.x, ONES, dot2(xb.y, ONES, dot2(xb.z, ONES, dot2(xb.w, ONES, 0.f))));
        float s2 = dot2(xb.x, xb.x, dot2(xb.y, xb.y, dot2(xb.z, xb.z, dot2(xb.w, xb.w, 0.f))));
        if (!act) { s1 = 0.f; s2 = 0.f; }
        s1 += ror8(s1); OPAQUE(s1); s2 += ror8(s2);
        s1 = rows_sum(s1); OPAQUE(s1); s2 = rows_sum(s2);
        if (!act) xb = v4u{__float_as_uint(s1), __float_as_uint(s2), 0u, 0u};
        if (lane < 56) stg(xb, rs, lane16, ab + ALY + A_X2 + (g * 12 + w) * KB);
        SB();
        if (QB > QA && opq_s(w) < 8) issue_window<QA, QB>(win, rs, lane16, QBASE());
        else SETTLE();
        STAMP(36);
        const bool stl = (lane >> 3) == 6;
        LAS unsigned char* Iw = act ? L + (c8 >> 2) * KB + ((c8 & 3) * 16 + (lane & 7)) * 16 : L + 126 * KB + lane16;
        LAS unsigned char* Sw = L + L_ST + (w * 80 + (lane & 7)) * 8;
        BAR();
        STAMP(37);
        v4u xv[10];
        const int vo = lane < 56 ? lane16 : lane16 + WS_BYTES;
        xv[0] = xb;
        RETRY_BEGIN();
#pragma unroll
        for (int i = 1; i < 10; ++i)
          xv[i] = ldg(rs, vo, ab + ALY + A_X2 + w * KB + (i < 8 ? (i ^ g) * 12 * KB : (3 + 4 * (i - 8)) * 12 * KB - (j > 0 ? ASL : 0)));
        SB();
#pragma unroll
        for (int i = 0; i < 10; ++i) {
          const int mi = i < 8 ? i ^ g : i;
          *(LAS v4u*)(Iw + (act ? (mi >> 1) * 18 * KB + (mi & 1) * 128 : 0)) = xv[i];
          if (i) seen_ = umax4(seen_, xv[i]);
        }
        RETRY_END();
        if (stl) {
#pragma unroll
          for (int i = 0; i < 10; ++i) *(LAS v2u*)(Sw + (i < 8 ? i ^ g : i) * 64) = v2u{xv[i].x, xv[i].y};
        }
        BAR();
        STAMP(18);
        if (QB < WIN && opq_s(w) < 8) issue_window<QB, WIN>(win, rs, lane16, QBASE());
        if (w == 11) row_table1<12>(L, 4, lane);
        if (w >= 8) row_table1<12>(L, w - 8, lane);
        if (opq_s(w) >= 8) issue_window<0, WIN>(win, rs, lane16, QBASE());
      } else {
        const rsrc_t ro = __builtin_amdgcn_make_buffer_rsrc((void*)p.out, 0, 2048 * 576 * 2, 0x00020000);
        __builtin_amdgcn_raw_buffer_store_b128(xb, ro, act ? mytok * 1152 + c8 * 16 : 0x10000000, 0, OUTAUX);
      }
    }
  }
  STAMP(40);
  STAMP_DUMP();
}
}

extern "C" void kernel_launch(void* const* d_in, const int* in_sizes, int n_in, void* d_out, int out_size, void* d_ws, size_t ws_size,
                              hipStream_t stream) {
  (void)in_sizes; (void)n_in; (void)out_size; (void)ws_size;
  Params p;
  p.x = (const unsigned short*)d_in[0];
  p.wqkv = (const unsigned short*)d_in[1];
  p.wo = (const unsigned short*)d_in[2];
  p.w1 = (const unsigned short*)d_in[4];
  p.b1 = (const unsigned short*)d_in[5];
  p.w2 = (const unsigned short*)d_in[6];
  p.b2 = (const unsigned short*)d_in[7];
  p.out = (unsigned short*)d_out;
  p.ws = (unsigned char*)d_ws;
  (void)hipFuncSetAttribute((const void*)fused_fwd, hipFuncAttributeMaxDynamicSharedMemorySize, LDS_BYTES);
  hipLaunchKernelGGL(fused_fwd, dim3(256), dim3(NTHR), LDS_BYTES, stream, p);
}
```

```cpp
#include <hip/hip_runtime.h>
#include <hip/hip_bf16.h>
#include <stdint.h>

namespace {
#define LAS __attribute__((address_space(3)))
#define GAS __attribute__((address_space(1)))
typedef unsigned v4u __attribute__((ext_vector_type(4)));
typedef unsigned v2u __attribute__((ext_vector_type(2)));
typedef float f32x4 __attribute__((ext_vector_type(4)));
typedef float f32x2 __attribute__((ext_vector_type(2)));
typedef __bf16 bf16x8 __attribute__((ext_vector_type(8)));
typedef __bf16 bf16x2 __attribute__((ext_vector_type(2)));
typedef short s16x4 __attribute__((ext_vector_type(4)));
typedef unsigned long long u64;
typedef __amdgpu_buffer_rsrc_t rsrc_t;

constexpr int KB = 1024;
constexpr int NTHR = 768;
constexpr int WIN = 18;
#define XB 2
#define P2N 9
#define QA 6
#define QB 12
#define PZD 20
#define BDELAY 50
#define CODEPF 35
#define NTW 1
constexpr int DST = 5;
constexpr float EPS = 1e-5f;
constexpr int SC1 = 16;
constexpr float GC0 = -2.0f * 0.7978845608028654f * 1.4426950408889634f;
constexpr int FLAGS = 4096;
constexpr int SFLAGS = 32 * KB;
constexpr int WFLAGS = 48 * KB;
constexpr int CS1 = 192 * KB;
constexpr int CSQ = 520 * KB;
constexpr int WBASE = 704 * KB;
constexpr int WSL = (252 + 1296) * KB;
constexpr int WOB = WBASE + 8 * WSL;
constexpr int ABASE = 14 * 1024 * KB;
constexpr int IMG = 72 * KB;
constexpr int A_X2 = 0, A_O = 96 * KB, A_X1 = A_O + IMG, A_PT = A_X1 + IMG, ALY = 1024 * KB, ASL = 2 * ALY;
constexpr int WS_BYTES = ABASE + 32 * ASL;
static_assert(WOB + 648 * KB <= ABASE && A_PT + 8 * IMG <= ALY && CSQ + 8 * 14 * 1152 + 256 <= WBASE, "workspace map");
constexpr int L_H = IMG;
constexpr int L_QK = 90 * KB;
constexpr int L_ST = 145 * KB;
constexpr int L_T1 = L_ST + 7680;
constexpr int L_TQ = L_T1 + 4608;
constexpr int L_RT = L_TQ + 896;
constexpr int L_HC = L_RT + 640;
constexpr int L_B2 = L_HC + 64;
constexpr int LDS_BYTES = L_B2 + 1152;
static_assert(LDS_BYTES <= 160 * KB, "LDS");
enum { C_H0 = 0, C_ST = 3, C_RT = 4, C_LC = 5, C_XA = 6, C_R1 = 10, C_P0 = 11 };

struct Params {
  const unsigned short *x, *wqkv, *wo, *w1, *b1, *w2, *b2;
  unsigned short *out;
  unsigned char *ws;
};

#define STAMP(i)
#define STAMP_INIT()
#define RETRY_COUNT()
#define STAMP_ARG
#define STAMP_PASS
#define STAMP_DUMP()
#define SB() __builtin_amdgcn_sched_barrier(0)
#define BAR() do { asm volatile("s_waitcnt lgkmcnt(0)" ::: "memory"); __builtin_amdgcn_s_barrier(); asm volatile("" ::: "memory"); } while (0)
#define DRAIN() asm volatile("s_waitcnt vmcnt(0)" ::: "memory")
#define SETTLE() DRAIN()
#define OPAQUE(x) asm("" : "+v"(x))
#define KEEP(x) asm volatile("" :: "v"(x))

__device__ __forceinline__ f32x4 mfma(v4u a, v4u b, f32x4 c) {
  return __builtin_amdgcn_mfma_f32_16x16x32_bf16(__builtin_bit_cast(bf16x8, a), __builtin_bit_cast(bf16x8, b), c, 0, 0, 0);
}
#define MF(acc, a, b) asm volatile("v_mfma_f32_16x16x32_bf16 %0, %1, %2, %0" : "+v"(acc) : "v"(a), "v"(b))
#define MF0(acc, a, b) asm volatile("v_mfma_f32_16x16x32_bf16 %0, %1, %2, 0" : "=&v"(acc) : "v"(a), "v"(b))
template <int NT, int MT> __device__ __forceinline__ void acc_fence(f32x4 (&acc)[NT][MT]) {
#pragma unroll
  for (int t = 0; t < NT; ++t)
#pragma unroll
    for (int m = 0; m < MT; ++m) asm volatile("" : "+v"(acc[t][m]));
}
#define ACC_IN(acc) do { acc_fence(acc); asm volatile("s_nop 1"); SB(); } while (0)
#define ACC_OUT(acc) do { SB(); asm volatile("s_nop 7\n\ts_nop 1"); acc_fence(acc); } while (0)
__device__ __forceinline__ unsigned pk(float a, float b) {
  f32x2 v = {a, b};
  return __builtin_bit_cast(unsigned, __builtin_convertvector(v, bf16x2));
}
__device__ __forceinline__ float dot2(unsigned a, unsigned b, float c) {
  return __builtin_amdgcn_fdot2_f32_bf16(__builtin_bit_cast(bf16x2, a), __builtin_bit_cast(bf16x2, b), c, false);
}
constexpr unsigned ONES = 0x3f803f80u;
__device__ __forceinline__ float blo(unsigned u) { return __uint_as_float(u << 16); }
__device__ __forceinline__ float bhi(unsigned u) { return __uint_as_float(u & 0xffff0000u); }
__device__ __forceinline__ v4u ldg(rsrc_t rs, int voff, int soff) { return __builtin_amdgcn_raw_buffer_load_b128(rs, voff, soff, SC1); }
__device__ __forceinline__ void stg(v4u v, rsrc_t rs, int voff, int soff) { __builtin_amdgcn_raw_buffer_store_b128(v, rs, voff + soff, 0, SC1); }
__device__ __forceinline__ v4u ldf(rsrc_t rs, int lane16, int base, int f) { return ldg(rs, lane16 + (f & 3) * KB, base + (f >> 2) * 4 * KB); }
__device__ __forceinline__ int opq_v(int x) { asm volatile("" : "+v"(x)); return x; }
__device__ __forceinline__ int lane_id() { int x; asm volatile("v_mbcnt_lo_u32_b32 %0, -1, 0\n\tv_mbcnt_hi_u32_b32 %0, -1, %0" : "=v"(x)); return x; }
template <unsigned C> __device__ __forceinline__ unsigned vconst() { unsigned x; asm volatile("v_mov_b32 %0, %1" : "=v"(x) : "i"(C)); return x; }
__device__ __forceinline__ int sel(bool c, int a, int b) { return c ? a : b; }
constexpr int OOR = 0x10000000;
__device__ __forceinline__ int opq_s(int x) { asm volatile("" : "+s"(x)); return x; }
#define LOCALS() const int lane = lane_id(), w = opq_s(w0), j = opq_s(j0), g = opq_s(g0); \
  const int lane16 = lane * 16, quad = lane >> 4, l15 = lane & 15, ab = ABASE + j * ASL + layer * ALY, wb = WBASE + g * WSL; \
  (void)lane16; (void)quad; (void)l15; (void)ab; (void)wb
#define OWNROWS(CPW) const int c8 = CPW * w + (lane >> 3); \
  const int piece = ((g >> 1) * 18 + (c8 >> 2)) * KB + ((c8 & 3) * 16 + 8 * (g & 1) + (lane & 7)) * 16; \
  const int mytok = 64 * j + 2 * (16 * ((g >> 1) & 1) + 8 * (g & 1) + (lane & 7)) + (g >> 2)
#define CPF_DONE() KEEP(cpf)
#define GET_TAG() CPF_DONE(); tag = 1 + ((u64)(unsigned)__builtin_amdgcn_readfirstlane((int)(unsigned)tag0) | \
                             ((u64)(unsigned)__builtin_amdgcn_readfirstlane((int)(unsigned)(tag0 >> 32)) << 32))
#define NBASE() opq_s(nb0)
#define QBASE() opq_s(qb0)
#define MBASE() opq_s(mb0)
__device__ __forceinline__ float rows_sum(float x) {
  auto s = __builtin_amdgcn_permlane16_swap(__float_as_uint(x), __float_as_uint(x), false, false);
  x = __uint_as_float(s[0]) + __uint_as_float(s[1]);
  auto t = __builtin_amdgcn_permlane32_swap(__float_as_uint(x), __float_as_uint(x), false, false);
  return __uint_as_float(t[0]) + __uint_as_float(t[1]);
}
__device__ __forceinline__ float vmax(unsigned a, unsigned b) { float r; asm("v_max_f32 %0, %1, %2\n\ts_nop 1" : "=v"(r) : "v"(a), "v"(b)); return r; }
__device__ __forceinline__ void rows_max2(float& a, float& b) {
  auto s = __builtin_amdgcn_permlane16_swap(__float_as_uint(a), __float_as_uint(b), false, false);
  float x = vmax(s[0], s[1]);
  auto t = __builtin_amdgcn_permlane32_swap(__float_as_uint(x), __float_as_uint(x), false, false);
  x = vmax(t[0], t[1]);
  auto u = __builtin_amdgcn_permlane16_swap(__float_as_uint(x), __float_as_uint(x), false, false);
  a = __uint_as_float(u[0]);
  b = __uint_as_float(u[1]);
}
template <int CTRL> __device__ __forceinline__ float dpp(float x) {
  return __int_as_float(__builtin_amdgcn_mov_dpp(__float_as_int(x), CTRL, 0xf, 0xf, true));
}
__device__ __forceinline__ float ror8(float x) { return dpp<0x128>(x); }
__device__ __forceinline__ void add4(f32x4& a, v4u b) {
  float a0 = a[0], a1 = a[1], a2 = a[2], a3 = a[3];
  asm("v_add_f32 %0, %0, %4\n\tv_add_f32 %1, %1, %5\n\tv_add_f32 %2, %2, %6\n\tv_add_f32 %3, %3, %7"
      : "+v"(a0), "+v"(a1), "+v"(a2), "+v"(a3) : "v"(b.x), "v"(b.y), "v"(b.z), "v"(b.w));
  a = f32x4{a0, a1, a2, a3};
}
__device__ __forceinline__ v4u widen(v2u a, v2u b) {
  auto s0 = __builtin_amdgcn_permlane16_swap(a.x, b.x, false, false);
  auto s1 = __builtin_amdgcn_permlane16_swap(a.y, b.y, false, false);
  return v4u{s0[0], s1[0], s0[1], s1[1]};
}
__device__ __forceinline__ v2u gelu4(f32x4 a, float k0, float k1, float is) {
  float s0, s1, s2, s3;
  unsigned u0, u1;
  asm("v_mul_f32 %2, %6, %6\n\tv_mul_f32 %3, %7, %7\n\tv_mul_f32 %4, %8, %8\n\tv_mul_f32 %5, %9, %9\n\t"
      "v_fma_f32 %2, %2, %11, %10\n\tv_fma_f32 %3, %3, %11, %10\n\tv_fma_f32 %4, %4, %11, %10\n\tv_fma_f32 %5, %5, %11, %10\n\t"
      "v_mul_f32 %2, %2, %6\n\tv_mul_f32 %3, %3, %7\n\tv_mul_f32 %4, %4, %8\n\tv_mul_f32 %5, %5, %9\n\t"
      "v_exp_f32 %2, %2\n\tv_exp_f32 %3, %3\n\tv_exp_f32 %4, %4\n\tv_exp_f32 %5, %5\n\t"
      "v_fma_f32 %2, %2, %12, %12\n\tv_fma_f32 %3, %3, %12, %12\n\tv_fma_f32 %4, %4, %12, %12\n\tv_fma_f32 %5, %5, %12, %12\n\t"
      "v_rcp_f32 %2, %2\n\tv_rcp_f32 %3, %3\n\tv_rcp_f32 %4, %4\n\tv_rcp_f32 %5, %5\n\t"
      "v_mul_f32 %2, %2, %6\n\tv_mul_f32 %3, %3, %7\n\tv_mul_f32 %4, %4, %8\n\tv_mul_f32 %5, %5, %9\n\t"
      "v_cvt_pk_bf16_f32 %0, %2, %3\n\tv_cvt_pk_bf16_f32 %1, %4, %5"
      : "=&v"(u0), "=&v"(u1), "=&v"(s0), "=&v"(s1), "=&v"(s2), "=&v"(s3)
      : "v"(a[0]), "v"(a[1]), "v"(a[2]), "v"(a[3]), "v"(k0), "v"(k1), "v"(is));
  return v2u{u0, u1};
}
__device__ __forceinline__ void shift4(f32x4& a, f32x4 cs, f32x4 b, float nmean, float is) {
  float a0 = a[0], a1 = a[1], a2 = a[2], a3 = a[3];
  asm("v_fmac_f32 %0, %12, %4\n\tv_fmac_f32 %1, %12, %5\n\tv_fmac_f32 %2, %12, %6\n\tv_fmac_f32 %3, %12, %7\n\t"
      "v_fmac_f32 %0, %13, %8\n\tv_fmac_f32 %1, %13, %9\n\tv_fmac_f32 %2, %13, %10\n\tv_fmac_f32 %3, %13, %11"
      : "+v"(a0), "+v"(a1), "+v"(a2), "+v"(a3)
      : "v"(cs[0]), "v"(cs[1]), "v"(cs[2]), "v"(cs[3]), "v"(b[0]), "v"(b[1]), "v"(b[2]), "v"(b[3]), "v"(nmean), "v"(is));
  a = f32x4{a0, a1, a2, a3};
}
__device__ __forceinline__ int cnt_get(LAS unsigned char* L, int i) { return ((volatile LAS int*)(L + L_HC))[i]; }
__device__ __forceinline__ void cnt_wait(LAS unsigned char* L, int i, int n) {
  while (__builtin_amdgcn_readfirstlane(cnt_get(L, i)) < n) __builtin_amdgcn_s_sleep(1);
  asm volatile("" ::: "memory");
}
__device__ __forceinline__ void cnt_add(LAS unsigned char* L, int i, int lane) {
  asm volatile("s_waitcnt lgkmcnt(0)" ::: "memory");
  if (lane == 0) __hip_atomic_fetch_add((LAS int*)(L + L_HC) + i, 1, __ATOMIC_RELAXED, __HIP_MEMORY_SCOPE_WORKGROUP);
}
template <int NP> __device__ __forceinline__ void row_table1(LAS unsigned char* L, int t5, int lane) {
  const int r = t5 * 16 + (lane & 15), q = lane >> 4;
  float t1 = 0.f, t2 = 0.f;
#pragma unroll
  for (int i = 0; i < (NP + 3) / 4; ++i)
    if (NP % 4 == 0 || q < NP) { const f32x2 v = *(LAS f32x2*)(L + L_ST + ((i * 4 + q) * 80 + r) * 8); t1 += v.x; OPAQUE(t1); t2 += v.y; }
  t1 = rows_sum(t1); OPAQUE(t1); t2 = rows_sum(t2);
  const float nmean = t1 * (-1.0f / 576.0f);
  const float var = fmaxf(__builtin_fmaf(-nmean, nmean, t2 * (1.0f / 576.0f)), 0.f);
  const float rstd = __builtin_amdgcn_rsqf(var + EPS);
  if (q < 2) *(LAS float*)(L + L_RT + q * 320 + r * 4) = q ? nmean * rstd : rstd;
  cnt_add(L, C_R1, lane);
}
__device__ __forceinline__ v2u ln_pack(f32x4 a, f32x4 r, f32x4 c, f32x4 nm) {
  float t0, t1, t2, t3;
  unsigned u0, u1;
  asm("v_mul_f32 %2, %14, %18\n\tv_mul_f32 %3, %15, %19\n\tv_mul_f32 %4, %16, %20\n\tv_mul_f32 %5, %17, %21\n\t"
      "v_fmac_f32 %2, %6, %10\n\tv_fmac_f32 %3, %7, %11\n\tv_fmac_f32 %4, %8, %12\n\tv_fmac_f32 %5, %9, %13\n\t"
      "v_cvt_pk_bf16_f32 %0, %2, %3\n\tv_cvt_pk_bf16_f32 %1, %4, %5"
      : "=&v"(u0), "=&v"(u1), "=&v"(t0), "=&v"(t1), "=&v"(t2), "=&v"(t3)
      : "v"(a[0]), "v"(a[1]), "v"(a[2]), "v"(a[3]), "v"(r[0]), "v"(r[1]), "v"(r[2]), "v"(r[3]), "v"(c[0]), "v"(c[1]), "v"(c[2]), "v"(c[3]),
        "v"(nm[0]), "v"(nm[1]), "v"(nm[2]), "v"(nm[3]));
  return v2u{u0, u1};
}
__device__ __forceinline__ f32x4 zero4() {
  float a, b, c, d;
  asm volatile("v_mov_b32 %0, 0\n\tv_mov_b32 %1, 0\n\tv_mov_b32 %2, 0\n\tv_mov_b32 %3, 0" : "=v"(a), "=v"(b), "=v"(c), "=v"(d));
  return f32x4{a, b, c, d};
}
__device__ __forceinline__ f32x4 splat(float x) { return f32x4{x, x, x, x}; }
__device__ __forceinline__ void row_table(LAS unsigned char* L, int lane) {
  cnt_wait(L, C_ST, 8);
  const f32x4 q = *(LAS f32x4*)(L + L_ST + lane * 16);
  float t1 = q[0] + q[2];
  OPAQUE(t1);
  float nmean = t1 * (-1.0f / 576.0f);
  const float var = fmaxf(__builtin_fmaf(-nmean, nmean, (q[1] + q[3]) * (1.0f / 576.0f)), 0.f);
  const float r1 = __builtin_amdgcn_rsqf(var + EPS);
  const float sc = r1 * __builtin_amdgcn_rsqf(var * r1 * r1 + EPS);
  float k0 = GC0 * sc;
  OPAQUE(k0);
  *(LAS f32x4*)(L + L_ST + KB + lane * 16) = f32x4{nmean, __builtin_amdgcn_rcpf(sc), k0, k0 * (0.044715f * sc * sc)};
  cnt_add(L, C_RT, lane);
}
__device__ __forceinline__ void shift_acc(f32x4 (&acc)[3][4], LAS unsigned char* L, int w, int quad, int l15) {
  f32x2 rt[4];
#pragma unroll
  for (int m = 0; m < 4; ++m) rt[m] = *(LAS f32x2*)(L + L_ST + KB + (m * 16 + l15) * 16);
#pragma unroll
  for (int t = 0; t < 3; ++t) {
    const f32x4 cs = *(LAS f32x4*)(L + L_T1 + (48 * w + 16 * t + 4 * quad) * 4), bb = *(LAS f32x4*)(L + L_T1 + 2304 + (48 * w + 16 * t + 4 * quad) * 4);
#pragma unroll
    for (int m = 0; m < 4; ++m) shift4(acc[t][m], cs, bb, rt[m][0], rt[m][1]);
  }
}
template <int SLEEP = 1> __device__ __forceinline__ void wait_flags(const unsigned char* ws, int off, bool active, u64 tag) {
  const GAS u64* f = (const GAS u64*)(ws + off);
  asm volatile("" : "+s"(tag));
#pragma unroll 1
  for (int spins = 0; spins < (1 << 16); ++spins) {
    const u64 v = active ? __hip_atomic_load(f, __ATOMIC_RELAXED, __HIP_MEMORY_SCOPE_AGENT) : tag;
    if (__all(v == tag)) break;
    __builtin_amdgcn_s_sleep(SLEEP);
  }
  asm volatile("" ::: "memory");
}
__device__ __forceinline__ void set_flag(unsigned char* ws, int off, u64 tag) {
  asm volatile("" : "+s"(tag));
  __hip_atomic_store((GAS u64*)(ws + off), tag, __ATOMIC_RELAXED, __HIP_MEMORY_SCOPE_AGENT);
}
constexpr unsigned POISON = 0xffffffffu;
__device__ __forceinline__ unsigned umax4(unsigned m, v4u v) { return max(max(max(m, v.x), v.y), max(v.z, v.w)); }
#define RETRY_BEGIN() _Pragma("unroll 1") for (int spins_ = 0; spins_ < (1 << 15); ++spins_) { unsigned seen_ = 0
#define RETRY_END() if (!__any(seen_ == POISON)) break; RETRY_COUNT(); __builtin_amdgcn_s_sleep(2); asm volatile("" ::: "memory"); }
template <int LO, int HI> __device__ __forceinline__ void issue_window(v4u (&win)[WIN], rsrc_t rs, int lane16, int base) {
#pragma unroll
  for (int i = LO; i < HI; ++i) win[i] = ldf(rs, lane16, base, i);
  SB();
}

template <int NT, int MT, int SWAP, int NNEXT, int R, int LO, int HI>
__device__ __forceinline__ void gemm18_steps(f32x4 (&acc)[NT][MT], v4u (&win)[WIN], v4u (&act)[R], LAS unsigned char* img, LAS unsigned char* img2, rsrc_t rs,
                                             int lane16, int wbase, int nvo, int nbase) {
#pragma unroll
  for (int n = LO; n < HI; ++n) {
    const int s = n / MT, m = n % MT, nn = n + R - 1;
    if (n < 18 * MT) {
#pragma unroll
      for (int t = 0; t < NT; ++t) {
        if (s == 0) {
          if ((SWAP >> t) & 1) MF0(acc[t][m], act[n % R], win[t]);
          else MF0(acc[t][m], win[t], act[n % R]);
        } else if ((SWAP >> t) & 1) MF(acc[t][m], act[n % R], win[(s * NT + t) % WIN]);
        else MF(acc[t][m], win[(s * NT + t) % WIN], act[n % R]);
      }
      SB();
    }
    if (nn < 18 * MT) act[nn % R] = *(LAS v4u*)(nn % MT < 3 ? img + ((nn % MT) * 18 + nn / MT) * KB : img2 + ((nn % MT - 3) * 18 + nn / MT) * KB);
    if (m == 0 && s > 0) {
#pragma unroll
      for (int t = 0; t < NT; ++t) {
        const int f = (s - 1) * NT + t;
        if (f + WIN < 18 * NT) win[f % WIN] = ldf(rs, lane16, wbase, f + WIN);
        else if (f % WIN < NNEXT) win[f % WIN] = ldf(rs, nvo, nbase, f % WIN);
      }
    }
    SB();
    if (n < 18 * MT) {
      KEEP(act[n % R]);
#pragma unroll
      for (int t = 0; t < NT; ++t) KEEP(win[(s * NT + t) % WIN]);
      SB();
    }
  }
}
template <int NT, int MT, int SWAP, int NNEXT, int R>
__device__ __forceinline__ void gemm18(f32x4 (&acc)[NT][MT], v4u (&win)[WIN], LAS unsigned char* img, rsrc_t rs, int lane16, int wbase, int nbase) {
  v4u act[R];
  LAS unsigned char* img2 = img + 54 * KB;
  if (MT > 3) asm volatile("" : "+v"(img2));
#pragma unroll
  for (int n = 0; n < R - 1; ++n) { act[n] = *(LAS v4u*)(n % MT < 3 ? img + ((n % MT) * 18 + n / MT) * KB : img2 + ((n % MT - 3) * 18 + n / MT) * KB); SB(); }
  gemm18_steps<NT, MT, SWAP, NNEXT, R, 0, 6 * MT>(acc, win, act, img, img2, rs, lane16, wbase, lane16, nbase);
  gemm18_steps<NT, MT, SWAP, NNEXT, R, 6 * MT, 12 * MT>(acc, win, act, img, img2, rs, lane16, wbase, lane16, nbase);
  gemm18_steps<NT, MT, SWAP, NNEXT, R, 12 * MT, 18 * MT + 1>(acc, win, act, img, img2, rs, lane16, wbase, lane16, nbase);
  ACC_OUT(acc);
}
template <int NT, int MT, int SWAP, int R, int tstep, int NNEXT>
__device__ __forceinline__ void qkv_tiles(v4u (&win)[WIN], LAS unsigned char* L, rsrc_t rs, int lane, int wbase, int nbase, int tile0, int rwait STAMP_ARG) {
  f32x4 acc[NT][MT];
  gemm18<NT, MT, SWAP, NNEXT, R>(acc, win, L + lane * 16, rs, lane * 16, wbase, nbase);
  STAMP(38);
  LAS unsigned char *Rq = L + L_RT + (lane & 48), *Rl = L + L_RT + (lane & 15) * 4, *Tq = L + L_TQ + tile0 * 64 + (lane & 48),
                    *Tl = L + L_TQ + tile0 * 64 + (lane & 15) * 4, *X = L + L_QK + lane * 8 + tile0 * 2560;
  asm volatile("" : "+v"(Rq), "+v"(Rl), "+v"(Tq), "+v"(Tl), "+v"(X));
  cnt_wait(L, C_R1, rwait);
  v2u u[NT][MT];
#pragma unroll
  for (int t = 0; t < NT; ++t) {
    if ((SWAP >> t) & 1) {
      const f32x4 cs = splat(*(LAS float*)(Tl + t * tstep * 64));
#pragma unroll
      for (int m = 0; m < MT; ++m) u[t][m] = ln_pack(acc[t][m], *(LAS f32x4*)(Rq + m * 64), cs, *(LAS f32x4*)(Rq + 320 + m * 64));
    } else {
      const f32x4 cs = *(LAS f32x4*)(Tq + t * tstep * 64);
#pragma unroll
      for (int m = 0; m < MT; ++m) u[t][m] = ln_pack(acc[t][m], splat(*(LAS float*)(Rl + m * 64)), cs, splat(*(LAS float*)(Rl + 320 + m * 64)));
    }
  }
#pragma unroll
  for (int t = 0; t < NT; ++t)
#pragma unroll
    for (int m = 0; m < MT; ++m) *(LAS v2u*)(X + (t * tstep * 5 + m) * 512) = u[t][m];
}
template <int C>
__device__ __forceinline__ void mlp_chunk(f32x4 (&acc)[3][4], v4u (&win)[WIN], v4u (&act)[8], LAS unsigned char* img, rsrc_t rs, int lane16, int rb,
                                          LAS unsigned char* L) {
  int hv = 4;
#pragma unroll
  for (int s = 0; s < 6; ++s) {
    if (C != 2 && s == 5) {
      if (__builtin_amdgcn_readfirstlane(hv) < 4) cnt_wait(L, C < 0 ? C_XA + 1 : C_H0 + C + 1, 4);
      asm volatile("" ::: "memory");
      SB();
    }
    if (C == 2 && s == 6 - DST) {
      if (lane16 == 0) __hip_atomic_fetch_add((LAS int*)(L + L_HC) + C_LC, 1, __ATOMIC_RELAXED, __HIP_MEMORY_SCOPE_WORKGROUP);
      SB();
    }
    asm volatile("" :: "v"(act[(s & 1) * 4]), "v"(act[(s & 1) * 4 + 1]), "v"(act[(s & 1) * 4 + 2]), "v"(act[(s & 1) * 4 + 3]), "v"(win[s * 3]),
                 "v"(win[s * 3 + 1]), "v"(win[s * 3 + 2]));
    SB();
#pragma unroll
    for (int t = 0; t < 3; ++t)
#pragma unroll
      for (int m = 0; m < 4; ++m) {
        const int mm = t == 1 ? 3 - m : m;
        if (C == 0 && s == 0) MF0(acc[t][mm], win[t], act[mm]);
        else MF(acc[t][mm], win[s * 3 + t], act[(s & 1) * 4 + mm]);
        SB();
        if (t == 0 && !(C == 2 && s == 5)) act[((s + 1) & 1) * 4 + m] = *(LAS v4u*)(img + (m * 18 + s + 1) * KB);
        if (C != 2 && s == 3 && t == 2 && m == 0) hv = cnt_get(L, C < 0 ? C_XA + 1 : C_H0 + C + 1);
        if (t == 1 && m < 3 && !(C == 2 && s + DST >= 6))
          win[((s + DST) % 6) * 3 + m] = ldf(rs, lane16, rb, (s + DST) * 3 + m);
        SB();
      }
  }
}
__device__ __forceinline__ void gemm_up(f32x4 (&acc)[3][4], v4u (&win)[WIN], LAS unsigned char* img, rsrc_t rs, int lane16, int rb, LAS unsigned char* L) {
  v4u act[8];
  cnt_wait(L, C_XA, 4);
#pragma unroll
  for (int n = 0; n < 4; ++n) { act[n] = *(LAS v4u*)(img + n * 18 * KB); SB(); }
  ACC_IN(acc);
#pragma unroll 1
  for (int c = 0; c < 3; ++c) {
    mlp_chunk<-1>(acc, win, act, img, rs, lane16, rb, L);
    img += 6 * KB;
    rb += 18 * KB;
  }
  ACC_OUT(acc);
}
__device__ __forceinline__ void gemm_down(f32x4 (&acc)[3][4], v4u (&win)[WIN], LAS unsigned char* img, rsrc_t rs, int lane16, int rb, LAS unsigned char* L STAMP_ARG) {
  v4u act[8];
  cnt_wait(L, C_H0, 4);
  STAMP(46);
#pragma unroll
  for (int n = 0; n < 4; ++n) { act[n] = *(LAS v4u*)(img + n * 18 * KB); SB(); }
  mlp_chunk<0>(acc, win, act, img, rs, lane16, rb, L);
  img += 6 * KB;
  asm volatile("" : "+v"(img));
  STAMP(47);
  mlp_chunk<1>(acc, win, act, img, rs, lane16, rb + 18 * KB, L);
  img += 6 * KB;
  asm volatile("" : "+v"(img));
  STAMP(48);
  mlp_chunk<2>(acc, win, act, img, rs, lane16, rb + 36 * KB, L);
  ACC_OUT(acc);
}

__global__ void __launch_bounds__(NTHR) fused_fwd(Params p) {
  extern __shared__ __attribute__((aligned(16))) unsigned char smem[];
  LAS unsigned char* L = (LAS unsigned char*)smem;
  const int w0 = __builtin_amdgcn_readfirstlane(threadIdx.x >> 6);
  const int bid = blockIdx.x;
  u64 pc0;
  asm volatile("s_getpc_b64 %0" : "=s"(pc0));
  unsigned cpf = 0;
  if (threadIdx.x < CODEPF * 16) cpf = *(const GAS unsigned*)(pc0 + threadIdx.x * 64);
  constexpr unsigned MPX = 8 / XB;
  const int j0 = ((blockIdx.x & 7) / XB) * (32 / MPX) + (blockIdx.x >> 3) / MPX;
  const int g0 = ((blockIdx.x & 7) % XB) * MPX + (blockIdx.x >> 3) % MPX;
  const rsrc_t rs = __builtin_amdgcn_make_buffer_rsrc((void*)p.ws, 0, WS_BYTES, 0x00020000);
  const int mb0 = WBASE + g0 * WSL + (252 + w0 * 108) * KB;
  const int nb0 = sel(w0 < 9, WOB + ((g0 & 3) * 9 + w0) * 18 * KB, mb0);
  const int qb0 = sel(w0 < 8, WBASE + g0 * WSL + sel(w0 < 6, w0 * 36, 108 + w0 * 18) * KB, nb0);
  STAMP_INIT();
  STAMP(0);
  const u64 tag0 = __hip_atomic_load((const GAS u64*)p.ws, __ATOMIC_RELAXED, __HIP_MEMORY_SCOPE_AGENT);
  u64 tag = 0;
  v4u win[WIN];
  {
    constexpr int layer = 0;
    LOCALS();
    const rsrc_t rx = __builtin_amdgcn_make_buffer_rsrc((void*)p.x, 0, 2048 * 576 * 2, 0x00020000);
    const int xrow = lane >> 2;
#define LOAD_X() v4u xr[9]; _Pragma("unroll") for (int i = 0; i < 9; ++i) xr[i] = __builtin_amdgcn_raw_buffer_load_b128(rx, xoff + i * 64, 0, 0); SB()
#define STAGE_X() do { float s1 = 0.f, s2 = 0.f; \
      _Pragma("unroll") for (int i = 0; i < 9; ++i) { \
        if (w < 10) *(LAS v4u*)(L + (w * 9 + i) * KB + (lane & 3) * 256 + xrow * 16) = xr[i]; \
        _Pragma("unroll") for (int e = 0; e < 4; ++e) { s1 = dot2(xr[i][e], ONES, s1); s2 = dot2(xr[i][e], xr[i][e], s2); } } \
      s1 += dpp<0xB1>(s1); s2 += dpp<0xB1>(s2); s1 += dpp<0x4E>(s1); s2 += dpp<0x4E>(s2); \
      if (w < 10 && (lane & 3) == 0) *(LAS f32x2*)(L + L_ST + ((w & 1) * 80 + (w >> 1) * 16 + xrow) * 8) = f32x2{s1, s2}; } while (0)
    if (opq_s(w) < 8) {
      LAS unsigned char* T = L + L_QK + w * 3072;
      const int uf = 8 * j + w, uc = sel(uf < 252, uf, 0), us = (uc * 293) >> 12, tp = uc - us * 14;
      const int tile = sel(tp < 4, 10 + tp, sel(tp == 4, 8, sel(tp == 13, 9, tp - 5)));
      const bool vx = tile == 9, ex = (tile & 14) == 8;
      const int c0 = 72 * g + sel(ex, 16, ((tile + sel(tile >= 10, 2, 0)) & 3) * 18) + sel(tile < 4, 576, sel(tile < 8 || vx, 1152, 0));
      const rsrc_t rq = __builtin_amdgcn_make_buffer_rsrc((void*)p.wqkv, 0, 576 * 1728 * 2, 0x00020000);
      const int qo = (lane >> 1) * 3456 + (lane & 1) * sel(ex, sel(vx, 0, 72), 16), qs = us * 32 * 3456 + c0 * 2;
      v4u qv;
      if (ex) {
        const int cA = sel(vx, 36, 1152), cB = sel(vx, 72, 36);
        qv = v4u{__builtin_amdgcn_raw_buffer_load_b32(rq, qo, qs, 0), __builtin_amdgcn_raw_buffer_load_b32(rq, qo, qs + cA, 0),
                 __builtin_amdgcn_raw_buffer_load_b32(rq, qo, qs + cB, 0), __builtin_amdgcn_raw_buffer_load_b32(rq, qo, qs + cA + cB, 0)};
      } else qv = __builtin_amdgcn_raw_buffer_load_b128(rq, qo, qs, 0);
      SB();
      const int of = 3 * j + w, oc = (g >> 2) * 81 + of, os = (oc * 57) >> 9, ot = oc - os * 9;
      const bool hasO = w < 3 && of < 81;
      const bool ox = os >= 16;
      const int orow = quad * sel(ox, 72, 4) + ((lane >> 1) & 1) + ((lane >> 2) & 1) * sel(ox, 18, 2) + ((lane >> 3) & 1) * sel(ox, 36, 18);
      const rsrc_t ro = __builtin_amdgcn_make_buffer_rsrc((void*)p.wo, 0, 576 * 576 * 2, 0x00020000);
      const v4u ov = __builtin_amdgcn_raw_buffer_load_b128(ro, orow * 1152 + (lane & 1) * 16 + sel(hasO, 0, OOR),
                                                           sel(ox, 288 * (os - 16) + 16, 36 * os) * 1152 + 32 * ((g & 3) * 9 + ot), 0);
      SB();
      const int xoff = (64 * j + 32 * ((w >> 1) & 1) + (w >> 2) + 2 * xrow) * 1152 + (w & 1) * 576 + (lane & 3) * 16;
      __builtin_amdgcn_s_barrier();
      LOAD_X();
      STAMP(41);
      *(LAS v4u*)(T + (lane >> 1) * 48 + (lane & 1) * 16) = (vx && (lane & 1)) ? v4u{0u, 0u, 0u, 0u} : qv;
      *(LAS v4u*)(T + 1536 + (lane >> 1) * 48 + (lane & 1) * 16) = ov;
      LAS unsigned char* R = T + (8 * quad + (l15 >> 2)) * 48 + (lane & 3) * 8;
      const v2u a = __builtin_bit_cast(v2u, __builtin_amdgcn_ds_read_tr16_b64_v4i16((LAS s16x4*)(R)));
      const v2u b = __builtin_bit_cast(v2u, __builtin_amdgcn_ds_read_tr16_b64_v4i16((LAS s16x4*)(R + 4 * 48)));
      const v2u c = __builtin_bit_cast(v2u, __builtin_amdgcn_ds_read_tr16_b64_v4i16((LAS s16x4*)(R + 1536)));
      const v2u d = __builtin_bit_cast(v2u, __builtin_amdgcn_ds_read_tr16_b64_v4i16((LAS s16x4*)(R + 1536 + 4 * 48)));
      stg(v4u{a.x, a.y, b.x, b.y}, rs, lane16,
          sel(uf >= 252, OOR, wb + sel(tile < 8, (tile & 3) * 36 + us * 2 + (tile >> 2), sel(tile < 10, 72 + tile * 18 + us, (tile >> 1) * 36 - 36 + us * 2 + (tile & 1))) * KB));
      stg(v4u{c.x, c.y, d.x, d.y}, rs, lane16, sel(hasO, WOB + ((g & 3) * 162 + ot * 18 + os) * KB, OOR));
      const float cq = rows_sum(dot2(a.x, ONES, dot2(a.y, ONES, dot2(b.x, ONES, dot2(b.y, ONES, 0.f)))));
      __builtin_amdgcn_raw_buffer_store_b32(__float_as_uint(cq), rs, sel(lane < 16, lane * 4, OOR) + sel(uf < 252, CSQ + ((g * 18 + us) * 14 + tile) * 64, OOR), 0, SC1);
      SB();
      STAMP(43);
      DRAIN();
      STAMP(44);
      GET_TAG();
      if (lane == 0 && __hip_atomic_fetch_add((LAS int*)(L + L_HC) + C_P0, 1, __ATOMIC_RELAXED, __HIP_MEMORY_SCOPE_WORKGROUP) == 7)
        set_flag(p.ws, SFLAGS + (g * 32 + j) * 8, tag);
      STAGE_X();
      STAMP(2);
      BAR();
      STAMP(3);
      issue_window<0, WIN>(win, rs, lane16, QBASE());
    }
    if (opq_s(w) >= 8) {
      LAS unsigned char* T = L + 125 * KB + (w - 8) * 4608;
      const int item = 4 * j + w - 8, rbk = (item * 57) >> 9, cb = item - rbk * 9, kind = sel(rbk >= 6, 1, 0), k3 = rbk - kind * 6;
      const int rn = sel(kind, 4, 8), ld = sel(kind, 1152, 9216);
      const rsrc_t rw = __builtin_amdgcn_make_buffer_rsrc((void*)(kind == 0 ? p.w1 : p.w2), 0, 576 * 4608 * 2, 0x00020000);
      const int jj = lane >> 3;
      const int woff = (jj & 3) * ld + (lane & 7) * 16 + sel(item < 108, 96 * k3 * ld + g * sel(kind, 576 * 1152, 1152) + 128 * cb, OOR);
      const int xoff = (sel(j > 0, 64 * j - 16, 0) + 2 * (xrow & 7) + (xrow >> 3)) * 1152 + (lane & 3) * 16 + sel(w < 10, (w & 1) * 576, OOR);
      if (w == 11 && lane < 16) ((volatile LAS int*)(L + L_HC))[lane] = 0;
      BAR();
      LOAD_X();
      v4u wv[3][4];
#pragma unroll
      for (int n = 0; n < 4; ++n)
#pragma unroll
        for (int c = 0; c < 3; ++c)
          wv[c][n] = __builtin_amdgcn_raw_buffer_load_b128(rw, woff + (jj >> 2) * (sel(kind, c < 2 ? 16 : 48, 4) * ld),
                                                           (n * rn + sel(kind, c < 2 ? 48 * c : 32, 32 * c)) * ld, NTW ? 2 : 0);
      SB();
      STAMP(41);
      GET_TAG();
      STAGE_X();
      float cs[4] = {0.f, 0.f, 0.f, 0.f};
      LAS unsigned char* R = T + (8 * quad + (l15 >> 2)) * 144 + (lane & 3) * 8;
#pragma unroll
      for (int c = 0; c < 3; ++c) {
#pragma unroll
        for (int n = 0; n < 4; ++n) *(LAS v4u*)(T + (n * 8 + (lane >> 3)) * 144 + (lane & 7) * 16) = wv[c][n];
#pragma unroll
        for (int tt = 0; tt < 4; ++tt) {
          const v2u a = __builtin_bit_cast(v2u, __builtin_amdgcn_ds_read_tr16_b64_v4i16((LAS s16x4*)(R + tt * 32)));
          const v2u b = __builtin_bit_cast(v2u, __builtin_amdgcn_ds_read_tr16_b64_v4i16((LAS s16x4*)(R + tt * 32 + 4 * 144)));
          wv[c][tt] = v4u{a.x, a.y, b.x, b.y};
          cs[tt] = dot2(a.x, ONES, dot2(a.y, ONES, dot2(b.x, ONES, dot2(b.y, ONES, cs[tt]))));
        }
      }
#pragma unroll
      for (int tt = 0; tt < 4; ++tt) { cs[tt] = rows_sum(cs[tt]); OPAQUE(cs[tt]); }
      float csum = quad == 0 ? cs[0] : quad == 1 ? cs[1] : quad == 2 ? cs[2] : cs[3];
      asm volatile("" : "+v"(csum));
      if (opq_s(w) == 11) {
        cnt_wait(L, C_P0, 8);
        wait_flags<4>(p.ws, SFLAGS + ((g ^ ((lane >> 3) & 4)) * 32 + (lane & 31)) * 8, true, tag);
#pragma unroll
        for (int i = 0; i < 18; ++i) win[i] = ldg(rs, lane16, CSQ + (g * 18 + i) * 896);
      }
      STAMP(2);
      BAR();
      STAMP(3);
      row_table1<2>(L, w - 8, lane);
      if (opq_s(w) == 11) {
        f32x4 cs = __builtin_bit_cast(f32x4, win[0]);
#pragma unroll
        for (int i = 1; i < 18; ++i) add4(cs, win[i]);
        if (lane < 56) *(LAS f32x4*)(L + L_TQ + lane16) = cs;
        row_table1<2>(L, 4, lane);
      }
      __builtin_amdgcn_s_sleep(BDELAY);
      STAMP(45);
      const int dst0 = sel(item < 108, wb + (252 + 54 * kind + 9 * k3) * KB, OOR);
#pragma unroll
      for (int tt = 0; tt < 4; ++tt)
#pragma unroll
        for (int c = 0; c < 3; ++c) stg(wv[c][tt], rs, lane16 + c * 3 * KB, dst0 + ((((4 * cb + tt) * 43) >> 7) * 105 + 4 * cb + tt) * KB);
      __builtin_amdgcn_raw_buffer_store_b32(__float_as_uint(csum), rs,
                                            lane * 4 + sel(kind == 0, CS1 + (k3 * 4608 + 576 * g + 64 * cb) * 4, OOR), 0, SC1);
#pragma unroll
      for (int i = 0; i < 5; ++i) {
        const int n = (w - 8) * 5 + i;
        stg(v4u{POISON, POISON, POISON, POISON}, rs, lane16, sel(n < 18, ab + sel(n < 9, A_O, A_X1 - 9 * KB) + (g * 9 + n) * KB, OOR));
      }
      SB();
      if (opq_s(w) == 8) issue_window<0, WIN>(win, rs, lane16, QBASE());
    }
  }
  int os0[3];
  const int wq = opq_s(w0);
  {
    const int T = 2 * (g0 >> 2) + sel(wq >= 6, 1, 0), s0 = 3 * sel(wq >= 6, wq - 6, wq);
#pragma unroll
    for (int i = 0; i < 3; ++i) {
      const int s = s0 + i;
      os0[i] = ABASE + j0 * ASL + A_O + sel(s < 16, ((s >> 1) * 9 + T * 2 + (s & 1)) * KB, ((s - 16) * 36 + 8) * KB + T * 256);
    }
  }
  const int oq0 = sel(wq == 5 || wq == 11, 9 * KB, 256);
  const int ft0 = (g0 & 3) * 9 + sel(wq < 9, wq, 0), xs0 = ((g0 >> 2) * 36 + (ft0 >> 1)) * KB + (ft0 & 1) * 512;

#pragma unroll 1
  for (int layer = 0; layer < 2; ++layer) {
    v4u xres;
    {
      LOCALS();
      STAMP(4 + layer * 16);
      {
        const int qbase = QBASE(), nbase = NBASE(), rw = 5 + 5 * layer;
        if (opq_s(w) < 4) qkv_tiles<2, 5, 2, 7, 4, WIN>(win, L, rs, lane, qbase, nbase, w, rw STAMP_PASS);
        if ((opq_s(w) & 14) == 4) qkv_tiles<2, 4, 0, 7, 1, 0>(win, L, rs, lane, qbase, nbase, 2 * w + 2, rw STAMP_PASS);
        if (opq_s(w) == 6) qkv_tiles<1, 5, 0, 10, 0, WIN>(win, L, rs, lane, qbase, nbase, 8, rw STAMP_PASS);
        if (opq_s(w) == 7) qkv_tiles<1, 5, 1, 10, 0, WIN>(win, L, rs, lane, qbase, nbase, 9, rw STAMP_PASS);
      }
      if (layer == 1 && bid == 0 && w == 10) {
#pragma unroll 1
        for (int i = 0; i < 4; ++i) wait_flags(p.ws, SFLAGS + (256 + i * 64 + lane) * 8, true, tag);
        if (lane == 0) set_flag(p.ws, 0, tag);
      }
      STAMP(5 + layer * 16);
      xres = *(LAS v4u*)(L + opq_s(xs0) + (quad & 1) * 18 * KB + (quad >> 1) * 256 + l15 * 16);
      if (layer == 0 && w >= 8) DRAIN();
      if ((opq_s(w) & 12) == 4) {
        BAR();
        if (w == 7 && layer == 0) {
          if (lane == 0) set_flag(p.ws, FLAGS + j * 128 + g * 8, tag);
          wait_flags(p.ws, FLAGS + j * 128 + (lane & 7) * 8, lane < 8, tag);
        }
        if (layer == 0 && w == 6) {
          if (lane == 0) set_flag(p.ws, SFLAGS + (256 + g * 32 + j) * 8, tag);
        }
        if (w < 6) issue_window<0, WIN>(win, rs, lane16, NBASE());
      }
      if ((opq_s(w) & 12) != 4) {
        const int T = w & 3, hp = w >> 3, Tp = (T & 1) ? T - 1 : 4;
        f32x4 mp, ms;
        const float neg = __uint_as_float(vconst<0xf149f2cau>());
        const int lo = l15 + ((T & 1) ? 9 : 1 + 4 * T), hi = (T & 1) ? 16 : j > 0 ? 8 + 4 * T : 0;
#pragma unroll
        for (int r = 0; r < 4; ++r) {
          const int jj = 4 * quad + r;
          mp[r] = ((jj >= lo) & (jj < hi)) ? 0.f : neg;
          ms[r] = (unsigned)(l15 - jj) <= 7u ? 0.f : neg;
        }
        int xs = L_QK + lane * 8 + T * 512, xp = L_QK + lane * 8 + Tp * 512, hs = xs + hp * 5120, hq = xp + hp * 5120;
        int om = ab + A_O + (g * 9 + T * 2 + hp) * KB + lane16;
        int oe = quad == hp ? ab + A_O + (g * 9 + 8) * KB + T * 256 + hp * 8 + l15 * 16 : WS_BYTES;
        unsigned qm0 = quad == 2 * hp ? ~0u : 0u, qm1 = quad == 2 * hp + 1 ? ~0u : 0u, zr = vconst<0>();
        v4u one4 = {vconst<ONES>(), vconst<ONES>(), vconst<ONES>(), vconst<ONES>()};
        asm volatile("" : "+v"(mp), "+v"(ms), "+v"(xs), "+v"(xp), "+v"(hs), "+v"(hq), "+v"(om), "+v"(oe), "+v"(qm0), "+v"(qm1), "+v"(zr), "+v"(one4));
        BAR();
        const v2u kxs = *(LAS v2u*)(L + xs + 40 * 512);
        const unsigned kxp = *(LAS unsigned*)(L + xp + 40 * 512 + 4);
        v2u q[2], ks[2], kp[2], vs[2], vp[2];
#pragma unroll
        for (int hh = 0; hh < 2; ++hh) {
          q[hh] = *(LAS v2u*)(L + hs + (50 + 5 * hh) * 512);
          kp[hh] = *(LAS v2u*)(L + hq + 5 * hh * 512);
          ks[hh] = *(LAS v2u*)(L + hs + 5 * hh * 512);
        }
        SB();
#pragma unroll
        for (int hh = 0; hh < 2; ++hh) {
          vp[hh] = *(LAS v2u*)(L + hq + (20 + 5 * hh) * 512);
          vs[hh] = *(LAS v2u*)(L + hs + (20 + 5 * hh) * 512);
        }
        const v2u vxp = *(LAS v2u*)(L + xp + 45 * 512), vxs = *(LAS v2u*)(L + xs + 45 * 512);
        SB();
        const f32x4 z = {0.f, 0.f, 0.f, 0.f};
        f32x4 sp[2], ss[2], o[2], ox[2], sm[2];
        float mx[2];
#pragma unroll
        for (int hh = 0; hh < 2; ++hh) {
          const v4u qf = {q[hh].x, q[hh].y, kxs.x & (hh ? qm1 : qm0), zr};
          sp[hh] = mfma(v4u{kp[hh].x, kp[hh].y, kxp, zr}, qf, mp);
          ss[hh] = mfma(v4u{ks[hh].x, ks[hh].y, kxs.y, zr}, qf, ms);
        }
#pragma unroll
        for (int hh = 0; hh < 2; ++hh) {
          const float m0 = __builtin_fmaxf(__builtin_fmaxf(sp[hh][0], sp[hh][1]), sp[hh][2]), m1 = __builtin_fmaxf(__builtin_fmaxf(ss[hh][0], ss[hh][1]), ss[hh][2]);
          mx[hh] = __builtin_fmaxf(__builtin_fmaxf(__builtin_fmaxf(__builtin_fmaxf(m0, sp[hh][3]), ss[hh][3]), m1), m1);
        }
        rows_max2(mx[0], mx[1]);
#pragma unroll
        for (int hh = 0; hh < 2; ++hh) {
          const float c = 0.23570226039551584f * 1.4426950408889634f, mc = -c * mx[hh];
          float e[8];
#pragma unroll
          for (int r = 0; r < 4; ++r) {
            e[r] = __builtin_amdgcn_exp2f(__builtin_fmaf(sp[hh][r], c, mc));
            e[4 + r] = __builtin_amdgcn_exp2f(__builtin_fmaf(ss[hh][r], c, mc));
          }
          const v4u pf = {pk(e[0], e[1]), pk(e[2], e[3]), pk(e[4], e[5]), pk(e[6], e[7])};
          sm[hh] = mfma(one4, pf, z);
          o[hh] = mfma(v4u{vp[hh].x, vp[hh].y, vs[hh].x, vs[hh].y}, pf, z);
          ox[hh] = mfma(v4u{vxp.x, vxp.y, vxs.x, vxs.y}, pf, z);
        }
        const float i0 = __builtin_amdgcn_rcpf(sm[0][0]), i1 = __builtin_amdgcn_rcpf(sm[1][0]);
        stg(v4u{pk(o[0][0] * i0, o[0][1] * i0), pk(o[0][2] * i0, o[0][3] * i0), pk(o[1][0] * i1, o[1][1] * i1), pk(o[1][2] * i1, o[1][3] * i1)}, rs, om, 0);
        __builtin_amdgcn_raw_buffer_store_b64(v2u{pk(ox[0][0] * i0, ox[0][1] * i0), pk(ox[1][2] * i1, ox[1][3] * i1)}, rs, oe, 0, SC1);
        STAMP(34);
        SETTLE();
      }
      STAMP(6 + layer * 16);
      BAR();
      STAMP(7 + layer * 16);
    }
    {
      LOCALS();
      const int th = g >> 2, ft = (g & 3) * 9 + w;
      {
        const int ov = quad * opq_s(oq0) + l15 * 16;
        RETRY_BEGIN();
        v4u orr[3];
#pragma unroll
        for (int i = 0; i < 3; ++i) orr[i] = ldg(rs, i ? ov : lane16, os0[i] + layer * ALY);
#pragma unroll
        for (int i = 0; i < 3; ++i) { *(LAS v4u*)(L + (w * 3 + i) * KB + lane16) = orr[i]; seen_ = umax4(seen_, orr[i]); }
        RETRY_END();
      }
      const v4u xr = xres;
      if (layer == 0 && w == 0) wait_flags<2>(p.ws, SFLAGS + (256 + g * 32 + (lane & 31)) * 8, lane < 32, tag);
      BAR();
      STAMP(8 + layer * 16);
      if (w < 9) {
        f32x4 acc[1][2];
        gemm18<1, 2, 0, P2N, 10>(acc, win, L + lane16, rs, lane16, 0, MBASE());
        float a[8];
#pragma unroll
        for (int r = 0; r < 4; ++r) {
          auto s = __builtin_amdgcn_permlane16_swap(__float_as_uint(acc[0][0][r]), __float_as_uint(acc[0][1][r]), false, false);
          a[r] = __uint_as_float(s[0]);
          a[4 + r] = __uint_as_float(s[1]);
        }
        const v4u o = {pk(a[0] + blo(xr.x), a[1] + bhi(xr.x)), pk(a[2] + blo(xr.y), a[3] + bhi(xr.y)),
                       pk(a[4] + blo(xr.z), a[5] + bhi(xr.z)), pk(a[6] + blo(xr.w), a[7] + bhi(xr.w))};
        stg(o, rs, lane16, ab + A_X1 + (th * 36 + ft) * KB);
        STAMP(35);
        SB();
        if (P2N < 3 * DST) issue_window<P2N, 3 * DST>(win, rs, lane16, MBASE());
        else SETTLE();
      }
      if (layer == 0 && opq_s(w) >= 9) {
        const int f4 = (w - 9) * 48 + (lane < 48 ? lane : 0);
        v4u cp[6];
#pragma unroll
        for (int i = 0; i < 6; ++i) cp[i] = ldg(rs, f4 * 16, CS1 + (i * 4608 + 576 * g) * 4);
        const v2u b = *(const GAS v2u*)(p.b1 + 576 * g + 4 * f4);
        *(LAS v2u*)(L + L_B2 + f4 * 8) = *(const GAS v2u*)(p.b2 + 4 * f4);
        f32x4 cs = __builtin_bit_cast(f32x4, cp[0]);
#pragma unroll
        for (int i = 1; i < 6; ++i) add4(cs, cp[i]);
        *(LAS f32x4*)(L + L_T1 + f4 * 16) = cs;
        *(LAS f32x4*)(L + L_T1 + 2304 + f4 * 16) = f32x4{blo(b.x), bhi(b.x), blo(b.y), bhi(b.y)};
      }
      if (w == 10 && lane < 10) ((volatile LAS int*)(L + L_HC))[lane] = 0;
      STAMP(9 + layer * 16);
      BAR();
      STAMP(10 + layer * 16);
    }
    {
      LOCALS();
      const int mbase = MBASE();
      const int T = w & 3, cls = w >> 2;
      if (opq_s(cls) == 0) {
        if (layer == 0) {
          __builtin_amdgcn_s_sleep(PZD);
          const v4u pz = {vconst<POISON>(), vconst<POISON>(), vconst<POISON>(), vconst<POISON>()};
#pragma unroll
          for (int i = 0; i < 8; ++i) {
            const int n = w * 8 + i;
            stg(pz, rs, lane16,
                sel(n < 30, ab + ALY + n * KB + sel(n < 12, A_X2 + g * 12 * KB, sel(n < 21, A_O, A_X1 - 9 * KB) + (g * 9 - 12) * KB), OOR));
          }
        }
      }
      if (opq_s(cls) > 0) {
        v4u xr[9];
        const int xo = (quad >> 1) * KB + ((quad & 1) * 2 + (T & 1)) * 256 + l15 * 16;
        RETRY_BEGIN();
#pragma unroll
        for (int i = 0; i < 9; ++i) xr[i] = ldg(rs, xo + (i & 1) * 2 * KB, ab + A_X1 + ((T >> 1) * 36 + 18 * (cls - 1) + (i >> 1) * 4) * KB);
#pragma unroll
        for (int i = 0; i < 9; ++i) { *(LAS v4u*)(L + (T * 18 + 9 * (cls - 1) + i) * KB + lane16) = xr[i]; seen_ = umax4(seen_, xr[i]); }
        RETRY_END();
        cnt_add(L, C_XA + cls - 1, lane);
        __builtin_amdgcn_s_setprio(3);
        const v4u one4 = {vconst<ONES>(), vconst<ONES>(), vconst<ONES>(), vconst<ONES>()};
        f32x4 sa = {0.f, 0.f, 0.f, 0.f}, sq = {0.f, 0.f, 0.f, 0.f};
        asm volatile("s_nop 1");
#pragma unroll
        for (int i = 0; i < 9; ++i) { sa = mfma(one4, xr[i], sa); sq = mfma(xr[i], xr[i], sq); }
        __builtin_amdgcn_s_setprio(0);
        if (layer == 0 && opq_s(w) >= 9) issue_window<0, 3 * DST>(win, rs, lane16, mbase);
        const float dg = (lane & 2) ? ((lane & 1) ? sq[3] : sq[2]) : ((lane & 1) ? sq[1] : sq[0]);
        if (quad == (l15 >> 2)) *(LAS f32x2*)(L + L_ST + ((T * 16 + l15) * 2 + cls - 1) * 8) = f32x2{sa[0], dg};
        cnt_add(L, C_ST, lane);
      }
      STAMP(11 + layer * 16);
      f32x4 acc[3][4];
#pragma unroll
      for (int t = 0; t < 3; ++t)
#pragma unroll
        for (int m = 0; m < 4; ++m) acc[t][m] = zero4();
      if (opq_s(cls) > 0) { row_table(L, lane); shift_acc(acc, L, w, quad, l15); }
      STAMP(49);
      __builtin_amdgcn_s_setprio(3);
      gemm_up(acc, win, L + lane16, rs, lane16, mbase, L);
      __builtin_amdgcn_s_setprio(0);
      STAMP(12 + layer * 16);
      {
        LAS unsigned char* H = L + L_H + lane16 + 3 * (w >> 1) * KB;
        if (opq_s(cls) == 0) {
          cnt_wait(L, C_RT, 1);
          shift_acc(acc, L, w, quad, l15);
        }
        f32x4 rt[4];
#pragma unroll
        for (int m = 0; m < 4; ++m) rt[m] = *(LAS f32x4*)(L + L_ST + KB + (m * 16 + l15) * 16);
#pragma unroll
        for (int m = 0; m < 4; ++m) {
          const v2u u0 = gelu4(acc[0][m], rt[m][2], rt[m][3], rt[m][1]), u1 = gelu4(acc[1][m], rt[m][2], rt[m][3], rt[m][1]);
          *(LAS v4u*)(H + (m * 18 + (w & 1)) * KB) = v4u{u0.x, u0.y, u1.x, u1.y};
          *(LAS v2u*)(H + (m * 18 + 2) * KB + (w & 1) * 8) = gelu4(acc[2][m], rt[m][2], rt[m][3], rt[m][1]);
        }
        cnt_add(L, C_H0 + cls, lane);
      }
      STAMP(13 + layer * 16);
      __builtin_amdgcn_s_setprio(2);
      gemm_down(acc, win, L + L_H + lane16, rs, lane16, mbase + 54 * KB, L STAMP_PASS);
      __builtin_amdgcn_s_setprio(0);
      STAMP(14 + layer * 16);
      const int pw = ab + A_PT + (g * 12 + w) * 6144;
      const int poff = (quad & 1) * 1536 + (quad >> 1) * 256 + l15 * 16;
#pragma unroll
      for (int t = 0; t < 3; ++t)
#pragma unroll
        for (int m = 0; m < 4; m += 2) {
          const v2u a = {pk(acc[t][m][0], acc[t][m][1]), pk(acc[t][m][2], acc[t][m][3])};
          const v2u b = {pk(acc[t][m + 1][0], acc[t][m + 1][1]), pk(acc[t][m + 1][2], acc[t][m + 1][3])};
          const v4u o = widen(a, b);
          const bool own = m + (quad & 1) == (g >> 1) && (l15 >> 3) == (g & 1);
          stg(o, rs, own ? WS_BYTES : poff + m * 1536 + t * 512, pw);
          if (own) *(LAS v4u*)(L + (((g >> 1) ^ 1) * 18 + w) * KB + ((2 * t + (quad >> 1)) * 8 + (lane & 7)) * 16) = o;
        }
      __builtin_amdgcn_s_setprio(0);
    }
    {
      LOCALS();
      OWNROWS(6);
      const bool act = lane < 48;
      float sum[8];
      {
        const v4u bb = *(LAS v4u*)(L + L_B2 + (act ? c8 : 0) * 16);
        const v4u xr = *(LAS v4u*)(L + (act ? piece : 0)), ow = *(LAS v4u*)(L + (((g >> 1) ^ 1) * 18 + w) * KB + lane16);
#pragma unroll
        for (int e = 0; e < 4; ++e) {
          sum[2 * e] = blo(xr[e]) + blo(ow[e]); OPAQUE(sum[2 * e]); sum[2 * e] += blo(bb[e]); OPAQUE(sum[2 * e]);
          sum[2 * e + 1] = bhi(xr[e]) + bhi(ow[e]) + bhi(bb[e]);
        }
      }
      int roff = (g >> 1) * 1536 + (lane >> 3) * 256 + (8 * (g & 1) + (lane & 7)) * 16 + (act ? 0 : OOR);
      int foff = WFLAGS + ((layer * 32 + j - (lane >> 3)) * 12 + w) * 128 + (lane < 8 ? lane : 3 + 4 * (lane & 1)) * 8;
      asm volatile("" : "+v"(roff), "+v"(foff), "+v"(sum[0]), "+v"(sum[1]), "+v"(sum[2]), "+v"(sum[3]), "+v"(sum[4]), "+v"(sum[5]), "+v"(sum[6]), "+v"(sum[7]));
      if (w >= 8) __builtin_amdgcn_s_barrier();
      DRAIN();
      if (lane == 0) set_flag(p.ws, WFLAGS + ((layer * 32 + j) * 12 + w) * 128 + g * 8, tag);
      STAMP(15 + layer * 16);
      if (layer == 0 && opq_s(w) < 8) {
        if (QA) cnt_wait(L, C_LC, 12);
        issue_window<0, QA>(win, rs, lane16, QBASE());
      }
      __builtin_amdgcn_s_barrier();
      wait_flags(p.ws, foff, lane < (layer == 0 && j > 0 ? 10 : 8), tag);
      if (w < 8) __builtin_amdgcn_s_barrier();
      asm volatile("" ::: "memory");
      STAMP(16 + layer * 16);
      v4u xb;
      {
        v4u pr[7];
#pragma unroll
        for (int i = 0; i < 7; ++i) pr[i] = ldg(rs, roff, ab + A_PT + (((i + 1 + g) & 7) * 12 + w) * 6144);
        SB();
        const unsigned sel_lo = opq_s(0x00003f80), sel_hi = opq_s(0x3f800000);
#pragma unroll
        for (int i = 0; i < 7; ++i)
#pragma unroll
          for (int e = 0; e < 4; ++e) { sum[2 * e] = dot2(pr[i][e], sel_lo, sum[2 * e]); sum[2 * e + 1] = dot2(pr[i][e], sel_hi, sum[2 * e + 1]); }
#pragma unroll
        for (int e = 0; e < 4; ++e) xb[e] = pk(sum[2 * e], sum[2 * e + 1]);
      }
      STAMP(17 + layer * 16);
      if (layer == 0) {
        float s1 = dot2(xb.x, ONES, dot2(xb.y, ONES, dot2(xb.z, ONES, dot2(xb.w, ONES, 0.f))));
        float s2 = dot2(xb.x, xb.x, dot2(xb.y, xb.y, dot2(xb.z, xb.z, dot2(xb.w, xb.w, 0.f))));
        if (!act) { s1 = 0.f; s2 = 0.f; }
        s1 += ror8(s1); OPAQUE(s1); s2 += ror8(s2);
        s1 = rows_sum(s1); OPAQUE(s1); s2 = rows_sum(s2);
        if (!act) xb = v4u{__float_as_uint(s1), __float_as_uint(s2), 0u, 0u};
        if (lane < 56) stg(xb, rs, lane16, ab + ALY + A_X2 + (g * 12 + w) * KB);
        SB();
        if (QB > QA && opq_s(w) < 8) issue_window<QA, QB>(win, rs, lane16, QBASE());
        else SETTLE();
        STAMP(36);
        const bool stl = (lane >> 3) == 6;
        LAS unsigned char* Iw = act ? L + (c8 >> 2) * KB + ((c8 & 3) * 16 + (lane & 7)) * 16 : L + 126 * KB + lane16;
        LAS unsigned char* Sw = L + L_ST + (w * 80 + (lane & 7)) * 8;
        BAR();
        STAMP(37);
        v4u xv[10];
        const int vo = lane < 56 ? lane16 : lane16 + WS_BYTES;
        xv[0] = xb;
        RETRY_BEGIN();
#pragma unroll
        for (int i = 1; i < 10; ++i)
          xv[i] = ldg(rs, vo, ab + ALY + A_X2 + w * KB + (i < 8 ? (i ^ g) * 12 * KB : (3 + 4 * (i - 8)) * 12 * KB - (j > 0 ? ASL : 0)));
        SB();
#pragma unroll
        for (int i = 0; i < 10; ++i) {
          const int mi = i < 8 ? i ^ g : i;
          *(LAS v4u*)(Iw + (act ? (mi >> 1) * 18 * KB + (mi & 1) * 128 : 0)) = xv[i];
          if (i) seen_ = umax4(seen_, xv[i]);
        }
        RETRY_END();
        if (stl) {
#pragma unroll
          for (int i = 0; i < 10; ++i) *(LAS v2u*)(Sw + (i < 8 ? i ^ g : i) * 64) = v2u{xv[i].x, xv[i].y};
        }
        BAR();
        STAMP(18);
        if (QB < WIN && opq_s(w) < 8) issue_window<QB, WIN>(win, rs, lane16, QBASE());
        if (w == 11) row_table1<12>(L, 4, lane);
        if (w >= 8) row_table1<12>(L, w - 8, lane);
        if (opq_s(w) == 8) issue_window<3 * DST, WIN>(win, rs, lane16, QBASE());
        if (opq_s(w) >= 8) issue_window<0, 3 * DST>(win, rs, lane16, QBASE());
      } else {
        const rsrc_t ro = __builtin_amdgcn_make_buffer_rsrc((void*)p.out, 0, 2048 * 576 * 2, 0x00020000);
        __builtin_amdgcn_raw_buffer_store_b128(xb, ro, act ? mytok * 1152 + c8 * 16 : OOR, 0, 0);
      }
    }
  }
  STAMP(40);
  STAMP_DUMP();
}
}

extern "C" void kernel_launch(void* const* d_in, const int* in_sizes, int n_in, void* d_out, int out_size, void* d_ws, size_t ws_size,
                              hipStream_t stream) {
  (void)in_sizes; (void)n_in; (void)out_size; (void)ws_size;
  Params p;
  p.x = (const unsigned short*)d_in[0];
  p.wqkv = (const unsigned short*)d_in[1];
  p.wo = (const unsigned short*)d_in[2];
  p.w1 = (const unsigned short*)d_in[4];
  p.b1 = (const unsigned short*)d_in[5];
  p.w2 = (const unsigned short*)d_in[6];
  p.b2 = (const unsigned short*)d_in[7];
  p.out = (unsigned short*)d_out;
  p.ws = (unsigned char*)d_ws;
  (void)hipFuncSetAttribute((const void*)fused_fwd, hipFuncAttributeMaxDynamicSharedMemorySize, LDS_BYTES);
  hipLaunchKernelGGL(fused_fwd, dim3(256), dim3(NTHR), LDS_BYTES, stream, p);
}
```

```cpp
#define ABS_AT 99
#define ABS_T 0
#include <hip/hip_runtime.h>
#include <hip/hip_bf16.h>
#include <stdint.h>

namespace {
#define LAS __attribute__((address_space(3)))
#define GAS __attribute__((address_space(1)))
typedef unsigned v4u __attribute__((ext_vector_type(4)));
typedef unsigned v2u __attribute__((ext_vector_type(2)));
typedef float f32x4 __attribute__((ext_vector_type(4)));
typedef float f32x2 __attribute__((ext_vector_type(2)));
typedef __bf16 bf16x8 __attribute__((ext_vector_type(8)));
typedef __bf16 bf16x2 __attribute__((ext_vector_type(2)));
typedef short s16x4 __attribute__((ext_vector_type(4)));
typedef unsigned long long u64;
typedef __amdgpu_buffer_rsrc_t rsrc_t;

constexpr int KB = 1024;
constexpr int NTHR = 768;
constexpr int WIN = 18;
#define XB 2
#define P2N 9
#define QA 6
#define QB 12
#define PZD 20
#define BDELAY 50
#define CODEPF 35
#define NTW 1
constexpr int DST = 5;
constexpr float EPS = 1e-5f;
constexpr int SC1 = 16;
constexpr float GC0 = -2.0f * 0.7978845608028654f * 1.4426950408889634f;
constexpr int FLAGS = 4096;
constexpr int SFLAGS = 32 * KB;
constexpr int WFLAGS = 48 * KB;
constexpr int CS1 = 192 * KB;
constexpr int CSQ = 520 * KB;
constexpr int WBASE = 704 * KB;
constexpr int WSL = (252 + 1296) * KB;
constexpr int WOB = WBASE + 8 * WSL;
constexpr int ABASE = 14 * 1024 * KB;
constexpr int IMG = 72 * KB;
constexpr int A_X2 = 0, A_O = 96 * KB, A_X1 = A_O + IMG, A_PT = A_X1 + IMG, ALY = 1024 * KB, ASL = 2 * ALY;
constexpr int WS_BYTES = ABASE + 32 * ASL;
static_assert(WOB + 648 * KB <= ABASE && A_PT + 8 * IMG <= ALY && CSQ + 8 * 14 * 1152 + 256 <= WBASE, "workspace map");
constexpr int L_H = IMG;
constexpr int L_QK = 90 * KB;
constexpr int L_ST = 145 * KB;
constexpr int L_T1 = L_ST + 7680;
constexpr int L_TQ = L_T1 + 4608;
constexpr int L_RT = L_TQ + 896;
constexpr int L_HC = L_RT + 640;
constexpr int L_B2 = L_HC + 64;
constexpr int LDS_BYTES = L_B2 + 1152;
static_assert(LDS_BYTES <= 160 * KB, "LDS");
enum { C_H0 = 0, C_ST = 3, C_RT = 4, C_LC = 5, C_XA = 6, C_R1 = 10, C_P0 = 11 };

struct Params {
  const unsigned short *x, *wqkv, *wo, *w1, *b1, *w2, *b2;
  unsigned short *out;
  unsigned char *ws;
};

#define STAMP(i) do { if ((i) == ABS_AT) { while ((unsigned)__builtin_amdgcn_s_memrealtime() - abs0_ < ABS_T * 100u) __builtin_amdgcn_s_sleep(1); } } while (0)
#define STAMP_INIT() const unsigned abs0_ = (unsigned)__builtin_amdgcn_s_memrealtime()
#define RETRY_COUNT()
#define STAMP_ARG , unsigned abs0_
#define STAMP_PASS , abs0_
#define STAMP_DUMP()
#define SB() __builtin_amdgcn_sched_barrier(0)
#define BAR() do { asm volatile("s_waitcnt lgkmcnt(0)" ::: "memory"); __builtin_amdgcn_s_barrier(); asm volatile("" ::: "memory"); } while (0)
#define DRAIN() asm volatile("s_waitcnt vmcnt(0)" ::: "memory")
#define SETTLE() DRAIN()
#define OPAQUE(x) asm("" : "+v"(x))
#define KEEP(x) asm volatile("" :: "v"(x))

__device__ __forceinline__ f32x4 mfma(v4u a, v4u b, f32x4 c) {
  return __builtin_amdgcn_mfma_f32_16x16x32_bf16(__builtin_bit_cast(bf16x8, a), __builtin_bit_cast(bf16x8, b), c, 0, 0, 0);
}
#define MF(acc, a, b) asm volatile("v_mfma_f32_16x16x32_bf16 %0, %1, %2, %0" : "+v"(acc) : "v"(a), "v"(b))
#define MF0(acc, a, b) asm volatile("v_mfma_f32_16x16x32_bf16 %0, %1, %2, 0" : "=&v"(acc) : "v"(a), "v"(b))
template <int NT, int MT> __device__ __forceinline__ void acc_fence(f32x4 (&acc)[NT][MT]) {
#pragma unroll
  for (int t = 0; t < NT; ++t)
#pragma unroll
    for (int m = 0; m < MT; ++m) asm volatile("" : "+v"(acc[t][m]));
}
#define ACC_IN(acc) do { acc_fence(acc); asm volatile("s_nop 1"); SB(); } while (0)
#define ACC_OUT(acc) do { SB(); asm volatile("s_nop 7\n\ts_nop 1"); acc_fence(acc); } while (0)
__device__ __forceinline__ unsigned pk(float a, float b) {
  f32x2 v = {a, b};
  return __builtin_bit_cast(unsigned, __builtin_convertvector(v, bf16x2));
}
__device__ __forceinline__ float dot2(unsigned a, unsigned b, float c) {
  return __builtin_amdgcn_fdot2_f32_bf16(__builtin_bit_cast(bf16x2, a), __builtin_bit_cast(bf16x2, b), c, false);
}
constexpr unsigned ONES = 0x3f803f80u;
__device__ __forceinline__ float blo(unsigned u) { return __uint_as_float(u << 16); }
__device__ __forceinline__ float bhi(unsigned u) { return __uint_as_float(u & 0xffff0000u); }
__device__ __forceinline__ v4u ldg(rsrc_t rs, int voff, int soff) { return __builtin_amdgcn_raw_buffer_load_b128(rs, voff, soff, SC1); }
__device__ __forceinline__ void stg(v4u v, rsrc_t rs, int voff, int soff) { __builtin_amdgcn_raw_buffer_store_b128(v, rs, voff + soff, 0, SC1); }
__device__ __forceinline__ v4u ldf(rsrc_t rs, int lane16, int base, int f) { return ldg(rs, lane16 + (f & 3) * KB, base + (f >> 2) * 4 * KB); }
__device__ __forceinline__ int opq_v(int x) { asm volatile("" : "+v"(x)); return x; }
__device__ __forceinline__ int lane_id() { int x; asm volatile("v_mbcnt_lo_u32_b32 %0, -1, 0\n\tv_mbcnt_hi_u32_b32 %0, -1, %0" : "=v"(x)); return x; }
template <unsigned C> __device__ __forceinline__ unsigned vconst() { unsigned x; asm volatile("v_mov_b32 %0, %1" : "=v"(x) : "i"(C)); return x; }
__device__ __forceinline__ int sel(bool c, int a, int b) { return c ? a : b; }
constexpr int OOR = 0x10000000;
__device__ __forceinline__ int opq_s(int x) { asm volatile("" : "+s"(x)); return x; }
#define LOCALS() const int lane = lane_id(), w = opq_s(w0), j = opq_s(j0), g = opq_s(g0); \
  const int lane16 = lane * 16, quad = lane >> 4, l15 = lane & 15, ab = ABASE + j * ASL + layer * ALY, wb = WBASE + g * WSL; \
  (void)lane16; (void)quad; (void)l15; (void)ab; (void)wb
#define OWNROWS(CPW) const int c8 = CPW * w + (lane >> 3); \
  const int piece = ((g >> 1) * 18 + (c8 >> 2)) * KB + ((c8 & 3) * 16 + 8 * (g & 1) + (lane & 7)) * 16; \
  const int mytok = 64 * j + 2 * (16 * ((g >> 1) & 1) + 8 * (g & 1) + (lane & 7)) + (g >> 2)
#define CPF_DONE() KEEP(cpf)
#define GET_TAG() CPF_DONE(); tag = 1 + ((u64)(unsigned)__builtin_amdgcn_readfirstlane((int)(unsigned)tag0) | \
                             ((u64)(unsigned)__builtin_amdgcn_readfirstlane((int)(unsigned)(tag0 >> 32)) << 32))
#define NBASE() opq_s(nb0)
#define QBASE() opq_s(qb0)
#define MBASE() opq_s(mb0)
__device__ __forceinline__ float rows_sum(float x) {
  auto s = __builtin_amdgcn_permlane16_swap(__float_as_uint(x), __float_as_uint(x), false, false);
  x = __uint_as_float(s[0]) + __uint_as_float(s[1]);
  auto t = __builtin_amdgcn_permlane32_swap(__float_as_uint(x), __float_as_uint(x), false, false);
  return __uint_as_float(t[0]) + __uint_as_float(t[1]);
}
__device__ __forceinline__ float vmax(unsigned a, unsigned b) { float r; asm("v_max_f32 %0, %1, %2\n\ts_nop 1" : "=v"(r) : "v"(a), "v"(b)); return r; }
__device__ __forceinline__ void rows_max2(float& a, float& b) {
  auto s = __builtin_amdgcn_permlane16_swap(__float_as_uint(a), __float_as_uint(b), false, false);
  float x = vmax(s[0], s[1]);
  auto t = __builtin_amdgcn_permlane32_swap(__float_as_uint(x), __float_as_uint(x), false, false);
  x = vmax(t[0], t[1]);
  auto u = __builtin_amdgcn_permlane16_swap(__float_as_uint(x), __float_as_uint(x), false, false);
  a = __uint_as_float(u[0]);
  b = __uint_as_float(u[1]);
}
template <int CTRL> __device__ __forceinline__ float dpp(float x) {
  return __int_as_float(__builtin_amdgcn_mov_dpp(__float_as_int(x), CTRL, 0xf, 0xf, true));
}
__device__ __forceinline__ float ror8(float x) { return dpp<0x128>(x); }
__device__ __forceinline__ void add4(f32x4& a, v4u b) {
  float a0 = a[0], a1 = a[1], a2 = a[2], a3 = a[3];
  asm("v_add_f32 %0, %0, %4\n\tv_add_f32 %1, %1, %5\n\tv_add_f32 %2, %2, %6\n\tv_add_f32 %3, %3, %7"
      : "+v"(a0), "+v"(a1), "+v"(a2), "+v"(a3) : "v"(b.x), "v"(b.y), "v"(b.z), "v"(b.w));
  a = f32x4{a0, a1, a2, a3};
}
__device__ __forceinline__ v4u widen(v2u a, v2u b) {
  auto s0 = __builtin_amdgcn_permlane16_swap(a.x, b.x, false, false);
  auto s1 = __builtin_amdgcn_permlane16_swap(a.y, b.y, false, false);
  return v4u{s0[0], s1[0], s0[1], s1[1]};
}
__device__ __forceinline__ v2u gelu4(f32x4 a, float k0, float k1, float is) {
  float s0, s1, s2, s3;
  unsigned u0, u1;
  asm("v_mul_f32 %2, %6, %6\n\tv_mul_f32 %3, %7, %7\n\tv_mul_f32 %4, %8, %8\n\tv_mul_f32 %5, %9, %9\n\t"
      "v_fma_f32 %2, %2, %11, %10\n\tv_fma_f32 %3, %3, %11, %10\n\tv_fma_f32 %4, %4, %11, %10\n\tv_fma_f32 %5, %5, %11, %10\n\t"
      "v_mul_f32 %2, %2, %6\n\tv_mul_f32 %3, %3, %7\n\tv_mul_f32 %4, %4, %8\n\tv_mul_f32 %5, %5, %9\n\t"
      "v_exp_f32 %2, %2\n\tv_exp_f32 %3, %3\n\tv_exp_f32 %4, %4\n\tv_exp_f32 %5, %5\n\t"
      "v_fma_f32 %2, %2, %12, %12\n\tv_fma_f32 %3, %3, %12, %12\n\tv_fma_f32 %4, %4, %12, %12\n\tv_fma_f32 %5, %5, %12, %12\n\t"
      "v_rcp_f32 %2, %2\n\tv_rcp_f32 %3, %3\n\tv_rcp_f32 %4, %4\n\tv_rcp_f32 %5, %5\n\t"
      "v_mul_f32 %2, %2, %6\n\tv_mul_f32 %3, %3, %7\n\tv_mul_f32 %4, %4, %8\n\tv_mul_f32 %5, %5, %9\n\t"
      "v_cvt_pk_bf16_f32 %0, %2, %3\n\tv_cvt_pk_bf16_f32 %1, %4, %5"
      : "=&v"(u0), "=&v"(u1), "=&v"(s0), "=&v"(s1), "=&v"(s2), "=&v"(s3)
      : "v"(a[0]), "v"(a[1]), "v"(a[2]), "v"(a[3]), "v"(k0), "v"(k1), "v"(is));
  return v2u{u0, u1};
}
__device__ __forceinline__ void shift4(f32x4& a, f32x4 cs, f32x4 b, float nmean, float is) {
  float a0 = a[0], a1 = a[1], a2 = a[2], a3 = a[3];
  asm("v_fmac_f32 %0, %12, %4\n\tv_fmac_f32 %1, %12, %5\n\tv_fmac_f32 %2, %12, %6\n\tv_fmac_f32 %3, %12, %7\n\t"
      "v_fmac_f32 %0, %13, %8\n\tv_fmac_f32 %1, %13, %9\n\tv_fmac_f32 %2, %13, %10\n\tv_fmac_f32 %3, %13, %11"
      : "+v"(a0), "+v"(a1), "+v"(a2), "+v"(a3)
      : "v"(cs[0]), "v"(cs[1]), "v"(cs[2]), "v"(cs[3]), "v"(b[0]), "v"(b[1]), "v"(b[2]), "v"(b[3]), "v"(nmean), "v"(is));
  a = f32x4{a0, a1, a2, a3};
}
__device__ __forceinline__ int cnt_get(LAS unsigned char* L, int i) { return ((volatile LAS int*)(L + L_HC))[i]; }
__device__ __forceinline__ void cnt_wait(LAS unsigned char* L, int i, int n) {
  while (__builtin_amdgcn_readfirstlane(cnt_get(L, i)) < n) __builtin_amdgcn_s_sleep(1);
  asm volatile("" ::: "memory");
}
__device__ __forceinline__ void cnt_add(LAS unsigned char* L, int i, int lane) {
  asm volatile("s_waitcnt lgkmcnt(0)" ::: "memory");
  if (lane == 0) __hip_atomic_fetch_add((LAS int*)(L + L_HC) + i, 1, __ATOMIC_RELAXED, __HIP_MEMORY_SCOPE_WORKGROUP);
}
template <int NP> __device__ __forceinline__ void row_table1(LAS unsigned char* L, int t5, int lane) {
  const int r = t5 * 16 + (lane & 15), q = lane >> 4;
  float t1 = 0.f, t2 = 0.f;
#pragma unroll
  for (int i = 0; i < (NP + 3) / 4; ++i)
    if (NP % 4 == 0 || q < NP) { const f32x2 v = *(LAS f32x2*)(L + L_ST + ((i * 4 + q) * 80 + r) * 8); t1 += v.x; OPAQUE(t1); t2 += v.y; }
  t1 = rows_sum(t1); OPAQUE(t1); t2 = rows_sum(t2);
  const float nmean = t1 * (-1.0f / 576.0f);
  const float var = fmaxf(__builtin_fmaf(-nmean, nmean, t2 * (1.0f / 576.0f)), 0.f);
  const float rstd = __builtin_amdgcn_rsqf(var + EPS);
  if (q < 2) *(LAS float*)(L + L_RT + q * 320 + r * 4) = q ? nmean * rstd : rstd;
  cnt_add(L, C_R1, lane);
}
__device__ __forceinline__ v2u ln_pack(f32x4 a, f32x4 r, f32x4 c, f32x4 nm) {
  float t0, t1, t2, t3;
  unsigned u0, u1;
  asm("v_mul_f32 %2, %14, %18\n\tv_mul_f32 %3, %15, %19\n\tv_mul_f32 %4, %16, %20\n\tv_mul_f32 %5, %17, %21\n\t"
      "v_fmac_f32 %2, %6, %10\n\tv_fmac_f32 %3, %7, %11\n\tv_fmac_f32 %4, %8, %12\n\tv_fmac_f32 %5, %9, %13\n\t"
      "v_cvt_pk_bf16_f32 %0, %2, %3\n\tv_cvt_pk_bf16_f32 %1, %4, %5"
      : "=&v"(u0), "=&v"(u1), "=&v"(t0), "=&v"(t1), "=&v"(t2), "=&v"(t3)
      : "v"(a[0]), "v"(a[1]), "v"(a[2]), "v"(a[3]), "v"(r[0]), "v"(r[1]), "v"(r[2]), "v"(r[3]), "v"(c[0]), "v"(c[1]), "v"(c[2]), "v"(c[3]),
        "v"(nm[0]), "v"(nm[1]), "v"(nm[2]), "v"(nm[3]));
  return v2u{u0, u1};
}
__device__ __forceinline__ f32x4 zero4() {
  float a, b, c, d;
  asm volatile("v_mov_b32 %0, 0\n\tv_mov_b32 %1, 0\n\tv_mov_b32 %2, 0\n\tv_mov_b32 %3, 0" : "=v"(a), "=v"(b), "=v"(c), "=v"(d));
  return f32x4{a, b, c, d};
}
__device__ __forceinline__ f32x4 splat(float x) { return f32x4{x, x, x, x}; }
__device__ __forceinline__ void row_table(LAS unsigned char* L, int lane) {
  cnt_wait(L, C_ST, 8);
  const f32x4 q = *(LAS f32x4*)(L + L_ST + lane * 16);
  float t1 = q[0] + q[2];
  OPAQUE(t1);
  float nmean = t1 * (-1.0f / 576.0f);
  const float var = fmaxf(__builtin_fmaf(-nmean, nmean, (q[1] + q[3]) * (1.0f / 576.0f)), 0.f);
  const float r1 = __builtin_amdgcn_rsqf(var + EPS);
  const float sc = r1 * __builtin_amdgcn_rsqf(var * r1 * r1 + EPS);
  float k0 = GC0 * sc;
  OPAQUE(k0);
  *(LAS f32x4*)(L + L_ST + KB + lane * 16) = f32x4{nmean, __builtin_amdgcn_rcpf(sc), k0, k0 * (0.044715f * sc * sc)};
  cnt_add(L, C_RT, lane);
}
__device__ __forceinline__ void shift_acc(f32x4 (&acc)[3][4], LAS unsigned char* L, int w, int quad, int l15) {
  f32x2 rt[4];
#pragma unroll
  for (int m = 0; m < 4; ++m) rt[m] = *(LAS f32x2*)(L + L_ST + KB + (m * 16 + l15) * 16);
#pragma unroll
  for (int t = 0; t < 3; ++t) {
    const f32x4 cs = *(LAS f32x4*)(L + L_T1 + (48 * w + 16 * t + 4 * quad) * 4), bb = *(LAS f32x4*)(L + L_T1 + 2304 + (48 * w + 16 * t + 4 * quad) * 4);
#pragma unroll
    for (int m = 0; m < 4; ++m) shift4(acc[t][m], cs, bb, rt[m][0], rt[m][1]);
  }
}
template <int SLEEP = 1> __device__ __forceinline__ void wait_flags(const unsigned char* ws, int off, bool active, u64 tag) {
  const GAS u64* f = (const GAS u64*)(ws + off);
  asm volatile("" : "+s"(tag));
#pragma unroll 1
  for (int spins = 0; spins < (1 << 16); ++spins) {
    const u64 v = active ? __hip_atomic_load(f, __ATOMIC_RELAXED, __HIP_MEMORY_SCOPE_AGENT) : tag;
    if (__all(v == tag)) break;
    __builtin_amdgcn_s_sleep(SLEEP);
  }
  asm volatile("" ::: "memory");
}
__device__ __forceinline__ void set_flag(unsigned char* ws, int off, u64 tag) {
  asm volatile("" : "+s"(tag));
  __hip_atomic_store((GAS u64*)(ws + off), tag, __ATOMIC_RELAXED, __HIP_MEMORY_SCOPE_AGENT);
}
constexpr unsigned POISON = 0xffffffffu;
__device__ __forceinline__ unsigned umax4(unsigned m, v4u v) { return max(max(max(m, v.x), v.y), max(v.z, v.w)); }
#define RETRY_BEGIN() _Pragma("unroll 1") for (int spins_ = 0; spins_ < (1 << 15); ++spins_) { unsigned seen_ = 0
#define RETRY_END() if (!__any(seen_ == POISON)) break; RETRY_COUNT(); __builtin_amdgcn_s_sleep(2); asm volatile("" ::: "memory"); }
template <int LO, int HI> __device__ __forceinline__ void issue_window(v4u (&win)[WIN], rsrc_t rs, int lane16, int base) {
#pragma unroll
  for (int i = LO; i < HI; ++i) win[i] = ldf(rs, lane16, base, i);
  SB();
}

template <int NT, int MT, int SWAP, int NNEXT, int R, int LO, int HI>
__device__ __forceinline__ void gemm18_steps(f32x4 (&acc)[NT][MT], v4u (&win)[WIN], v4u (&act)[R], LAS unsigned char* img, LAS unsigned char* img2, rsrc_t rs,
                                             int lane16, int wbase, int nvo, int nbase) {
#pragma unroll
  for (int n = LO; n < HI; ++n) {
    const int s = n / MT, m = n % MT, nn = n + R - 1;
    if (n < 18 * MT) {
#pragma unroll
      for (int t = 0; t < NT; ++t) {
        if (s == 0) {
          if ((SWAP >> t) & 1) MF0(acc[t][m], act[n % R], win[t]);
          else MF0(acc[t][m], win[t], act[n % R]);
        } else if ((SWAP >> t) & 1) MF(acc[t][m], act[n % R], win[(s * NT + t) % WIN]);
        else MF(acc[t][m], win[(s * NT + t) % WIN], act[n % R]);
      }
      SB();
    }
    if (nn < 18 * MT) act[nn % R] = *(LAS v4u*)(nn % MT < 3 ? img + ((nn % MT) * 18 + nn / MT) * KB : img2 + ((nn % MT - 3) * 18 + nn / MT) * KB);
    if (m == 0 && s > 0) {
#pragma unroll
      for (int t = 0; t < NT; ++t) {
        const int f = (s - 1) * NT + t;
        if (f + WIN < 18 * NT) win[f % WIN] = ldf(rs, lane16, wbase, f + WIN);
        else if (f % WIN < NNEXT) win[f % WIN] = ldf(rs, nvo, nbase, f % WIN);
      }
    }
    SB();
    if (n < 18 * MT) {
      KEEP(act[n % R]);
#pragma unroll
      for (int t = 0; t < NT; ++t) KEEP(win[(s * NT + t) % WIN]);
      SB();
    }
  }
}
template <int NT, int MT, int SWAP, int NNEXT, int R>
__device__ __forceinline__ void gemm18(f32x4 (&acc)[NT][MT], v4u (&win)[WIN], LAS unsigned char* img, rsrc_t rs, int lane16, int wbase, int nbase) {
  v4u act[R];
  LAS unsigned char* img2 = img + 54 * KB;
  if (MT > 3) asm volatile("" : "+v"(img2));
#pragma unroll
  for (int n = 0; n < R - 1; ++n) { act[n] = *(LAS v4u*)(n % MT < 3 ? img + ((n % MT) * 18 + n / MT) * KB : img2 + ((n % MT - 3) * 18 + n / MT) * KB); SB(); }
  gemm18_steps<NT, MT, SWAP, NNEXT, R, 0, 6 * MT>(acc, win, act, img, img2, rs, lane16, wbase, lane16, nbase);
  gemm18_steps<NT, MT, SWAP, NNEXT, R, 6 * MT, 12 * MT>(acc, win, act, img, img2, rs, lane16, wbase, lane16, nbase);
  gemm18_steps<NT, MT, SWAP, NNEXT, R, 12 * MT, 18 * MT + 1>(acc, win, act, img, img2, rs, lane16, wbase, lane16, nbase);
  ACC_OUT(acc);
}
template <int NT, int MT, int SWAP, int R, int tstep, int NNEXT>
__device__ __forceinline__ void qkv_tiles(v4u (&win)[WIN], LAS unsigned char* L, rsrc_t rs, int lane, int wbase, int nbase, int tile0, int rwait STAMP_ARG) {
  f32x4 acc[NT][MT];
  gemm18<NT, MT, SWAP, NNEXT, R>(acc, win, L + lane * 16, rs, lane * 16, wbase, nbase);
  STAMP(38);
  LAS unsigned char *Rq = L + L_RT + (lane & 48), *Rl = L + L_RT + (lane & 15) * 4, *Tq = L + L_TQ + tile0 * 64 + (lane & 48),
                    *Tl = L + L_TQ + tile0 * 64 + (lane & 15) * 4, *X = L + L_QK + lane * 8 + tile0 * 2560;
  asm volatile("" : "+v"(Rq), "+v"(Rl), "+v"(Tq), "+v"(Tl), "+v"(X));
  cnt_wait(L, C_R1, rwait);
  v2u u[NT][MT];
#pragma unroll
  for (int t = 0; t < NT; ++t) {
    if ((SWAP >> t) & 1) {
      const f32x4 cs = splat(*(LAS float*)(Tl + t * tstep * 64));
#pragma unroll
      for (int m = 0; m < MT; ++m) u[t][m] = ln_pack(acc[t][m], *(LAS f32x4*)(Rq + m * 64), cs, *(LAS f32x4*)(Rq + 320 + m * 64));
    } else {
      const f32x4 cs = *(LAS f32x4*)(Tq + t * tstep * 64);
#pragma unroll
      for (int m = 0; m < MT; ++m) u[t][m] = ln_pack(acc[t][m], splat(*(LAS float*)(Rl + m * 64)), cs, splat(*(LAS float*)(Rl + 320 + m * 64)));
    }
  }
#pragma unroll
  for (int t = 0; t < NT; ++t)
#pragma unroll
    for (int m = 0; m < MT; ++m) *(LAS v2u*)(X + (t * tstep * 5 + m) * 512) = u[t][m];
}
template <int C>
__device__ __forceinline__ void mlp_chunk(f32x4 (&acc)[3][4], v4u (&win)[WIN], v4u (&act)[8], LAS unsigned char* img, rsrc_t rs, int lane16, int rb,
                                          LAS unsigned char* L) {
  int hv = 4;
#pragma unroll
  for (int s = 0; s < 6; ++s) {
    if (C != 2 && s == 5) {
      if (__builtin_amdgcn_readfirstlane(hv) < 4) cnt_wait(L, C < 0 ? C_XA + 1 : C_H0 + C + 1, 4);
      asm volatile("" ::: "memory");
      SB();
    }
    if (C == 2 && s == 6 - DST) {
      if (lane16 == 0) __hip_atomic_fetch_add((LAS int*)(L + L_HC) + C_LC, 1, __ATOMIC_RELAXED, __HIP_MEMORY_SCOPE_WORKGROUP);
      SB();
    }
    asm volatile("" :: "v"(act[(s & 1) * 4]), "v"(act[(s & 1) * 4 + 1]), "v"(act[(s & 1) * 4 + 2]), "v"(act[(s & 1) * 4 + 3]), "v"(win[s * 3]),
                 "v"(win[s * 3 + 1]), "v"(win[s * 3 + 2]));
    SB();
#pragma unroll
    for (int t = 0; t < 3; ++t)
#pragma unroll
      for (int m = 0; m < 4; ++m) {
        const int mm = t == 1 ? 3 - m : m;
        if (C == 0 && s == 0) MF0(acc[t][mm], win[t], act[mm]);
        else MF(acc[t][mm], win[s * 3 + t], act[(s & 1) * 4 + mm]);
        SB();
        if (t == 0 && !(C == 2 && s == 5)) act[((s + 1) & 1) * 4 + m] = *(LAS v4u*)(img + (m * 18 + s + 1) * KB);
        if (C != 2 && s == 3 && t == 2 && m == 0) hv = cnt_get(L, C < 0 ? C_XA + 1 : C_H0 + C + 1);
        if (t == 1 && m < 3 && !(C == 2 && s + DST >= 6))
          win[((s + DST) % 6) * 3 + m] = ldf(rs, lane16, rb, (s + DST) * 3 + m);
        SB();
      }
  }
}
__device__ __forceinline__ void gemm_up(f32x4 (&acc)[3][4], v4u (&win)[WIN], LAS unsigned char* img, rsrc_t rs, int lane16, int rb, LAS unsigned char* L) {
  v4u act[8];
  cnt_wait(L, C_XA, 4);
#pragma unroll
  for (int n = 0; n < 4; ++n) { act[n] = *(LAS v4u*)(img + n * 18 * KB); SB(); }
  ACC_IN(acc);
#pragma unroll 1
  for (int c = 0; c < 3; ++c) {
    mlp_chunk<-1>(acc, win, act, img, rs, lane16, rb, L);
    img += 6 * KB;
    rb += 18 * KB;
  }
  ACC_OUT(acc);
}
__device__ __forceinline__ void gemm_down(f32x4 (&acc)[3][4], v4u (&win)[WIN], LAS unsigned char* img, rsrc_t rs, int lane16, int rb, LAS unsigned char* L STAMP_ARG) {
  v4u act[8];
  cnt_wait(L, C_H0, 4);
  STAMP(46);
#pragma unroll
  for (int n = 0; n < 4; ++n) { act[n] = *(LAS v4u*)(img + n * 18 * KB); SB(); }
  mlp_chunk<0>(acc, win, act, img, rs, lane16, rb, L);
  img += 6 * KB;
  asm volatile("" : "+v"(img));
  STAMP(47);
  mlp_chunk<1>(acc, win, act, img, rs, lane16, rb + 18 * KB, L);
  img += 6 * KB;
  asm volatile("" : "+v"(img));
  STAMP(48);
  mlp_chunk<2>(acc, win, act, img, rs, lane16, rb + 36 * KB, L);
  ACC_OUT(acc);
}

__global__ void __launch_bounds__(NTHR) fused_fwd(Params p) {
  extern __shared__ __attribute__((aligned(16))) unsigned char smem[];
  LAS unsigned char* L = (LAS unsigned char*)smem;
  const int w0 = __builtin_amdgcn_readfirstlane(threadIdx.x >> 6);
  const int bid = blockIdx.x;
  u64 pc0;
  asm volatile("s_getpc_b64 %0" : "=s"(pc0));
  unsigned cpf = 0;
  if (threadIdx.x < CODEPF * 16) cpf = *(const GAS unsigned*)(pc0 + threadIdx.x * 64);
  constexpr unsigned MPX = 8 / XB;
  const int j0 = ((blockIdx.x & 7) / XB) * (32 / MPX) + (blockIdx.x >> 3) / MPX;
  const int g0 = ((blockIdx.x & 7) % XB) * MPX + (blockIdx.x >> 3) % MPX;
  const rsrc_t rs = __builtin_amdgcn_make_buffer_rsrc((void*)p.ws, 0, WS_BYTES, 0x00020000);
  const int mb0 = WBASE + g0 * WSL + (252 + w0 * 108) * KB;
  const int nb0 = sel(w0 < 9, WOB + ((g0 & 3) * 9 + w0) * 18 * KB, mb0);
  const int qb0 = sel(w0 < 8, WBASE + g0 * WSL + sel(w0 < 6, w0 * 36, 108 + w0 * 18) * KB, nb0);
  STAMP_INIT();
  STAMP(0);
  const u64 tag0 = __hip_atomic_load((const GAS u64*)p.ws, __ATOMIC_RELAXED, __HIP_MEMORY_SCOPE_AGENT);
  u64 tag = 0;
  v4u win[WIN];
  {
    constexpr int layer = 0;
    LOCALS();
    const rsrc_t rx = __builtin_amdgcn_make_buffer_rsrc((void*)p.x, 0, 2048 * 576 * 2, 0x00020000);
    const int xrow = lane >> 2;
#define LOAD_X() v4u xr[9]; _Pragma("unroll") for (int i = 0; i < 9; ++i) xr[i] = __builtin_amdgcn_raw_buffer_load_b128(rx, xoff + i * 64, 0, 0); SB()
#define STAGE_X() do { float s1 = 0.f, s2 = 0.f; \
      _Pragma("unroll") for (int i = 0; i < 9; ++i) { \
        if (w < 10) *(LAS v4u*)(L + (w * 9 + i) * KB + (lane & 3) * 256 + xrow * 16) = xr[i]; \
        _Pragma("unroll") for (int e = 0; e < 4; ++e) { s1 = dot2(xr[i][e], ONES, s1); s2 = dot2(xr[i][e], xr[i][e], s2); } } \
      s1 += dpp<0xB1>(s1); s2 += dpp<0xB1>(s2); s1 += dpp<0x4E>(s1); s2 += dpp<0x4E>(s2); \
      if (w < 10 && (lane & 3) == 0) *(LAS f32x2*)(L + L_ST + ((w & 1) * 80 + (w >> 1) * 16 + xrow) * 8) = f32x2{s1, s2}; } while (0)
    if (opq_s(w) < 8) {
      LAS unsigned char* T = L + L_QK + w * 3072;
      const int uf = 8 * j + w, uc = sel(uf < 252, uf, 0), us = (uc * 293) >> 12, tp = uc - us * 14;
      const int tile = sel(tp < 4, 10 + tp, sel(tp == 4, 8, sel(tp == 13, 9, tp - 5)));
      const bool vx = tile == 9, ex = (tile & 14) == 8;
      const int c0 = 72 * g + sel(ex, 16, ((tile + sel(tile >= 10, 2, 0)) & 3) * 18) + sel(tile < 4, 576, sel(tile < 8 || vx, 1152, 0));
      const rsrc_t rq = __builtin_amdgcn_make_buffer_rsrc((void*)p.wqkv, 0, 576 * 1728 * 2, 0x00020000);
      const int qo = (lane >> 1) * 3456 + (lane & 1) * sel(ex, sel(vx, 0, 72), 16), qs = us * 32 * 3456 + c0 * 2;
      v4u qv;
      if (ex) {
        const int cA = sel(vx, 36, 1152), cB = sel(vx, 72, 36);
        qv = v4u{__builtin_amdgcn_raw_buffer_load_b32(rq, qo, qs, 0), __builtin_amdgcn_raw_buffer_load_b32(rq, qo, qs + cA, 0),
                 __builtin_amdgcn_raw_buffer_load_b32(rq, qo, qs + cB, 0), __builtin_amdgcn_raw_buffer_load_b32(rq, qo, qs + cA + cB, 0)};
      } else qv = __builtin_amdgcn_raw_buffer_load_b128(rq, qo, qs, 0);
      SB();
      const int of = 3 * j + w, oc = (g >> 2) * 81 + of, os = (oc * 57) >> 9, ot = oc - os * 9;
      const bool hasO = w < 3 && of < 81;
      const bool ox = os >= 16;
      const int orow = quad * sel(ox, 72, 4) + ((lane >> 1) & 1) + ((lane >> 2) & 1) * sel(ox, 18, 2) + ((lane >> 3) & 1) * sel(ox, 36, 18);
      const rsrc_t ro = __builtin_amdgcn_make_buffer_rsrc((void*)p.wo, 0, 576 * 576 * 2, 0x00020000);
      const v4u ov = __builtin_amdgcn_raw_buffer_load_b128(ro, orow * 1152 + (lane & 1) * 16 + sel(hasO, 0, OOR),
                                                           sel(ox, 288 * (os - 16) + 16, 36 * os) * 1152 + 32 * ((g & 3) * 9 + ot), 0);
      SB();
      const int xoff = (64 * j + 32 * ((w >> 1) & 1) + (w >> 2) + 2 * xrow) * 1152 + (w & 1) * 576 + (lane & 3) * 16;
      __builtin_amdgcn_s_barrier();
      LOAD_X();
      STAMP(41);
      *(LAS v4u*)(T + (lane >> 1) * 48 + (lane & 1) * 16) = (vx && (lane & 1)) ? v4u{0u, 0u, 0u, 0u} : qv;
      *(LAS v4u*)(T + 1536 + (lane >> 1) * 48 + (lane & 1) * 16) = ov;
      LAS unsigned char* R = T + (8 * quad + (l15 >> 2)) * 48 + (lane & 3) * 8;
      const v2u a = __builtin_bit_cast(v2u, __builtin_amdgcn_ds_read_tr16_b64_v4i16((LAS s16x4*)(R)));
      const v2u b = __builtin_bit_cast(v2u, __builtin_amdgcn_ds_read_tr16_b64_v4i16((LAS s16x4*)(R + 4 * 48)));
      const v2u c = __builtin_bit_cast(v2u, __builtin_amdgcn_ds_read_tr16_b64_v4i16((LAS s16x4*)(R + 1536)));
      const v2u d = __builtin_bit_cast(v2u, __builtin_amdgcn_ds_read_tr16_b64_v4i16((LAS s16x4*)(R + 1536 + 4 * 48)));
      stg(v4u{a.x, a.y, b.x, b.y}, rs, lane16,
          sel(uf >= 252, OOR, wb + sel(tile < 8, (tile & 3) * 36 + us * 2 + (tile >> 2), sel(tile < 10, 72 + tile * 18 + us, (tile >> 1) * 36 - 36 + us * 2 + (tile & 1))) * KB));
      stg(v4u{c.x, c.y, d.x, d.y}, rs, lane16, sel(hasO, WOB + ((g & 3) * 162 + ot * 18 + os) * KB, OOR));
      const float cq = rows_sum(dot2(a.x, ONES, dot2(a.y, ONES, dot2(b.x, ONES, dot2(b.y, ONES, 0.f)))));
      __builtin_amdgcn_raw_buffer_store_b32(__float_as_uint(cq), rs, sel(lane < 16, lane * 4, OOR) + sel(uf < 252, CSQ + ((g * 18 + us) * 14 + tile) * 64, OOR), 0, SC1);
      SB();
      STAMP(43);
      DRAIN();
      STAMP(44);
      GET_TAG();
      if (lane == 0 && __hip_atomic_fetch_add((LAS int*)(L + L_HC) + C_P0, 1, __ATOMIC_RELAXED, __HIP_MEMORY_SCOPE_WORKGROUP) == 7)
        set_flag(p.ws, SFLAGS + (g * 32 + j) * 8, tag);
      STAGE_X();
      STAMP(2);
      BAR();
      STAMP(3);
      issue_window<0, WIN>(win, rs, lane16, QBASE());
    }
    if (opq_s(w) >= 8) {
      LAS unsigned char* T = L + 125 * KB + (w - 8) * 4608;
      const int item = 4 * j + w - 8, rbk = (item * 57) >> 9, cb = item - rbk * 9, kind = sel(rbk >= 6, 1, 0), k3 = rbk - kind * 6;
      const int rn = sel(kind, 4, 8), ld = sel(kind, 1152, 9216);
      const rsrc_t rw = __builtin_amdgcn_make_buffer_rsrc((void*)(kind == 0 ? p.w1 : p.w2), 0, 576 * 4608 * 2, 0x00020000);
      const int jj = lane >> 3;
      const int woff = (jj & 3) * ld + (lane & 7) * 16 + sel(item < 108, 96 * k3 * ld + g * sel(kind, 576 * 1152, 1152) + 128 * cb, OOR);
      const int xoff = (sel(j > 0, 64 * j - 16, 0) + 2 * (xrow & 7) + (xrow >> 3)) * 1152 + (lane & 3) * 16 + sel(w < 10, (w & 1) * 576, OOR);
      if (w == 11 && lane < 16) ((volatile LAS int*)(L + L_HC))[lane] = 0;
      BAR();
      LOAD_X();
      v4u wv[3][4];
#pragma unroll
      for (int n = 0; n < 4; ++n)
#pragma unroll
        for (int c = 0; c < 3; ++c)
          wv[c][n] = __builtin_amdgcn_raw_buffer_load_b128(rw, woff + (jj >> 2) * (sel(kind, c < 2 ? 16 : 48, 4) * ld),
                                                           (n * rn + sel(kind, c < 2 ? 48 * c : 32, 32 * c)) * ld, NTW ? 2 : 0);
      SB();
      STAMP(41);
      GET_TAG();
      STAGE_X();
      float cs[4] = {0.f, 0.f, 0.f, 0.f};
      LAS unsigned char* R = T + (8 * quad + (l15 >> 2)) * 144 + (lane & 3) * 8;
#pragma unroll
      for (int c = 0; c < 3; ++c) {
#pragma unroll
        for (int n = 0; n < 4; ++n) *(LAS v4u*)(T + (n * 8 + (lane >> 3)) * 144 + (lane & 7) * 16) = wv[c][n];
#pragma unroll
        for (int tt = 0; tt < 4; ++tt) {
          const v2u a = __builtin_bit_cast(v2u, __builtin_amdgcn_ds_read_tr16_b64_v4i16((LAS s16x4*)(R + tt * 32)));
          const v2u b = __builtin_bit_cast(v2u, __builtin_amdgcn_ds_read_tr16_b64_v4i16((LAS s16x4*)(R + tt * 32 + 4 * 144)));
          wv[c][tt] = v4u{a.x, a.y, b.x, b.y};
          cs[tt] = dot2(a.x, ONES, dot2(a.y, ONES, dot2(b.x, ONES, dot2(b.y, ONES, cs[tt]))));
        }
      }
#pragma unroll
      for (int tt = 0; tt < 4; ++tt) { cs[tt] = rows_sum(cs[tt]); OPAQUE(cs[tt]); }
      float csum = quad == 0 ? cs[0] : quad == 1 ? cs[1] : quad == 2 ? cs[2] : cs[3];
      asm volatile("" : "+v"(csum));
      if (opq_s(w) == 11) {
        cnt_wait(L, C_P0, 8);
        wait_flags<4>(p.ws, SFLAGS + ((g ^ ((lane >> 3) & 4)) * 32 + (lane & 31)) * 8, true, tag);
#pragma unroll
        for (int i = 0; i < 18; ++i) win[i] = ldg(rs, lane16, CSQ + (g * 18 + i) * 896);
      }
      STAMP(2);
      BAR();
      STAMP(3);
      row_table1<2>(L, w - 8, lane);
      if (opq_s(w) == 11) {
        f32x4 cs = __builtin_bit_cast(f32x4, win[0]);
#pragma unroll
        for (int i = 1; i < 18; ++i) add4(cs, win[i]);
        if (lane < 56) *(LAS f32x4*)(L + L_TQ + lane16) = cs;
        row_table1<2>(L, 4, lane);
      }
      __builtin_amdgcn_s_sleep(BDELAY);
      STAMP(45);
      const int dst0 = sel(item < 108, wb + (252 + 54 * kind + 9 * k3) * KB, OOR);
#pragma unroll
      for (int tt = 0; tt < 4; ++tt)
#pragma unroll
        for (int c = 0; c < 3; ++c) stg(wv[c][tt], rs, lane16 + c * 3 * KB, dst0 + ((((4 * cb + tt) * 43) >> 7) * 105 + 4 * cb + tt) * KB);
      __builtin_amdgcn_raw_buffer_store_b32(__float_as_uint(csum), rs,
                                            lane * 4 + sel(kind == 0, CS1 + (k3 * 4608 + 576 * g + 64 * cb) * 4, OOR), 0, SC1);
#pragma unroll
      for (int i = 0; i < 5; ++i) {
        const int n = (w - 8) * 5 + i;
        stg(v4u{POISON, POISON, POISON, POISON}, rs, lane16, sel(n < 18, ab + sel(n < 9, A_O, A_X1 - 9 * KB) + (g * 9 + n) * KB, OOR));
      }
      SB();
      if (opq_s(w) == 8) issue_window<0, WIN>(win, rs, lane16, QBASE());
    }
  }
  int os0[3];
  const int wq = opq_s(w0);
  {
    const int T = 2 * (g0 >> 2) + sel(wq >= 6, 1, 0), s0 = 3 * sel(wq >= 6, wq - 6, wq);
#pragma unroll
    for (int i = 0; i < 3; ++i) {
      const int s = s0 + i;
      os0[i] = ABASE + j0 * ASL + A_O + sel(s < 16, ((s >> 1) * 9 + T * 2 + (s & 1)) * KB, ((s - 16) * 36 + 8) * KB + T * 256);
    }
  }
  const int oq0 = sel(wq == 5 || wq == 11, 9 * KB, 256);
  const int ft0 = (g0 & 3) * 9 + sel(wq < 9, wq, 0), xs0 = ((g0 >> 2) * 36 + (ft0 >> 1)) * KB + (ft0 & 1) * 512;

#pragma unroll 1
  for (int layer = 0; layer < 2; ++layer) {
    v4u xres;
    {
      LOCALS();
      STAMP(4 + layer * 16);
      {
        const int qbase = QBASE(), nbase = NBASE(), rw = 5 + 5 * layer;
        if (opq_s(w) < 4) qkv_tiles<2, 5, 2, 7, 4, WIN>(win, L, rs, lane, qbase, nbase, w, rw STAMP_PASS);
        if ((opq_s(w) & 14) == 4) qkv_tiles<2, 4, 0, 7, 1, 0>(win, L, rs, lane, qbase, nbase, 2 * w + 2, rw STAMP_PASS);
        if (opq_s(w) == 6) qkv_tiles<1, 5, 0, 10, 0, WIN>(win, L, rs, lane, qbase, nbase, 8, rw STAMP_PASS);
        if (opq_s(w) == 7) qkv_tiles<1, 5, 1, 10, 0, WIN>(win, L, rs, lane, qbase, nbase, 9, rw STAMP_PASS);
      }
      if (layer == 1 && bid == 0 && w == 10) {
#pragma unroll 1
        for (int i = 0; i < 4; ++i) wait_flags(p.ws, SFLAGS + (256 + i * 64 + lane) * 8, true, tag);
        if (lane == 0) set_flag(p.ws, 0, tag);
      }
      STAMP(5 + layer * 16);
      xres = *(LAS v4u*)(L + opq_s(xs0) + (quad & 1) * 18 * KB + (quad >> 1) * 256 + l15 * 16);
      if (layer == 0 && w >= 8) DRAIN();
      if ((opq_s(w) & 12) == 4) {
        BAR();
        if (w == 7 && layer == 0) {
          if (lane == 0) set_flag(p.ws, FLAGS + j * 128 + g * 8, tag);
          wait_flags(p.ws, FLAGS + j * 128 + (lane & 7) * 8, lane < 8, tag);
        }
        if (layer == 0 && w == 6) {
          if (lane == 0) set_flag(p.ws, SFLAGS + (256 + g * 32 + j) * 8, tag);
        }
        if (w < 6) issue_window<0, WIN>(win, rs, lane16, NBASE());
      }
      if ((opq_s(w) & 12) != 4) {
        const int T = w & 3, hp = w >> 3, Tp = (T & 1) ? T - 1 : 4;
        f32x4 mp, ms;
        const float neg = __uint_as_float(vconst<0xf149f2cau>());
        const int lo = l15 + ((T & 1) ? 9 : 1 + 4 * T), hi = (T & 1) ? 16 : j > 0 ? 8 + 4 * T : 0;
#pragma unroll
        for (int r = 0; r < 4; ++r) {
          const int jj = 4 * quad + r;
          mp[r] = ((jj >= lo) & (jj < hi)) ? 0.f : neg;
          ms[r] = (unsigned)(l15 - jj) <= 7u ? 0.f : neg;
        }
        int xs = L_QK + lane * 8 + T * 512, xp = L_QK + lane * 8 + Tp * 512, hs = xs + hp * 5120, hq = xp + hp * 5120;
        int om = ab + A_O + (g * 9 + T * 2 + hp) * KB + lane16;
        int oe = quad == hp ? ab + A_O + (g * 9 + 8) * KB + T * 256 + hp * 8 + l15 * 16 : WS_BYTES;
        unsigned qm0 = quad == 2 * hp ? ~0u : 0u, qm1 = quad == 2 * hp + 1 ? ~0u : 0u, zr = vconst<0>();
        v4u one4 = {vconst<ONES>(), vconst<ONES>(), vconst<ONES>(), vconst<ONES>()};
        asm volatile("" : "+v"(mp), "+v"(ms), "+v"(xs), "+v"(xp), "+v"(hs), "+v"(hq), "+v"(om), "+v"(oe), "+v"(qm0), "+v"(qm1), "+v"(zr), "+v"(one4));
        BAR();
        const v2u kxs = *(LAS v2u*)(L + xs + 40 * 512);
        const unsigned kxp = *(LAS unsigned*)(L + xp + 40 * 512 + 4);
        v2u q[2], ks[2], kp[2], vs[2], vp[2];
#pragma unroll
        for (int hh = 0; hh < 2; ++hh) {
          q[hh] = *(LAS v2u*)(L + hs + (50 + 5 * hh) * 512);
          kp[hh] = *(LAS v2u*)(L + hq + 5 * hh * 512);
          ks[hh] = *(LAS v2u*)(L + hs + 5 * hh * 512);
        }
        SB();
#pragma unroll
        for (int hh = 0; hh < 2; ++hh) {
          vp[hh] = *(LAS v2u*)(L + hq + (20 + 5 * hh) * 512);
          vs[hh] = *(LAS v2u*)(L + hs + (20 + 5 * hh) * 512);
        }
        const v2u vxp = *(LAS v2u*)(L + xp + 45 * 512), vxs = *(LAS v2u*)(L + xs + 45 * 512);
        SB();
        const f32x4 z = {0.f, 0.f, 0.f, 0.f};
        f32x4 sp[2], ss[2], o[2], ox[2], sm[2];
        float mx[2];
#pragma unroll
        for (int hh = 0; hh < 2; ++hh) {
          const v4u qf = {q[hh].x, q[hh].y, kxs.x & (hh ? qm1 : qm0), zr};
          sp[hh] = mfma(v4u{kp[hh].x, kp[hh].y, kxp, zr}, qf, mp);
          ss[hh] = mfma(v4u{ks[hh].x, ks[hh].y, kxs.y, zr}, qf, ms);
        }
#pragma unroll
        for (int hh = 0; hh < 2; ++hh) {
          const float m0 = __builtin_fmaxf(__builtin_fmaxf(sp[hh][0], sp[hh][1]), sp[hh][2]), m1 = __builtin_fmaxf(__builtin_fmaxf(ss[hh][0], ss[hh][1]), ss[hh][2]);
          mx[hh] = __builtin_fmaxf(__builtin_fmaxf(__builtin_fmaxf(__builtin_fmaxf(m0, sp[hh][3]), ss[hh][3]), m1), m1);
        }
        rows_max2(mx[0], mx[1]);
#pragma unroll
        for (int hh = 0; hh < 2; ++hh) {
          const float c = 0.23570226039551584f * 1.4426950408889634f, mc = -c * mx[hh];
          float e[8];
#pragma unroll
          for (int r = 0; r < 4; ++r) {
            e[r] = __builtin_amdgcn_exp2f(__builtin_fmaf(sp[hh][r], c, mc));
            e[4 + r] = __builtin_amdgcn_exp2f(__builtin_fmaf(ss[hh][r], c, mc));
          }
          const v4u pf = {pk(e[0], e[1]), pk(e[2], e[3]), pk(e[4], e[5]), pk(e[6], e[7])};
          sm[hh] = mfma(one4, pf, z);
          o[hh] = mfma(v4u{vp[hh].x, vp[hh].y, vs[hh].x, vs[hh].y}, pf, z);
          ox[hh] = mfma(v4u{vxp.x, vxp.y, vxs.x, vxs.y}, pf, z);
        }
        const float i0 = __builtin_amdgcn_rcpf(sm[0][0]), i1 = __builtin_amdgcn_rcpf(sm[1][0]);
        stg(v4u{pk(o[0][0] * i0, o[0][1] * i0), pk(o[0][2] * i0, o[0][3] * i0), pk(o[1][0] * i1, o[1][1] * i1), pk(o[1][2] * i1, o[1][3] * i1)}, rs, om, 0);
        __builtin_amdgcn_raw_buffer_store_b64(v2u{pk(ox[0][0] * i0, ox[0][1] * i0), pk(ox[1][2] * i1, ox[1][3] * i1)}, rs, oe, 0, SC1);
        STAMP(34);
        SETTLE();
      }
      STAMP(6 + layer * 16);
      BAR();
      STAMP(7 + layer * 16);
    }
    {
      LOCALS();
      const int th = g >> 2, ft = (g & 3) * 9 + w;
      {
        const int ov = quad * opq_s(oq0) + l15 * 16;
        RETRY_BEGIN();
        v4u orr[3];
#pragma unroll
        for (int i = 0; i < 3; ++i) orr[i] = ldg(rs, i ? ov : lane16, os0[i] + layer * ALY);
#pragma unroll
        for (int i = 0; i < 3; ++i) { *(LAS v4u*)(L + (w * 3 + i) * KB + lane16) = orr[i]; seen_ = umax4(seen_, orr[i]); }
        RETRY_END();
      }
      const v4u xr = xres;
      if (layer == 0 && w == 0) wait_flags<2>(p.ws, SFLAGS + (256 + g * 32 + (lane & 31)) * 8, lane < 32, tag);
      BAR();
      STAMP(8 + layer * 16);
      if (w < 9) {
        f32x4 acc[1][2];
        gemm18<1, 2, 0, P2N, 10>(acc, win, L + lane16, rs, lane16, 0, MBASE());
        float a[8];
#pragma unroll
        for (int r = 0; r < 4; ++r) {
          auto s = __builtin_amdgcn_permlane16_swap(__float_as_uint(acc[0][0][r]), __float_as_uint(acc[0][1][r]), false, false);
          a[r] = __uint_as_float(s[0]);
          a[4 + r] = __uint_as_float(s[1]);
        }
        const v4u o = {pk(a[0] + blo(xr.x), a[1] + bhi(xr.x)), pk(a[2] + blo(xr.y), a[3] + bhi(xr.y)),
                       pk(a[4] + blo(xr.z), a[5] + bhi(xr.z)), pk(a[6] + blo(xr.w), a[7] + bhi(xr.w))};
        stg(o, rs, lane16, ab + A_X1 + (th * 36 + ft) * KB);
        STAMP(35);
        SB();
        if (P2N < 3 * DST) issue_window<P2N, 3 * DST>(win, rs, lane16, MBASE());
        else SETTLE();
      }
      if (layer == 0 && opq_s(w) >= 9) {
        const int f4 = (w - 9) * 48 + (lane < 48 ? lane : 0);
        v4u cp[6];
#pragma unroll
        for (int i = 0; i < 6; ++i) cp[i] = ldg(rs, f4 * 16, CS1 + (i * 4608 + 576 * g) * 4);
        const v2u b = *(const GAS v2u*)(p.b1 + 576 * g + 4 * f4);
        *(LAS v2u*)(L + L_B2 + f4 * 8) = *(const GAS v2u*)(p.b2 + 4 * f4);
        f32x4 cs = __builtin_bit_cast(f32x4, cp[0]);
#pragma unroll
        for (int i = 1; i < 6; ++i) add4(cs, cp[i]);
        *(LAS f32x4*)(L + L_T1 + f4 * 16) = cs;
        *(LAS f32x4*)(L + L_T1 + 2304 + f4 * 16) = f32x4{blo(b.x), bhi(b.x), blo(b.y), bhi(b.y)};
      }
      if (w == 10 && lane < 10) ((volatile LAS int*)(L + L_HC))[lane] = 0;
      STAMP(9 + layer * 16);
      BAR();
      STAMP(10 + layer * 16);
    }
    {
      LOCALS();
      const int mbase = MBASE();
      const int T = w & 3, cls = w >> 2;
      if (opq_s(cls) == 0) {
        if (layer == 0) {
          __builtin_amdgcn_s_sleep(PZD);
          const v4u pz = {vconst<POISON>(), vconst<POISON>(), vconst<POISON>(), vconst<POISON>()};
#pragma unroll
          for (int i = 0; i < 8; ++i) {
            const int n = w * 8 + i;
            stg(pz, rs, lane16,
                sel(n < 30, ab + ALY + n * KB + sel(n < 12, A_X2 + g * 12 * KB, sel(n < 21, A_O, A_X1 - 9 * KB) + (g * 9 - 12) * KB), OOR));
          }
        }
      }
      if (opq_s(cls) > 0) {
        v4u xr[9];
        const int xo = (quad >> 1) * KB + ((quad & 1) * 2 + (T & 1)) * 256 + l15 * 16;
        RETRY_BEGIN();
#pragma unroll
        for (int i = 0; i < 9; ++i) xr[i] = ldg(rs, xo + (i & 1) * 2 * KB, ab + A_X1 + ((T >> 1) * 36 + 18 * (cls - 1) + (i >> 1) * 4) * KB);
#pragma unroll
        for (int i = 0; i < 9; ++i) { *(LAS v4u*)(L + (T * 18 + 9 * (cls - 1) + i) * KB + lane16) = xr[i]; seen_ = umax4(seen_, xr[i]); }
        RETRY_END();
        cnt_add(L, C_XA + cls - 1, lane);
        __builtin_amdgcn_s_setprio(3);
        const v4u one4 = {vconst<ONES>(), vconst<ONES>(), vconst<ONES>(), vconst<ONES>()};
        f32x4 sa = {0.f, 0.f, 0.f, 0.f}, sq = {0.f, 0.f, 0.f, 0.f};
        asm volatile("s_nop 1");
#pragma unroll
        for (int i = 0; i < 9; ++i) { sa = mfma(one4, xr[i], sa); sq = mfma(xr[i], xr[i], sq); }
        __builtin_amdgcn_s_setprio(0);
        if (layer == 0 && opq_s(w) >= 9) issue_window<0, 3 * DST>(win, rs, lane16, mbase);
        const float dg = (lane & 2) ? ((lane & 1) ? sq[3] : sq[2]) : ((lane & 1) ? sq[1] : sq[0]);
        if (quad == (l15 >> 2)) *(LAS f32x2*)(L + L_ST + ((T * 16 + l15) * 2 + cls - 1) * 8) = f32x2{sa[0], dg};
        cnt_add(L, C_ST, lane);
      }
      STAMP(11 + layer * 16);
      f32x4 acc[3][4];
#pragma unroll
      for (int t = 0; t < 3; ++t)
#pragma unroll
        for (int m = 0; m < 4; ++m) acc[t][m] = zero4();
      if (opq_s(cls) > 0) { row_table(L, lane); shift_acc(acc, L, w, quad, l15); }
      STAMP(49);
      __builtin_amdgcn_s_setprio(3);
      gemm_up(acc, win, L + lane16, rs, lane16, mbase, L);
      __builtin_amdgcn_s_setprio(0);
      STAMP(12 + layer * 16);
      {
        LAS unsigned char* H = L + L_H + lane16 + 3 * (w >> 1) * KB;
        if (opq_s(cls) == 0) {
          cnt_wait(L, C_RT, 1);
          shift_acc(acc, L, w, quad, l15);
        }
        f32x4 rt[4];
#pragma unroll
        for (int m = 0; m < 4; ++m) rt[m] = *(LAS f32x4*)(L + L_ST + KB + (m * 16 + l15) * 16);
#pragma unroll
        for (int m = 0; m < 4; ++m) {
          const v2u u0 = gelu4(acc[0][m], rt[m][2], rt[m][3], rt[m][1]), u1 = gelu4(acc[1][m], rt[m][2], rt[m][3], rt[m][1]);
          *(LAS v4u*)(H + (m * 18 + (w & 1)) * KB) = v4u{u0.x, u0.y, u1.x, u1.y};
          *(LAS v2u*)(H + (m * 18 + 2) * KB + (w & 1) * 8) = gelu4(acc[2][m], rt[m][2], rt[m][3], rt[m][1]);
        }
        cnt_add(L, C_H0 + cls, lane);
      }
      STAMP(13 + layer * 16);
      __builtin_amdgcn_s_setprio(2);
      gemm_down(acc, win, L + L_H + lane16, rs, lane16, mbase + 54 * KB, L STAMP_PASS);
      __builtin_amdgcn_s_setprio(0);
      STAMP(14 + layer * 16);
      const int pw = ab + A_PT + (g * 12 + w) * 6144;
      const int poff = (quad & 1) * 1536 + (quad >> 1) * 256 + l15 * 16;
#pragma unroll
      for (int t = 0; t < 3; ++t)
#pragma unroll
        for (int m = 0; m < 4; m += 2) {
          const v2u a = {pk(acc[t][m][0], acc[t][m][1]), pk(acc[t][m][2], acc[t][m][3])};
          const v2u b = {pk(acc[t][m + 1][0], acc[t][m + 1][1]), pk(acc[t][m + 1][2], acc[t][m + 1][3])};
          const v4u o = widen(a, b);
          const bool own = m + (quad & 1) == (g >> 1) && (l15 >> 3) == (g & 1);
          stg(o, rs, own ? WS_BYTES : poff + m * 1536 + t * 512, pw);
          if (own) *(LAS v4u*)(L + (((g >> 1) ^ 1) * 18 + w) * KB + ((2 * t + (quad >> 1)) * 8 + (lane & 7)) * 16) = o;
        }
      __builtin_amdgcn_s_setprio(0);
    }
    {
      LOCALS();
      OWNROWS(6);
      const bool act = lane < 48;
      float sum[8];
      {
        const v4u bb = *(LAS v4u*)(L + L_B2 + (act ? c8 : 0) * 16);
        const v4u xr = *(LAS v4u*)(L + (act ? piece : 0)), ow = *(LAS v4u*)(L + (((g >> 1) ^ 1) * 18 + w) * KB + lane16);
#pragma unroll
        for (int e = 0; e < 4; ++e) {
          sum[2 * e] = blo(xr[e]) + blo(ow[e]); OPAQUE(sum[2 * e]); sum[2 * e] += blo(bb[e]); OPAQUE(sum[2 * e]);
          sum[2 * e + 1] = bhi(xr[e]) + bhi(ow[e]) + bhi(bb[e]);
        }
      }
      int roff = (g >> 1) * 1536 + (lane >> 3) * 256 + (8 * (g & 1) + (lane & 7)) * 16 + (act ? 0 : OOR);
      int foff = WFLAGS + ((layer * 32 + j - (lane >> 3)) * 12 + w) * 128 + (lane < 8 ? lane : 3 + 4 * (lane & 1)) * 8;
      asm volatile("" : "+v"(roff), "+v"(foff), "+v"(sum[0]), "+v"(sum[1]), "+v"(sum[2]), "+v"(sum[3]), "+v"(sum[4]), "+v"(sum[5]), "+v"(sum[6]), "+v"(sum[7]));
      if (w >= 8) __builtin_amdgcn_s_barrier();
      DRAIN();
      if (lane == 0) set_flag(p.ws, WFLAGS + ((layer * 32 + j) * 12 + w) * 128 + g * 8, tag);
      STAMP(15 + layer * 16);
      if (layer == 0 && opq_s(w) < 8) {
        if (QA) cnt_wait(L, C_LC, 12);
        issue_window<0, QA>(win, rs, lane16, QBASE());
      }
      __builtin_amdgcn_s_barrier();
      wait_flags(p.ws, foff, lane < (layer == 0 && j > 0 ? 10 : 8), tag);
      if (w < 8) __builtin_amdgcn_s_barrier();
      asm volatile("" ::: "memory");
      STAMP(16 + layer * 16);
      v4u xb;
      {
        v4u pr[7];
#pragma unroll
        for (int i = 0; i < 7; ++i) pr[i] = ldg(rs, roff, ab + A_PT + (((i + 1 + g) & 7) * 12 + w) * 6144);
        SB();
        const unsigned sel_lo = opq_s(0x00003f80), sel_hi = opq_s(0x3f800000);
#pragma unroll
        for (int i = 0; i < 7; ++i)
#pragma unroll
          for (int e = 0; e < 4; ++e) { sum[2 * e] = dot2(pr[i][e], sel_lo, sum[2 * e]); sum[2 * e + 1] = dot2(pr[i][e], sel_hi, sum[2 * e + 1]); }
#pragma unroll
        for (int e = 0; e < 4; ++e) xb[e] = pk(sum[2 * e], sum[2 * e + 1]);
      }
      STAMP(17 + layer * 16);
      if (layer == 0) {
        float s1 = dot2(xb.x, ONES, dot2(xb.y, ONES, dot2(xb.z, ONES, dot2(xb.w, ONES, 0.f))));
        float s2 = dot2(xb.x, xb.x, dot2(xb.y, xb.y, dot2(xb.z, xb.z, dot2(xb.w, xb.w, 0.f))));
        if (!act) { s1 = 0.f; s2 = 0.f; }
        s1 += ror8(s1); OPAQUE(s1); s2 += ror8(s2);
        s1 = rows_sum(s1); OPAQUE(s1); s2 = rows_sum(s2);
        if (!act) xb = v4u{__float_as_uint(s1), __float_as_uint(s2), 0u, 0u};
        if (lane < 56) stg(xb, rs, lane16, ab + ALY + A_X2 + (g * 12 + w) * KB);
        SB();
        if (QB > QA && opq_s(w) < 8) issue_window<QA, QB>(win, rs, lane16, QBASE());
        else SETTLE();
        STAMP(36);
        const bool stl = (lane >> 3) == 6;
        LAS unsigned char* Iw = act ? L + (c8 >> 2) * KB + ((c8 & 3) * 16 + (lane & 7)) * 16 : L + 126 * KB + lane16;
        LAS unsigned char* Sw = L + L_ST + (w * 80 + (lane & 7)) * 8;
        BAR();
        STAMP(37);
        v4u xv[10];
        const int vo = lane < 56 ? lane16 : lane16 + WS_BYTES;
        xv[0] = xb;
        RETRY_BEGIN();
#pragma unroll
        for (int i = 1; i < 10; ++i)
          xv[i] = ldg(rs, vo, ab + ALY + A_X2 + w * KB + (i < 8 ? (i ^ g) * 12 * KB : (3 + 4 * (i - 8)) * 12 * KB - (j > 0 ? ASL : 0)));
        SB();
#pragma unroll
        for (int i = 0; i < 10; ++i) {
          const int mi = i < 8 ? i ^ g : i;
          *(LAS v4u*)(Iw + (act ? (mi >> 1) * 18 * KB + (mi & 1) * 128 : 0)) = xv[i];
          if (i) seen_ = umax4(seen_, xv[i]);
        }
        RETRY_END();
        if (stl) {
#pragma unroll
          for (int i = 0; i < 10; ++i) *(LAS v2u*)(Sw + (i < 8 ? i ^ g : i) * 64) = v2u{xv[i].x, xv[i].y};
        }
        BAR();
        STAMP(18);
        if (QB < WIN && opq_s(w) < 8) issue_window<QB, WIN>(win, rs, lane16, QBASE());
        if (w == 11) row_table1<12>(L, 4, lane);
        if (w >= 8) row_table1<12>(L, w - 8, lane);
        if (opq_s(w) == 8) issue_window<3 * DST, WIN>(win, rs, lane16, QBASE());
        if (opq_s(w) >= 8) issue_window<0, 3 * DST>(win, rs, lane16, QBASE());
      } else {
        const rsrc_t ro = __builtin_amdgcn_make_buffer_rsrc((void*)p.out, 0, 2048 * 576 * 2, 0x00020000);
        __builtin_amdgcn_raw_buffer_store_b128(xb, ro, act ? mytok * 1152 + c8 * 16 : OOR, 0, 0);
      }
    }
  }
  STAMP(40);
  STAMP_DUMP();
}
}

extern "C" void kernel_launch(void* const* d_in, const int* in_sizes, int n_in, void* d_out, int out_size, void* d_ws, size_t ws_size,
                              hipStream_t stream) {
  (void)in_sizes; (void)n_in; (void)out_size; (void)ws_size;
  Params p;
  p.x = (const unsigned short*)d_in[0];
  p.wqkv = (const unsigned short*)d_in[1];
  p.wo = (const unsigned short*)d_in[2];
  p.w1 = (const unsigned short*)d_in[4];
  p.b1 = (const unsigned short*)d_in[5];
  p.w2 = (const unsigned short*)d_in[6];
  p.b2 = (const unsigned short*)d_in[7];
  p.out = (unsigned short*)d_out;
  p.ws = (unsigned char*)d_ws;
  (void)hipFuncSetAttribute((const void*)fused_fwd, hipFuncAttributeMaxDynamicSharedMemorySize, LDS_BYTES);
  hipLaunchKernelGGL(fused_fwd, dim3(256), dim3(NTHR), LDS_BYTES, stream, p);
}
```

```cpp
#define ABS_AT 99
#define ABS_T 100
#include <hip/hip_runtime.h>
#include <hip/hip_bf16.h>
#include <stdint.h>

namespace {
#define LAS __attribute__((address_space(3)))
#define GAS __attribute__((address_space(1)))
typedef unsigned v4u __attribute__((ext_vector_type(4)));
typedef unsigned v2u __attribute__((ext_vector_type(2)));
typedef float f32x4 __attribute__((ext_vector_type(4)));
typedef float f32x2 __attribute__((ext_vector_type(2)));
typedef __bf16 bf16x8 __attribute__((ext_vector_type(8)));
typedef __bf16 bf16x2 __attribute__((ext_vector_type(2)));
typedef short s16x4 __attribute__((ext_vector_type(4)));
typedef unsigned long long u64;
typedef __amdgpu_buffer_rsrc_t rsrc_t;

constexpr int KB = 1024;
constexpr int NTHR = 768;
constexpr int WIN = 18;
#define XB 2
#define P2N 9
#define QA 6
#define QB 12
#define PZD 20
#define BDELAY 50
#define CODEPF 35
#define NTW 1
constexpr int DST = 5;
constexpr float EPS = 1e-5f;
constexpr int SC1 = 16;
constexpr float GC0 = -2.0f * 0.7978845608028654f * 1.4426950408889634f;
constexpr int FLAGS = 4096;
constexpr int SFLAGS = 32 * KB;
constexpr int WFLAGS = 48 * KB;
constexpr int CS1 = 192 * KB;
constexpr int CSQ = 520 * KB;
constexpr int WBASE = 704 * KB;
constexpr int WSL = (252 + 1296) * KB;
constexpr int WOB = WBASE + 8 * WSL;
constexpr int ABASE = 14 * 1024 * KB;
constexpr int IMG = 72 * KB;
constexpr int A_X2 = 0, A_O = 96 * KB, A_X1 = A_O + IMG, A_PT = A_X1 + IMG, ALY = 1024 * KB, ASL = 2 * ALY;
constexpr int WS_BYTES = ABASE + 32 * ASL;
static_assert(WOB + 648 * KB <= ABASE && A_PT + 8 * IMG <= ALY && CSQ + 8 * 14 * 1152 + 256 <= WBASE, "workspace map");
constexpr int L_H = IMG;
constexpr int L_QK = 90 * KB;
constexpr int L_ST = 145 * KB;
constexpr int L_T1 = L_ST + 7680;
constexpr int L_TQ = L_T1 + 4608;
constexpr int L_RT = L_TQ + 896;
constexpr int L_HC = L_RT + 640;
constexpr int L_B2 = L_HC + 64;
constexpr int LDS_BYTES = L_B2 + 1152;
static_assert(LDS_BYTES <= 160 * KB, "LDS");
enum { C_H0 = 0, C_ST = 3, C_RT = 4, C_LC = 5, C_XA = 6, C_R1 = 10, C_P0 = 11 };

struct Params {
  const unsigned short *x, *wqkv, *wo, *w1, *b1, *w2, *b2;
  unsigned short *out;
  unsigned char *ws;
};

#define STAMP(i) do { if ((i) == ABS_AT) { while ((unsigned)__builtin_amdgcn_s_memrealtime() - abs0_ < ABS_T * 100u) __builtin_amdgcn_s_sleep(1); } } while (0)
#define STAMP_INIT()
#define RETRY_COUNT()
#define STAMP_ARG , unsigned abs0_
#define STAMP_PASS , abs0_
#define STAMP_DUMP()
#define SB() __builtin_amdgcn_sched_barrier(0)
#define BAR() do { asm volatile("s_waitcnt lgkmcnt(0)" ::: "memory"); __builtin_amdgcn_s_barrier(); asm volatile("" ::: "memory"); } while (0)
#define DRAIN() asm volatile("s_waitcnt vmcnt(0)" ::: "memory")
#define SETTLE() DRAIN()
#define OPAQUE(x) asm("" : "+v"(x))
#define KEEP(x) asm volatile("" :: "v"(x))

__device__ __forceinline__ f32x4 mfma(v4u a, v4u b, f32x4 c) {
  return __builtin_amdgcn_mfma_f32_16x16x32_bf16(__builtin_bit_cast(bf16x8, a), __builtin_bit_cast(bf16x8, b), c, 0, 0, 0);
}
#define MF(acc, a, b) asm volatile("v_mfma_f32_16x16x32_bf16 %0, %1, %2, %0" : "+v"(acc) : "v"(a), "v"(b))
#define MF0(acc, a, b) asm volatile("v_mfma_f32_16x16x32_bf16 %0, %1, %2, 0" : "=&v"(acc) : "v"(a), "v"(b))
template <int NT, int MT> __device__ __forceinline__ void acc_fence(f32x4 (&acc)[NT][MT]) {
#pragma unroll
  for (int t = 0; t < NT; ++t)
#pragma unroll
    for (int m = 0; m < MT; ++m) asm volatile("" : "+v"(acc[t][m]));
}
#define ACC_IN(acc) do { acc_fence(acc); asm volatile("s_nop 1"); SB(); } while (0)
#define ACC_OUT(acc) do { SB(); asm volatile("s_nop 7\n\ts_nop 1"); acc_fence(acc); } while (0)
__device__ __forceinline__ unsigned pk(float a, float b) {
  f32x2 v = {a, b};
  return __builtin_bit_cast(unsigned, __builtin_convertvector(v, bf16x2));
}
__device__ __forceinline__ float dot2(unsigned a, unsigned b, float c) {
  return __builtin_amdgcn_fdot2_f32_bf16(__builtin_bit_cast(bf16x2, a), __builtin_bit_cast(bf16x2, b), c, false);
}
constexpr unsigned ONES = 0x3f803f80u;
__device__ __forceinline__ float blo(unsigned u) { return __uint_as_float(u << 16); }
__device__ __forceinline__ float bhi(unsigned u) { return __uint_as_float(u & 0xffff0000u); }
__device__ __forceinline__ v4u ldg(rsrc_t rs, int voff, int soff) { return __builtin_amdgcn_raw_buffer_load_b128(rs, voff, soff, SC1); }
__device__ __forceinline__ void stg(v4u v, rsrc_t rs, int voff, int soff) { __builtin_amdgcn_raw_buffer_store_b128(v, rs, voff + soff, 0, SC1); }
__device__ __forceinline__ v4u ldf(rsrc_t rs, int lane16, int base, int f) { return ldg(rs, lane16 + (f & 3) * KB, base + (f >> 2) * 4 * KB); }
__device__ __forceinline__ int opq_v(int x) { asm volatile("" : "+v"(x)); return x; }
__device__ __forceinline__ int lane_id() { int x; asm volatile("v_mbcnt_lo_u32_b32 %0, -1, 0\n\tv_mbcnt_hi_u32_b32 %0, -1, %0" : "=v"(x)); return x; }
template <unsigned C> __device__ __forceinline__ unsigned vconst() { unsigned x; asm volatile("v_mov_b32 %0, %1" : "=v"(x) : "i"(C)); return x; }
__device__ __forceinline__ int sel(bool c, int a, int b) { return c ? a : b; }
constexpr int OOR = 0x10000000;
__device__ __forceinline__ int opq_s(int x) { asm volatile("" : "+s"(x)); return x; }
#define LOCALS() const int lane = lane_id(), w = opq_s(w0), j = opq_s(j0), g = opq_s(g0); \
  const int lane16 = lane * 16, quad = lane >> 4, l15 = lane & 15, ab = ABASE + j * ASL + layer * ALY, wb = WBASE + g * WSL; \
  (void)lane16; (void)quad; (void)l15; (void)ab; (void)wb
#define OWNROWS(CPW) const int c8 = CPW * w + (lane >> 3); \
  const int piece = ((g >> 1) * 18 + (c8 >> 2)) * KB + ((c8 & 3) * 16 + 8 * (g & 1) + (lane & 7)) * 16; \
  const int mytok = 64 * j + 2 * (16 * ((g >> 1) & 1) + 8 * (g & 1) + (lane & 7)) + (g >> 2)
#define CPF_DONE() KEEP(cpf)
#define GET_TAG() CPF_DONE(); tag = 1 + ((u64)(unsigned)__builtin_amdgcn_readfirstlane((int)(unsigned)tag0) | \
                             ((u64)(unsigned)__builtin_amdgcn_readfirstlane((int)(unsigned)(tag0 >> 32)) << 32))
#define NBASE() opq_s(nb0)
#define QBASE() opq_s(qb0)
#define MBASE() opq_s(mb0)
__device__ __forceinline__ float rows_sum(float x) {
  auto s = __builtin_amdgcn_permlane16_swap(__float_as_uint(x), __float_as_uint(x), false, false);
  x = __uint_as_float(s[0]) + __uint_as_float(s[1]);
  auto t = __builtin_amdgcn_permlane32_swap(__float_as_uint(x), __float_as_uint(x), false, false);
  return __uint_as_float(t[0]) + __uint_as_float(t[1]);
}
__device__ __forceinline__ float vmax(unsigned a, unsigned b) { float r; asm("v_max_f32 %0, %1, %2\n\ts_nop 1" : "=v"(r) : "v"(a), "v"(b)); return r; }
__device__ __forceinline__ void rows_max2(float& a, float& b) {
  auto s = __builtin_amdgcn_permlane16_swap(__float_as_uint(a), __float_as_uint(b), false, false);
  float x = vmax(s[0], s[1]);
  auto t = __builtin_amdgcn_permlane32_swap(__float_as_uint(x), __float_as_uint(x), false, false);
  x = vmax(t[0], t[1]);
  auto u = __builtin_amdgcn_permlane16_swap(__float_as_uint(x), __float_as_uint(x), false, false);
  a = __uint_as_float(u[0]);
  b = __uint_as_float(u[1]);
}
template <int CTRL> __device__ __forceinline__ float dpp(float x) {
  return __int_as_float(__builtin_amdgcn_mov_dpp(__float_as_int(x), CTRL, 0xf, 0xf, true));
}
__device__ __forceinline__ float ror8(float x) { return dpp<0x128>(x); }
__device__ __forceinline__ void add4(f32x4& a, v4u b) {
  float a0 = a[0], a1 = a[1], a2 = a[2], a3 = a[3];
  asm("v_add_f32 %0, %0, %4\n\tv_add_f32 %1, %1, %5\n\tv_add_f32 %2, %2, %6\n\tv_add_f32 %3, %3, %7"
      : "+v"(a0), "+v"(a1), "+v"(a2), "+v"(a3) : "v"(b.x), "v"(b.y), "v"(b.z), "v"(b.w));
  a = f32x4{a0, a1, a2, a3};
}
__device__ __forceinline__ v4u widen(v2u a, v2u b) {
  auto s0 = __builtin_amdgcn_permlane16_swap(a.x, b.x, false, false);
  auto s1 = __builtin_amdgcn_permlane16_swap(a.y, b.y, false, false);
  return v4u{s0[0], s1[0], s0[1], s1[1]};
}
__device__ __forceinline__ v2u gelu4(f32x4 a, float k0, float k1, float is) {
  float s0, s1, s2, s3;
  unsigned u0, u1;
  asm("v_mul_f32 %2, %6, %6\n\tv_mul_f32 %3, %7, %7\n\tv_mul_f32 %4, %8, %8\n\tv_mul_f32 %5, %9, %9\n\t"
      "v_fma_f32 %2, %2, %11, %10\n\tv_fma_f32 %3, %3, %11, %10\n\tv_fma_f32 %4, %4, %11, %10\n\tv_fma_f32 %5, %5, %11, %10\n\t"
      "v_mul_f32 %2, %2, %6\n\tv_mul_f32 %3, %3, %7\n\tv_mul_f32 %4, %4, %8\n\tv_mul_f32 %5, %5, %9\n\t"
      "v_exp_f32 %2, %2\n\tv_exp_f32 %3, %3\n\tv_exp_f32 %4, %4\n\tv_exp_f32 %5, %5\n\t"
      "v_fma_f32 %2, %2, %12, %12\n\tv_fma_f32 %3, %3, %12, %12\n\tv_fma_f32 %4, %4, %12, %12\n\tv_fma_f32 %5, %5, %12, %12\n\t"
      "v_rcp_f32 %2, %2\n\tv_rcp_f32 %3, %3\n\tv_rcp_f32 %4, %4\n\tv_rcp_f32 %5, %5\n\t"
      "v_mul_f32 %2, %2, %6\n\tv_mul_f32 %3, %3, %7\n\tv_mul_f32 %4, %4, %8\n\tv_mul_f32 %5, %5, %9\n\t"
      "v_cvt_pk_bf16_f32 %0, %2, %3\n\tv_cvt_pk_bf16_f32 %1, %4, %5"
      : "=&v"(u0), "=&v"(u1), "=&v"(s0), "=&v"(s1), "=&v"(s2), "=&v"(s3)
      : "v"(a[0]), "v"(a[1]), "v"(a[2]), "v"(a[3]), "v"(k0), "v"(k1), "v"(is));
  return v2u{u0, u1};
}
__device__ __forceinline__ void shift4(f32x4& a, f32x4 cs, f32x4 b, float nmean, float is) {
  float a0 = a[0], a1 = a[1], a2 = a[2], a3 = a[3];
  asm("v_fmac_f32 %0, %12, %4\n\tv_fmac_f32 %1, %12, %5\n\tv_fmac_f32 %2, %12, %6\n\tv_fmac_f32 %3, %12, %7\n\t"
      "v_fmac_f32 %0, %13, %8\n\tv_fmac_f32 %1, %13, %9\n\tv_fmac_f32 %2, %13, %10\n\tv_fmac_f32 %3, %13, %11"
      : "+v"(a0), "+v"(a1), "+v"(a2), "+v"(a3)
      : "v"(cs[0]), "v"(cs[1]), "v"(cs[2]), "v"(cs[3]), "v"(b[0]), "v"(b[1]), "v"(b[2]), "v"(b[3]), "v"(nmean), "v"(is));
  a = f32x4{a0, a1, a2, a3};
}
__device__ __forceinline__ int cnt_get(LAS unsigned char* L, int i) { return ((volatile LAS int*)(L + L_HC))[i]; }
__device__ __forceinline__ void cnt_wait(LAS unsigned char* L, int i, int n) {
  while (__builtin_amdgcn_readfirstlane(cnt_get(L, i)) < n) __builtin_amdgcn_s_sleep(1);
  asm volatile("" ::: "memory");
}
__device__ __forceinline__ void cnt_add(LAS unsigned char* L, int i, int lane) {
  asm volatile("s_waitcnt lgkmcnt(0)" ::: "memory");
  if (lane == 0) __hip_atomic_fetch_add((LAS int*)(L + L_HC) + i, 1, __ATOMIC_RELAXED, __HIP_MEMORY_SCOPE_WORKGROUP);
}
template <int NP> __device__ __forceinline__ void row_table1(LAS unsigned char* L, int t5, int lane) {
  const int r = t5 * 16 + (lane & 15), q = lane >> 4;
  float t1 = 0.f, t2 = 0.f;
#pragma unroll
  for (int i = 0; i < (NP + 3) / 4; ++i)
    if (NP % 4 == 0 || q < NP) { const f32x2 v = *(LAS f32x2*)(L + L_ST + ((i * 4 + q) * 80 + r) * 8); t1 += v.x; OPAQUE(t1); t2 += v.y; }
  t1 = rows_sum(t1); OPAQUE(t1); t2 = rows_sum(t2);
  const float nmean = t1 * (-1.0f / 576.0f);
  const float var = fmaxf(__builtin_fmaf(-nmean, nmean, t2 * (1.0f / 576.0f)), 0.f);
  const float rstd = __builtin_amdgcn_rsqf(var + EPS);
  if (q < 2) *(LAS float*)(L + L_RT + q * 320 + r * 4) = q ? nmean * rstd : rstd;
  cnt_add(L, C_R1, lane);
}
__device__ __forceinline__ v2u ln_pack(f32x4 a, f32x4 r, f32x4 c, f32x4 nm) {
  float t0, t1, t2, t3;
  unsigned u0, u1;
  asm("v_mul_f32 %2, %14, %18\n\tv_mul_f32 %3, %15, %19\n\tv_mul_f32 %4, %16, %20\n\tv_mul_f32 %5, %17, %21\n\t"
      "v_fmac_f32 %2, %6, %10\n\tv_fmac_f32 %3, %7, %11\n\tv_fmac_f32 %4, %8, %12\n\tv_fmac_f32 %5, %9, %13\n\t"
      "v_cvt_pk_bf16_f32 %0, %2, %3\n\tv_cvt_pk_bf16_f32 %1, %4, %5"
      : "=&v"(u0), "=&v"(u1), "=&v"(t0), "=&v"(t1), "=&v"(t2), "=&v"(t3)
      : "v"(a[0]), "v"(a[1]), "v"(a[2]), "v"(a[3]), "v"(r[0]), "v"(r[1]), "v"(r[2]), "v"(r[3]), "v"(c[0]), "v"(c[1]), "v"(c[2]), "v"(c[3]),
        "v"(nm[0]), "v"(nm[1]), "v"(nm[2]), "v"(nm[3]));
  return v2u{u0, u1};
}
__device__ __forceinline__ f32x4 zero4() {
  float a, b, c, d;
  asm volatile("v_mov_b32 %0, 0\n\tv_mov_b32 %1, 0\n\tv_mov_b32 %2, 0\n\tv_mov_b32 %3, 0" : "=v"(a), "=v"(b), "=v"(c), "=v"(d));
  return f32x4{a, b, c, d};
}
__device__ __forceinline__ f32x4 splat(float x) { return f32x4{x, x, x, x}; }
__device__ __forceinline__ void row_table(LAS unsigned char* L, int lane) {
  cnt_wait(L, C_ST, 8);
  const f32x4 q = *(LAS f32x4*)(L + L_ST + lane * 16);
  float t1 = q[0] + q[2];
  OPAQUE(t1);
  float nmean = t1 * (-1.0f / 576.0f);
  const float var = fmaxf(__builtin_fmaf(-nmean, nmean, (q[1] + q[3]) * (1.0f / 576.0f)), 0.f);
  const float r1 = __builtin_amdgcn_rsqf(var + EPS);
  const float sc = r1 * __builtin_amdgcn_rsqf(var * r1 * r1 + EPS);
  float k0 = GC0 * sc;
  OPAQUE(k0);
  *(LAS f32x4*)(L + L_ST + KB + lane * 16) = f32x4{nmean, __builtin_amdgcn_rcpf(sc), k0, k0 * (0.044715f * sc * sc)};
  cnt_add(L, C_RT, lane);
}
__device__ __forceinline__ void shift_acc(f32x4 (&acc)[3][4], LAS unsigned char* L, int w, int quad, int l15) {
  f32x2 rt[4];
#pragma unroll
  for (int m = 0; m < 4; ++m) rt[m] = *(LAS f32x2*)(L + L_ST + KB + (m * 16 + l15) * 16);
#pragma unroll
  for (int t = 0; t < 3; ++t) {
    const f32x4 cs = *(LAS f32x4*)(L + L_T1 + (48 * w + 16 * t + 4 * quad) * 4), bb = *(LAS f32x4*)(L + L_T1 + 2304 + (48 * w + 16 * t + 4 * quad) * 4);
#pragma unroll
    for (int m = 0; m < 4; ++m) shift4(acc[t][m], cs, bb, rt[m][0], rt[m][1]);
  }
}
template <int SLEEP = 1> __device__ __forceinline__ void wait_flags(const unsigned char* ws, int off, bool active, u64 tag) {
  const GAS u64* f = (const GAS u64*)(ws + off);
  asm volatile("" : "+s"(tag));
#pragma unroll 1
  for (int spins = 0; spins < (1 << 16); ++spins) {
    const u64 v = active ? __hip_atomic_load(f, __ATOMIC_RELAXED, __HIP_MEMORY_SCOPE_AGENT) : tag;
    if (__all(v == tag)) break;
    __builtin_amdgcn_s_sleep(SLEEP);
  }
  asm volatile("" ::: "memory");
}
__device__ __forceinline__ void set_flag(unsigned char* ws, int off, u64 tag) {
  asm volatile("" : "+s"(tag));
  __hip_atomic_store((GAS u64*)(ws + off), tag, __ATOMIC_RELAXED, __HIP_MEMORY_SCOPE_AGENT);
}
constexpr unsigned POISON = 0xffffffffu;
__device__ __forceinline__ unsigned umax4(unsigned m, v4u v) { return max(max(max(m, v.x), v.y), max(v.z, v.w)); }
#define RETRY_BEGIN() _Pragma("unroll 1") for (int spins_ = 0; spins_ < (1 << 15); ++spins_) { unsigned seen_ = 0
#define RETRY_END() if (!__any(seen_ == POISON)) break; RETRY_COUNT(); __builtin_amdgcn_s_sleep(2); asm volatile("" ::: "memory"); }
template <int LO, int HI> __device__ __forceinline__ void issue_window(v4u (&win)[WIN], rsrc_t rs, int lane16, int base) {
#pragma unroll
  for (int i = LO; i < HI; ++i) win[i] = ldf(rs, lane16, base, i);
  SB();
}

template <int NT, int MT, int SWAP, int NNEXT, int R, int LO, int HI>
__device__ __forceinline__ void gemm18_steps(f32x4 (&acc)[NT][MT], v4u (&win)[WIN], v4u (&act)[R], LAS unsigned char* img, LAS unsigned char* img2, rsrc_t rs,
                                             int lane16, int wbase, int nvo, int nbase) {
#pragma unroll
  for (int n = LO; n < HI; ++n) {
    const int s = n / MT, m = n % MT, nn = n + R - 1;
    if (n < 18 * MT) {
#pragma unroll
      for (int t = 0; t < NT; ++t) {
        if (s == 0) {
          if ((SWAP >> t) & 1) MF0(acc[t][m], act[n % R], win[t]);
          else MF0(acc[t][m], win[t], act[n % R]);
        } else if ((SWAP >> t) & 1) MF(acc[t][m], act[n % R], win[(s * NT + t) % WIN]);
        else MF(acc[t][m], win[(s * NT + t) % WIN], act[n % R]);
      }
      SB();
    }
    if (nn < 18 * MT) act[nn % R] = *(LAS v4u*)(nn % MT < 3 ? img + ((nn % MT) * 18 + nn / MT) * KB : img2 + ((nn % MT - 3) * 18 + nn / MT) * KB);
    if (m == 0 && s > 0) {
#pragma unroll
      for (int t = 0; t < NT; ++t) {
        const int f = (s - 1) * NT + t;
        if (f + WIN < 18 * NT) win[f % WIN] = ldf(rs, lane16, wbase, f + WIN);
        else if (f % WIN < NNEXT) win[f % WIN] = ldf(rs, nvo, nbase, f % WIN);
      }
    }
    SB();
    if (n < 18 * MT) {
      KEEP(act[n % R]);
#pragma unroll
      for (int t = 0; t < NT; ++t) KEEP(win[(s * NT + t) % WIN]);
      SB();
    }
  }
}
template <int NT, int MT, int SWAP, int NNEXT, int R>
__device__ __forceinline__ void gemm18(f32x4 (&acc)[NT][MT], v4u (&win)[WIN], LAS unsigned char* img, rsrc_t rs, int lane16, int wbase, int nbase) {
  v4u act[R];
  LAS unsigned char* img2 = img + 54 * KB;
  if (MT > 3) asm volatile("" : "+v"(img2));
#pragma unroll
  for (int n = 0; n < R - 1; ++n) { act[n] = *(LAS v4u*)(n % MT < 3 ? img + ((n % MT) * 18 + n / MT) * KB : img2 + ((n % MT - 3) * 18 + n / MT) * KB); SB(); }
  gemm18_steps<NT, MT, SWAP, NNEXT, R, 0, 6 * MT>(acc, win, act, img, img2, rs, lane16, wbase, lane16, nbase);
  gemm18_steps<NT, MT, SWAP, NNEXT, R, 6 * MT, 12 * MT>(acc, win, act, img, img2, rs, lane16, wbase, lane16, nbase);
  gemm18_steps<NT, MT, SWAP, NNEXT, R, 12 * MT, 18 * MT + 1>(acc, win, act, img, img2, rs, lane16, wbase, lane16, nbase);
  ACC_OUT(acc);
}
template <int NT, int MT, int SWAP, int R, int tstep, int NNEXT>
__device__ __forceinline__ void qkv_tiles(v4u (&win)[WIN], LAS unsigned char* L, rsrc_t rs, int lane, int wbase, int nbase, int tile0, int rwait STAMP_ARG) {
  f32x4 acc[NT][MT];
  gemm18<NT, MT, SWAP, NNEXT, R>(acc, win, L + lane * 16, rs, lane * 16, wbase, nbase);
  STAMP(38);
  LAS unsigned char *Rq = L + L_RT + (lane & 48), *Rl = L + L_RT + (lane & 15) * 4, *Tq = L + L_TQ + tile0 * 64 + (lane & 48),
                    *Tl = L + L_TQ + tile0 * 64 + (lane & 15) * 4, *X = L + L_QK + lane * 8 + tile0 * 2560;
  asm volatile("" : "+v"(Rq), "+v"(Rl), "+v"(Tq), "+v"(Tl), "+v"(X));
  cnt_wait(L, C_R1, rwait);
  v2u u[NT][MT];
#pragma unroll
  for (int t = 0; t < NT; ++t) {
    if ((SWAP >> t) & 1) {
      const f32x4 cs = splat(*(LAS float*)(Tl + t * tstep * 64));
#pragma unroll
      for (int m = 0; m < MT; ++m) u[t][m] = ln_pack(acc[t][m], *(LAS f32x4*)(Rq + m * 64), cs, *(LAS f32x4*)(Rq + 320 + m * 64));
    } else {
      const f32x4 cs = *(LAS f32x4*)(Tq + t * tstep * 64);
#pragma unroll
      for (int m = 0; m < MT; ++m) u[t][m] = ln_pack(acc[t][m], splat(*(LAS float*)(Rl + m * 64)), cs, splat(*(LAS float*)(Rl + 320 + m * 64)));
    }
  }
#pragma unroll
  for (int t = 0; t < NT; ++t)
#pragma unroll
    for (int m = 0; m < MT; ++m) *(LAS v2u*)(X + (t * tstep * 5 + m) * 512) = u[t][m];
}
template <int C>
__device__ __forceinline__ void mlp_chunk(f32x4 (&acc)[3][4], v4u (&win)[WIN], v4u (&act)[8], LAS unsigned char* img, rsrc_t rs, int lane16, int rb,
                                          LAS unsigned char* L) {
  int hv = 4;
#pragma unroll
  for (int s = 0; s < 6; ++s) {
    if (C != 2 && s == 5) {
      if (__builtin_amdgcn_readfirstlane(hv) < 4) cnt_wait(L, C < 0 ? C_XA + 1 : C_H0 + C + 1, 4);
      asm volatile("" ::: "memory");
      SB();
    }
    if (C == 2 && s == 6 - DST) {
      if (lane16 == 0) __hip_atomic_fetch_add((LAS int*)(L + L_HC) + C_LC, 1, __ATOMIC_RELAXED, __HIP_MEMORY_SCOPE_WORKGROUP);
      SB();
    }
    asm volatile("" :: "v"(act[(s & 1) * 4]), "v"(act[(s & 1) * 4 + 1]), "v"(act[(s & 1) * 4 + 2]), "v"(act[(s & 1) * 4 + 3]), "v"(win[s * 3]),
                 "v"(win[s * 3 + 1]), "v"(win[s * 3 + 2]));
    SB();
#pragma unroll
    for (int t = 0; t < 3; ++t)
#pragma unroll
      for (int m = 0; m < 4; ++m) {
        const int mm = t == 1 ? 3 - m : m;
        if (C == 0 && s == 0) MF0(acc[t][mm], win[t], act[mm]);
        else MF(acc[t][mm], win[s * 3 + t], act[(s & 1) * 4 + mm]);
        SB();
        if (t == 0 && !(C == 2 && s == 5)) act[((s + 1) & 1) * 4 + m] = *(LAS v4u*)(img + (m * 18 + s + 1) * KB);
        if (C != 2 && s == 3 && t == 2 && m == 0) hv = cnt_get(L, C < 0 ? C_XA + 1 : C_H0 + C + 1);
        if (t == 1 && m < 3 && !(C == 2 && s + DST >= 6))
          win[((s + DST) % 6) * 3 + m] = ldf(rs, lane16, rb, (s + DST) * 3 + m);
        SB();
      }
  }
}
__device__ __forceinline__ void gemm_up(f32x4 (&acc)[3][4], v4u (&win)[WIN], LAS unsigned char* img, rsrc_t rs, int lane16, int rb, LAS unsigned char* L) {
  v4u act[8];
  cnt_wait(L, C_XA, 4);
#pragma unroll
  for (int n = 0; n < 4; ++n) { act[n] = *(LAS v4u*)(img + n * 18 * KB); SB(); }
  ACC_IN(acc);
#pragma unroll 1
  for (int c = 0; c < 3; ++c) {
    mlp_chunk<-1>(acc, win, act, img, rs, lane16, rb, L);
    img += 6 * KB;
    rb += 18 * KB;
  }
  ACC_OUT(acc);
}
__device__ __forceinline__ void gemm_down(f32x4 (&acc)[3][4], v4u (&win)[WIN], LAS unsigned char* img, rsrc_t rs, int lane16, int rb, LAS unsigned char* L STAMP_ARG) {
  v4u act[8];
  cnt_wait(L, C_H0, 4);
  STAMP(46);
#pragma unroll
  for (int n = 0; n < 4; ++n) { act[n] = *(LAS v4u*)(img + n * 18 * KB); SB(); }
  mlp_chunk<0>(acc, win, act, img, rs, lane16, rb, L);
  img += 6 * KB;
  asm volatile("" : "+v"(img));
  STAMP(47);
  mlp_chunk<1>(acc, win, act, img, rs, lane16, rb + 18 * KB, L);
  img += 6 * KB;
  asm volatile("" : "+v"(img));
  STAMP(48);
  mlp_chunk<2>(acc, win, act, img, rs, lane16, rb + 36 * KB, L);
  ACC_OUT(acc);
}

__global__ void __launch_bounds__(NTHR) fused_fwd(Params p_) {
  u64 tt_;
  const __attribute__((address_space(4))) Params* pp_ = (const __attribute__((address_space(4))) Params*)__builtin_amdgcn_kernarg_segment_ptr();
  asm volatile("s_memrealtime %0\n\ts_waitcnt lgkmcnt(0)" : "=s"(tt_), "+s"(pp_));
  const unsigned abs0_ = (unsigned)tt_;
  const Params p = {pp_->x, pp_->wqkv, pp_->wo, pp_->w1, pp_->b1, pp_->w2, pp_->b2, pp_->out, pp_->ws};
  extern __shared__ __attribute__((aligned(16))) unsigned char smem[];
  LAS unsigned char* L = (LAS unsigned char*)smem;
  const int w0 = __builtin_amdgcn_readfirstlane(threadIdx.x >> 6);
  const int bid = blockIdx.x;
  u64 pc0;
  asm volatile("s_getpc_b64 %0" : "=s"(pc0));
  unsigned cpf = 0;
  if (threadIdx.x < CODEPF * 16) cpf = *(const GAS unsigned*)(pc0 + threadIdx.x * 64);
  constexpr unsigned MPX = 8 / XB;
  const int j0 = ((blockIdx.x & 7) / XB) * (32 / MPX) + (blockIdx.x >> 3) / MPX;
  const int g0 = ((blockIdx.x & 7) % XB) * MPX + (blockIdx.x >> 3) % MPX;
  const rsrc_t rs = __builtin_amdgcn_make_buffer_rsrc((void*)p.ws, 0, WS_BYTES, 0x00020000);
  const int mb0 = WBASE + g0 * WSL + (252 + w0 * 108) * KB;
  const int nb0 = sel(w0 < 9, WOB + ((g0 & 3) * 9 + w0) * 18 * KB, mb0);
  const int qb0 = sel(w0 < 8, WBASE + g0 * WSL + sel(w0 < 6, w0 * 36, 108 + w0 * 18) * KB, nb0);
  STAMP_INIT();
  STAMP(0);
  const u64 tag0 = __hip_atomic_load((const GAS u64*)p.ws, __ATOMIC_RELAXED, __HIP_MEMORY_SCOPE_AGENT);
  u64 tag = 0;
  v4u win[WIN];
  {
    constexpr int layer = 0;
    LOCALS();
    const rsrc_t rx = __builtin_amdgcn_make_buffer_rsrc((void*)p.x, 0, 2048 * 576 * 2, 0x00020000);
    const int xrow = lane >> 2;
#define LOAD_X() v4u xr[9]; _Pragma("unroll") for (int i = 0; i < 9; ++i) xr[i] = __builtin_amdgcn_raw_buffer_load_b128(rx, xoff + i * 64, 0, 0); SB()
#define STAGE_X() do { float s1 = 0.f, s2 = 0.f; \
      _Pragma("unroll") for (int i = 0; i < 9; ++i) { \
        if (w < 10) *(LAS v4u*)(L + (w * 9 + i) * KB + (lane & 3) * 256 + xrow * 16) = xr[i]; \
        _Pragma("unroll") for (int e = 0; e < 4; ++e) { s1 = dot2(xr[i][e], ONES, s1); s2 = dot2(xr[i][e], xr[i][e], s2); } } \
      s1 += dpp<0xB1>(s1); s2 += dpp<0xB1>(s2); s1 += dpp<0x4E>(s1); s2 += dpp<0x4E>(s2); \
      if (w < 10 && (lane & 3) == 0) *(LAS f32x2*)(L + L_ST + ((w & 1) * 80 + (w >> 1) * 16 + xrow) * 8) = f32x2{s1, s2}; } while (0)
    if (opq_s(w) < 8) {
      LAS unsigned char* T = L + L_QK + w * 3072;
      const int uf = 8 * j + w, uc = sel(uf < 252, uf, 0), us = (uc * 293) >> 12, tp = uc - us * 14;
      const int tile = sel(tp < 4, 10 + tp, sel(tp == 4, 8, sel(tp == 13, 9, tp - 5)));
      const bool vx = tile == 9, ex = (tile & 14) == 8;
      const int c0 = 72 * g + sel(ex, 16, ((tile + sel(tile >= 10, 2, 0)) & 3) * 18) + sel(tile < 4, 576, sel(tile < 8 || vx, 1152, 0));
      const rsrc_t rq = __builtin_amdgcn_make_buffer_rsrc((void*)p.wqkv, 0, 576 * 1728 * 2, 0x00020000);
      const int qo = (lane >> 1) * 3456 + (lane & 1) * sel(ex, sel(vx, 0, 72), 16), qs = us * 32 * 3456 + c0 * 2;
      v4u qv;
      if (ex) {
        const int cA = sel(vx, 36, 1152), cB = sel(vx, 72, 36);
        qv = v4u{__builtin_amdgcn_raw_buffer_load_b32(rq, qo, qs, 0), __builtin_amdgcn_raw_buffer_load_b32(rq, qo, qs + cA, 0),
                 __builtin_amdgcn_raw_buffer_load_b32(rq, qo, qs + cB, 0), __builtin_amdgcn_raw_buffer_load_b32(rq, qo, qs + cA + cB, 0)};
      } else qv = __builtin_amdgcn_raw_buffer_load_b128(rq, qo, qs, 0);
      SB();
      const int of = 3 * j + w, oc = (g >> 2) * 81 + of, os = (oc * 57) >> 9, ot = oc - os * 9;
      const bool hasO = w < 3 && of < 81;
      const bool ox = os >= 16;
      const int orow = quad * sel(ox, 72, 4) + ((lane >> 1) & 1) + ((lane >> 2) & 1) * sel(ox, 18, 2) + ((lane >> 3) & 1) * sel(ox, 36, 18);
      const rsrc_t ro = __builtin_amdgcn_make_buffer_rsrc((void*)p.wo, 0, 576 * 576 * 2, 0x00020000);
      const v4u ov = __builtin_amdgcn_raw_buffer_load_b128(ro, orow * 1152 + (lane & 1) * 16 + sel(hasO, 0, OOR),
                                                           sel(ox, 288 * (os - 16) + 16, 36 * os) * 1152 + 32 * ((g & 3) * 9 + ot), 0);
      SB();
      const int xoff = (64 * j + 32 * ((w >> 1) & 1) + (w >> 2) + 2 * xrow) * 1152 + (w & 1) * 576 + (lane & 3) * 16;
      __builtin_amdgcn_s_barrier();
      LOAD_X();
      STAMP(41);
      *(LAS v4u*)(T + (lane >> 1) * 48 + (lane & 1) * 16) = (vx && (lane & 1)) ? v4u{0u, 0u, 0u, 0u} : qv;
      *(LAS v4u*)(T + 1536 + (lane >> 1) * 48 + (lane & 1) * 16) = ov;
      LAS unsigned char* R = T + (8 * quad + (l15 >> 2)) * 48 + (lane & 3) * 8;
      const v2u a = __builtin_bit_cast(v2u, __builtin_amdgcn_ds_read_tr16_b64_v4i16((LAS s16x4*)(R)));
      const v2u b = __builtin_bit_cast(v2u, __builtin_amdgcn_ds_read_tr16_b64_v4i16((LAS s16x4*)(R + 4 * 48)));
      const v2u c = __builtin_bit_cast(v2u, __builtin_amdgcn_ds_read_tr16_b64_v4i16((LAS s16x4*)(R + 1536)));
      const v2u d = __builtin_bit_cast(v2u, __builtin_amdgcn_ds_read_tr16_b64_v4i16((LAS s16x4*)(R + 1536 + 4 * 48)));
      stg(v4u{a.x, a.y, b.x, b.y}, rs, lane16,
          sel(uf >= 252, OOR, wb + sel(tile < 8, (tile & 3) * 36 + us * 2 + (tile >> 2), sel(tile < 10, 72 + tile * 18 + us, (tile >> 1) * 36 - 36 + us * 2 + (tile & 1))) * KB));
      stg(v4u{c.x, c.y, d.x, d.y}, rs, lane16, sel(hasO, WOB + ((g & 3) * 162 + ot * 18 + os) * KB, OOR));
      const float cq = rows_sum(dot2(a.x, ONES, dot2(a.y, ONES, dot2(b.x, ONES, dot2(b.y, ONES, 0.f)))));
      __builtin_amdgcn_raw_buffer_store_b32(__float_as_uint(cq), rs, sel(lane < 16, lane * 4, OOR) + sel(uf < 252, CSQ + ((g * 18 + us) * 14 + tile) * 64, OOR), 0, SC1);
      SB();
      STAMP(43);
      DRAIN();
      STAMP(44);
      GET_TAG();
      if (lane == 0 && __hip_atomic_fetch_add((LAS int*)(L + L_HC) + C_P0, 1, __ATOMIC_RELAXED, __HIP_MEMORY_SCOPE_WORKGROUP) == 7)
        set_flag(p.ws, SFLAGS + (g * 32 + j) * 8, tag);
      STAGE_X();
      STAMP(2);
      BAR();
      STAMP(3);
      issue_window<0, WIN>(win, rs, lane16, QBASE());
    }
    if (opq_s(w) >= 8) {
      LAS unsigned char* T = L + 125 * KB + (w - 8) * 4608;
      const int item = 4 * j + w - 8, rbk = (item * 57) >> 9, cb = item - rbk * 9, kind = sel(rbk >= 6, 1, 0), k3 = rbk - kind * 6;
      const int rn = sel(kind, 4, 8), ld = sel(kind, 1152, 9216);
      const rsrc_t rw = __builtin_amdgcn_make_buffer_rsrc((void*)(kind == 0 ? p.w1 : p.w2), 0, 576 * 4608 * 2, 0x00020000);
      const int jj = lane >> 3;
      const int woff = (jj & 3) * ld + (lane & 7) * 16 + sel(item < 108, 96 * k3 * ld + g * sel(kind, 576 * 1152, 1152) + 128 * cb, OOR);
      const int xoff = (sel(j > 0, 64 * j - 16, 0) + 2 * (xrow & 7) + (xrow >> 3)) * 1152 + (lane & 3) * 16 + sel(w < 10, (w & 1) * 576, OOR);
      if (w == 11 && lane < 16) ((volatile LAS int*)(L + L_HC))[lane] = 0;
      BAR();
      LOAD_X();
      v4u wv[3][4];
#pragma unroll
      for (int n = 0; n < 4; ++n)
#pragma unroll
        for (int c = 0; c < 3; ++c)
          wv[c][n] = __builtin_amdgcn_raw_buffer_load_b128(rw, woff + (jj >> 2) * (sel(kind, c < 2 ? 16 : 48, 4) * ld),
                                                           (n * rn + sel(kind, c < 2 ? 48 * c : 32, 32 * c)) * ld, NTW ? 2 : 0);
      SB();
      STAMP(41);
      GET_TAG();
      STAGE_X();
      float cs[4] = {0.f, 0.f, 0.f, 0.f};
      LAS unsigned char* R = T + (8 * quad + (l15 >> 2)) * 144 + (lane & 3) * 8;
#pragma unroll
      for (int c = 0; c < 3; ++c) {
#pragma unroll
        for (int n = 0; n < 4; ++n) *(LAS v4u*)(T + (n * 8 + (lane >> 3)) * 144 + (lane & 7) * 16) = wv[c][n];
#pragma unroll
        for (int tt = 0; tt < 4; ++tt) {
          const v2u a = __builtin_bit_cast(v2u, __builtin_amdgcn_ds_read_tr16_b64_v4i16((LAS s16x4*)(R + tt * 32)));
          const v2u b = __builtin_bit_cast(v2u, __builtin_amdgcn_ds_read_tr16_b64_v4i16((LAS s16x4*)(R + tt * 32 + 4 * 144)));
          wv[c][tt] = v4u{a.x, a.y, b.x, b.y};
          cs[tt] = dot2(a.x, ONES, dot2(a.y, ONES, dot2(b.x, ONES, dot2(b.y, ONES, cs[tt]))));
        }
      }
#pragma unroll
      for (int tt = 0; tt < 4; ++tt) { cs[tt] = rows_sum(cs[tt]); OPAQUE(cs[tt]); }
      float csum = quad == 0 ? cs[0] : quad == 1 ? cs[1] : quad == 2 ? cs[2] : cs[3];
      asm volatile("" : "+v"(csum));
      if (opq_s(w) == 11) {
        cnt_wait(L, C_P0, 8);
        wait_flags<4>(p.ws, SFLAGS + ((g ^ ((lane >> 3) & 4)) * 32 + (lane & 31)) * 8, true, tag);
#pragma unroll
        for (int i = 0; i < 18; ++i) win[i] = ldg(rs, lane16, CSQ + (g * 18 + i) * 896);
      }
      STAMP(2);
      BAR();
      STAMP(3);
      row_table1<2>(L, w - 8, lane);
      if (opq_s(w) == 11) {
        f32x4 cs = __builtin_bit_cast(f32x4, win[0]);
#pragma unroll
        for (int i = 1; i < 18; ++i) add4(cs, win[i]);
        if (lane < 56) *(LAS f32x4*)(L + L_TQ + lane16) = cs;
        row_table1<2>(L, 4, lane);
      }
      __builtin_amdgcn_s_sleep(BDELAY);
      STAMP(45);
      const int dst0 = sel(item < 108, wb + (252 + 54 * kind + 9 * k3) * KB, OOR);
#pragma unroll
      for (int tt = 0; tt < 4; ++tt)
#pragma unroll
        for (int c = 0; c < 3; ++c) stg(wv[c][tt], rs, lane16 + c * 3 * KB, dst0 + ((((4 * cb + tt) * 43) >> 7) * 105 + 4 * cb + tt) * KB);
      __builtin_amdgcn_raw_buffer_store_b32(__float_as_uint(csum), rs,
                                            lane * 4 + sel(kind == 0, CS1 + (k3 * 4608 + 576 * g + 64 * cb) * 4, OOR), 0, SC1);
#pragma unroll
      for (int i = 0; i < 5; ++i) {
        const int n = (w - 8) * 5 + i;
        stg(v4u{POISON, POISON, POISON, POISON}, rs, lane16, sel(n < 18, ab + sel(n < 9, A_O, A_X1 - 9 * KB) + (g * 9 + n) * KB, OOR));
      }
      SB();
      if (opq_s(w) == 8) issue_window<0, WIN>(win, rs, lane16, QBASE());
    }
  }
  int os0[3];
  const int wq = opq_s(w0);
  {
    const int T = 2 * (g0 >> 2) + sel(wq >= 6, 1, 0), s0 = 3 * sel(wq >= 6, wq - 6, wq);
#pragma unroll
    for (int i = 0; i < 3; ++i) {
      const int s = s0 + i;
      os0[i] = ABASE + j0 * ASL + A_O + sel(s < 16, ((s >> 1) * 9 + T * 2 + (s & 1)) * KB, ((s - 16) * 36 + 8) * KB + T * 256);
    }
  }
  const int oq0 = sel(wq == 5 || wq == 11, 9 * KB, 256);
  const int ft0 = (g0 & 3) * 9 + sel(wq < 9, wq, 0), xs0 = ((g0 >> 2) * 36 + (ft0 >> 1)) * KB + (ft0 & 1) * 512;

#pragma unroll 1
  for (int layer = 0; layer < 2; ++layer) {
    v4u xres;
    {
      LOCALS();
      STAMP(4 + layer * 16);
      {
        const int qbase = QBASE(), nbase = NBASE(), rw = 5 + 5 * layer;
        if (opq_s(w) < 4) qkv_tiles<2, 5, 2, 7, 4, WIN>(win, L, rs, lane, qbase, nbase, w, rw STAMP_PASS);
        if ((opq_s(w) & 14) == 4) qkv_tiles<2, 4, 0, 7, 1, 0>(win, L, rs, lane, qbase, nbase, 2 * w + 2, rw STAMP_PASS);
        if (opq_s(w) == 6) qkv_tiles<1, 5, 0, 10, 0, WIN>(win, L, rs, lane, qbase, nbase, 8, rw STAMP_PASS);
        if (opq_s(w) == 7) qkv_tiles<1, 5, 1, 10, 0, WIN>(win, L, rs, lane, qbase, nbase, 9, rw STAMP_PASS);
      }
      if (layer == 1 && bid == 0 && w == 10) {
#pragma unroll 1
        for (int i = 0; i < 4; ++i) wait_flags(p.ws, SFLAGS + (256 + i * 64 + lane) * 8, true, tag);
        if (lane == 0) set_flag(p.ws, 0, tag);
      }
      STAMP(5 + layer * 16);
      xres = *(LAS v4u*)(L + opq_s(xs0) + (quad & 1) * 18 * KB + (quad >> 1) * 256 + l15 * 16);
      if (layer == 0 && w >= 8) DRAIN();
      if ((opq_s(w) & 12) == 4) {
        BAR();
        if (w == 7 && layer == 0) {
          if (lane == 0) set_flag(p.ws, FLAGS + j * 128 + g * 8, tag);
          wait_flags(p.ws, FLAGS + j * 128 + (lane & 7) * 8, lane < 8, tag);
        }
        if (layer == 0 && w == 6) {
          if (lane == 0) set_flag(p.ws, SFLAGS + (256 + g * 32 + j) * 8, tag);
        }
        if (w < 6) issue_window<0, WIN>(win, rs, lane16, NBASE());
      }
      if ((opq_s(w) & 12) != 4) {
        const int T = w & 3, hp = w >> 3, Tp = (T & 1) ? T - 1 : 4;
        f32x4 mp, ms;
        const float neg = __uint_as_float(vconst<0xf149f2cau>());
        const int lo = l15 + ((T & 1) ? 9 : 1 + 4 * T), hi = (T & 1) ? 16 : j > 0 ? 8 + 4 * T : 0;
#pragma unroll
        for (int r = 0; r < 4; ++r) {
          const int jj = 4 * quad + r;
          mp[r] = ((jj >= lo) & (jj < hi)) ? 0.f : neg;
          ms[r] = (unsigned)(l15 - jj) <= 7u ? 0.f : neg;
        }
        int xs = L_QK + lane * 8 + T * 512, xp = L_QK + lane * 8 + Tp * 512, hs = xs + hp * 5120, hq = xp + hp * 5120;
        int om = ab + A_O + (g * 9 + T * 2 + hp) * KB + lane16;
        int oe = quad == hp ? ab + A_O + (g * 9 + 8) * KB + T * 256 + hp * 8 + l15 * 16 : WS_BYTES;
        unsigned qm0 = quad == 2 * hp ? ~0u : 0u, qm1 = quad == 2 * hp + 1 ? ~0u : 0u, zr = vconst<0>();
        v4u one4 = {vconst<ONES>(), vconst<ONES>(), vconst<ONES>(), vconst<ONES>()};
        asm volatile("" : "+v"(mp), "+v"(ms), "+v"(xs), "+v"(xp), "+v"(hs), "+v"(hq), "+v"(om), "+v"(oe), "+v"(qm0), "+v"(qm1), "+v"(zr), "+v"(one4));
        BAR();
        const v2u kxs = *(LAS v2u*)(L + xs + 40 * 512);
        const unsigned kxp = *(LAS unsigned*)(L + xp + 40 * 512 + 4);
        v2u q[2], ks[2], kp[2], vs[2], vp[2];
#pragma unroll
        for (int hh = 0; hh < 2; ++hh) {
          q[hh] = *(LAS v2u*)(L + hs + (50 + 5 * hh) * 512);
          kp[hh] = *(LAS v2u*)(L + hq + 5 * hh * 512);
          ks[hh] = *(LAS v2u*)(L + hs + 5 * hh * 512);
        }
        SB();
#pragma unroll
        for (int hh = 0; hh < 2; ++hh) {
          vp[hh] = *(LAS v2u*)(L + hq + (20 + 5 * hh) * 512);
          vs[hh] = *(LAS v2u*)(L + hs + (20 + 5 * hh) * 512);
        }
        const v2u vxp = *(LAS v2u*)(L + xp + 45 * 512), vxs = *(LAS v2u*)(L + xs + 45 * 512);
        SB();
        const f32x4 z = {0.f, 0.f, 0.f, 0.f};
        f32x4 sp[2], ss[2], o[2], ox[2], sm[2];
        float mx[2];
#pragma unroll
        for (int hh = 0; hh < 2; ++hh) {
          const v4u qf = {q[hh].x, q[hh].y, kxs.x & (hh ? qm1 : qm0), zr};
          sp[hh] = mfma(v4u{kp[hh].x, kp[hh].y, kxp, zr}, qf, mp);
          ss[hh] = mfma(v4u{ks[hh].x, ks[hh].y, kxs.y, zr}, qf, ms);
        }
#pragma unroll
        for (int hh = 0; hh < 2; ++hh) {
          const float m0 = __builtin_fmaxf(__builtin_fmaxf(sp[hh][0], sp[hh][1]), sp[hh][2]), m1 = __builtin_fmaxf(__builtin_fmaxf(ss[hh][0], ss[hh][1]), ss[hh][2]);
          mx[hh] = __builtin_fmaxf(__builtin_fmaxf(__builtin_fmaxf(__builtin_fmaxf(m0, sp[hh][3]), ss[hh][3]), m1), m1);
        }
        rows_max2(mx[0], mx[1]);
#pragma unroll
        for (int hh = 0; hh < 2; ++hh) {
          const float c = 0.23570226039551584f * 1.4426950408889634f, mc = -c * mx[hh];
          float e[8];
#pragma unroll
          for (int r = 0; r < 4; ++r) {
            e[r] = __builtin_amdgcn_exp2f(__builtin_fmaf(sp[hh][r], c, mc));
            e[4 + r] = __builtin_amdgcn_exp2f(__builtin_fmaf(ss[hh][r], c, mc));
          }
          const v4u pf = {pk(e[0], e[1]), pk(e[2], e[3]), pk(e[4], e[5]), pk(e[6], e[7])};
          sm[hh] = mfma(one4, pf, z);
          o[hh] = mfma(v4u{vp[hh].x, vp[hh].y, vs[hh].x, vs[hh].y}, pf, z);
          ox[hh] = mfma(v4u{vxp.x, vxp.y, vxs.x, vxs.y}, pf, z);
        }
        const float i0 = __builtin_amdgcn_rcpf(sm[0][0]), i1 = __builtin_amdgcn_rcpf(sm[1][0]);
        stg(v4u{pk(o[0][0] * i0, o[0][1] * i0), pk(o[0][2] * i0, o[0][3] * i0), pk(o[1][0] * i1, o[1][1] * i1), pk(o[1][2] * i1, o[1][3] * i1)}, rs, om, 0);
        __builtin_amdgcn_raw_buffer_store_b64(v2u{pk(ox[0][0] * i0, ox[0][1] * i0), pk(ox[1][2] * i1, ox[1][3] * i1)}, rs, oe, 0, SC1);
        STAMP(34);
        SETTLE();
      }
      STAMP(6 + layer * 16);
      BAR();
      STAMP(7 + layer * 16);
    }
    {
      LOCALS();
      const int th = g >> 2, ft = (g & 3) * 9 + w;
      {
        const int ov = quad * opq_s(oq0) + l15 * 16;
        RETRY_BEGIN();
        v4u orr[3];
#pragma unroll
        for (int i = 0; i < 3; ++i) orr[i] = ldg(rs, i ? ov : lane16, os0[i] + layer * ALY);
#pragma unroll
        for (int i = 0; i < 3; ++i) { *(LAS v4u*)(L + (w * 3 + i) * KB + lane16) = orr[i]; seen_ = umax4(seen_, orr[i]); }
        RETRY_END();
      }
      const v4u xr = xres;
      if (layer == 0 && w == 0) wait_flags<2>(p.ws, SFLAGS + (256 + g * 32 + (lane & 31)) * 8, lane < 32, tag);
      BAR();
      STAMP(8 + layer * 16);
      if (w < 9) {
        f32x4 acc[1][2];
        gemm18<1, 2, 0, P2N, 10>(acc, win, L + lane16, rs, lane16, 0, MBASE());
        float a[8];
#pragma unroll
        for (int r = 0; r < 4; ++r) {
          auto s = __builtin_amdgcn_permlane16_swap(__float_as_uint(acc[0][0][r]), __float_as_uint(acc[0][1][r]), false, false);
          a[r] = __uint_as_float(s[0]);
          a[4 + r] = __uint_as_float(s[1]);
        }
        const v4u o = {pk(a[0] + blo(xr.x), a[1] + bhi(xr.x)), pk(a[2] + blo(xr.y), a[3] + bhi(xr.y)),
                       pk(a[4] + blo(xr.z), a[5] + bhi(xr.z)), pk(a[6] + blo(xr.w), a[7] + bhi(xr.w))};
        stg(o, rs, lane16, ab + A_X1 + (th * 36 + ft) * KB);
        STAMP(35);
        SB();
        if (P2N < 3 * DST) issue_window<P2N, 3 * DST>(win, rs, lane16, MBASE());
        else SETTLE();
      }
      if (layer == 0 && opq_s(w) >= 9) {
        const int f4 = (w - 9) * 48 + (lane < 48 ? lane : 0);
        v4u cp[6];
#pragma unroll
        for (int i = 0; i < 6; ++i) cp[i] = ldg(rs, f4 * 16, CS1 + (i * 4608 + 576 * g) * 4);
        const v2u b = *(const GAS v2u*)(p.b1 + 576 * g + 4 * f4);
        *(LAS v2u*)(L + L_B2 + f4 * 8) = *(const GAS v2u*)(p.b2 + 4 * f4);
        f32x4 cs = __builtin_bit_cast(f32x4, cp[0]);
#pragma unroll
        for (int i = 1; i < 6; ++i) add4(cs, cp[i]);
        *(LAS f32x4*)(L + L_T1 + f4 * 16) = cs;
        *(LAS f32x4*)(L + L_T1 + 2304 + f4 * 16) = f32x4{blo(b.x), bhi(b.x), blo(b.y), bhi(b.y)};
      }
      if (w == 10 && lane < 10) ((volatile LAS int*)(L + L_HC))[lane] = 0;
      STAMP(9 + layer * 16);
      BAR();
      STAMP(10 + layer * 16);
    }
    {
      LOCALS();
      const int mbase = MBASE();
      const int T = w & 3, cls = w >> 2;
      if (opq_s(cls) == 0) {
        if (layer == 0) {
          __builtin_amdgcn_s_sleep(PZD);
          const v4u pz = {vconst<POISON>(), vconst<POISON>(), vconst<POISON>(), vconst<POISON>()};
#pragma unroll
          for (int i = 0; i < 8; ++i) {
            const int n = w * 8 + i;
            stg(pz, rs, lane16,
                sel(n < 30, ab + ALY + n * KB + sel(n < 12, A_X2 + g * 12 * KB, sel(n < 21, A_O, A_X1 - 9 * KB) + (g * 9 - 12) * KB), OOR));
          }
        }
      }
      if (opq_s(cls) > 0) {
        v4u xr[9];
        const int xo = (quad >> 1) * KB + ((quad & 1) * 2 + (T & 1)) * 256 + l15 * 16;
        RETRY_BEGIN();
#pragma unroll
        for (int i = 0; i < 9; ++i) xr[i] = ldg(rs, xo + (i & 1) * 2 * KB, ab + A_X1 + ((T >> 1) * 36 + 18 * (cls - 1) + (i >> 1) * 4) * KB);
#pragma unroll
        for (int i = 0; i < 9; ++i) { *(LAS v4u*)(L + (T * 18 + 9 * (cls - 1) + i) * KB + lane16) = xr[i]; seen_ = umax4(seen_, xr[i]); }
        RETRY_END();
        cnt_add(L, C_XA + cls - 1, lane);
        __builtin_amdgcn_s_setprio(3);
        const v4u one4 = {vconst<ONES>(), vconst<ONES>(), vconst<ONES>(), vconst<ONES>()};
        f32x4 sa = {0.f, 0.f, 0.f, 0.f}, sq = {0.f, 0.f, 0.f, 0.f};
        asm volatile("s_nop 1");
#pragma unroll
        for (int i = 0; i < 9; ++i) { sa = mfma(one4, xr[i], sa); sq = mfma(xr[i], xr[i], sq); }
        __builtin_amdgcn_s_setprio(0);
        if (layer == 0 && opq_s(w) >= 9) issue_window<0, 3 * DST>(win, rs, lane16, mbase);
        const float dg = (lane & 2) ? ((lane & 1) ? sq[3] : sq[2]) : ((lane & 1) ? sq[1] : sq[0]);
        if (quad == (l15 >> 2)) *(LAS f32x2*)(L + L_ST + ((T * 16 + l15) * 2 + cls - 1) * 8) = f32x2{sa[0], dg};
        cnt_add(L, C_ST, lane);
      }
      STAMP(11 + layer * 16);
      f32x4 acc[3][4];
#pragma unroll
      for (int t = 0; t < 3; ++t)
#pragma unroll
        for (int m = 0; m < 4; ++m) acc[t][m] = zero4();
      if (opq_s(cls) > 0) { row_table(L, lane); shift_acc(acc, L, w, quad, l15); }
      STAMP(49);
      __builtin_amdgcn_s_setprio(3);
      gemm_up(acc, win, L + lane16, rs, lane16, mbase, L);
      __builtin_amdgcn_s_setprio(0);
      STAMP(12 + layer * 16);
      {
        LAS unsigned char* H = L + L_H + lane16 + 3 * (w >> 1) * KB;
        if (opq_s(cls) == 0) {
          cnt_wait(L, C_RT, 1);
          shift_acc(acc, L, w, quad, l15);
        }
        f32x4 rt[4];
#pragma unroll
        for (int m = 0; m < 4; ++m) rt[m] = *(LAS f32x4*)(L + L_ST + KB + (m * 16 + l15) * 16);
#pragma unroll
        for (int m = 0; m < 4; ++m) {
          const v2u u0 = gelu4(acc[0][m], rt[m][2], rt[m][3], rt[m][1]), u1 = gelu4(acc[1][m], rt[m][2], rt[m][3], rt[m][1]);
          *(LAS v4u*)(H + (m * 18 + (w & 1)) * KB) = v4u{u0.x, u0.y, u1.x, u1.y};
          *(LAS v2u*)(H + (m * 18 + 2) * KB + (w & 1) * 8) = gelu4(acc[2][m], rt[m][2], rt[m][3], rt[m][1]);
        }
        cnt_add(L, C_H0 + cls, lane);
      }
      STAMP(13 + layer * 16);
      __builtin_amdgcn_s_setprio(2);
      gemm_down(acc, win, L + L_H + lane16, rs, lane16, mbase + 54 * KB, L STAMP_PASS);
      __builtin_amdgcn_s_setprio(0);
      STAMP(14 + layer * 16);
      const int pw = ab + A_PT + (g * 12 + w) * 6144;
      const int poff = (quad & 1) * 1536 + (quad >> 1) * 256 + l15 * 16;
#pragma unroll
      for (int t = 0; t < 3; ++t)
#pragma unroll
        for (int m = 0; m < 4; m += 2) {
          const v2u a = {pk(acc[t][m][0], acc[t][m][1]), pk(acc[t][m][2], acc[t][m][3])};
          const v2u b = {pk(acc[t][m + 1][0], acc[t][m + 1][1]), pk(acc[t][m + 1][2], acc[t][m + 1][3])};
          const v4u o = widen(a, b);
          const bool own = m + (quad & 1) == (g >> 1) && (l15 >> 3) == (g & 1);
          stg(o, rs, own ? WS_BYTES : poff + m * 1536 + t * 512, pw);
          if (own) *(LAS v4u*)(L + (((g >> 1) ^ 1) * 18 + w) * KB + ((2 * t + (quad >> 1)) * 8 + (lane & 7)) * 16) = o;
        }
      __builtin_amdgcn_s_setprio(0);
    }
    {
      LOCALS();
      OWNROWS(6);
      const bool act = lane < 48;
      float sum[8];
      {
        const v4u bb = *(LAS v4u*)(L + L_B2 + (act ? c8 : 0) * 16);
        const v4u xr = *(LAS v4u*)(L + (act ? piece : 0)), ow = *(LAS v4u*)(L + (((g >> 1) ^ 1) * 18 + w) * KB + lane16);
#pragma unroll
        for (int e = 0; e < 4; ++e) {
          sum[2 * e] = blo(xr[e]) + blo(ow[e]); OPAQUE(sum[2 * e]); sum[2 * e] += blo(bb[e]); OPAQUE(sum[2 * e]);
          sum[2 * e + 1] = bhi(xr[e]) + bhi(ow[e]) + bhi(bb[e]);
        }
      }
      int roff = (g >> 1) * 1536 + (lane >> 3) * 256 + (8 * (g & 1) + (lane & 7)) * 16 + (act ? 0 : OOR);
      int foff = WFLAGS + ((layer * 32 + j - (lane >> 3)) * 12 + w) * 128 + (lane < 8 ? lane : 3 + 4 * (lane & 1)) * 8;
      asm volatile("" : "+v"(roff), "+v"(foff), "+v"(sum[0]), "+v"(sum[1]), "+v"(sum[2]), "+v"(sum[3]), "+v"(sum[4]), "+v"(sum[5]), "+v"(sum[6]), "+v"(sum[7]));
      if (w >= 8) __builtin_amdgcn_s_barrier();
      DRAIN();
      if (lane == 0) set_flag(p.ws, WFLAGS + ((layer * 32 + j) * 12 + w) * 128 + g * 8, tag);
      STAMP(15 + layer * 16);
      if (layer == 0 && opq_s(w) < 8) {
        if (QA) cnt_wait(L, C_LC, 12);
        issue_window<0, QA>(win, rs, lane16, QBASE());
      }
      __builtin_amdgcn_s_barrier();
      wait_flags(p.ws, foff, lane < (layer == 0 && j > 0 ? 10 : 8), tag);
      if (w < 8) __builtin_amdgcn_s_barrier();
      asm volatile("" ::: "memory");
      STAMP(16 + layer * 16);
      v4u xb;
      {
        v4u pr[7];
#pragma unroll
        for (int i = 0; i < 7; ++i) pr[i] = ldg(rs, roff, ab + A_PT + (((i + 1 + g) & 7) * 12 + w) * 6144);
        SB();
        const unsigned sel_lo = opq_s(0x00003f80), sel_hi = opq_s(0x3f800000);
#pragma unroll
        for (int i = 0; i < 7; ++i)
#pragma unroll
          for (int e = 0; e < 4; ++e) { sum[2 * e] = dot2(pr[i][e], sel_lo, sum[2 * e]); sum[2 * e + 1] = dot2(pr[i][e], sel_hi, sum[2 * e + 1]); }
#pragma unroll
        for (int e = 0; e < 4; ++e) xb[e] = pk(sum[2 * e], sum[2 * e + 1]);
      }
      STAMP(17 + layer * 16);
      if (layer == 0) {
        float s1 = dot2(xb.x, ONES, dot2(xb.y, ONES, dot2(xb.z, ONES, dot2(xb.w, ONES, 0.f))));
        float s2 = dot2(xb.x, xb.x, dot2(xb.y, xb.y, dot2(xb.z, xb.z, dot2(xb.w, xb.w, 0.f))));
        if (!act) { s1 = 0.f; s2 = 0.f; }
        s1 += ror8(s1); OPAQUE(s1); s2 += ror8(s2);
        s1 = rows_sum(s1); OPAQUE(s1); s2 = rows_sum(s2);
        if (!act) xb = v4u{__float_as_uint(s1), __float_as_uint(s2), 0u, 0u};
        if (lane < 56) stg(xb, rs, lane16, ab + ALY + A_X2 + (g * 12 + w) * KB);
        SB();
        if (QB > QA && opq_s(w) < 8) issue_window<QA, QB>(win, rs, lane16, QBASE());
        else SETTLE();
        STAMP(36);
        const bool stl = (lane >> 3) == 6;
        LAS unsigned char* Iw = act ? L + (c8 >> 2) * KB + ((c8 & 3) * 16 + (lane & 7)) * 16 : L + 126 * KB + lane16;
        LAS unsigned char* Sw = L + L_ST + (w * 80 + (lane & 7)) * 8;
        BAR();
        STAMP(37);
        v4u xv[10];
        const int vo = lane < 56 ? lane16 : lane16 + WS_BYTES;
        xv[0] = xb;
        RETRY_BEGIN();
#pragma unroll
        for (int i = 1; i < 10; ++i)
          xv[i] = ldg(rs, vo, ab + ALY + A_X2 + w * KB + (i < 8 ? (i ^ g) * 12 * KB : (3 + 4 * (i - 8)) * 12 * KB - (j > 0 ? ASL : 0)));
        SB();
#pragma unroll
        for (int i = 0; i < 10; ++i) {
          const int mi = i < 8 ? i ^ g : i;
          *(LAS v4u*)(Iw + (act ? (mi >> 1) * 18 * KB + (mi & 1) * 128 : 0)) = xv[i];
          if (i) seen_ = umax4(seen_, xv[i]);
        }
        RETRY_END();
        if (stl) {
#pragma unroll
          for (int i = 0; i < 10; ++i) *(LAS v2u*)(Sw + (i < 8 ? i ^ g : i) * 64) = v2u{xv[i].x, xv[i].y};
        }
        BAR();
        STAMP(18);
        if (QB < WIN && opq_s(w) < 8) issue_window<QB, WIN>(win, rs, lane16, QBASE());
        if (w == 11) row_table1<12>(L, 4, lane);
        if (w >= 8) row_table1<12>(L, w - 8, lane);
        if (opq_s(w) == 8) issue_window<3 * DST, WIN>(win, rs, lane16, QBASE());
        if (opq_s(w) >= 8) issue_window<0, 3 * DST>(win, rs, lane16, QBASE());
      } else {
        const rsrc_t ro = __builtin_amdgcn_make_buffer_rsrc((void*)p.out, 0, 2048 * 576 * 2, 0x00020000);
        __builtin_amdgcn_raw_buffer_store_b128(xb, ro, act ? mytok * 1152 + c8 * 16 : OOR, 0, 0);
      }
    }
  }
  STAMP(40);
  STAMP_DUMP();
}
}

extern "C" void kernel_launch(void* const* d_in, const int* in_sizes, int n_in, void* d_out, int out_size, void* d_ws, size_t ws_size,
                              hipStream_t stream) {
  (void)in_sizes; (void)n_in; (void)out_size; (void)ws_size;
  Params p;
  p.x = (const unsigned short*)d_in[0];
  p.wqkv = (const unsigned short*)d_in[1];
  p.wo = (const unsigned short*)d_in[2];
  p.w1 = (const unsigned short*)d_in[4];
  p.b1 = (const unsigned short*)d_in[5];
  p.w2 = (const unsigned short*)d_in[6];
  p.b2 = (const unsigned short*)d_in[7];
  p.out = (unsigned short*)d_out;
  p.ws = (unsigned char*)d_ws;
  (void)hipFuncSetAttribute((const void*)fused_fwd, hipFuncAttributeMaxDynamicSharedMemorySize, LDS_BYTES);
  hipLaunchKernelGGL(fused_fwd, dim3(256), dim3(NTHR), LDS_BYTES, stream, p);
}
```

```cpp
#define ABS_AT 40
#define ABS_T 100
#include <hip/hip_runtime.h>
#include <hip/hip_bf16.h>
#include <stdint.h>

namespace {
#define LAS __attribute__((address_space(3)))
#define GAS __attribute__((address_space(1)))
typedef unsigned v4u __attribute__((ext_vector_type(4)));
typedef unsigned v2u __attribute__((ext_vector_type(2)));
typedef float f32x4 __attribute__((ext_vector_type(4)));
typedef float f32x2 __attribute__((ext_vector_type(2)));
typedef __bf16 bf16x8 __attribute__((ext_vector_type(8)));
typedef __bf16 bf16x2 __attribute__((ext_vector_type(2)));
typedef short s16x4 __attribute__((ext_vector_type(4)));
typedef unsigned long long u64;
typedef __amdgpu_buffer_rsrc_t rsrc_t;

constexpr int KB = 1024;
constexpr int NTHR = 768;
constexpr int WIN = 18;
#define XB 2
#define P2N 9
#define QA 6
#define QB 12
#define PZD 20
#define BDELAY 50
#define CODEPF 35
#define NTW 1
constexpr int DST = 5;
constexpr float EPS = 1e-5f;
constexpr int SC1 = 16;
constexpr float GC0 = -2.0f * 0.7978845608028654f * 1.4426950408889634f;
constexpr int FLAGS = 4096;
constexpr int SFLAGS = 32 * KB;
constexpr int WFLAGS = 48 * KB;
constexpr int CS1 = 192 * KB;
constexpr int CSQ = 520 * KB;
constexpr int WBASE = 704 * KB;
constexpr int WSL = (252 + 1296) * KB;
constexpr int WOB = WBASE + 8 * WSL;
constexpr int ABASE = 14 * 1024 * KB;
constexpr int IMG = 72 * KB;
constexpr int A_X2 = 0, A_O = 96 * KB, A_X1 = A_O + IMG, A_PT = A_X1 + IMG, ALY = 1024 * KB, ASL = 2 * ALY;
constexpr int WS_BYTES = ABASE + 32 * ASL;
static_assert(WOB + 648 * KB <= ABASE && A_PT + 8 * IMG <= ALY && CSQ + 8 * 14 * 1152 + 256 <= WBASE, "workspace map");
constexpr int L_H = IMG;
constexpr int L_QK = 90 * KB;
constexpr int L_ST = 145 * KB;
constexpr int L_T1 = L_ST + 7680;
constexpr int L_TQ = L_T1 + 4608;
constexpr int L_RT = L_TQ + 896;
constexpr int L_HC = L_RT + 640;
constexpr int L_B2 = L_HC + 64;
constexpr int LDS_BYTES = L_B2 + 1152;
static_assert(LDS_BYTES <= 160 * KB, "LDS");
enum { C_H0 = 0, C_ST = 3, C_RT = 4, C_LC = 5, C_XA = 6, C_R1 = 10, C_P0 = 11 };

struct Params {
  const unsigned short *x, *wqkv, *wo, *w1, *b1, *w2, *b2;
  unsigned short *out;
  unsigned char *ws;
};

#define STAMP(i) do { if ((i) == ABS_AT) { while ((unsigned)__builtin_amdgcn_s_memrealtime() - abs0_ < ABS_T * 100u) __builtin_amdgcn_s_sleep(1); } } while (0)
#define STAMP_INIT() const unsigned abs0_ = (unsigned)__builtin_amdgcn_s_memrealtime()
#define RETRY_COUNT()
#define STAMP_ARG , unsigned abs0_
#define STAMP_PASS , abs0_
#define STAMP_DUMP()
#define SB() __builtin_amdgcn_sched_barrier(0)
#define BAR() do { asm volatile("s_waitcnt lgkmcnt(0)" ::: "memory"); __builtin_amdgcn_s_barrier(); asm volatile("" ::: "memory"); } while (0)
#define DRAIN() asm volatile("s_waitcnt vmcnt(0)" ::: "memory")
#define SETTLE() DRAIN()
#define OPAQUE(x) asm("" : "+v"(x))
#define KEEP(x) asm volatile("" :: "v"(x))

__device__ __forceinline__ f32x4 mfma(v4u a, v4u b, f32x4 c) {
  return __builtin_amdgcn_mfma_f32_16x16x32_bf16(__builtin_bit_cast(bf16x8, a), __builtin_bit_cast(bf16x8, b), c, 0, 0, 0);
}
#define MF(acc, a, b) asm volatile("v_mfma_f32_16x16x32_bf16 %0, %1, %2, %0" : "+v"(acc) : "v"(a), "v"(b))
#define MF0(acc, a, b) asm volatile("v_mfma_f32_16x16x32_bf16 %0, %1, %2, 0" : "=&v"(acc) : "v"(a), "v"(b))
template <int NT, int MT> __device__ __forceinline__ void acc_fence(f32x4 (&acc)[NT][MT]) {
#pragma unroll
  for (int t = 0; t < NT; ++t)
#pragma unroll
    for (int m = 0; m < MT; ++m) asm volatile("" : "+v"(acc[t][m]));
}
#define ACC_IN(acc) do { acc_fence(acc); asm volatile("s_nop 1"); SB(); } while (0)
#define ACC_OUT(acc) do { SB(); asm volatile("s_nop 7\n\ts_nop 1"); acc_fence(acc); } while (0)
__device__ __forceinline__ unsigned pk(float a, float b) {
  f32x2 v = {a, b};
  return __builtin_bit_cast(unsigned, __builtin_convertvector(v, bf16x2));
}
__device__ __forceinline__ float dot2(unsigned a, unsigned b, float c) {
  return __builtin_amdgcn_fdot2_f32_bf16(__builtin_bit_cast(bf16x2, a), __builtin_bit_cast(bf16x2, b), c, false);
}
constexpr unsigned ONES = 0x3f803f80u;
__device__ __forceinline__ float blo(unsigned u) { return __uint_as_float(u << 16); }
__device__ __forceinline__ float bhi(unsigned u) { return __uint_as_float(u & 0xffff0000u); }
__device__ __forceinline__ v4u ldg(rsrc_t rs, int voff, int soff) { return __builtin_amdgcn_raw_buffer_load_b128(rs, voff, soff, SC1); }
__device__ __forceinline__ void stg(v4u v, rsrc_t rs, int voff, int soff) { __builtin_amdgcn_raw_buffer_store_b128(v, rs, voff + soff, 0, SC1); }
__device__ __forceinline__ v4u ldf(rsrc_t rs, int lane16, int base, int f) { return ldg(rs, lane16 + (f & 3) * KB, base + (f >> 2) * 4 * KB); }
__device__ __forceinline__ int opq_v(int x) { asm volatile("" : "+v"(x)); return x; }
__device__ __forceinline__ int lane_id() { int x; asm volatile("v_mbcnt_lo_u32_b32 %0, -1, 0\n\tv_mbcnt_hi_u32_b32 %0, -1, %0" : "=v"(x)); return x; }
template <unsigned C> __device__ __forceinline__ unsigned vconst() { unsigned x; asm volatile("v_mov_b32 %0, %1" : "=v"(x) : "i"(C)); return x; }
__device__ __forceinline__ int sel(bool c, int a, int b) { return c ? a : b; }
constexpr int OOR = 0x10000000;
__device__ __forceinline__ int opq_s(int x) { asm volatile("" : "+s"(x)); return x; }
#define LOCALS() const int lane = lane_id(), w = opq_s(w0), j = opq_s(j0), g = opq_s(g0); \
  const int lane16 = lane * 16, quad = lane >> 4, l15 = lane & 15, ab = ABASE + j * ASL + layer * ALY, wb = WBASE + g * WSL; \
  (void)lane16; (void)quad; (void)l15; (void)ab; (void)wb
#define OWNROWS(CPW) const int c8 = CPW * w + (lane >> 3); \
  const int piece = ((g >> 1) * 18 + (c8 >> 2)) * KB + ((c8 & 3) * 16 + 8 * (g & 1) + (lane & 7)) * 16; \
  const int mytok = 64 * j + 2 * (16 * ((g >> 1) & 1) + 8 * (g & 1) + (lane & 7)) + (g >> 2)
#define CPF_DONE() KEEP(cpf)
#define GET_TAG() CPF_DONE(); tag = 1 + ((u64)(unsigned)__builtin_amdgcn_readfirstlane((int)(unsigned)tag0) | \
                             ((u64)(unsigned)__builtin_amdgcn_readfirstlane((int)(unsigned)(tag0 >> 32)) << 32))
#define NBASE() opq_s(nb0)
#define QBASE() opq_s(qb0)
#define MBASE() opq_s(mb0)
__device__ __forceinline__ float rows_sum(float x) {
  auto s = __builtin_amdgcn_permlane16_swap(__float_as_uint(x), __float_as_uint(x), false, false);
  x = __uint_as_float(s[0]) + __uint_as_float(s[1]);
  auto t = __builtin_amdgcn_permlane32_swap(__float_as_uint(x), __float_as_uint(x), false, false);
  return __uint_as_float(t[0]) + __uint_as_float(t[1]);
}
__device__ __forceinline__ float vmax(unsigned a, unsigned b) { float r; asm("v_max_f32 %0, %1, %2\n\ts_nop 1" : "=v"(r) : "v"(a), "v"(b)); return r; }
__device__ __forceinline__ void rows_max2(float& a, float& b) {
  auto s = __builtin_amdgcn_permlane16_swap(__float_as_uint(a), __float_as_uint(b), false, false);
  float x = vmax(s[0], s[1]);
  auto t = __builtin_amdgcn_permlane32_swap(__float_as_uint(x), __float_as_uint(x), false, false);
  x = vmax(t[0], t[1]);
  auto u = __builtin_amdgcn_permlane16_swap(__float_as_uint(x), __float_as_uint(x), false, false);
  a = __uint_as_float(u[0]);
  b = __uint_as_float(u[1]);
}
template <int CTRL> __device__ __forceinline__ float dpp(float x) {
  return __int_as_float(__builtin_amdgcn_mov_dpp(__float_as_int(x), CTRL, 0xf, 0xf, true));
}
__device__ __forceinline__ float ror8(float x) { return dpp<0x128>(x); }
__device__ __forceinline__ void add4(f32x4& a, v4u b) {
  float a0 = a[0], a1 = a[1], a2 = a[2], a3 = a[3];
  asm("v_add_f32 %0, %0, %4\n\tv_add_f32 %1, %1, %5\n\tv_add_f32 %2, %2, %6\n\tv_add_f32 %3, %3, %7"
      : "+v"(a0), "+v"(a1), "+v"(a2), "+v"(a3) : "v"(b.x), "v"(b.y), "v"(b.z), "v"(b.w));
  a = f32x4{a0, a1, a2, a3};
}
__device__ __forceinline__ v4u widen(v2u a, v2u b) {
  auto s0 = __builtin_amdgcn_permlane16_swap(a.x, b.x, false, false);
  auto s1 = __builtin_amdgcn_permlane16_swap(a.y, b.y, false, false);
  return v4u{s0[0], s1[0], s0[1], s1[1]};
}
__device__ __forceinline__ v2u gelu4(f32x4 a, float k0, float k1, float is) {
  float s0, s1, s2, s3;
  unsigned u0, u1;
  asm("v_mul_f32 %2, %6, %6\n\tv_mul_f32 %3, %7, %7\n\tv_mul_f32 %4, %8, %8\n\tv_mul_f32 %5, %9, %9\n\t"
      "v_fma_f32 %2, %2, %11, %10\n\tv_fma_f32 %3, %3, %11, %10\n\tv_fma_f32 %4, %4, %11, %10\n\tv_fma_f32 %5, %5, %11, %10\n\t"
      "v_mul_f32 %2, %2, %6\n\tv_mul_f32 %3, %3, %7\n\tv_mul_f32 %4, %4, %8\n\tv_mul_f32 %5, %5, %9\n\t"
      "v_exp_f32 %2, %2\n\tv_exp_f32 %3, %3\n\tv_exp_f32 %4, %4\n\tv_exp_f32 %5, %5\n\t"
      "v_fma_f32 %2, %2, %12, %12\n\tv_fma_f32 %3, %3, %12, %12\n\tv_fma_f32 %4, %4, %12, %12\n\tv_fma_f32 %5, %5, %12, %12\n\t"
      "v_rcp_f32 %2, %2\n\tv_rcp_f32 %3, %3\n\tv_rcp_f32 %4, %4\n\tv_rcp_f32 %5, %5\n\t"
      "v_mul_f32 %2, %2, %6\n\tv_mul_f32 %3, %3, %7\n\tv_mul_f32 %4, %4, %8\n\tv_mul_f32 %5, %5, %9\n\t"
      "v_cvt_pk_bf16_f32 %0, %2, %3\n\tv_cvt_pk_bf16_f32 %1, %4, %5"
      : "=&v"(u0), "=&v"(u1), "=&v"(s0), "=&v"(s1), "=&v"(s2), "=&v"(s3)
      : "v"(a[0]), "v"(a[1]), "v"(a[2]), "v"(a[3]), "v"(k0), "v"(k1), "v"(is));
  return v2u{u0, u1};
}
__device__ __forceinline__ void shift4(f32x4& a, f32x4 cs, f32x4 b, float nmean, float is) {
  float a0 = a[0], a1 = a[1], a2 = a[2], a3 = a[3];
  asm("v_fmac_f32 %0, %12, %4\n\tv_fmac_f32 %1, %12, %5\n\tv_fmac_f32 %2, %12, %6\n\tv_fmac_f32 %3, %12, %7\n\t"
      "v_fmac_f32 %0, %13, %8\n\tv_fmac_f32 %1, %13, %9\n\tv_fmac_f32 %2, %13, %10\n\tv_fmac_f32 %3, %13, %11"
      : "+v"(a0), "+v"(a1), "+v"(a2), "+v"(a3)
      : "v"(cs[0]), "v"(cs[1]), "v"(cs[2]), "v"(cs[3]), "v"(b[0]), "v"(b[1]), "v"(b[2]), "v"(b[3]), "v"(nmean), "v"(is));
  a = f32x4{a0, a1, a2, a3};
}
__device__ __forceinline__ int cnt_get(LAS unsigned char* L, int i) { return ((volatile LAS int*)(L + L_HC))[i]; }
__device__ __forceinline__ void cnt_wait(LAS unsigned char* L, int i, int n) {
  while (__builtin_amdgcn_readfirstlane(cnt_get(L, i)) < n) __builtin_amdgcn_s_sleep(1);
  asm volatile("" ::: "memory");
}
__device__ __forceinline__ void cnt_add(LAS unsigned char* L, int i, int lane) {
  asm volatile("s_waitcnt lgkmcnt(0)" ::: "memory");
  if (lane == 0) __hip_atomic_fetch_add((LAS int*)(L + L_HC) + i, 1, __ATOMIC_RELAXED, __HIP_MEMORY_SCOPE_WORKGROUP);
}
template <int NP> __device__ __forceinline__ void row_table1(LAS unsigned char* L, int t5, int lane) {
  const int r = t5 * 16 + (lane & 15), q = lane >> 4;
  float t1 = 0.f, t2 = 0.f;
#pragma unroll
  for (int i = 0; i < (NP + 3) / 4; ++i)
    if (NP % 4 == 0 || q < NP) { const f32x2 v = *(LAS f32x2*)(L + L_ST + ((i * 4 + q) * 80 + r) * 8); t1 += v.x; OPAQUE(t1); t2 += v.y; }
  t1 = rows_sum(t1); OPAQUE(t1); t2 = rows_sum(t2);
  const float nmean = t1 * (-1.0f / 576.0f);
  const float var = fmaxf(__builtin_fmaf(-nmean, nmean, t2 * (1.0f / 576.0f)), 0.f);
  const float rstd = __builtin_amdgcn_rsqf(var + EPS);
  if (q < 2) *(LAS float*)(L + L_RT + q * 320 + r * 4) = q ? nmean * rstd : rstd;
  cnt_add(L, C_R1, lane);
}
__device__ __forceinline__ v2u ln_pack(f32x4 a, f32x4 r, f32x4 c, f32x4 nm) {
  float t0, t1, t2, t3;
  unsigned u0, u1;
  asm("v_mul_f32 %2, %14, %18\n\tv_mul_f32 %3, %15, %19\n\tv_mul_f32 %4, %16, %20\n\tv_mul_f32 %5, %17, %21\n\t"
      "v_fmac_f32 %2, %6, %10\n\tv_fmac_f32 %3, %7, %11\n\tv_fmac_f32 %4, %8, %12\n\tv_fmac_f32 %5, %9, %13\n\t"
      "v_cvt_pk_bf16_f32 %0, %2, %3\n\tv_cvt_pk_bf16_f32 %1, %4, %5"
      : "=&v"(u0), "=&v"(u1), "=&v"(t0), "=&v"(t1), "=&v"(t2), "=&v"(t3)
      : "v"(a[0]), "v"(a[1]), "v"(a[2]), "v"(a[3]), "v"(r[0]), "v"(r[1]), "v"(r[2]), "v"(r[3]), "v"(c[0]), "v"(c[1]), "v"(c[2]), "v"(c[3]),
        "v"(nm[0]), "v"(nm[1]), "v"(nm[2]), "v"(nm[3]));
  return v2u{u0, u1};
}
__device__ __forceinline__ f32x4 zero4() {
  float a, b, c, d;
  asm volatile("v_mov_b32 %0, 0\n\tv_mov_b32 %1, 0\n\tv_mov_b32 %2, 0\n\tv_mov_b32 %3, 0" : "=v"(a), "=v"(b), "=v"(c), "=v"(d));
  return f32x4{a, b, c, d};
}
__device__ __forceinline__ f32x4 splat(float x) { return f32x4{x, x, x, x}; }
__device__ __forceinline__ void row_table(LAS unsigned char* L, int lane) {
  cnt_wait(L, C_ST, 8);
  const f32x4 q = *(LAS f32x4*)(L + L_ST + lane * 16);
  float t1 = q[0] + q[2];
  OPAQUE(t1);
  float nmean = t1 * (-1.0f / 576.0f);
  const float var = fmaxf(__builtin_fmaf(-nmean, nmean, (q[1] + q[3]) * (1.0f / 576.0f)), 0.f);
  const float r1 = __builtin_amdgcn_rsqf(var + EPS);
  const float sc = r1 * __builtin_amdgcn_rsqf(var * r1 * r1 + EPS);
  float k0 = GC0 * sc;
  OPAQUE(k0);
  *(LAS f32x4*)(L + L_ST + KB + lane * 16) = f32x4{nmean, __builtin_amdgcn_rcpf(sc), k0, k0 * (0.044715f * sc * sc)};
  cnt_add(L, C_RT, lane);
}
__device__ __forceinline__ void shift_acc(f32x4 (&acc)[3][4], LAS unsigned char* L, int w, int quad, int l15) {
  f32x2 rt[4];
#pragma unroll
  for (int m = 0; m < 4; ++m) rt[m] = *(LAS f32x2*)(L + L_ST + KB + (m * 16 + l15) * 16);
#pragma unroll
  for (int t = 0; t < 3; ++t) {
    const f32x4 cs = *(LAS f32x4*)(L + L_T1 + (48 * w + 16 * t + 4 * quad) * 4), bb = *(LAS f32x4*)(L + L_T1 + 2304 + (48 * w + 16 * t + 4 * quad) * 4);
#pragma unroll
    for (int m = 0; m < 4; ++m) shift4(acc[t][m], cs, bb, rt[m][0], rt[m][1]);
  }
}
template <int SLEEP = 1> __device__ __forceinline__ void wait_flags(const unsigned char* ws, int off, bool active, u64 tag) {
  const GAS u64* f = (const GAS u64*)(ws + off);
  asm volatile("" : "+s"(tag));
#pragma unroll 1
  for (int spins = 0; spins < (1 << 16); ++spins) {
    const u64 v = active ? __hip_atomic_load(f, __ATOMIC_RELAXED, __HIP_MEMORY_SCOPE_AGENT) : tag;
    if (__all(v == tag)) break;
    __builtin_amdgcn_s_sleep(SLEEP);
  }
  asm volatile("" ::: "memory");
}
__device__ __forceinline__ void set_flag(unsigned char* ws, int off, u64 tag) {
  asm volatile("" : "+s"(tag));
  __hip_atomic_store((GAS u64*)(ws + off), tag, __ATOMIC_RELAXED, __HIP_MEMORY_SCOPE_AGENT);
}
constexpr unsigned POISON = 0xffffffffu;
__device__ __forceinline__ unsigned umax4(unsigned m, v4u v) { return max(max(max(m, v.x), v.y), max(v.z, v.w)); }
#define RETRY_BEGIN() _Pragma("unroll 1") for (int spins_ = 0; spins_ < (1 << 15); ++spins_) { unsigned seen_ = 0
#define RETRY_END() if (!__any(seen_ == POISON)) break; RETRY_COUNT(); __builtin_amdgcn_s_sleep(2); asm volatile("" ::: "memory"); }
template <int LO, int HI> __device__ __forceinline__ void issue_window(v4u (&win)[WIN], rsrc_t rs, int lane16, int base) {
#pragma unroll
  for (int i = LO; i < HI; ++i) win[i] = ldf(rs, lane16, base, i);
  SB();
}

template <int NT, int MT, int SWAP, int NNEXT, int R, int LO, int HI>
__device__ __forceinline__ void gemm18_steps(f32x4 (&acc)[NT][MT], v4u (&win)[WIN], v4u (&act)[R], LAS unsigned char* img, LAS unsigned char* img2, rsrc_t rs,
                                             int lane16, int wbase, int nvo, int nbase) {
#pragma unroll
  for (int n = LO; n < HI; ++n) {
    const int s = n / MT, m = n % MT, nn = n + R - 1;
    if (n < 18 * MT) {
#pragma unroll
      for (int t = 0; t < NT; ++t) {
        if (s == 0) {
          if ((SWAP >> t) & 1) MF0(acc[t][m], act[n % R], win[t]);
          else MF0(acc[t][m], win[t], act[n % R]);
        } else if ((SWAP >> t) & 1) MF(acc[t][m], act[n % R], win[(s * NT + t) % WIN]);
        else MF(acc[t][m], win[(s * NT + t) % WIN], act[n % R]);
      }
      SB();
    }
    if (nn < 18 * MT) act[nn % R] = *(LAS v4u*)(nn % MT < 3 ? img + ((nn % MT) * 18 + nn / MT) * KB : img2 + ((nn % MT - 3) * 18 + nn / MT) * KB);
    if (m == 0 && s > 0) {
#pragma unroll
      for (int t = 0; t < NT; ++t) {
        const int f = (s - 1) * NT + t;
        if (f + WIN < 18 * NT) win[f % WIN] = ldf(rs, lane16, wbase, f + WIN);
        else if (f % WIN < NNEXT) win[f % WIN] = ldf(rs, nvo, nbase, f % WIN);
      }
    }
    SB();
    if (n < 18 * MT) {
      KEEP(act[n % R]);
#pragma unroll
      for (int t = 0; t < NT; ++t) KEEP(win[(s * NT + t) % WIN]);
      SB();
    }
  }
}
template <int NT, int MT, int SWAP, int NNEXT, int R>
__device__ __forceinline__ void gemm18(f32x4 (&acc)[NT][MT], v4u (&win)[WIN], LAS unsigned char* img, rsrc_t rs, int lane16, int wbase, int nbase) {
  v4u act[R];
  LAS unsigned char* img2 = img + 54 * KB;
  if (MT > 3) asm volatile("" : "+v"(img2));
#pragma unroll
  for (int n = 0; n < R - 1; ++n) { act[n] = *(LAS v4u*)(n % MT < 3 ? img + ((n % MT) * 18 + n / MT) * KB : img2 + ((n % MT - 3) * 18 + n / MT) * KB); SB(); }
  gemm18_steps<NT, MT, SWAP, NNEXT, R, 0, 6 * MT>(acc, win, act, img, img2, rs, lane16, wbase, lane16, nbase);
  gemm18_steps<NT, MT, SWAP, NNEXT, R, 6 * MT, 12 * MT>(acc, win, act, img, img2, rs, lane16, wbase, lane16, nbase);
  gemm18_steps<NT, MT, SWAP, NNEXT, R, 12 * MT, 18 * MT + 1>(acc, win, act, img, img2, rs, lane16, wbase, lane16, nbase);
  ACC_OUT(acc);
}
template <int NT, int MT, int SWAP, int R, int tstep, int NNEXT>
__device__ __forceinline__ void qkv_tiles(v4u (&win)[WIN], LAS unsigned char* L, rsrc_t rs, int lane, int wbase, int nbase, int tile0, int rwait STAMP_ARG) {
  f32x4 acc[NT][MT];
  gemm18<NT, MT, SWAP, NNEXT, R>(acc, win, L + lane * 16, rs, lane * 16, wbase, nbase);
  STAMP(38);
  LAS unsigned char *Rq = L + L_RT + (lane & 48), *Rl = L + L_RT + (lane & 15) * 4, *Tq = L + L_TQ + tile0 * 64 + (lane & 48),
                    *Tl = L + L_TQ + tile0 * 64 + (lane & 15) * 4, *X = L + L_QK + lane * 8 + tile0 * 2560;
  asm volatile("" : "+v"(Rq), "+v"(Rl), "+v"(Tq), "+v"(Tl), "+v"(X));
  cnt_wait(L, C_R1, rwait);
  v2u u[NT][MT];
#pragma unroll
  for (int t = 0; t < NT; ++t) {
    if ((SWAP >> t) & 1) {
      const f32x4 cs = splat(*(LAS float*)(Tl + t * tstep * 64));
#pragma unroll
      for (int m = 0; m < MT; ++m) u[t][m] = ln_pack(acc[t][m], *(LAS f32x4*)(Rq + m * 64), cs, *(LAS f32x4*)(Rq + 320 + m * 64));
    } else {
      const f32x4 cs = *(LAS f32x4*)(Tq + t * tstep * 64);
#pragma unroll
      for (int m = 0; m < MT; ++m) u[t][m] = ln_pack(acc[t][m], splat(*(LAS float*)(Rl + m * 64)), cs, splat(*(LAS float*)(Rl + 320 + m * 64)));
    }
  }
#pragma unroll
  for (int t = 0; t < NT; ++t)
#pragma unroll
    for (int m = 0; m < MT; ++m) *(LAS v2u*)(X + (t * tstep * 5 + m) * 512) = u[t][m];
}
template <int C>
__device__ __forceinline__ void mlp_chunk(f32x4 (&acc)[3][4], v4u (&win)[WIN], v4u (&act)[8], LAS unsigned char* img, rsrc_t rs, int lane16, int rb,
                                          LAS unsigned char* L) {
  int hv = 4;
#pragma unroll
  for (int s = 0; s < 6; ++s) {
    if (C != 2 && s == 5) {
      if (__builtin_amdgcn_readfirstlane(hv) < 4) cnt_wait(L, C < 0 ? C_XA + 1 : C_H0 + C + 1, 4);
      asm volatile("" ::: "memory");
      SB();
    }
    if (C == 2 && s == 6 - DST) {
      if (lane16 == 0) __hip_atomic_fetch_add((LAS int*)(L + L_HC) + C_LC, 1, __ATOMIC_RELAXED, __HIP_MEMORY_SCOPE_WORKGROUP);
      SB();
    }
    asm volatile("" :: "v"(act[(s & 1) * 4]), "v"(act[(s & 1) * 4 + 1]), "v"(act[(s & 1) * 4 + 2]), "v"(act[(s & 1) * 4 + 3]), "v"(win[s * 3]),
                 "v"(win[s * 3 + 1]), "v"(win[s * 3 + 2]));
    SB();
#pragma unroll
    for (int t = 0; t < 3; ++t)
#pragma unroll
      for (int m = 0; m < 4; ++m) {
        const int mm = t == 1 ? 3 - m : m;
        if (C == 0 && s == 0) MF0(acc[t][mm], win[t], act[mm]);
        else MF(acc[t][mm], win[s * 3 + t], act[(s & 1) * 4 + mm]);
        SB();
        if (t == 0 && !(C == 2 && s == 5)) act[((s + 1) & 1) * 4 + m] = *(LAS v4u*)(img + (m * 18 + s + 1) * KB);
        if (C != 2 && s == 3 && t == 2 && m == 0) hv = cnt_get(L, C < 0 ? C_XA + 1 : C_H0 + C + 1);
        if (t == 1 && m < 3 && !(C == 2 && s + DST >= 6))
          win[((s + DST) % 6) * 3 + m] = ldf(rs, lane16, rb, (s + DST) * 3 + m);
        SB();
      }
  }
}
__device__ __forceinline__ void gemm_up(f32x4 (&acc)[3][4], v4u (&win)[WIN], LAS unsigned char* img, rsrc_t rs, int lane16, int rb, LAS unsigned char* L) {
  v4u act[8];
  cnt_wait(L, C_XA, 4);
#pragma unroll
  for (int n = 0; n < 4; ++n) { act[n] = *(LAS v4u*)(img + n * 18 * KB); SB(); }
  ACC_IN(acc);
#pragma unroll 1
  for (int c = 0; c < 3; ++c) {
    mlp_chunk<-1>(acc, win, act, img, rs, lane16, rb, L);
    img += 6 * KB;
    rb += 18 * KB;
  }
  ACC_OUT(acc);
}
__device__ __forceinline__ void gemm_down(f32x4 (&acc)[3][4], v4u (&win)[WIN], LAS unsigned char* img, rsrc_t rs, int lane16, int rb, LAS unsigned char* L STAMP_ARG) {
  v4u act[8];
  cnt_wait(L, C_H0, 4);
  STAMP(46);
#pragma unroll
  for (int n = 0; n < 4; ++n) { act[n] = *(LAS v4u*)(img + n * 18 * KB); SB(); }
  mlp_chunk<0>(acc, win, act, img, rs, lane16, rb, L);
  img += 6 * KB;
  asm volatile("" : "+v"(img));
  STAMP(47);
  mlp_chunk<1>(acc, win, act, img, rs, lane16, rb + 18 * KB, L);
  img += 6 * KB;
  asm volatile("" : "+v"(img));
  STAMP(48);
  mlp_chunk<2>(acc, win, act, img, rs, lane16, rb + 36 * KB, L);
  ACC_OUT(acc);
}

__global__ void __launch_bounds__(NTHR) fused_fwd(Params p) {
  extern __shared__ __attribute__((aligned(16))) unsigned char smem[];
  LAS unsigned char* L = (LAS unsigned char*)smem;
  const int w0 = __builtin_amdgcn_readfirstlane(threadIdx.x >> 6);
  const int bid = blockIdx.x;
  u64 pc0;
  asm volatile("s_getpc_b64 %0" : "=s"(pc0));
  unsigned cpf = 0;
  if (threadIdx.x < CODEPF * 16) cpf = *(const GAS unsigned*)(pc0 + threadIdx.x * 64);
  constexpr unsigned MPX = 8 / XB;
  const int j0 = ((blockIdx.x & 7) / XB) * (32 / MPX) + (blockIdx.x >> 3) / MPX;
  const int g0 = ((blockIdx.x & 7) % XB) * MPX + (blockIdx.x >> 3) % MPX;
  const rsrc_t rs = __builtin_amdgcn_make_buffer_rsrc((void*)p.ws, 0, WS_BYTES, 0x00020000);
  const int mb0 = WBASE + g0 * WSL + (252 + w0 * 108) * KB;
  const int nb0 = sel(w0 < 9, WOB + ((g0 & 3) * 9 + w0) * 18 * KB, mb0);
  const int qb0 = sel(w0 < 8, WBASE + g0 * WSL + sel(w0 < 6, w0 * 36, 108 + w0 * 18) * KB, nb0);
  STAMP_INIT();
  STAMP(0);
  const u64 tag0 = __hip_atomic_load((const GAS u64*)p.ws, __ATOMIC_RELAXED, __HIP_MEMORY_SCOPE_AGENT);
  u64 tag = 0;
  v4u win[WIN];
  {
    constexpr int layer = 0;
    LOCALS();
    const rsrc_t rx = __builtin_amdgcn_make_buffer_rsrc((void*)p.x, 0, 2048 * 576 * 2, 0x00020000);
    const int xrow = lane >> 2;
#define LOAD_X() v4u xr[9]; _Pragma("unroll") for (int i = 0; i < 9; ++i) xr[i] = __builtin_amdgcn_raw_buffer_load_b128(rx, xoff + i * 64, 0, 0); SB()
#define STAGE_X() do { float s1 = 0.f, s2 = 0.f; \
      _Pragma("unroll") for (int i = 0; i < 9; ++i) { \
        if (w < 10) *(LAS v4u*)(L + (w * 9 + i) * KB + (lane & 3) * 256 + xrow * 16) = xr[i]; \
        _Pragma("unroll") for (int e = 0; e < 4; ++e) { s1 = dot2(xr[i][e], ONES, s1); s2 = dot2(xr[i][e], xr[i][e], s2); } } \
      s1 += dpp<0xB1>(s1); s2 += dpp<0xB1>(s2); s1 += dpp<0x4E>(s1); s2 += dpp<0x4E>(s2); \
      if (w < 10 && (lane & 3) == 0) *(LAS f32x2*)(L + L_ST + ((w & 1) * 80 + (w >> 1) * 16 + xrow) * 8) = f32x2{s1, s2}; } while (0)
    if (opq_s(w) < 8) {
      LAS unsigned char* T = L + L_QK + w * 3072;
      const int uf = 8 * j + w, uc = sel(uf < 252, uf, 0), us = (uc * 293) >> 12, tp = uc - us * 14;
      const int tile = sel(tp < 4, 10 + tp, sel(tp == 4, 8, sel(tp == 13, 9, tp - 5)));
      const bool vx = tile == 9, ex = (tile & 14) == 8;
      const int c0 = 72 * g + sel(ex, 16, ((tile + sel(tile >= 10, 2, 0)) & 3) * 18) + sel(tile < 4, 576, sel(tile < 8 || vx, 1152, 0));
      const rsrc_t rq = __builtin_amdgcn_make_buffer_rsrc((void*)p.wqkv, 0, 576 * 1728 * 2, 0x00020000);
      const int qo = (lane >> 1) * 3456 + (lane & 1) * sel(ex, sel(vx, 0, 72), 16), qs = us * 32 * 3456 + c0 * 2;
      v4u qv;
      if (ex) {
        const int cA = sel(vx, 36, 1152), cB = sel(vx, 72, 36);
        qv = v4u{__builtin_amdgcn_raw_buffer_load_b32(rq, qo, qs, 0), __builtin_amdgcn_raw_buffer_load_b32(rq, qo, qs + cA, 0),
                 __builtin_amdgcn_raw_buffer_load_b32(rq, qo, qs + cB, 0), __builtin_amdgcn_raw_buffer_load_b32(rq, qo, qs + cA + cB, 0)};
      } else qv = __builtin_amdgcn_raw_buffer_load_b128(rq, qo, qs, 0);
      SB();
      const int of = 3 * j + w, oc = (g >> 2) * 81 + of, os = (oc * 57) >> 9, ot = oc - os * 9;
      const bool hasO = w < 3 && of < 81;
      const bool ox = os >= 16;
      const int orow = quad * sel(ox, 72, 4) + ((lane >> 1) & 1) + ((lane >> 2) & 1) * sel(ox, 18, 2) + ((lane >> 3) & 1) * sel(ox, 36, 18);
      const rsrc_t ro = __builtin_amdgcn_make_buffer_rsrc((void*)p.wo, 0, 576 * 576 * 2, 0x00020000);
      const v4u ov = __builtin_amdgcn_raw_buffer_load_b128(ro, orow * 1152 + (lane & 1) * 16 + sel(hasO, 0, OOR),
                                                           sel(ox, 288 * (os - 16) + 16, 36 * os) * 1152 + 32 * ((g & 3) * 9 + ot), 0);
      SB();
      const int xoff = (64 * j + 32 * ((w >> 1) & 1) + (w >> 2) + 2 * xrow) * 1152 + (w & 1) * 576 + (lane & 3) * 16;
      __builtin_amdgcn_s_barrier();
      LOAD_X();
      STAMP(41);
      *(LAS v4u*)(T + (lane >> 1) * 48 + (lane & 1) * 16) = (vx && (lane & 1)) ? v4u{0u, 0u, 0u, 0u} : qv;
      *(LAS v4u*)(T + 1536 + (lane >> 1) * 48 + (lane & 1) * 16) = ov;
      LAS unsigned char* R = T + (8 * quad + (l15 >> 2)) * 48 + (lane & 3) * 8;
      const v2u a = __builtin_bit_cast(v2u, __builtin_amdgcn_ds_read_tr16_b64_v4i16((LAS s16x4*)(R)));
      const v2u b = __builtin_bit_cast(v2u, __builtin_amdgcn_ds_read_tr16_b64_v4i16((LAS s16x4*)(R + 4 * 48)));
      const v2u c = __builtin_bit_cast(v2u, __builtin_amdgcn_ds_read_tr16_b64_v4i16((LAS s16x4*)(R + 1536)));
      const v2u d = __builtin_bit_cast(v2u, __builtin_amdgcn_ds_read_tr16_b64_v4i16((LAS s16x4*)(R + 1536 + 4 * 48)));
      stg(v4u{a.x, a.y, b.x, b.y}, rs, lane16,
          sel(uf >= 252, OOR, wb + sel(tile < 8, (tile & 3) * 36 + us * 2 + (tile >> 2), sel(tile < 10, 72 + tile * 18 + us, (tile >> 1) * 36 - 36 + us * 2 + (tile & 1))) * KB));
      stg(v4u{c.x, c.y, d.x, d.y}, rs, lane16, sel(hasO, WOB + ((g & 3) * 162 + ot * 18 + os) * KB, OOR));
      const float cq = rows_sum(dot2(a.x, ONES, dot2(a.y, ONES, dot2(b.x, ONES, dot2(b.y, ONES, 0.f)))));
      __builtin_amdgcn_raw_buffer_store_b32(__float_as_uint(cq), rs, sel(lane < 16, lane * 4, OOR) + sel(uf < 252, CSQ + ((g * 18 + us) * 14 + tile) * 64, OOR), 0, SC1);
      SB();
      STAMP(43);
      DRAIN();
      STAMP(44);
      GET_TAG();
      if (lane == 0 && __hip_atomic_fetch_add((LAS int*)(L + L_HC) + C_P0, 1, __ATOMIC_RELAXED, __HIP_MEMORY_SCOPE_WORKGROUP) == 7)
        set_flag(p.ws, SFLAGS + (g * 32 + j) * 8, tag);
      STAGE_X();
      STAMP(2);
      BAR();
      STAMP(3);
      issue_window<0, WIN>(win, rs, lane16, QBASE());
    }
    if (opq_s(w) >= 8) {
      LAS unsigned char* T = L + 125 * KB + (w - 8) * 4608;
      const int item = 4 * j + w - 8, rbk = (item * 57) >> 9, cb = item - rbk * 9, kind = sel(rbk >= 6, 1, 0), k3 = rbk - kind * 6;
      const int rn = sel(kind, 4, 8), ld = sel(kind, 1152, 9216);
      const rsrc_t rw = __builtin_amdgcn_make_buffer_rsrc((void*)(kind == 0 ? p.w1 : p.w2), 0, 576 * 4608 * 2, 0x00020000);
      const int jj = lane >> 3;
      const int woff = (jj & 3) * ld + (lane & 7) * 16 + sel(item < 108, 96 * k3 * ld + g * sel(kind, 576 * 1152, 1152) + 128 * cb, OOR);
      const int xoff = (sel(j > 0, 64 * j - 16, 0) + 2 * (xrow & 7) + (xrow >> 3)) * 1152 + (lane & 3) * 16 + sel(w < 10, (w & 1) * 576, OOR);
      if (w == 11 && lane < 16) ((volatile LAS int*)(L + L_HC))[lane] = 0;
      BAR();
      LOAD_X();
      v4u wv[3][4];
#pragma unroll
      for (int n = 0; n < 4; ++n)
#pragma unroll
        for (int c = 0; c < 3; ++c)
          wv[c][n] = __builtin_amdgcn_raw_buffer_load_b128(rw, woff + (jj >> 2) * (sel(kind, c < 2 ? 16 : 48, 4) * ld),
                                                           (n * rn + sel(kind, c < 2 ? 48 * c : 32, 32 * c)) * ld, NTW ? 2 : 0);
      SB();
      STAMP(41);
      GET_TAG();
      STAGE_X();
      float cs[4] = {0.f, 0.f, 0.f, 0.f};
      LAS unsigned char* R = T + (8 * quad + (l15 >> 2)) * 144 + (lane & 3) * 8;
#pragma unroll
      for (int c = 0; c < 3; ++c) {
#pragma unroll
        for (int n = 0; n < 4; ++n) *(LAS v4u*)(T + (n * 8 + (lane >> 3)) * 144 + (lane & 7) * 16) = wv[c][n];
#pragma unroll
        for (int tt = 0; tt < 4; ++tt) {
          const v2u a = __builtin_bit_cast(v2u, __builtin_amdgcn_ds_read_tr16_b64_v4i16((LAS s16x4*)(R + tt * 32)));
          const v2u b = __builtin_bit_cast(v2u, __builtin_amdgcn_ds_read_tr16_b64_v4i16((LAS s16x4*)(R + tt * 32 + 4 * 144)));
          wv[c][tt] = v4u{a.x, a.y, b.x, b.y};
          cs[tt] = dot2(a.x, ONES, dot2(a.y, ONES, dot2(b.x, ONES, dot2(b.y, ONES, cs[tt]))));
        }
      }
#pragma unroll
      for (int tt = 0; tt < 4; ++tt) { cs[tt] = rows_sum(cs[tt]); OPAQUE(cs[tt]); }
      float csum = quad == 0 ? cs[0] : quad == 1 ? cs[1] : quad == 2 ? cs[2] : cs[3];
      asm volatile("" : "+v"(csum));
      if (opq_s(w) == 11) {
        cnt_wait(L, C_P0, 8);
        wait_flags<4>(p.ws, SFLAGS + ((g ^ ((lane >> 3) & 4)) * 32 + (lane & 31)) * 8, true, tag);
#pragma unroll
        for (int i = 0; i < 18; ++i) win[i] = ldg(rs, lane16, CSQ + (g * 18 + i) * 896);
      }
      STAMP(2);
      BAR();
      STAMP(3);
      row_table1<2>(L, w - 8, lane);
      if (opq_s(w) == 11) {
        f32x4 cs = __builtin_bit_cast(f32x4, win[0]);
#pragma unroll
        for (int i = 1; i < 18; ++i) add4(cs, win[i]);
        if (lane < 56) *(LAS f32x4*)(L + L_TQ + lane16) = cs;
        row_table1<2>(L, 4, lane);
      }
      __builtin_amdgcn_s_sleep(BDELAY);
      STAMP(45);
      const int dst0 = sel(item < 108, wb + (252 + 54 * kind + 9 * k3) * KB, OOR);
#pragma unroll
      for (int tt = 0; tt < 4; ++tt)
#pragma unroll
        for (int c = 0; c < 3; ++c) stg(wv[c][tt], rs, lane16 + c * 3 * KB, dst0 + ((((4 * cb + tt) * 43) >> 7) * 105 + 4 * cb + tt) * KB);
      __builtin_amdgcn_raw_buffer_store_b32(__float_as_uint(csum), rs,
                                            lane * 4 + sel(kind == 0, CS1 + (k3 * 4608 + 576 * g + 64 * cb) * 4, OOR), 0, SC1);
#pragma unroll
      for (int i = 0; i < 5; ++i) {
        const int n = (w - 8) * 5 + i;
        stg(v4u{POISON, POISON, POISON, POISON}, rs, lane16, sel(n < 18, ab + sel(n < 9, A_O, A_X1 - 9 * KB) + (g * 9 + n) * KB, OOR));
      }
      SB();
      if (opq_s(w) == 8) issue_window<0, WIN>(win, rs, lane16, QBASE());
    }
  }
  int os0[3];
  const int wq = opq_s(w0);
  {
    const int T = 2 * (g0 >> 2) + sel(wq >= 6, 1, 0), s0 = 3 * sel(wq >= 6, wq - 6, wq);
#pragma unroll
    for (int i = 0; i < 3; ++i) {
      const int s = s0 + i;
      os0[i] = ABASE + j0 * ASL + A_O + sel(s < 16, ((s >> 1) * 9 + T * 2 + (s & 1)) * KB, ((s - 16) * 36 + 8) * KB + T * 256);
    }
  }
  const int oq0 = sel(wq == 5 || wq == 11, 9 * KB, 256);
  const int ft0 = (g0 & 3) * 9 + sel(wq < 9, wq, 0), xs0 = ((g0 >> 2) * 36 + (ft0 >> 1)) * KB + (ft0 & 1) * 512;

#pragma unroll 1
  for (int layer = 0; layer < 2; ++layer) {
    v4u xres;
    {
      LOCALS();
      STAMP(4 + layer * 16);
      {
        const int qbase = QBASE(), nbase = NBASE(), rw = 5 + 5 * layer;
        if (opq_s(w) < 4) qkv_tiles<2, 5, 2, 7, 4, WIN>(win, L, rs, lane, qbase, nbase, w, rw STAMP_PASS);
        if ((opq_s(w) & 14) == 4) qkv_tiles<2, 4, 0, 7, 1, 0>(win, L, rs, lane, qbase, nbase, 2 * w + 2, rw STAMP_PASS);
        if (opq_s(w) == 6) qkv_tiles<1, 5, 0, 10, 0, WIN>(win, L, rs, lane, qbase, nbase, 8, rw STAMP_PASS);
        if (opq_s(w) == 7) qkv_tiles<1, 5, 1, 10, 0, WIN>(win, L, rs, lane, qbase, nbase, 9, rw STAMP_PASS);
      }
      if (layer == 1 && bid == 0 && w == 10) {
#pragma unroll 1
        for (int i = 0; i < 4; ++i) wait_flags(p.ws, SFLAGS + (256 + i * 64 + lane) * 8, true, tag);
        if (lane == 0) set_flag(p.ws, 0, tag);
      }
      STAMP(5 + layer * 16);
      xres = *(LAS v4u*)(L + opq_s(xs0) + (quad & 1) * 18 * KB + (quad >> 1) * 256 + l15 * 16);
      if (layer == 0 && w >= 8) DRAIN();
      if ((opq_s(w) & 12) == 4) {
        BAR();
        if (w == 7 && layer == 0) {
          if (lane == 0) set_flag(p.ws, FLAGS + j * 128 + g * 8, tag);
          wait_flags(p.ws, FLAGS + j * 128 + (lane & 7) * 8, lane < 8, tag);
        }
        if (layer == 0 && w == 6) {
          if (lane == 0) set_flag(p.ws, SFLAGS + (256 + g * 32 + j) * 8, tag);
        }
        if (w < 6) issue_window<0, WIN>(win, rs, lane16, NBASE());
      }
      if ((opq_s(w) & 12) != 4) {
        const int T = w & 3, hp = w >> 3, Tp = (T & 1) ? T - 1 : 4;
        f32x4 mp, ms;
        const float neg = __uint_as_float(vconst<0xf149f2cau>());
        const int lo = l15 + ((T & 1) ? 9 : 1 + 4 * T), hi = (T & 1) ? 16 : j > 0 ? 8 + 4 * T : 0;
#pragma unroll
        for (int r = 0; r < 4; ++r) {
          const int jj = 4 * quad + r;
          mp[r] = ((jj >= lo) & (jj < hi)) ? 0.f : neg;
          ms[r] = (unsigned)(l15 - jj) <= 7u ? 0.f : neg;
        }
        int xs = L_QK + lane * 8 + T * 512, xp = L_QK + lane * 8 + Tp * 512, hs = xs + hp * 5120, hq = xp + hp * 5120;
        int om = ab + A_O + (g * 9 + T * 2 + hp) * KB + lane16;
        int oe = quad == hp ? ab + A_O + (g * 9 + 8) * KB + T * 256 + hp * 8 + l15 * 16 : WS_BYTES;
        unsigned qm0 = quad == 2 * hp ? ~0u : 0u, qm1 = quad == 2 * hp + 1 ? ~0u : 0u, zr = vconst<0>();
        v4u one4 = {vconst<ONES>(), vconst<ONES>(), vconst<ONES>(), vconst<ONES>()};
        asm volatile("" : "+v"(mp), "+v"(ms), "+v"(xs), "+v"(xp), "+v"(hs), "+v"(hq), "+v"(om), "+v"(oe), "+v"(qm0), "+v"(qm1), "+v"(zr), "+v"(one4));
        BAR();
        const v2u kxs = *(LAS v2u*)(L + xs + 40 * 512);
        const unsigned kxp = *(LAS unsigned*)(L + xp + 40 * 512 + 4);
        v2u q[2], ks[2], kp[2], vs[2], vp[2];
#pragma unroll
        for (int hh = 0; hh < 2; ++hh) {
          q[hh] = *(LAS v2u*)(L + hs + (50 + 5 * hh) * 512);
          kp[hh] = *(LAS v2u*)(L + hq + 5 * hh * 512);
          ks[hh] = *(LAS v2u*)(L + hs + 5 * hh * 512);
        }
        SB();
#pragma unroll
        for (int hh = 0; hh < 2; ++hh) {
          vp[hh] = *(LAS v2u*)(L + hq + (20 + 5 * hh) * 512);
          vs[hh] = *(LAS v2u*)(L + hs + (20 + 5 * hh) * 512);
        }
        const v2u vxp = *(LAS v2u*)(L + xp + 45 * 512), vxs = *(LAS v2u*)(L + xs + 45 * 512);
        SB();
        const f32x4 z = {0.f, 0.f, 0.f, 0.f};
        f32x4 sp[2], ss[2], o[2], ox[2], sm[2];
        float mx[2];
#pragma unroll
        for (int hh = 0; hh < 2; ++hh) {
          const v4u qf = {q[hh].x, q[hh].y, kxs.x & (hh ? qm1 : qm0), zr};
          sp[hh] = mfma(v4u{kp[hh].x, kp[hh].y, kxp, zr}, qf, mp);
          ss[hh] = mfma(v4u{ks[hh].x, ks[hh].y, kxs.y, zr}, qf, ms);
        }
#pragma unroll
        for (int hh = 0; hh < 2; ++hh) {
          const float m0 = __builtin_fmaxf(__builtin_fmaxf(sp[hh][0], sp[hh][1]), sp[hh][2]), m1 = __builtin_fmaxf(__builtin_fmaxf(ss[hh][0], ss[hh][1]), ss[hh][2]);
          mx[hh] = __builtin_fmaxf(__builtin_fmaxf(__builtin_fmaxf(__builtin_fmaxf(m0, sp[hh][3]), ss[hh][3]), m1), m1);
        }
        rows_max2(mx[0], mx[1]);
#pragma unroll
        for (int hh = 0; hh < 2; ++hh) {
          const float c = 0.23570226039551584f * 1.4426950408889634f, mc = -c * mx[hh];
          float e[8];
#pragma unroll
          for (int r = 0; r < 4; ++r) {
            e[r] = __builtin_amdgcn_exp2f(__builtin_fmaf(sp[hh][r], c, mc));
            e[4 + r] = __builtin_amdgcn_exp2f(__builtin_fmaf(ss[hh][r], c, mc));
          }
          const v4u pf = {pk(e[0], e[1]), pk(e[2], e[3]), pk(e[4], e[5]), pk(e[6], e[7])};
          sm[hh] = mfma(one4, pf, z);
          o[hh] = mfma(v4u{vp[hh].x, vp[hh].y, vs[hh].x, vs[hh].y}, pf, z);
          ox[hh] = mfma(v4u{vxp.x, vxp.y, vxs.x, vxs.y}, pf, z);
        }
        const float i0 = __builtin_amdgcn_rcpf(sm[0][0]), i1 = __builtin_amdgcn_rcpf(sm[1][0]);
        stg(v4u{pk(o[0][0] * i0, o[0][1] * i0), pk(o[0][2] * i0, o[0][3] * i0), pk(o[1][0] * i1, o[1][1] * i1), pk(o[1][2] * i1, o[1][3] * i1)}, rs, om, 0);
        __builtin_amdgcn_raw_buffer_store_b64(v2u{pk(ox[0][0] * i0, ox[0][1] * i0), pk(ox[1][2] * i1, ox[1][3] * i1)}, rs, oe, 0, SC1);
        STAMP(34);
        SETTLE();
      }
      STAMP(6 + layer * 16);
      BAR();
      STAMP(7 + layer * 16);
    }
    {
      LOCALS();
      const int th = g >> 2, ft = (g & 3) * 9 + w;
      {
        const int ov = quad * opq_s(oq0) + l15 * 16;
        RETRY_BEGIN();
        v4u orr[3];
#pragma unroll
        for (int i = 0; i < 3; ++i) orr[i] = ldg(rs, i ? ov : lane16, os0[i] + layer * ALY);
#pragma unroll
        for (int i = 0; i < 3; ++i) { *(LAS v4u*)(L + (w * 3 + i) * KB + lane16) = orr[i]; seen_ = umax4(seen_, orr[i]); }
        RETRY_END();
      }
      const v4u xr = xres;
      if (layer == 0 && w == 0) wait_flags<2>(p.ws, SFLAGS + (256 + g * 32 + (lane & 31)) * 8, lane < 32, tag);
      BAR();
      STAMP(8 + layer * 16);
      if (w < 9) {
        f32x4 acc[1][2];
        gemm18<1, 2, 0, P2N, 10>(acc, win, L + lane16, rs, lane16, 0, MBASE());
        float a[8];
#pragma unroll
        for (int r = 0; r < 4; ++r) {
          auto s = __builtin_amdgcn_permlane16_swap(__float_as_uint(acc[0][0][r]), __float_as_uint(acc[0][1][r]), false, false);
          a[r] = __uint_as_float(s[0]);
          a[4 + r] = __uint_as_float(s[1]);
        }
        const v4u o = {pk(a[0] + blo(xr.x), a[1] + bhi(xr.x)), pk(a[2] + blo(xr.y), a[3] + bhi(xr.y)),
                       pk(a[4] + blo(xr.z), a[5] + bhi(xr.z)), pk(a[6] + blo(xr.w), a[7] + bhi(xr.w))};
        stg(o, rs, lane16, ab + A_X1 + (th * 36 + ft) * KB);
        STAMP(35);
        SB();
        if (P2N < 3 * DST) issue_window<P2N, 3 * DST>(win, rs, lane16, MBASE());
        else SETTLE();
      }
      if (layer == 0 && opq_s(w) >= 9) {
        const int f4 = (w - 9) * 48 + (lane < 48 ? lane : 0);
        v4u cp[6];
#pragma unroll
        for (int i = 0; i < 6; ++i) cp[i] = ldg(rs, f4 * 16, CS1 + (i * 4608 + 576 * g) * 4);
        const v2u b = *(const GAS v2u*)(p.b1 + 576 * g + 4 * f4);
        *(LAS v2u*)(L + L_B2 + f4 * 8) = *(const GAS v2u*)(p.b2 + 4 * f4);
        f32x4 cs = __builtin_bit_cast(f32x4, cp[0]);
#pragma unroll
        for (int i = 1; i < 6; ++i) add4(cs, cp[i]);
        *(LAS f32x4*)(L + L_T1 + f4 * 16) = cs;
        *(LAS f32x4*)(L + L_T1 + 2304 + f4 * 16) = f32x4{blo(b.x), bhi(b.x), blo(b.y), bhi(b.y)};
      }
      if (w == 10 && lane < 10) ((volatile LAS int*)(L + L_HC))[lane] = 0;
      STAMP(9 + layer * 16);
      BAR();
      STAMP(10 + layer * 16);
    }
    {
      LOCALS();
      const int mbase = MBASE();
      const int T = w & 3, cls = w >> 2;
      if (opq_s(cls) == 0) {
        if (layer == 0) {
          __builtin_amdgcn_s_sleep(PZD);
          const v4u pz = {vconst<POISON>(), vconst<POISON>(), vconst<POISON>(), vconst<POISON>()};
#pragma unroll
          for (int i = 0; i < 8; ++i) {
            const int n = w * 8 + i;
            stg(pz, rs, lane16,
                sel(n < 30, ab + ALY + n * KB + sel(n < 12, A_X2 + g * 12 * KB, sel(n < 21, A_O, A_X1 - 9 * KB) + (g * 9 - 12) * KB), OOR));
          }
        }
      }
      if (opq_s(cls) > 0) {
        v4u xr[9];
        const int xo = (quad >> 1) * KB + ((quad & 1) * 2 + (T & 1)) * 256 + l15 * 16;
        RETRY_BEGIN();
#pragma unroll
        for (int i = 0; i < 9; ++i) xr[i] = ldg(rs, xo + (i & 1) * 2 * KB, ab + A_X1 + ((T >> 1) * 36 + 18 * (cls - 1) + (i >> 1) * 4) * KB);
#pragma unroll
        for (int i = 0; i < 9; ++i) { *(LAS v4u*)(L + (T * 18 + 9 * (cls - 1) + i) * KB + lane16) = xr[i]; seen_ = umax4(seen_, xr[i]); }
        RETRY_END();
        cnt_add(L, C_XA + cls - 1, lane);
        __builtin_amdgcn_s_setprio(3);
        const v4u one4 = {vconst<ONES>(), vconst<ONES>(), vconst<ONES>(), vconst<ONES>()};
        f32x4 sa = {0.f, 0.f, 0.f, 0.f}, sq = {0.f, 0.f, 0.f, 0.f};
        asm volatile("s_nop 1");
#pragma unroll
        for (int i = 0; i < 9; ++i) { sa = mfma(one4, xr[i], sa); sq = mfma(xr[i], xr[i], sq); }
        __builtin_amdgcn_s_setprio(0);
        if (layer == 0 && opq_s(w) >= 9) issue_window<0, 3 * DST>(win, rs, lane16, mbase);
        const float dg = (lane & 2) ? ((lane & 1) ? sq[3] : sq[2]) : ((lane & 1) ? sq[1] : sq[0]);
        if (quad == (l15 >> 2)) *(LAS f32x2*)(L + L_ST + ((T * 16 + l15) * 2 + cls - 1) * 8) = f32x2{sa[0], dg};
        cnt_add(L, C_ST, lane);
      }
      STAMP(11 + layer * 16);
      f32x4 acc[3][4];
#pragma unroll
      for (int t = 0; t < 3; ++t)
#pragma unroll
        for (int m = 0; m < 4; ++m) acc[t][m] = zero4();
      if (opq_s(cls) > 0) { row_table(L, lane); shift_acc(acc, L, w, quad, l15); }
      STAMP(49);
      __builtin_amdgcn_s_setprio(3);
      gemm_up(acc, win, L + lane16, rs, lane16, mbase, L);
      __builtin_amdgcn_s_setprio(0);
      STAMP(12 + layer * 16);
      {
        LAS unsigned char* H = L + L_H + lane16 + 3 * (w >> 1) * KB;
        if (opq_s(cls) == 0) {
          cnt_wait(L, C_RT, 1);
          shift_acc(acc, L, w, quad, l15);
        }
        f32x4 rt[4];
#pragma unroll
        for (int m = 0; m < 4; ++m) rt[m] = *(LAS f32x4*)(L + L_ST + KB + (m * 16 + l15) * 16);
#pragma unroll
        for (int m = 0; m < 4; ++m) {
          const v2u u0 = gelu4(acc[0][m], rt[m][2], rt[m][3], rt[m][1]), u1 = gelu4(acc[1][m], rt[m][2], rt[m][3], rt[m][1]);
          *(LAS v4u*)(H + (m * 18 + (w & 1)) * KB) = v4u{u0.x, u0.y, u1.x, u1.y};
          *(LAS v2u*)(H + (m * 18 + 2) * KB + (w & 1) * 8) = gelu4(acc[2][m], rt[m][2], rt[m][3], rt[m][1]);
        }
        cnt_add(L, C_H0 + cls, lane);
      }
      STAMP(13 + layer * 16);
      __builtin_amdgcn_s_setprio(2);
      gemm_down(acc, win, L + L_H + lane16, rs, lane16, mbase + 54 * KB, L STAMP_PASS);
      __builtin_amdgcn_s_setprio(0);
      STAMP(14 + layer * 16);
      const int pw = ab + A_PT + (g * 12 + w) * 6144;
      const int poff = (quad & 1) * 1536 + (quad >> 1) * 256 + l15 * 16;
#pragma unroll
      for (int t = 0; t < 3; ++t)
#pragma unroll
        for (int m = 0; m < 4; m += 2) {
          const v2u a = {pk(acc[t][m][0], acc[t][m][1]), pk(acc[t][m][2], acc[t][m][3])};
          const v2u b = {pk(acc[t][m + 1][0], acc[t][m + 1][1]), pk(acc[t][m + 1][2], acc[t][m + 1][3])};
          const v4u o = widen(a, b);
          const bool own = m + (quad & 1) == (g >> 1) && (l15 >> 3) == (g & 1);
          stg(o, rs, own ? WS_BYTES : poff + m * 1536 + t * 512, pw);
          if (own) *(LAS v4u*)(L + (((g >> 1) ^ 1) * 18 + w) * KB + ((2 * t + (quad >> 1)) * 8 + (lane & 7)) * 16) = o;
        }
      __builtin_amdgcn_s_setprio(0);
    }
    {
      LOCALS();
      OWNROWS(6);
      const bool act = lane < 48;
      float sum[8];
      {
        const v4u bb = *(LAS v4u*)(L + L_B2 + (act ? c8 : 0) * 16);
        const v4u xr = *(LAS v4u*)(L + (act ? piece : 0)), ow = *(LAS v4u*)(L + (((g >> 1) ^ 1) * 18 + w) * KB + lane16);
#pragma unroll
        for (int e = 0; e < 4; ++e) {
          sum[2 * e] = blo(xr[e]) + blo(ow[e]); OPAQUE(sum[2 * e]); sum[2 * e] += blo(bb[e]); OPAQUE(sum[2 * e]);
          sum[2 * e + 1] = bhi(xr[e]) + bhi(ow[e]) + bhi(bb[e]);
        }
      }
      int roff = (g >> 1) * 1536 + (lane >> 3) * 256 + (8 * (g & 1) + (lane & 7)) * 16 + (act ? 0 : OOR);
      int foff = WFLAGS + ((layer * 32 + j - (lane >> 3)) * 12 + w) * 128 + (lane < 8 ? lane : 3 + 4 * (lane & 1)) * 8;
      asm volatile("" : "+v"(roff), "+v"(foff), "+v"(sum[0]), "+v"(sum[1]), "+v"(sum[2]), "+v"(sum[3]), "+v"(sum[4]), "+v"(sum[5]), "+v"(sum[6]), "+v"(sum[7]));
      if (w >= 8) __builtin_amdgcn_s_barrier();
      DRAIN();
      if (lane == 0) set_flag(p.ws, WFLAGS + ((layer * 32 + j) * 12 + w) * 128 + g * 8, tag);
      STAMP(15 + layer * 16);
      if (layer == 0 && opq_s(w) < 8) {
        if (QA) cnt_wait(L, C_LC, 12);
        issue_window<0, QA>(win, rs, lane16, QBASE());
      }
      __builtin_amdgcn_s_barrier();
      wait_flags(p.ws, foff, lane < (layer == 0 && j > 0 ? 10 : 8), tag);
      if (w < 8) __builtin_amdgcn_s_barrier();
      asm volatile("" ::: "memory");
      STAMP(16 + layer * 16);
      v4u xb;
      {
        v4u pr[7];
#pragma unroll
        for (int i = 0; i < 7; ++i) pr[i] = ldg(rs, roff, ab + A_PT + (((i + 1 + g) & 7) * 12 + w) * 6144);
        SB();
        const unsigned sel_lo = opq_s(0x00003f80), sel_hi = opq_s(0x3f800000);
#pragma unroll
        for (int i = 0; i < 7; ++i)
#pragma unroll
          for (int e = 0; e < 4; ++e) { sum[2 * e] = dot2(pr[i][e], sel_lo, sum[2 * e]); sum[2 * e + 1] = dot2(pr[i][e], sel_hi, sum[2 * e + 1]); }
#pragma unroll
        for (int e = 0; e < 4; ++e) xb[e] = pk(sum[2 * e], sum[2 * e + 1]);
      }
      STAMP(17 + layer * 16);
      if (layer == 0) {
        float s1 = dot2(xb.x, ONES, dot2(xb.y, ONES, dot2(xb.z, ONES, dot2(xb.w, ONES, 0.f))));
        float s2 = dot2(xb.x, xb.x, dot2(xb.y, xb.y, dot2(xb.z, xb.z, dot2(xb.w, xb.w, 0.f))));
        if (!act) { s1 = 0.f; s2 = 0.f; }
        s1 += ror8(s1); OPAQUE(s1); s2 += ror8(s2);
        s1 = rows_sum(s1); OPAQUE(s1); s2 = rows_sum(s2);
        if (!act) xb = v4u{__float_as_uint(s1), __float_as_uint(s2), 0u, 0u};
        if (lane < 56) stg(xb, rs, lane16, ab + ALY + A_X2 + (g * 12 + w) * KB);
        SB();
        if (QB > QA && opq_s(w) < 8) issue_window<QA, QB>(win, rs, lane16, QBASE());
        else SETTLE();
        STAMP(36);
        const bool stl = (lane >> 3) == 6;
        LAS unsigned char* Iw = act ? L + (c8 >> 2) * KB + ((c8 & 3) * 16 + (lane & 7)) * 16 : L + 126 * KB + lane16;
        LAS unsigned char* Sw = L + L_ST + (w * 80 + (lane & 7)) * 8;
        BAR();
        STAMP(37);
        v4u xv[10];
        const int vo = lane < 56 ? lane16 : lane16 + WS_BYTES;
        xv[0] = xb;
        RETRY_BEGIN();
#pragma unroll
        for (int i = 1; i < 10; ++i)
          xv[i] = ldg(rs, vo, ab + ALY + A_X2 + w * KB + (i < 8 ? (i ^ g) * 12 * KB : (3 + 4 * (i - 8)) * 12 * KB - (j > 0 ? ASL : 0)));
        SB();
#pragma unroll
        for (int i = 0; i < 10; ++i) {
          const int mi = i < 8 ? i ^ g : i;
          *(LAS v4u*)(Iw + (act ? (mi >> 1) * 18 * KB + (mi & 1) * 128 : 0)) = xv[i];
          if (i) seen_ = umax4(seen_, xv[i]);
        }
        RETRY_END();
        if (stl) {
#pragma unroll
          for (int i = 0; i < 10; ++i) *(LAS v2u*)(Sw + (i < 8 ? i ^ g : i) * 64) = v2u{xv[i].x, xv[i].y};
        }
        BAR();
        STAMP(18);
        if (QB < WIN && opq_s(w) < 8) issue_window<QB, WIN>(win, rs, lane16, QBASE());
        if (w == 11) row_table1<12>(L, 4, lane);
        if (w >= 8) row_table1<12>(L, w - 8, lane);
        if (opq_s(w) == 8) issue_window<3 * DST, WIN>(win, rs, lane16, QBASE());
        if (opq_s(w) >= 8) issue_window<0, 3 * DST>(win, rs, lane16, QBASE());
      } else {
        const rsrc_t ro = __builtin_amdgcn_make_buffer_rsrc((void*)p.out, 0, 2048 * 576 * 2, 0x00020000);
        __builtin_amdgcn_raw_buffer_store_b128(xb, ro, act ? mytok * 1152 + c8 * 16 : OOR, 0, 0);
      }
    }
  }
  STAMP(40);
  STAMP_DUMP();
}
}

extern "C" void kernel_launch(void* const* d_in, const int* in_sizes, int n_in, void* d_out, int out_size, void* d_ws, size_t ws_size,
                              hipStream_t stream) {
  (void)in_sizes; (void)n_in; (void)out_size; (void)ws_size;
  Params p;
  p.x = (const unsigned short*)d_in[0];
  p.wqkv = (const unsigned short*)d_in[1];
  p.wo = (const unsigned short*)d_in[2];
  p.w1 = (const unsigned short*)d_in[4];
  p.b1 = (const unsigned short*)d_in[5];
  p.w2 = (const unsigned short*)d_in[6];
  p.b2 = (const unsigned short*)d_in[7];
  p.out = (unsigned short*)d_out;
  p.ws = (unsigned char*)d_ws;
  (void)hipFuncSetAttribute((const void*)fused_fwd, hipFuncAttributeMaxDynamicSharedMemorySize, LDS_BYTES);
  hipLaunchKernelGGL(fused_fwd, dim3(256), dim3(NTHR), LDS_BYTES, stream, p);
}
```

```cpp
#include <hip/hip_runtime.h>
#include <hip/hip_bf16.h>
#include <stdint.h>

namespace {
#define LAS __attribute__((address_space(3)))
#define GAS __attribute__((address_space(1)))
typedef unsigned v4u __attribute__((ext_vector_type(4)));
typedef unsigned v2u __attribute__((ext_vector_type(2)));
typedef float f32x4 __attribute__((ext_vector_type(4)));
typedef float f32x2 __attribute__((ext_vector_type(2)));
typedef __bf16 bf16x8 __attribute__((ext_vector_type(8)));
typedef __bf16 bf16x2 __attribute__((ext_vector_type(2)));
typedef short s16x4 __attribute__((ext_vector_type(4)));
typedef unsigned long long u64;
typedef __amdgpu_buffer_rsrc_t rsrc_t;

constexpr int KB = 1024;
constexpr int NTHR = 768;
constexpr int WIN = 18;
constexpr int XB = 2;
constexpr int P2N = 9;
constexpr int QA = 6;
constexpr int QB = 12;
constexpr int PZD = 20;
constexpr int BDELAY = 10;
constexpr int NTW = 1;
constexpr int DST = 5;
constexpr float EPS = 1e-5f;
constexpr int SC1 = 16;
constexpr float GC0 = -2.0f * 0.7978845608028654f * 1.4426950408889634f;
constexpr int FLAGS = 4096;
constexpr int SFLAGS = 32 * KB;
constexpr int WFLAGS = 48 * KB;
constexpr int CS1 = 192 * KB;
constexpr int CSQ = 520 * KB;
constexpr int WBASE = 704 * KB;
constexpr int WSL = (252 + 1296) * KB;
constexpr int WOB = WBASE + 8 * WSL;
constexpr int ABASE = 14 * 1024 * KB;
constexpr int IMG = 72 * KB;
constexpr int A_X2 = 0, A_O = 96 * KB, A_X1 = A_O + IMG, A_PT = A_X1 + IMG, ALY = 1024 * KB, ASL = 2 * ALY;
constexpr int WS_BYTES = ABASE + 32 * ASL;
static_assert(WOB + 648 * KB <= ABASE && A_PT + 8 * IMG <= ALY && CSQ + 8 * 14 * 1152 + 256 <= WBASE, "workspace map");
constexpr int L_H = IMG;
constexpr int L_QK = 90 * KB;
constexpr int L_ST = 145 * KB;
constexpr int L_T1 = L_ST + 7680;
constexpr int L_TQ = L_T1 + 4608;
constexpr int L_RT = L_TQ + 896;
constexpr int L_HC = L_RT + 640;
constexpr int L_B2 = L_HC + 64;
constexpr int LDS_BYTES = L_B2 + 1152;
static_assert(LDS_BYTES <= 160 * KB, "LDS");
enum { C_H0 = 0, C_ST = 3, C_RT = 4, C_LC = 5, C_XA = 6, C_R1 = 10, C_P0 = 11 };

struct Params {
  const unsigned short *x, *wqkv, *wo, *w1, *b1, *w2, *b2;
  unsigned short *out;
  unsigned char *ws;
};

#define SB() __builtin_amdgcn_sched_barrier(0)
#define BAR() do { asm volatile("s_waitcnt lgkmcnt(0)" ::: "memory"); __builtin_amdgcn_s_barrier(); asm volatile("" ::: "memory"); } while (0)
#define DRAIN() asm volatile("s_waitcnt vmcnt(0)" ::: "memory")
#define SETTLE() DRAIN()
#define OPAQUE(x) asm("" : "+v"(x))
#define KEEP(x) asm volatile("" :: "v"(x))

__device__ __forceinline__ f32x4 mfma(v4u a, v4u b, f32x4 c) {
  return __builtin_amdgcn_mfma_f32_16x16x32_bf16(__builtin_bit_cast(bf16x8, a), __builtin_bit_cast(bf16x8, b), c, 0, 0, 0);
}
#define MF(acc, a, b) asm volatile("v_mfma_f32_16x16x32_bf16 %0, %1, %2, %0" : "+v"(acc) : "v"(a), "v"(b))
#define MF0(acc, a, b) asm volatile("v_mfma_f32_16x16x32_bf16 %0, %1, %2, 0" : "=&v"(acc) : "v"(a), "v"(b))
template <int NT, int MT> __device__ __forceinline__ void acc_fence(f32x4 (&acc)[NT][MT]) {
#pragma unroll
  for (int t = 0; t < NT; ++t)
#pragma unroll
    for (int m = 0; m < MT; ++m) asm volatile("" : "+v"(acc[t][m]));
}
#define ACC_IN(acc) do { acc_fence(acc); asm volatile("s_nop 1"); SB(); } while (0)
#define ACC_OUT(acc) do { SB(); asm volatile("s_nop 7\n\ts_nop 1"); acc_fence(acc); } while (0)
__device__ __forceinline__ unsigned pk(float a, float b) {
  f32x2 v = {a, b};
  return __builtin_bit_cast(unsigned, __builtin_convertvector(v, bf16x2));
}
__device__ __forceinline__ float dot2(unsigned a, unsigned b, float c) {
  return __builtin_amdgcn_fdot2_f32_bf16(__builtin_bit_cast(bf16x2, a), __builtin_bit_cast(bf16x2, b), c, false);
}
constexpr unsigned ONES = 0x3f803f80u;
__device__ __forceinline__ float blo(unsigned u) { return __uint_as_float(u << 16); }
__device__ __forceinline__ float bhi(unsigned u) { return __uint_as_float(u & 0xffff0000u); }
__device__ __forceinline__ v4u ldg(rsrc_t rs, int voff, int soff) { return __builtin_amdgcn_raw_buffer_load_b128(rs, voff, soff, SC1); }
__device__ __forceinline__ void stg(v4u v, rsrc_t rs, int voff, int soff) { __builtin_amdgcn_raw_buffer_store_b128(v, rs, voff + soff, 0, SC1); }
__device__ __forceinline__ v4u ldf(rsrc_t rs, int lane16, int base, int f) { return ldg(rs, lane16 + (f & 3) * KB, base + (f >> 2) * 4 * KB); }
__device__ __forceinline__ int opq_v(int x) { asm volatile("" : "+v"(x)); return x; }
__device__ __forceinline__ int lane_id() { int x; asm volatile("v_mbcnt_lo_u32_b32 %0, -1, 0\n\tv_mbcnt_hi_u32_b32 %0, -1, %0" : "=v"(x)); return x; }
template <unsigned C> __device__ __forceinline__ unsigned vconst() { unsigned x; asm volatile("v_mov_b32 %0, %1" : "=v"(x) : "i"(C)); return x; }
__device__ __forceinline__ int sel(bool c, int a, int b) { return c ? a : b; }
constexpr int OOR = 0x10000000;
__device__ __forceinline__ int opq_s(int x) { asm volatile("" : "+s"(x)); return x; }
#define LOCALS() const int lane = lane_id(), w = opq_s(w0), j = opq_s(j0), g = opq_s(g0); \
  const int lane16 = lane * 16, quad = lane >> 4, l15 = lane & 15, ab = ABASE + j * ASL + layer * ALY, wb = WBASE + g * WSL; \
  (void)lane16; (void)quad; (void)l15; (void)ab; (void)wb
#define OWNROWS(CPW) const int c8 = CPW * w + (lane >> 3); \
  const int piece = ((g >> 1) * 18 + (c8 >> 2)) * KB + ((c8 & 3) * 16 + 8 * (g & 1) + (lane & 7)) * 16; \
  const int mytok = 64 * j + 2 * (16 * ((g >> 1) & 1) + 8 * (g & 1) + (lane & 7)) + (g >> 2)
#define GET_TAG() KEEP(cpf); tag = 1 + ((u64)(unsigned)__builtin_amdgcn_readfirstlane((int)(unsigned)tag0) | \
                             ((u64)(unsigned)__builtin_amdgcn_readfirstlane((int)(unsigned)(tag0 >> 32)) << 32))
#define NBASE() opq_s(nb0)
#define QBASE() opq_s(qb0)
#define MBASE() opq_s(mb0)
__device__ __forceinline__ float rows_sum(float x) {
  auto s = __builtin_amdgcn_permlane16_swap(__float_as_uint(x), __float_as_uint(x), false, false);
  x = __uint_as_float(s[0]) + __uint_as_float(s[1]);
  auto t = __builtin_amdgcn_permlane32_swap(__float_as_uint(x), __float_as_uint(x), false, false);
  return __uint_as_float(t[0]) + __uint_as_float(t[1]);
}
__device__ __forceinline__ float vmax(unsigned a, unsigned b) { float r; asm("v_max_f32 %0, %1, %2\n\ts_nop 1" : "=v"(r) : "v"(a), "v"(b)); return r; }
__device__ __forceinline__ void rows_max2(float& a, float& b) {
  auto s = __builtin_amdgcn_permlane16_swap(__float_as_uint(a), __float_as_uint(b), false, false);
  float x = vmax(s[0], s[1]);
  auto t = __builtin_amdgcn_permlane32_swap(__float_as_uint(x), __float_as_uint(x), false, false);
  x = vmax(t[0], t[1]);
  auto u = __builtin_amdgcn_permlane16_swap(__float_as_uint(x), __float_as_uint(x), false, false);
  a = __uint_as_float(u[0]);
  b = __uint_as_float(u[1]);
}
template <int CTRL> __device__ __forceinline__ float dpp(float x) {
  return __int_as_float(__builtin_amdgcn_mov_dpp(__float_as_int(x), CTRL, 0xf, 0xf, true));
}
__device__ __forceinline__ float ror8(float x) { return dpp<0x128>(x); }
__device__ __forceinline__ void add4(f32x4& a, v4u b) {
  float a0 = a[0], a1 = a[1], a2 = a[2], a3 = a[3];
  asm("v_add_f32 %0, %0, %4\n\tv_add_f32 %1, %1, %5\n\tv_add_f32 %2, %2, %6\n\tv_add_f32 %3, %3, %7"
      : "+v"(a0), "+v"(a1), "+v"(a2), "+v"(a3) : "v"(b.x), "v"(b.y), "v"(b.z), "v"(b.w));
  a = f32x4{a0, a1, a2, a3};
}
__device__ __forceinline__ v4u widen(v2u a, v2u b) {
  auto s0 = __builtin_amdgcn_permlane16_swap(a.x, b.x, false, false);
  auto s1 = __builtin_amdgcn_permlane16_swap(a.y, b.y, false, false);
  return v4u{s0[0], s1[0], s0[1], s1[1]};
}
__device__ __forceinline__ v2u gelu4(f32x4 a, float k0, float k1, float is) {
  float s0, s1, s2, s3;
  unsigned u0, u1;
  asm("v_mul_f32 %2, %6, %6\n\tv_mul_f32 %3, %7, %7\n\tv_mul_f32 %4, %8, %8\n\tv_mul_f32 %5, %9, %9\n\t"
      "v_fma_f32 %2, %2, %11, %10\n\tv_fma_f32 %3, %3, %11, %10\n\tv_fma_f32 %4, %4, %11, %10\n\tv_fma_f32 %5, %5, %11, %10\n\t"
      "v_mul_f32 %2, %2, %6\n\tv_mul_f32 %3, %3, %7\n\tv_mul_f32 %4, %4, %8\n\tv_mul_f32 %5, %5, %9\n\t"
      "v_exp_f32 %2, %2\n\tv_exp_f32 %3, %3\n\tv_exp_f32 %4, %4\n\tv_exp_f32 %5, %5\n\t"
      "v_fma_f32 %2, %2, %12, %12\n\tv_fma_f32 %3, %3, %12, %12\n\tv_fma_f32 %4, %4, %12, %12\n\tv_fma_f32 %5, %5, %12, %12\n\t"
      "v_rcp_f32 %2, %2\n\tv_rcp_f32 %3, %3\n\tv_rcp_f32 %4, %4\n\tv_rcp_f32 %5, %5\n\t"
      "v_mul_f32 %2, %2, %6\n\tv_mul_f32 %3, %3, %7\n\tv_mul_f32 %4, %4, %8\n\tv_mul_f32 %5, %5, %9\n\t"
      "v_cvt_pk_bf16_f32 %0, %2, %3\n\tv_cvt_pk_bf16_f32 %1, %4, %5"
      : "=&v"(u0), "=&v"(u1), "=&v"(s0), "=&v"(s1), "=&v"(s2), "=&v"(s3)
      : "v"(a[0]), "v"(a[1]), "v"(a[2]), "v"(a[3]), "v"(k0), "v"(k1), "v"(is));
  return v2u{u0, u1};
}
__device__ __forceinline__ void shift4(f32x4& a, f32x4 cs, f32x4 b, float nmean, float is) {
  float a0 = a[0], a1 = a[1], a2 = a[2], a3 = a[3];
  asm("v_fmac_f32 %0, %12, %4\n\tv_fmac_f32 %1, %12, %5\n\tv_fmac_f32 %2, %12, %6\n\tv_fmac_f32 %3, %12, %7\n\t"
      "v_fmac_f32 %0, %13, %8\n\tv_fmac_f32 %1, %13, %9\n\tv_fmac_f32 %2, %13, %10\n\tv_fmac_f32 %3, %13, %11"
      : "+v"(a0), "+v"(a1), "+v"(a2), "+v"(a3)
      : "v"(cs[0]), "v"(cs[1]), "v"(cs[2]), "v"(cs[3]), "v"(b[0]), "v"(b[1]), "v"(b[2]), "v"(b[3]), "v"(nmean), "v"(is));
  a = f32x4{a0, a1, a2, a3};
}
__device__ __forceinline__ int cnt_get(LAS unsigned char* L, int i) { return ((volatile LAS int*)(L + L_HC))[i]; }
__device__ __forceinline__ void cnt_wait(LAS unsigned char* L, int i, int n) {
  while (__builtin_amdgcn_readfirstlane(cnt_get(L, i)) < n) __builtin_amdgcn_s_sleep(1);
  asm volatile("" ::: "memory");
}
__device__ __forceinline__ void cnt_add(LAS unsigned char* L, int i, int lane) {
  asm volatile("s_waitcnt lgkmcnt(0)" ::: "memory");
  if (lane == 0) __hip_atomic_fetch_add((LAS int*)(L + L_HC) + i, 1, __ATOMIC_RELAXED, __HIP_MEMORY_SCOPE_WORKGROUP);
}
template <int NP> __device__ __forceinline__ void row_table1(LAS unsigned char* L, int t5, int lane) {
  const int r = t5 * 16 + (lane & 15), q = lane >> 4;
  float t1 = 0.f, t2 = 0.f;
#pragma unroll
  for (int i = 0; i < (NP + 3) / 4; ++i)
    if (NP % 4 == 0 || q < NP) { const f32x2 v = *(LAS f32x2*)(L + L_ST + ((i * 4 + q) * 80 + r) * 8); t1 += v.x; OPAQUE(t1); t2 += v.y; }
  t1 = rows_sum(t1); OPAQUE(t1); t2 = rows_sum(t2);
  const float nmean = t1 * (-1.0f / 576.0f);
  const float var = fmaxf(__builtin_fmaf(-nmean, nmean, t2 * (1.0f / 576.0f)), 0.f);
  const float rstd = __builtin_amdgcn_rsqf(var + EPS);
  if (q < 2) *(LAS float*)(L + L_RT + q * 320 + r * 4) = q ? nmean * rstd : rstd;
  cnt_add(L, C_R1, lane);
}
__device__ __forceinline__ v2u ln_pack(f32x4 a, f32x4 r, f32x4 c, f32x4 nm) {
  float t0, t1, t2, t3;
  unsigned u0, u1;
  asm("v_mul_f32 %2, %14, %18\n\tv_mul_f32 %3, %15, %19\n\tv_mul_f32 %4, %16, %20\n\tv_mul_f32 %5, %17, %21\n\t"
      "v_fmac_f32 %2, %6, %10\n\tv_fmac_f32 %3, %7, %11\n\tv_fmac_f32 %4, %8, %12\n\tv_fmac_f32 %5, %9, %13\n\t"
      "v_cvt_pk_bf16_f32 %0, %2, %3\n\tv_cvt_pk_bf16_f32 %1, %4, %5"
      : "=&v"(u0), "=&v"(u1), "=&v"(t0), "=&v"(t1), "=&v"(t2), "=&v"(t3)
      : "v"(a[0]), "v"(a[1]), "v"(a[2]), "v"(a[3]), "v"(r[0]), "v"(r[1]), "v"(r[2]), "v"(r[3]), "v"(c[0]), "v"(c[1]), "v"(c[2]), "v"(c[3]),
        "v"(nm[0]), "v"(nm[1]), "v"(nm[2]), "v"(nm[3]));
  return v2u{u0, u1};
}
__device__ __forceinline__ f32x4 zero4() {
  float a, b, c, d;
  asm volatile("v_mov_b32 %0, 0\n\tv_mov_b32 %1, 0\n\tv_mov_b32 %2, 0\n\tv_mov_b32 %3, 0" : "=v"(a), "=v"(b), "=v"(c), "=v"(d));
  return f32x4{a, b, c, d};
}
__device__ __forceinline__ f32x4 splat(float x) { return f32x4{x, x, x, x}; }
__device__ __forceinline__ void row_table(LAS unsigned char* L, int lane) {
  cnt_wait(L, C_ST, 8);
  const f32x4 q = *(LAS f32x4*)(L + L_ST + lane * 16);
  float t1 = q[0] + q[2];
  OPAQUE(t1);
  float nmean = t1 * (-1.0f / 576.0f);
  const float var = fmaxf(__builtin_fmaf(-nmean, nmean, (q[1] + q[3]) * (1.0f / 576.0f)), 0.f);
  const float r1 = __builtin_amdgcn_rsqf(var + EPS);
  const float sc = r1 * __builtin_amdgcn_rsqf(var * r1 * r1 + EPS);
  float k0 = GC0 * sc;
  OPAQUE(k0);
  *(LAS f32x4*)(L + L_ST + KB + lane * 16) = f32x4{nmean, __builtin_amdgcn_rcpf(sc), k0, k0 * (0.044715f * sc * sc)};
  cnt_add(L, C_RT, lane);
}
__device__ __forceinline__ void shift_acc(f32x4 (&acc)[3][4], LAS unsigned char* L, int w, int quad, int l15) {
  f32x2 rt[4];
#pragma unroll
  for (int m = 0; m < 4; ++m) rt[m] = *(LAS f32x2*)(L + L_ST + KB + (m * 16 + l15) * 16);
#pragma unroll
  for (int t = 0; t < 3; ++t) {
    const f32x4 cs = *(LAS f32x4*)(L + L_T1 + (48 * w + 16 * t + 4 * quad) * 4), bb = *(LAS f32x4*)(L + L_T1 + 2304 + (48 * w + 16 * t + 4 * quad) * 4);
#pragma unroll
    for (int m = 0; m < 4; ++m) shift4(acc[t][m], cs, bb, rt[m][0], rt[m][1]);
  }
}
template <int SLEEP = 1> __device__ __forceinline__ void wait_flags(const unsigned char* ws, int off, bool active, u64 tag) {
  const GAS u64* f = (const GAS u64*)(ws + off);
  asm volatile("" : "+s"(tag));
#pragma unroll 1
  for (int spins = 0; spins < (1 << 16); ++spins) {
    const u64 v = active ? __hip_atomic_load(f, __ATOMIC_RELAXED, __HIP_MEMORY_SCOPE_AGENT) : tag;
    if (__all(v == tag)) break;
    __builtin_amdgcn_s_sleep(SLEEP);
  }
  asm volatile("" ::: "memory");
}
__device__ __forceinline__ void set_flag(unsigned char* ws, int off, u64 tag) {
  asm volatile("" : "+s"(tag));
  __hip_atomic_store((GAS u64*)(ws + off), tag, __ATOMIC_RELAXED, __HIP_MEMORY_SCOPE_AGENT);
}
constexpr unsigned POISON = 0xffffffffu;
__device__ __forceinline__ unsigned umax4(unsigned m, v4u v) { return max(max(max(m, v.x), v.y), max(v.z, v.w)); }
#define RETRY_BEGIN() _Pragma("unroll 1") for (int spins_ = 0; spins_ < (1 << 15); ++spins_) { unsigned seen_ = 0
#define RETRY_END() if (!__any(seen_ == POISON)) break; __builtin_amdgcn_s_sleep(2); asm volatile("" ::: "memory"); }
template <int LO, int HI> __device__ __forceinline__ void issue_window(v4u (&win)[WIN], rsrc_t rs, int lane16, int base) {
#pragma unroll
  for (int i = LO; i < HI; ++i) win[i] = ldf(rs, lane16, base, i);
  SB();
}

template <int NT, int MT, int SWAP, int NNEXT, int R, int LO, int HI>
__device__ __forceinline__ void gemm18_steps(f32x4 (&acc)[NT][MT], v4u (&win)[WIN], v4u (&act)[R], LAS unsigned char* img, LAS unsigned char* img2, rsrc_t rs,
                                             int lane16, int wbase, int nvo, int nbase) {
#pragma unroll
  for (int n = LO; n < HI; ++n) {
    const int s = n / MT, m = n % MT, nn = n + R - 1;
    if (n < 18 * MT) {
#pragma unroll
      for (int t = 0; t < NT; ++t) {
        if (s == 0) {
          if ((SWAP >> t) & 1) MF0(acc[t][m], act[n % R], win[t]);
          else MF0(acc[t][m], win[t], act[n % R]);
        } else if ((SWAP >> t) & 1) MF(acc[t][m], act[n % R], win[(s * NT + t) % WIN]);
        else MF(acc[t][m], win[(s * NT + t) % WIN], act[n % R]);
      }
      SB();
    }
    if (nn < 18 * MT) act[nn % R] = *(LAS v4u*)(nn % MT < 3 ? img + ((nn % MT) * 18 + nn / MT) * KB : img2 + ((nn % MT - 3) * 18 + nn / MT) * KB);
    if (m == 0 && s > 0) {
#pragma unroll
      for (int t = 0; t < NT; ++t) {
        const int f = (s - 1) * NT + t;
        if (f + WIN < 18 * NT) win[f % WIN] = ldf(rs, lane16, wbase, f + WIN);
        else if (f % WIN < NNEXT) win[f % WIN] = ldf(rs, nvo, nbase, f % WIN);
      }
    }
    SB();
    if (n < 18 * MT) {
      KEEP(act[n % R]);
#pragma unroll
      for (int t = 0; t < NT; ++t) KEEP(win[(s * NT + t) % WIN]);
      SB();
    }
  }
}
template <int NT, int MT, int SWAP, int NNEXT, int R>
__device__ __forceinline__ void gemm18(f32x4 (&acc)[NT][MT], v4u (&win)[WIN], LAS unsigned char* img, rsrc_t rs, int lane16, int wbase, int nbase) {
  v4u act[R];
  LAS unsigned char* img2 = img + 54 * KB;
  if (MT > 3) asm volatile("" : "+v"(img2));
#pragma unroll
  for (int n = 0; n < R - 1; ++n) { act[n] = *(LAS v4u*)(n % MT < 3 ? img + ((n % MT) * 18 + n / MT) * KB : img2 + ((n % MT - 3) * 18 + n / MT) * KB); SB(); }
  gemm18_steps<NT, MT, SWAP, NNEXT, R, 0, 6 * MT>(acc, win, act, img, img2, rs, lane16, wbase, lane16, nbase);
  gemm18_steps<NT, MT, SWAP, NNEXT, R, 6 * MT, 12 * MT>(acc, win, act, img, img2, rs, lane16, wbase, lane16, nbase);
  gemm18_steps<NT, MT, SWAP, NNEXT, R, 12 * MT, 18 * MT + 1>(acc, win, act, img, img2, rs, lane16, wbase, lane16, nbase);
  ACC_OUT(acc);
}
template <int NT, int MT, int SWAP, int R, int tstep, int NNEXT>
__device__ __forceinline__ void qkv_tiles(v4u (&win)[WIN], LAS unsigned char* L, rsrc_t rs, int lane, int wbase, int nbase, int tile0, int rwait) {
  f32x4 acc[NT][MT];
  gemm18<NT, MT, SWAP, NNEXT, R>(acc, win, L + lane * 16, rs, lane * 16, wbase, nbase);
  LAS unsigned char *Rq = L + L_RT + (lane & 48), *Rl = L + L_RT + (lane & 15) * 4, *Tq = L + L_TQ + tile0 * 64 + (lane & 48),
                    *Tl = L + L_TQ + tile0 * 64 + (lane & 15) * 4, *X = L + L_QK + lane * 8 + tile0 * 2560;
  asm volatile("" : "+v"(Rq), "+v"(Rl), "+v"(Tq), "+v"(Tl), "+v"(X));
  cnt_wait(L, C_R1, rwait);
  v2u u[NT][MT];
#pragma unroll
  for (int t = 0; t < NT; ++t) {
    if ((SWAP >> t) & 1) {
      const f32x4 cs = splat(*(LAS float*)(Tl + t * tstep * 64));
#pragma unroll
      for (int m = 0; m < MT; ++m) u[t][m] = ln_pack(acc[t][m], *(LAS f32x4*)(Rq + m * 64), cs, *(LAS f32x4*)(Rq + 320 + m * 64));
    } else {
      const f32x4 cs = *(LAS f32x4*)(Tq + t * tstep * 64);
#pragma unroll
      for (int m = 0; m < MT; ++m) u[t][m] = ln_pack(acc[t][m], splat(*(LAS float*)(Rl + m * 64)), cs, splat(*(LAS float*)(Rl + 320 + m * 64)));
    }
  }
#pragma unroll
  for (int t = 0; t < NT; ++t)
#pragma unroll
    for (int m = 0; m < MT; ++m) *(LAS v2u*)(X + (t * tstep * 5 + m) * 512) = u[t][m];
}
template <int C>
__device__ __forceinline__ void mlp_chunk(f32x4 (&acc)[3][4], v4u (&win)[WIN], v4u (&act)[8], LAS unsigned char* img, rsrc_t rs, int lane16, int rb,
                                          LAS unsigned char* L) {
  int hv = 4;
#pragma unroll
  for (int s = 0; s < 6; ++s) {
    if (C != 2 && s == 5) {
      if (__builtin_amdgcn_readfirstlane(hv) < 4) cnt_wait(L, C < 0 ? C_XA + 1 : C_H0 + C + 1, 4);
      asm volatile("" ::: "memory");
      SB();
    }
    if (C == 2 && s == 6 - DST) {
      if (lane16 == 0) __hip_atomic_fetch_add((LAS int*)(L + L_HC) + C_LC, 1, __ATOMIC_RELAXED, __HIP_MEMORY_SCOPE_WORKGROUP);
      SB();
    }
    asm volatile("" :: "v"(act[(s & 1) * 4]), "v"(act[(s & 1) * 4 + 1]), "v"(act[(s & 1) * 4 + 2]), "v"(act[(s & 1) * 4 + 3]), "v"(win[s * 3]),
                 "v"(win[s * 3 + 1]), "v"(win[s * 3 + 2]));
    SB();
#pragma unroll
    for (int t = 0; t < 3; ++t)
#pragma unroll
      for (int m = 0; m < 4; ++m) {
        const int mm = t == 1 ? 3 - m : m;
        if (C == 0 && s == 0) MF0(acc[t][mm], win[t], act[mm]);
        else MF(acc[t][mm], win[s * 3 + t], act[(s & 1) * 4 + mm]);
        SB();
        if (t == 0 && !(C == 2 && s == 5)) act[((s + 1) & 1) * 4 + m] = *(LAS v4u*)(img + (m * 18 + s + 1) * KB);
        if (C != 2 && s == 3 && t == 2 && m == 0) hv = cnt_get(L, C < 0 ? C_XA + 1 : C_H0 + C + 1);
        if (t == 1 && m < 3 && !(C == 2 && s + DST >= 6))
          win[((s + DST) % 6) * 3 + m] = ldf(rs, lane16, rb, (s + DST) * 3 + m);
        SB();
      }
  }
}
__device__ __forceinline__ void gemm_up(f32x4 (&acc)[3][4], v4u (&win)[WIN], LAS unsigned char* img, rsrc_t rs, int lane16, int rb, LAS unsigned char* L) {
  v4u act[8];
  cnt_wait(L, C_XA, 4);
#pragma unroll
  for (int n = 0; n < 4; ++n) { act[n] = *(LAS v4u*)(img + n * 18 * KB); SB(); }
  ACC_IN(acc);
#pragma unroll 1
  for (int c = 0; c < 3; ++c) {
    mlp_chunk<-1>(acc, win, act, img, rs, lane16, rb, L);
    img += 6 * KB;
    rb += 18 * KB;
  }
  ACC_OUT(acc);
}
__device__ __forceinline__ void gemm_down(f32x4 (&acc)[3][4], v4u (&win)[WIN], LAS unsigned char* img, rsrc_t rs, int lane16, int rb, LAS unsigned char* L) {
  v4u act[8];
  cnt_wait(L, C_H0, 4);
#pragma unroll
  for (int n = 0; n < 4; ++n) { act[n] = *(LAS v4u*)(img + n * 18 * KB); SB(); }
  mlp_chunk<0>(acc, win, act, img, rs, lane16, rb, L);
  img += 6 * KB;
  asm volatile("" : "+v"(img));
  mlp_chunk<1>(acc, win, act, img, rs, lane16, rb + 18 * KB, L);
  img += 6 * KB;
  asm volatile("" : "+v"(img));
  mlp_chunk<2>(acc, win, act, img, rs, lane16, rb + 36 * KB, L);
  ACC_OUT(acc);
}

__global__ void __launch_bounds__(NTHR, 3) fused_fwd(Params p) {
  extern __shared__ __attribute__((aligned(16))) unsigned char smem[];
  LAS unsigned char* L = (LAS unsigned char*)smem;
  const int w0 = __builtin_amdgcn_readfirstlane(threadIdx.x >> 6);
  const int bid = blockIdx.x;
  u64 pc0;
  int clen;
  asm volatile("s_getpc_b64 %0\n.Lfwd_begin:\n\ts_mov_b32 %1, .Lfwd_end-.Lfwd_begin" : "=s"(pc0), "=s"(clen));
  unsigned cpf = 0;
  const int coff = (((blockIdx.x >> 3) % 11) * 64 + (threadIdx.x & 63)) * 64;
  if (w0 == 11 && coff < clen) cpf = *(const GAS unsigned*)(pc0 + coff);
  constexpr unsigned MPX = 8 / XB;
  const int j0 = ((blockIdx.x & 7) / XB) * (32 / MPX) + (blockIdx.x >> 3) / MPX;
  const int g0 = ((blockIdx.x & 7) % XB) * MPX + (blockIdx.x >> 3) % MPX;
  const rsrc_t rs = __builtin_amdgcn_make_buffer_rsrc((void*)p.ws, 0, WS_BYTES, 0x00020000);
  const int mb0 = WBASE + g0 * WSL + (252 + w0 * 108) * KB;
  const int nb0 = sel(w0 < 9, WOB + ((g0 & 3) * 9 + w0) * 18 * KB, mb0);
  const int qb0 = sel(w0 < 8, WBASE + g0 * WSL + sel(w0 < 6, w0 * 36, 108 + w0 * 18) * KB, nb0);
  const u64 tag0 = __hip_atomic_load((const GAS u64*)p.ws, __ATOMIC_RELAXED, __HIP_MEMORY_SCOPE_AGENT);
  u64 tag = 0;
  v4u win[WIN];
  {
    constexpr int layer = 0;
    LOCALS();
    const rsrc_t rx = __builtin_amdgcn_make_buffer_rsrc((void*)p.x, 0, 2048 * 576 * 2, 0x00020000);
    const int xrow = lane >> 2;
#define LOAD_X() v4u xr[9]; _Pragma("unroll") for (int i = 0; i < 9; ++i) xr[i] = __builtin_amdgcn_raw_buffer_load_b128(rx, xoff + i * 64, 0, 0); SB()
#define STAGE_X() do { float s1 = 0.f, s2 = 0.f; \
      _Pragma("unroll") for (int i = 0; i < 9; ++i) { \
        if (w < 10) *(LAS v4u*)(L + (w * 9 + i) * KB + (lane & 3) * 256 + xrow * 16) = xr[i]; \
        _Pragma("unroll") for (int e = 0; e < 4; ++e) { s1 = dot2(xr[i][e], ONES, s1); s2 = dot2(xr[i][e], xr[i][e], s2); } } \
      s1 += dpp<0xB1>(s1); s2 += dpp<0xB1>(s2); s1 += dpp<0x4E>(s1); s2 += dpp<0x4E>(s2); \
      if (w < 10 && (lane & 3) == 0) *(LAS f32x2*)(L + L_ST + ((w & 1) * 80 + (w >> 1) * 16 + xrow) * 8) = f32x2{s1, s2}; } while (0)
    if (opq_s(w) < 8) {
      LAS unsigned char* T = L + L_QK + w * 3072;
      const int uf = 8 * j + w, uc = sel(uf < 252, uf, 0), us = (uc * 293) >> 12, tp = uc - us * 14;
      const int tile = sel(tp < 4, 10 + tp, sel(tp == 4, 8, sel(tp == 13, 9, tp - 5)));
      const bool vx = tile == 9, ex = (tile & 14) == 8;
      const int c0 = 72 * g + sel(ex, 16, ((tile + sel(tile >= 10, 2, 0)) & 3) * 18) + sel(tile < 4, 576, sel(tile < 8 || vx, 1152, 0));
      const rsrc_t rq = __builtin_amdgcn_make_buffer_rsrc((void*)p.wqkv, 0, 576 * 1728 * 2, 0x00020000);
      const int qo = (lane >> 1) * 3456 + (lane & 1) * sel(ex, sel(vx, 0, 72), 16), qs = us * 32 * 3456 + c0 * 2;
      v4u qv;
      if (ex) {
        const int cA = sel(vx, 36, 1152), cB = sel(vx, 72, 36);
        qv = v4u{__builtin_amdgcn_raw_buffer_load_b32(rq, qo, qs, 0), __builtin_amdgcn_raw_buffer_load_b32(rq, qo, qs + cA, 0),
                 __builtin_amdgcn_raw_buffer_load_b32(rq, qo, qs + cB, 0), __builtin_amdgcn_raw_buffer_load_b32(rq, qo, qs + cA + cB, 0)};
      } else qv = __builtin_amdgcn_raw_buffer_load_b128(rq, qo, qs, 0);
      SB();
      const int of = 3 * j + w, oc = (g >> 2) * 81 + of, os = (oc * 57) >> 9, ot = oc - os * 9;
      const bool hasO = w < 3 && of < 81;
      const bool ox = os >= 16;
      const int orow = quad * sel(ox, 72, 4) + ((lane >> 1) & 1) + ((lane >> 2) & 1) * sel(ox, 18, 2) + ((lane >> 3) & 1) * sel(ox, 36, 18);
      const rsrc_t ro = __builtin_amdgcn_make_buffer_rsrc((void*)p.wo, 0, 576 * 576 * 2, 0x00020000);
      const v4u ov = __builtin_amdgcn_raw_buffer_load_b128(ro, orow * 1152 + (lane & 1) * 16 + sel(hasO, 0, OOR),
                                                           sel(ox, 288 * (os - 16) + 16, 36 * os) * 1152 + 32 * ((g & 3) * 9 + ot), 0);
      SB();
      const int xoff = (64 * j + 32 * ((w >> 1) & 1) + (w >> 2) + 2 * xrow) * 1152 + (w & 1) * 576 + (lane & 3) * 16;
      __builtin_amdgcn_s_barrier();
      LOAD_X();
      *(LAS v4u*)(T + (lane >> 1) * 48 + (lane & 1) * 16) = (vx && (lane & 1)) ? v4u{0u, 0u, 0u, 0u} : qv;
      *(LAS v4u*)(T + 1536 + (lane >> 1) * 48 + (lane & 1) * 16) = ov;
      LAS unsigned char* R = T + (8 * quad + (l15 >> 2)) * 48 + (lane & 3) * 8;
      const v2u a = __builtin_bit_cast(v2u, __builtin_amdgcn_ds_read_tr16_b64_v4i16((LAS s16x4*)(R)));
      const v2u b = __builtin_bit_cast(v2u, __builtin_amdgcn_ds_read_tr16_b64_v4i16((LAS s16x4*)(R + 4 * 48)));
      const v2u c = __builtin_bit_cast(v2u, __builtin_amdgcn_ds_read_tr16_b64_v4i16((LAS s16x4*)(R + 1536)));
      const v2u d = __builtin_bit_cast(v2u, __builtin_amdgcn_ds_read_tr16_b64_v4i16((LAS s16x4*)(R + 1536 + 4 * 48)));
      stg(v4u{a.x, a.y, b.x, b.y}, rs, lane16,
          sel(uf >= 252, OOR, wb + sel(tile < 8, (tile & 3) * 36 + us * 2 + (tile >> 2), sel(tile < 10, 72 + tile * 18 + us, (tile >> 1) * 36 - 36 + us * 2 + (tile & 1))) * KB));
      stg(v4u{c.x, c.y, d.x, d.y}, rs, lane16, sel(hasO, WOB + ((g & 3) * 162 + ot * 18 + os) * KB, OOR));
      const float cq = rows_sum(dot2(a.x, ONES, dot2(a.y, ONES, dot2(b.x, ONES, dot2(b.y, ONES, 0.f)))));
      __builtin_amdgcn_raw_buffer_store_b32(__float_as_uint(cq), rs, sel(lane < 16, lane * 4, OOR) + sel(uf < 252, CSQ + ((g * 18 + us) * 14 + tile) * 64, OOR), 0, SC1);
      SB();
      DRAIN();
      GET_TAG();
      if (lane == 0 && __hip_atomic_fetch_add((LAS int*)(L + L_HC) + C_P0, 1, __ATOMIC_RELAXED, __HIP_MEMORY_SCOPE_WORKGROUP) == 7)
        set_flag(p.ws, SFLAGS + (g * 32 + j) * 8, tag);
      STAGE_X();
      BAR();
      issue_window<0, WIN>(win, rs, lane16, QBASE());
    }
    if (opq_s(w) >= 8) {
      LAS unsigned char* T = L + 125 * KB + (w - 8) * 4608;
      const int item = sel(w < 11, 3 * j + w - 8, sel((j & 7) < 3, 96 + 3 * (j >> 3) + (j & 7), 127)), rbk = (item * 57) >> 9, cb = item - rbk * 9, kind = sel(rbk >= 6, 1, 0), k3 = rbk - kind * 6;
      const int rn = sel(kind, 4, 8), ld = sel(kind, 1152, 9216);
      const rsrc_t rw = __builtin_amdgcn_make_buffer_rsrc((void*)(kind == 0 ? p.w1 : p.w2), 0, 576 * 4608 * 2, 0x00020000);
      const int jj = lane >> 3;
      const int woff = (jj & 3) * ld + (lane & 7) * 16 + sel(item < 108, 96 * k3 * ld + g * sel(kind, 576 * 1152, 1152) + 128 * cb, OOR);
      const int xoff = (sel(j > 0, 64 * j - 16, 0) + 2 * (xrow & 7) + (xrow >> 3)) * 1152 + (lane & 3) * 16 + sel(w < 10, (w & 1) * 576, OOR);
      if (w == 11 && lane < 16) ((volatile LAS int*)(L + L_HC))[lane] = 0;
      BAR();
      LOAD_X();
      v4u wv[3][4];
#pragma unroll
      for (int n = 0; n < 4; ++n)
#pragma unroll
        for (int c = 0; c < 3; ++c)
          wv[c][n] = __builtin_amdgcn_raw_buffer_load_b128(rw, woff + (jj >> 2) * (sel(kind, c < 2 ? 16 : 48, 4) * ld),
                                                           (n * rn + sel(kind, c < 2 ? 48 * c : 32, 32 * c)) * ld, NTW ? 2 : 0);
      SB();
      GET_TAG();
      STAGE_X();
      float cs[4] = {0.f, 0.f, 0.f, 0.f};
      LAS unsigned char* R = T + (8 * quad + (l15 >> 2)) * 144 + (lane & 3) * 8;
#pragma unroll
      for (int c = 0; c < 3; ++c) {
#pragma unroll
        for (int n = 0; n < 4; ++n) *(LAS v4u*)(T + (n * 8 + (lane >> 3)) * 144 + (lane & 7) * 16) = wv[c][n];
#pragma unroll
        for (int tt = 0; tt < 4; ++tt) {
          const v2u a = __builtin_bit_cast(v2u, __builtin_amdgcn_ds_read_tr16_b64_v4i16((LAS s16x4*)(R + tt * 32)));
          const v2u b = __builtin_bit_cast(v2u, __builtin_amdgcn_ds_read_tr16_b64_v4i16((LAS s16x4*)(R + tt * 32 + 4 * 144)));
          wv[c][tt] = v4u{a.x, a.y, b.x, b.y};
          cs[tt] = dot2(a.x, ONES, dot2(a.y, ONES, dot2(b.x, ONES, dot2(b.y, ONES, cs[tt]))));
        }
      }
#pragma unroll
      for (int tt = 0; tt < 4; ++tt) { cs[tt] = rows_sum(cs[tt]); OPAQUE(cs[tt]); }
      float csum = quad == 0 ? cs[0] : quad == 1 ? cs[1] : quad == 2 ? cs[2] : cs[3];
      asm volatile("" : "+v"(csum));
      if (opq_s(w) == 11) {
        cnt_wait(L, C_P0, 8);
        wait_flags<4>(p.ws, SFLAGS + ((g ^ ((lane >> 3) & 4)) * 32 + (lane & 31)) * 8, true, tag);
#pragma unroll
        for (int i = 0; i < 18; ++i) win[i] = ldg(rs, lane16, CSQ + (g * 18 + i) * 896);
      }
      BAR();
      row_table1<2>(L, w - 8, lane);
      if (opq_s(w) == 11) {
        f32x4 cs = __builtin_bit_cast(f32x4, win[0]);
#pragma unroll
        for (int i = 1; i < 18; ++i) add4(cs, win[i]);
        if (lane < 56) *(LAS f32x4*)(L + L_TQ + lane16) = cs;
        row_table1<2>(L, 4, lane);
      }
#pragma unroll
      for (int i = 0; i < 5; ++i) {
        const int n = (w - 8) * 5 + i;
        stg(v4u{POISON, POISON, POISON, POISON}, rs, lane16, sel(n < 18, ab + sel(n < 9, A_O, A_X1 - 9 * KB) + (g * 9 + n) * KB, OOR));
      }
      __builtin_amdgcn_s_sleep(BDELAY);
      DRAIN();
      if (lane == 0 && __hip_atomic_fetch_add((LAS int*)(L + L_HC) + C_P0, 1, __ATOMIC_RELAXED, __HIP_MEMORY_SCOPE_WORKGROUP) == 11)
        set_flag(p.ws, FLAGS + j * 128 + g * 8, tag);
      const int dst0 = sel(item < 108, wb + (252 + 54 * kind + 9 * k3) * KB, OOR);
#pragma unroll
      for (int tt = 0; tt < 4; ++tt)
#pragma unroll
        for (int c = 0; c < 3; ++c) stg(wv[c][tt], rs, lane16 + c * 3 * KB, dst0 + ((((4 * cb + tt) * 43) >> 7) * 105 + 4 * cb + tt) * KB);
      __builtin_amdgcn_raw_buffer_store_b32(__float_as_uint(csum), rs,
                                            lane * 4 + sel(kind == 0, CS1 + (k3 * 4608 + 576 * g + 64 * cb) * 4, OOR), 0, SC1);
      SB();
      if (opq_s(w) == 8) issue_window<0, WIN>(win, rs, lane16, QBASE());
    }
  }
  int os0[3];
  const int wq = opq_s(w0);
  {
    const int T = 2 * (g0 >> 2) + sel(wq >= 6, 1, 0), s0 = 3 * sel(wq >= 6, wq - 6, wq);
#pragma unroll
    for (int i = 0; i < 3; ++i) {
      const int s = s0 + i;
      os0[i] = ABASE + j0 * ASL + A_O + sel(s < 16, ((s >> 1) * 9 + T * 2 + (s & 1)) * KB, ((s - 16) * 36 + 8) * KB + T * 256);
    }
  }
  const int oq0 = sel(wq == 5 || wq == 11, 9 * KB, 256);
  const int ft0 = (g0 & 3) * 9 + sel(wq < 9, wq, 0), xs0 = ((g0 >> 2) * 36 + (ft0 >> 1)) * KB + (ft0 & 1) * 512;

#pragma unroll 1
  for (int layer = 0; layer < 2; ++layer) {
    v4u xres;
    {
      LOCALS();
      {
        const int qbase = QBASE(), nbase = NBASE(), rw = 5 + 5 * layer;
        if (opq_s(w) < 4) qkv_tiles<2, 5, 2, 7, 4, WIN>(win, L, rs, lane, qbase, nbase, w, rw);
        if ((opq_s(w) & 14) == 4) qkv_tiles<2, 4, 0, 7, 1, 0>(win, L, rs, lane, qbase, nbase, 2 * w + 2, rw);
        if (opq_s(w) == 6) qkv_tiles<1, 5, 0, 10, 0, WIN>(win, L, rs, lane, qbase, nbase, 8, rw);
        if (opq_s(w) == 7) qkv_tiles<1, 5, 1, 10, 0, WIN>(win, L, rs, lane, qbase, nbase, 9, rw);
      }
      if (layer == 1 && bid == 0 && w == 10) {
#pragma unroll 1
        for (int i = 0; i < 4; ++i) wait_flags(p.ws, SFLAGS + (256 + i * 64 + lane) * 8, true, tag);
        if (lane == 0) set_flag(p.ws, 0, tag);
      }
      xres = *(LAS v4u*)(L + opq_s(xs0) + (quad & 1) * 18 * KB + (quad >> 1) * 256 + l15 * 16);
      if (layer == 0 && w >= 8) DRAIN();
      if ((opq_s(w) & 12) == 4) {
        BAR();
        if (layer == 0 && w == 6 && lane == 0) set_flag(p.ws, SFLAGS + (256 + g * 32 + j) * 8, tag);
        if (w == 7 && layer == 0) {
          wait_flags(p.ws, FLAGS + j * 128 + (lane & 7) * 8, lane < 8, tag);
        }
        if (w < 6) issue_window<0, WIN>(win, rs, lane16, NBASE());
      }
      if ((opq_s(w) & 12) != 4) {
        const int T = w & 3, hp = w >> 3, Tp = (T & 1) ? T - 1 : 4;
        f32x4 mp, ms;
        const float neg = __uint_as_float(vconst<0xf149f2cau>());
        const int lo = l15 + ((T & 1) ? 9 : 1 + 4 * T), hi = (T & 1) ? 16 : j > 0 ? 8 + 4 * T : 0;
#pragma unroll
        for (int r = 0; r < 4; ++r) {
          const int jj = 4 * quad + r;
          mp[r] = ((jj >= lo) & (jj < hi)) ? 0.f : neg;
          ms[r] = (unsigned)(l15 - jj) <= 7u ? 0.f : neg;
        }
        int xs = L_QK + lane * 8 + T * 512, xp = L_QK + lane * 8 + Tp * 512, hs = xs + hp * 5120, hq = xp + hp * 5120;
        int om = ab + A_O + (g * 9 + T * 2 + hp) * KB + lane16;
        int oe = quad == hp ? ab + A_O + (g * 9 + 8) * KB + T * 256 + hp * 8 + l15 * 16 : WS_BYTES;
        unsigned qm0 = quad == 2 * hp ? ~0u : 0u, qm1 = quad == 2 * hp + 1 ? ~0u : 0u, zr = vconst<0>();
        v4u one4 = {vconst<ONES>(), vconst<ONES>(), vconst<ONES>(), vconst<ONES>()};
        asm volatile("" : "+v"(mp), "+v"(ms), "+v"(xs), "+v"(xp), "+v"(hs), "+v"(hq), "+v"(om), "+v"(oe), "+v"(qm0), "+v"(qm1), "+v"(zr), "+v"(one4));
        BAR();
        const v2u kxs = *(LAS v2u*)(L + xs + 40 * 512);
        const unsigned kxp = *(LAS unsigned*)(L + xp + 40 * 512 + 4);
        v2u q[2], ks[2], kp[2], vs[2], vp[2];
#pragma unroll
        for (int hh = 0; hh < 2; ++hh) {
          q[hh] = *(LAS v2u*)(L + hs + (50 + 5 * hh) * 512);
          kp[hh] = *(LAS v2u*)(L + hq + 5 * hh * 512);
          ks[hh] = *(LAS v2u*)(L + hs + 5 * hh * 512);
        }
        SB();
#pragma unroll
        for (int hh = 0; hh < 2; ++hh) {
          vp[hh] = *(LAS v2u*)(L + hq + (20 + 5 * hh) * 512);
          vs[hh] = *(LAS v2u*)(L + hs + (20 + 5 * hh) * 512);
        }
        const v2u vxp = *(LAS v2u*)(L + xp + 45 * 512), vxs = *(LAS v2u*)(L + xs + 45 * 512);
        SB();
        const f32x4 z = {0.f, 0.f, 0.f, 0.f};
        f32x4 sp[2], ss[2], o[2], ox[2], sm[2];
        float mx[2];
#pragma unroll
        for (int hh = 0; hh < 2; ++hh) {
          const v4u qf = {q[hh].x, q[hh].y, kxs.x & (hh ? qm1 : qm0), zr};
          sp[hh] = mfma(v4u{kp[hh].x, kp[hh].y, kxp, zr}, qf, mp);
          ss[hh] = mfma(v4u{ks[hh].x, ks[hh].y, kxs.y, zr}, qf, ms);
        }
#pragma unroll
        for (int hh = 0; hh < 2; ++hh) {
          const float m0 = __builtin_fmaxf(__builtin_fmaxf(sp[hh][0], sp[hh][1]), sp[hh][2]), m1 = __builtin_fmaxf(__builtin_fmaxf(ss[hh][0], ss[hh][1]), ss[hh][2]);
          mx[hh] = __builtin_fmaxf(__builtin_fmaxf(__builtin_fmaxf(__builtin_fmaxf(m0, sp[hh][3]), ss[hh][3]), m1), m1);
        }
        rows_max2(mx[0], mx[1]);
#pragma unroll
        for (int hh = 0; hh < 2; ++hh) {
          const float c = 0.23570226039551584f * 1.4426950408889634f, mc = -c * mx[hh];
          float e[8];
#pragma unroll
          for (int r = 0; r < 4; ++r) {
            e[r] = __builtin_amdgcn_exp2f(__builtin_fmaf(sp[hh][r], c, mc));
            e[4 + r] = __builtin_amdgcn_exp2f(__builtin_fmaf(ss[hh][r], c, mc));
          }
          const v4u pf = {pk(e[0], e[1]), pk(e[2], e[3]), pk(e[4], e[5]), pk(e[6], e[7])};
          sm[hh] = mfma(one4, pf, z);
          o[hh] = mfma(v4u{vp[hh].x, vp[hh].y, vs[hh].x, vs[hh].y}, pf, z);
          ox[hh] = mfma(v4u{vxp.x, vxp.y, vxs.x, vxs.y}, pf, z);
        }
        const float i0 = __builtin_amdgcn_rcpf(sm[0][0]), i1 = __builtin_amdgcn_rcpf(sm[1][0]);
        stg(v4u{pk(o[0][0] * i0, o[0][1] * i0), pk(o[0][2] * i0, o[0][3] * i0), pk(o[1][0] * i1, o[1][1] * i1), pk(o[1][2] * i1, o[1][3] * i1)}, rs, om, 0);
        __builtin_amdgcn_raw_buffer_store_b64(v2u{pk(ox[0][0] * i0, ox[0][1] * i0), pk(ox[1][2] * i1, ox[1][3] * i1)}, rs, oe, 0, SC1);
        SETTLE();
      }
      BAR();
    }
    {
      LOCALS();
      const int th = g >> 2, ft = (g & 3) * 9 + w;
      {
        const int ov = quad * opq_s(oq0) + l15 * 16;
        RETRY_BEGIN();
        v4u orr[3];
#pragma unroll
        for (int i = 0; i < 3; ++i) orr[i] = ldg(rs, i ? ov : lane16, os0[i] + layer * ALY);
#pragma unroll
        for (int i = 0; i < 3; ++i) { *(LAS v4u*)(L + (w * 3 + i) * KB + lane16) = orr[i]; seen_ = umax4(seen_, orr[i]); }
        RETRY_END();
      }
      const v4u xr = xres;
      if (layer == 0 && w == 0) wait_flags<2>(p.ws, SFLAGS + (256 + g * 32 + (lane & 31)) * 8, lane < 32, tag);
      BAR();
      if (w < 9) {
        f32x4 acc[1][2];
        gemm18<1, 2, 0, P2N, 10>(acc, win, L + lane16, rs, lane16, 0, MBASE());
        float a[8];
#pragma unroll
        for (int r = 0; r < 4; ++r) {
          auto s = __builtin_amdgcn_permlane16_swap(__float_as_uint(acc[0][0][r]), __float_as_uint(acc[0][1][r]), false, false);
          a[r] = __uint_as_float(s[0]);
          a[4 + r] = __uint_as_float(s[1]);
        }
        const v4u o = {pk(a[0] + blo(xr.x), a[1] + bhi(xr.x)), pk(a[2] + blo(xr.y), a[3] + bhi(xr.y)),
                       pk(a[4] + blo(xr.z), a[5] + bhi(xr.z)), pk(a[6] + blo(xr.w), a[7] + bhi(xr.w))};
        stg(o, rs, lane16, ab + A_X1 + (th * 36 + ft) * KB);
        SB();
        if (P2N < 3 * DST) issue_window<P2N, 3 * DST>(win, rs, lane16, MBASE());
        else SETTLE();
      }
      if (layer == 0 && opq_s(w) >= 9) {
        const int f4 = (w - 9) * 48 + (lane < 48 ? lane : 0);
        v4u cp[6];
#pragma unroll
        for (int i = 0; i < 6; ++i) cp[i] = ldg(rs, f4 * 16, CS1 + (i * 4608 + 576 * g) * 4);
        const v2u b = *(const GAS v2u*)(p.b1 + 576 * g + 4 * f4);
        *(LAS v2u*)(L + L_B2 + f4 * 8) = *(const GAS v2u*)(p.b2 + 4 * f4);
        f32x4 cs = __builtin_bit_cast(f32x4, cp[0]);
#pragma unroll
        for (int i = 1; i < 6; ++i) add4(cs, cp[i]);
        *(LAS f32x4*)(L + L_T1 + f4 * 16) = cs;
        *(LAS f32x4*)(L + L_T1 + 2304 + f4 * 16) = f32x4{blo(b.x), bhi(b.x), blo(b.y), bhi(b.y)};
      }
      if (w == 10 && lane < 10) ((volatile LAS int*)(L + L_HC))[lane] = 0;
      BAR();
    }
    {
      LOCALS();
      const int mbase = MBASE();
      const int T = w & 3, cls = w >> 2;
      if (opq_s(cls) == 0) {
        if (layer == 0) {
          __builtin_amdgcn_s_sleep(PZD);
          const v4u pz = {vconst<POISON>(), vconst<POISON>(), vconst<POISON>(), vconst<POISON>()};
#pragma unroll
          for (int i = 0; i < 8; ++i) {
            const int n = w * 8 + i;
            stg(pz, rs, lane16,
                sel(n < 30, ab + ALY + n * KB + sel(n < 12, A_X2 + g * 12 * KB, sel(n < 21, A_O, A_X1 - 9 * KB) + (g * 9 - 12) * KB), OOR));
          }
        }
      }
      if (opq_s(cls) > 0) {
        v4u xr[9];
        const int xo = (quad >> 1) * KB + ((quad & 1) * 2 + (T & 1)) * 256 + l15 * 16;
        RETRY_BEGIN();
#pragma unroll
        for (int i = 0; i < 9; ++i) xr[i] = ldg(rs, xo + (i & 1) * 2 * KB, ab + A_X1 + ((T >> 1) * 36 + 18 * (cls - 1) + (i >> 1) * 4) * KB);
#pragma unroll
        for (int i = 0; i < 9; ++i) { *(LAS v4u*)(L + (T * 18 + 9 * (cls - 1) + i) * KB + lane16) = xr[i]; seen_ = umax4(seen_, xr[i]); }
        RETRY_END();
        cnt_add(L, C_XA + cls - 1, lane);
        __builtin_amdgcn_s_setprio(3);
        const v4u one4 = {vconst<ONES>(), vconst<ONES>(), vconst<ONES>(), vconst<ONES>()};
        f32x4 sa = {0.f, 0.f, 0.f, 0.f}, sq = {0.f, 0.f, 0.f, 0.f};
        asm volatile("s_nop 1");
#pragma unroll
        for (int i = 0; i < 9; ++i) { sa = mfma(one4, xr[i], sa); sq = mfma(xr[i], xr[i], sq); }
        __builtin_amdgcn_s_setprio(0);
        if (layer == 0 && opq_s(w) >= 9) issue_window<0, 3 * DST>(win, rs, lane16, mbase);
        const float dg = (lane & 2) ? ((lane & 1) ? sq[3] : sq[2]) : ((lane & 1) ? sq[1] : sq[0]);
        if (quad == (l15 >> 2)) *(LAS f32x2*)(L + L_ST + ((T * 16 + l15) * 2 + cls - 1) * 8) = f32x2{sa[0], dg};
        cnt_add(L, C_ST, lane);
      }
      f32x4 acc[3][4];
#pragma unroll
      for (int t = 0; t < 3; ++t)
#pragma unroll
        for (int m = 0; m < 4; ++m) acc[t][m] = zero4();
      if (opq_s(cls) > 0) { row_table(L, lane); shift_acc(acc, L, w, quad, l15); }
      __builtin_amdgcn_s_setprio(3);
      gemm_up(acc, win, L + lane16, rs, lane16, mbase, L);
      __builtin_amdgcn_s_setprio(0);
      {
        LAS unsigned char* H = L + L_H + lane16 + 3 * (w >> 1) * KB;
        if (opq_s(cls) == 0) {
          cnt_wait(L, C_RT, 1);
          shift_acc(acc, L, w, quad, l15);
        }
        f32x4 rt[4];
#pragma unroll
        for (int m = 0; m < 4; ++m) rt[m] = *(LAS f32x4*)(L + L_ST + KB + (m * 16 + l15) * 16);
#pragma unroll
        for (int m = 0; m < 4; ++m) {
          const v2u u0 = gelu4(acc[0][m], rt[m][2], rt[m][3], rt[m][1]), u1 = gelu4(acc[1][m], rt[m][2], rt[m][3], rt[m][1]);
          *(LAS v4u*)(H + (m * 18 + (w & 1)) * KB) = v4u{u0.x, u0.y, u1.x, u1.y};
          *(LAS v2u*)(H + (m * 18 + 2) * KB + (w & 1) * 8) = gelu4(acc[2][m], rt[m][2], rt[m][3], rt[m][1]);
        }
        cnt_add(L, C_H0 + cls, lane);
      }
      __builtin_amdgcn_s_setprio(2);
      gemm_down(acc, win, L + L_H + lane16, rs, lane16, mbase + 54 * KB, L);
      __builtin_amdgcn_s_setprio(0);
      const int pw = ab + A_PT + (g * 12 + w) * 6144;
      const int poff = (quad & 1) * 1536 + (quad >> 1) * 256 + l15 * 16;
#pragma unroll
      for (int t = 0; t < 3; ++t)
#pragma unroll
        for (int m = 0; m < 4; m += 2) {
          const v2u a = {pk(acc[t][m][0], acc[t][m][1]), pk(acc[t][m][2], acc[t][m][3])};
          const v2u b = {pk(acc[t][m + 1][0], acc[t][m + 1][1]), pk(acc[t][m + 1][2], acc[t][m + 1][3])};
          const v4u o = widen(a, b);
          const bool own = m + (quad & 1) == (g >> 1) && (l15 >> 3) == (g & 1);
          stg(o, rs, own ? WS_BYTES : poff + m * 1536 + t * 512, pw);
          if (own) *(LAS v4u*)(L + (((g >> 1) ^ 1) * 18 + w) * KB + ((2 * t + (quad >> 1)) * 8 + (lane & 7)) * 16) = o;
        }
      __builtin_amdgcn_s_setprio(0);
    }
    {
      LOCALS();
      OWNROWS(6);
      const bool act = lane < 48;
      float sum[8];
      {
        const v4u bb = *(LAS v4u*)(L + L_B2 + (act ? c8 : 0) * 16);
        const v4u xr = *(LAS v4u*)(L + (act ? piece : 0)), ow = *(LAS v4u*)(L + (((g >> 1) ^ 1) * 18 + w) * KB + lane16);
#pragma unroll
        for (int e = 0; e < 4; ++e) {
          sum[2 * e] = blo(xr[e]) + blo(ow[e]); OPAQUE(sum[2 * e]); sum[2 * e] += blo(bb[e]); OPAQUE(sum[2 * e]);
          sum[2 * e + 1] = bhi(xr[e]) + bhi(ow[e]) + bhi(bb[e]);
        }
      }
      int roff = (g >> 1) * 1536 + (lane >> 3) * 256 + (8 * (g & 1) + (lane & 7)) * 16 + (act ? 0 : OOR);
      int foff = WFLAGS + ((layer * 32 + j - (lane >> 3)) * 12 + w) * 128 + (lane < 8 ? lane : 3 + 4 * (lane & 1)) * 8;
      asm volatile("" : "+v"(roff), "+v"(foff), "+v"(sum[0]), "+v"(sum[1]), "+v"(sum[2]), "+v"(sum[3]), "+v"(sum[4]), "+v"(sum[5]), "+v"(sum[6]), "+v"(sum[7]));
      if (w >= 8) __builtin_amdgcn_s_barrier();
      DRAIN();
      if (lane == 0) set_flag(p.ws, WFLAGS + ((layer * 32 + j) * 12 + w) * 128 + g * 8, tag);
      if (layer == 0 && opq_s(w) < 8) {
        if (QA) cnt_wait(L, C_LC, 12);
        issue_window<0, QA>(win, rs, lane16, QBASE());
      }
      __builtin_amdgcn_s_barrier();
      wait_flags(p.ws, foff, lane < (layer == 0 && j > 0 ? 10 : 8), tag);
      if (w < 8) __builtin_amdgcn_s_barrier();
      asm volatile("" ::: "memory");
      v4u xb;
      {
        v4u pr[7];
#pragma unroll
        for (int i = 0; i < 7; ++i) pr[i] = ldg(rs, roff, ab + A_PT + (((i + 1 + g) & 7) * 12 + w) * 6144);
        SB();
        const unsigned sel_lo = opq_s(0x00003f80), sel_hi = opq_s(0x3f800000);
#pragma unroll
        for (int i = 0; i < 7; ++i)
#pragma unroll
          for (int e = 0; e < 4; ++e) { sum[2 * e] = dot2(pr[i][e], sel_lo, sum[2 * e]); sum[2 * e + 1] = dot2(pr[i][e], sel_hi, sum[2 * e + 1]); }
#pragma unroll
        for (int e = 0; e < 4; ++e) xb[e] = pk(sum[2 * e], sum[2 * e + 1]);
      }
      if (layer == 0) {
        float s1 = dot2(xb.x, ONES, dot2(xb.y, ONES, dot2(xb.z, ONES, dot2(xb.w, ONES, 0.f))));
        float s2 = dot2(xb.x, xb.x, dot2(xb.y, xb.y, dot2(xb.z, xb.z, dot2(xb.w, xb.w, 0.f))));
        if (!act) { s1 = 0.f; s2 = 0.f; }
        s1 += ror8(s1); OPAQUE(s1); s2 += ror8(s2);
        s1 = rows_sum(s1); OPAQUE(s1); s2 = rows_sum(s2);
        if (!act) xb = v4u{__float_as_uint(s1), __float_as_uint(s2), 0u, 0u};
        if (lane < 56) stg(xb, rs, lane16, ab + ALY + A_X2 + (g * 12 + w) * KB);
        SB();
        if (QB > QA && opq_s(w) < 8) issue_window<QA, QB>(win, rs, lane16, QBASE());
        else SETTLE();
        const bool stl = (lane >> 3) == 6;
        LAS unsigned char* Iw = act ? L + (c8 >> 2) * KB + ((c8 & 3) * 16 + (lane & 7)) * 16 : L + 126 * KB + lane16;
        LAS unsigned char* Sw = L + L_ST + (w * 80 + (lane & 7)) * 8;
        BAR();
        v4u xv[10];
        const int vo = lane < 56 ? lane16 : lane16 + WS_BYTES;
        xv[0] = xb;
        RETRY_BEGIN();
#pragma unroll
        for (int i = 1; i < 10; ++i)
          xv[i] = ldg(rs, vo, ab + ALY + A_X2 + w * KB + (i < 8 ? (i ^ g) * 12 * KB : (3 + 4 * (i - 8)) * 12 * KB - (j > 0 ? ASL : 0)));
        SB();
#pragma unroll
        for (int i = 0; i < 10; ++i) {
          const int mi = i < 8 ? i ^ g : i;
          *(LAS v4u*)(Iw + (act ? (mi >> 1) * 18 * KB + (mi & 1) * 128 : 0)) = xv[i];
          if (i) seen_ = umax4(seen_, xv[i]);
        }
        RETRY_END();
        if (stl) {
#pragma unroll
          for (int i = 0; i < 10; ++i) *(LAS v2u*)(Sw + (i < 8 ? i ^ g : i) * 64) = v2u{xv[i].x, xv[i].y};
        }
        BAR();
        if (QB < WIN && opq_s(w) < 8) issue_window<QB, WIN>(win, rs, lane16, QBASE());
        if (w == 11) row_table1<12>(L, 4, lane);
        if (w >= 8) row_table1<12>(L, w - 8, lane);
        if (opq_s(w) == 8) issue_window<3 * DST, WIN>(win, rs, lane16, QBASE());
        if (opq_s(w) >= 8) issue_window<0, 3 * DST>(win, rs, lane16, QBASE());
      } else {
        const rsrc_t ro = __builtin_amdgcn_make_buffer_rsrc((void*)p.out, 0, 2048 * 576 * 2, 0x00020000);
        __builtin_amdgcn_raw_buffer_store_b128(xb, ro, act ? mytok * 1152 + c8 * 16 : OOR, 0, 0);
      }
    }
  }
  asm volatile(".Lfwd_end:");
}
}

extern "C" void kernel_launch(void* const* d_in, const int* in_sizes, int n_in, void* d_out, int out_size, void* d_ws, size_t ws_size,
                              hipStream_t stream) {
  (void)in_sizes; (void)n_in; (void)out_size; (void)ws_size;
  Params p;
  p.x = (const unsigned short*)d_in[0];
  p.wqkv = (const unsigned short*)d_in[1];
  p.wo = (const unsigned short*)d_in[2];
  p.w1 = (const unsigned short*)d_in[4];
  p.b1 = (const unsigned short*)d_in[5];
  p.w2 = (const unsigned short*)d_in[6];
  p.b2 = (const unsigned short*)d_in[7];
  p.out = (unsigned short*)d_out;
  p.ws = (unsigned char*)d_ws;
  (void)hipFuncSetAttribute((const void*)fused_fwd, hipFuncAttributeMaxDynamicSharedMemorySize, LDS_BYTES);
  hipLaunchKernelGGL(fused_fwd, dim3(256), dim3(NTHR), LDS_BYTES, stream, p);
}
```

```cpp
#include <hip/hip_runtime.h>
#include <hip/hip_bf16.h>
#include <stdint.h>

namespace {
#define LAS __attribute__((address_space(3)))
#define GAS __attribute__((address_space(1)))
typedef unsigned v4u __attribute__((ext_vector_type(4)));
typedef unsigned v2u __attribute__((ext_vector_type(2)));
typedef float f32x4 __attribute__((ext_vector_type(4)));
typedef float f32x2 __attribute__((ext_vector_type(2)));
typedef __bf16 bf16x8 __attribute__((ext_vector_type(8)));
typedef __bf16 bf16x2 __attribute__((ext_vector_type(2)));
typedef short s16x4 __attribute__((ext_vector_type(4)));
typedef unsigned long long u64;
typedef __amdgpu_buffer_rsrc_t rsrc_t;

constexpr int KB = 1024;
constexpr int NTHR = 768;
constexpr int WIN = 18;
constexpr int XB = 2;
constexpr int P2N = 9;
constexpr int QA = 6;
constexpr int QB = 12;
constexpr int PZD = 20;
constexpr int BDELAY = 10;
constexpr int NTW = 1;
constexpr int DST = 5;
constexpr float EPS = 1e-5f;
constexpr int SC1 = 16;
constexpr float GC0 = -2.0f * 0.7978845608028654f * 1.4426950408889634f;
constexpr int FLAGS = 4096;
constexpr int SFLAGS = 32 * KB;
constexpr int WFLAGS = 48 * KB;
constexpr int CS1 = 192 * KB;
constexpr int CSQ = 520 * KB;
constexpr int WBASE = 704 * KB;
constexpr int WSL = (252 + 1296) * KB;
constexpr int WOB = WBASE + 8 * WSL;
constexpr int ABASE = 14 * 1024 * KB;
constexpr int IMG = 72 * KB;
constexpr int A_X2 = 0, A_O = 96 * KB, A_X1 = A_O + IMG, A_PT = A_X1 + IMG, ALY = 1024 * KB, ASL = 2 * ALY;
constexpr int WS_BYTES = ABASE + 32 * ASL;
static_assert(WOB + 648 * KB <= ABASE && A_PT + 8 * IMG <= ALY && CSQ + 8 * 14 * 1152 + 256 <= WBASE, "workspace map");
constexpr int L_H = IMG;
constexpr int L_QK = 90 * KB;
constexpr int L_ST = 145 * KB;
constexpr int L_T1 = L_ST + 7680;
constexpr int L_TQ = L_T1 + 4608;
constexpr int L_RT = L_TQ + 896;
constexpr int L_HC = L_RT + 640;
constexpr int L_B2 = L_HC + 64;
constexpr int LDS_BYTES = L_B2 + 1152;
static_assert(LDS_BYTES <= 160 * KB, "LDS");
enum { C_H0 = 0, C_ST = 3, C_RT = 4, C_LC = 5, C_XA = 6, C_R1 = 10, C_P0 = 11 };

struct Params {
  const unsigned short *x, *wqkv, *wo, *w1, *b1, *w2, *b2;
  unsigned short *out;
  unsigned char *ws;
};

#define SB() __builtin_amdgcn_sched_barrier(0)
#define BAR() do { asm volatile("s_waitcnt lgkmcnt(0)" ::: "memory"); __builtin_amdgcn_s_barrier(); asm volatile("" ::: "memory"); } while (0)
#define DRAIN() asm volatile("s_waitcnt vmcnt(0)" ::: "memory")
#define SETTLE() DRAIN()
#define OPAQUE(x) asm("" : "+v"(x))
#define KEEP(x) asm volatile("" :: "v"(x))

__device__ __forceinline__ f32x4 mfma(v4u a, v4u b, f32x4 c) {
  return __builtin_amdgcn_mfma_f32_16x16x32_bf16(__builtin_bit_cast(bf16x8, a), __builtin_bit_cast(bf16x8, b), c, 0, 0, 0);
}
#define MF(acc, a, b) asm volatile("v_mfma_f32_16x16x32_bf16 %0, %1, %2, %0" : "+v"(acc) : "v"(a), "v"(b))
#define MF0(acc, a, b) asm volatile("v_mfma_f32_16x16x32_bf16 %0, %1, %2, 0" : "=&v"(acc) : "v"(a), "v"(b))
template <int NT, int MT> __device__ __forceinline__ void acc_fence(f32x4 (&acc)[NT][MT]) {
#pragma unroll
  for (int t = 0; t < NT; ++t)
#pragma unroll
    for (int m = 0; m < MT; ++m) asm volatile("" : "+v"(acc[t][m]));
}
#define ACC_IN(acc) do { acc_fence(acc); asm volatile("s_nop 1"); SB(); } while (0)
#define ACC_OUT(acc) do { SB(); asm volatile("s_nop 7\n\ts_nop 1"); acc_fence(acc); } while (0)
__device__ __forceinline__ unsigned pk(float a, float b) {
  f32x2 v = {a, b};
  return __builtin_bit_cast(unsigned, __builtin_convertvector(v, bf16x2));
}
__device__ __forceinline__ float dot2(unsigned a, unsigned b, float c) {
  return __builtin_amdgcn_fdot2_f32_bf16(__builtin_bit_cast(bf16x2, a), __builtin_bit_cast(bf16x2, b), c, false);
}
constexpr unsigned ONES = 0x3f803f80u;
__device__ __forceinline__ float blo(unsigned u) { return __uint_as_float(u << 16); }
__device__ __forceinline__ float bhi(unsigned u) { return __uint_as_float(u & 0xffff0000u); }
__device__ __forceinline__ v4u ldg(rsrc_t rs, int voff, int soff) { return __builtin_amdgcn_raw_buffer_load_b128(rs, voff, soff, SC1); }
__device__ __forceinline__ void stg(v4u v, rsrc_t rs, int voff, int soff) { __builtin_amdgcn_raw_buffer_store_b128(v, rs, voff + soff, 0, SC1); }
__device__ __forceinline__ v4u ldf(rsrc_t rs, int lane16, int base, int f) { return ldg(rs, lane16 + (f & 3) * KB, base + (f >> 2) * 4 * KB); }
__device__ __forceinline__ int lane_id() { int x; asm volatile("v_mbcnt_lo_u32_b32 %0, -1, 0\n\tv_mbcnt_hi_u32_b32 %0, -1, %0" : "=v"(x)); return x; }
template <unsigned C> __device__ __forceinline__ unsigned vconst() { unsigned x; asm volatile("v_mov_b32 %0, %1" : "=v"(x) : "i"(C)); return x; }
__device__ __forceinline__ int sel(bool c, int a, int b) { return c ? a : b; }
constexpr int OOR = 0x10000000;
__device__ __forceinline__ int opq_s(int x) { asm volatile("" : "+s"(x)); return x; }
#define LOCALS() const int lane = lane_id(), w = opq_s(w0), j = opq_s(j0), g = opq_s(g0); \
  const int lane16 = lane * 16, quad = lane >> 4, l15 = lane & 15, ab = ABASE + j * ASL + layer * ALY, wb = WBASE + g * WSL; \
  (void)lane16; (void)quad; (void)l15; (void)ab; (void)wb
#define OWNROWS(CPW) const int c8 = CPW * w + (lane >> 3); \
  const int piece = ((g >> 1) * 18 + (c8 >> 2)) * KB + ((c8 & 3) * 16 + 8 * (g & 1) + (lane & 7)) * 16; \
  const int mytok = 64 * j + 2 * (16 * ((g >> 1) & 1) + 8 * (g & 1) + (lane & 7)) + (g >> 2)
#define GET_TAG() KEEP(cpf); tag = 1 + ((u64)(unsigned)__builtin_amdgcn_readfirstlane((int)(unsigned)tag0) | \
                             ((u64)(unsigned)__builtin_amdgcn_readfirstlane((int)(unsigned)(tag0 >> 32)) << 32))
#define NBASE() opq_s(nb0)
#define QBASE() opq_s(qb0)
#define MBASE() opq_s(mb0)
__device__ __forceinline__ float rows_sum(float x) {
  auto s = __builtin_amdgcn_permlane16_swap(__float_as_uint(x), __float_as_uint(x), false, false);
  x = __uint_as_float(s[0]) + __uint_as_float(s[1]);
  auto t = __builtin_amdgcn_permlane32_swap(__float_as_uint(x), __float_as_uint(x), false, false);
  return __uint_as_float(t[0]) + __uint_as_float(t[1]);
}
__device__ __forceinline__ float vmax(unsigned a, unsigned b) { float r; asm("v_max_f32 %0, %1, %2\n\ts_nop 1" : "=v"(r) : "v"(a), "v"(b)); return r; }
__device__ __forceinline__ void rows_max2(float& a, float& b) {
  auto s = __builtin_amdgcn_permlane16_swap(__float_as_uint(a), __float_as_uint(b), false, false);
  float x = vmax(s[0], s[1]);
  auto t = __builtin_amdgcn_permlane32_swap(__float_as_uint(x), __float_as_uint(x), false, false);
  x = vmax(t[0], t[1]);
  auto u = __builtin_amdgcn_permlane16_swap(__float_as_uint(x), __float_as_uint(x), false, false);
  a = __uint_as_float(u[0]);
  b = __uint_as_float(u[1]);
}
template <int CTRL> __device__ __forceinline__ float dpp(float x) {
  return __int_as_float(__builtin_amdgcn_mov_dpp(__float_as_int(x), CTRL, 0xf, 0xf, true));
}
__device__ __forceinline__ float ror8(float x) { return dpp<0x128>(x); }
__device__ __forceinline__ void add4(f32x4& a, v4u b) {
  float a0 = a[0], a1 = a[1], a2 = a[2], a3 = a[3];
  asm("v_add_f32 %0, %0, %4\n\tv_add_f32 %1, %1, %5\n\tv_add_f32 %2, %2, %6\n\tv_add_f32 %3, %3, %7"
      : "+v"(a0), "+v"(a1), "+v"(a2), "+v"(a3) : "v"(b.x), "v"(b.y), "v"(b.z), "v"(b.w));
  a = f32x4{a0, a1, a2, a3};
}
__device__ __forceinline__ v4u widen(v2u a, v2u b) {
  auto s0 = __builtin_amdgcn_permlane16_swap(a.x, b.x, false, false);
  auto s1 = __builtin_amdgcn_permlane16_swap(a.y, b.y, false, false);
  return v4u{s0[0], s1[0], s0[1], s1[1]};
}
__device__ __forceinline__ v2u gelu4(f32x4 a, float k0, float k1, float is) {
  float s0, s1, s2, s3;
  unsigned u0, u1;
  asm("v_mul_f32 %2, %6, %6\n\tv_mul_f32 %3, %7, %7\n\tv_mul_f32 %4, %8, %8\n\tv_mul_f32 %5, %9, %9\n\t"
      "v_fma_f32 %2, %2, %11, %10\n\tv_fma_f32 %3, %3, %11, %10\n\tv_fma_f32 %4, %4, %11, %10\n\tv_fma_f32 %5, %5, %11, %10\n\t"
      "v_mul_f32 %2, %2, %6\n\tv_mul_f32 %3, %3, %7\n\tv_mul_f32 %4, %4, %8\n\tv_mul_f32 %5, %5, %9\n\t"
      "v_exp_f32 %2, %2\n\tv_exp_f32 %3, %3\n\tv_exp_f32 %4, %4\n\tv_exp_f32 %5, %5\n\t"
      "v_fma_f32 %2, %2, %12, %12\n\tv_fma_f32 %3, %3, %12, %12\n\tv_fma_f32 %4, %4, %12, %12\n\tv_fma_f32 %5, %5, %12, %12\n\t"
      "v_rcp_f32 %2, %2\n\tv_rcp_f32 %3, %3\n\tv_rcp_f32 %4, %4\n\tv_rcp_f32 %5, %5\n\t"
      "v_mul_f32 %2, %2, %6\n\tv_mul_f32 %3, %3, %7\n\tv_mul_f32 %4, %4, %8\n\tv_mul_f32 %5, %5, %9\n\t"
      "v_cvt_pk_bf16_f32 %0, %2, %3\n\tv_cvt_pk_bf16_f32 %1, %4, %5"
      : "=&v"(u0), "=&v"(u1), "=&v"(s0), "=&v"(s1), "=&v"(s2), "=&v"(s3)
      : "v"(a[0]), "v"(a[1]), "v"(a[2]), "v"(a[3]), "v"(k0), "v"(k1), "v"(is));
  return v2u{u0, u1};
}
__device__ __forceinline__ void shift4(f32x4& a, f32x4 cs, f32x4 b, float nmean, float is) {
  float a0 = a[0], a1 = a[1], a2 = a[2], a3 = a[3];
  asm("v_fmac_f32 %0, %12, %4\n\tv_fmac_f32 %1, %12, %5\n\tv_fmac_f32 %2, %12, %6\n\tv_fmac_f32 %3, %12, %7\n\t"
      "v_fmac_f32 %0, %13, %8\n\tv_fmac_f32 %1, %13, %9\n\tv_fmac_f32 %2, %13, %10\n\tv_fmac_f32 %3, %13, %11"
      : "+v"(a0), "+v"(a1), "+v"(a2), "+v"(a3)
      : "v"(cs[0]), "v"(cs[1]), "v"(cs[2]), "v"(cs[3]), "v"(b[0]), "v"(b[1]), "v"(b[2]), "v"(b[3]), "v"(nmean), "v"(is));
  a = f32x4{a0, a1, a2, a3};
}
__device__ __forceinline__ int cnt_get(LAS unsigned char* L, int i) { return ((volatile LAS int*)(L + L_HC))[i]; }
__device__ __forceinline__ void cnt_wait(LAS unsigned char* L, int i, int n) {
  while (__builtin_amdgcn_readfirstlane(cnt_get(L, i)) < n) __builtin_amdgcn_s_sleep(1);
  asm volatile("" ::: "memory");
}
__device__ __forceinline__ void cnt_add(LAS unsigned char* L, int i, int lane) {
  asm volatile("s_waitcnt lgkmcnt(0)" ::: "memory");
  if (lane == 0) __hip_atomic_fetch_add((LAS int*)(L + L_HC) + i, 1, __ATOMIC_RELAXED, __HIP_MEMORY_SCOPE_WORKGROUP);
}
template <int NP> __device__ __forceinline__ void row_table1(LAS unsigned char* L, int t5, int lane) {
  const int r = t5 * 16 + (lane & 15), q = lane >> 4;
  float t1 = 0.f, t2 = 0.f;
#pragma unroll
  for (int i = 0; i < (NP + 3) / 4; ++i)
    if (NP % 4 == 0 || q < NP) { const f32x2 v = *(LAS f32x2*)(L + L_ST + ((i * 4 + q) * 80 + r) * 8); t1 += v.x; OPAQUE(t1); t2 += v.y; }
  t1 = rows_sum(t1); OPAQUE(t1); t2 = rows_sum(t2);
  const float nmean = t1 * (-1.0f / 576.0f);
  const float var = fmaxf(__builtin_fmaf(-nmean, nmean, t2 * (1.0f / 576.0f)), 0.f);
  const float rstd = __builtin_amdgcn_rsqf(var + EPS);
  if (q < 2) *(LAS float*)(L + L_RT + q * 320 + r * 4) = q ? nmean * rstd : rstd;
  cnt_add(L, C_R1, lane);
}
__device__ __forceinline__ v2u ln_pack(f32x4 a, f32x4 r, f32x4 c, f32x4 nm) {
  float t0, t1, t2, t3;
  unsigned u0, u1;
  asm("v_mul_f32 %2, %14, %18\n\tv_mul_f32 %3, %15, %19\n\tv_mul_f32 %4, %16, %20\n\tv_mul_f32 %5, %17, %21\n\t"
      "v_fmac_f32 %2, %6, %10\n\tv_fmac_f32 %3, %7, %11\n\tv_fmac_f32 %4, %8, %12\n\tv_fmac_f32 %5, %9, %13\n\t"
      "v_cvt_pk_bf16_f32 %0, %2, %3\n\tv_cvt_pk_bf16_f32 %1, %4, %5"
      : "=&v"(u0), "=&v"(u1), "=&v"(t0), "=&v"(t1), "=&v"(t2), "=&v"(t3)
      : "v"(a[0]), "v"(a[1]), "v"(a[2]), "v"(a[3]), "v"(r[0]), "v"(r[1]), "v"(r[2]), "v"(r[3]), "v"(c[0]), "v"(c[1]), "v"(c[2]), "v"(c[3]),
        "v"(nm[0]), "v"(nm[1]), "v"(nm[2]), "v"(nm[3]));
  return v2u{u0, u1};
}
__device__ __forceinline__ f32x4 zero4() {
  float a, b, c, d;
  asm volatile("v_mov_b32 %0, 0\n\tv_mov_b32 %1, 0\n\tv_mov_b32 %2, 0\n\tv_mov_b32 %3, 0" : "=v"(a), "=v"(b), "=v"(c), "=v"(d));
  return f32x4{a, b, c, d};
}
__device__ __forceinline__ f32x4 splat(float x) { return f32x4{x, x, x, x}; }
__device__ __forceinline__ void row_table(LAS unsigned char* L, int lane) {
  cnt_wait(L, C_ST, 8);
  const f32x4 q = *(LAS f32x4*)(L + L_ST + lane * 16);
  float t1 = q[0] + q[2];
  OPAQUE(t1);
  float nmean = t1 * (-1.0f / 576.0f);
  const float var = fmaxf(__builtin_fmaf(-nmean, nmean, (q[1] + q[3]) * (1.0f / 576.0f)), 0.f);
  const float r1 = __builtin_amdgcn_rsqf(var + EPS);
  const float sc = r1 * __builtin_amdgcn_rsqf(var * r1 * r1 + EPS);
  float k0 = GC0 * sc;
  OPAQUE(k0);
  *(LAS f32x4*)(L + L_ST + KB + lane * 16) = f32x4{nmean, __builtin_amdgcn_rcpf(sc), k0, k0 * (0.044715f * sc * sc)};
  cnt_add(L, C_RT, lane);
}
__device__ __forceinline__ void shift_acc(f32x4 (&acc)[3][4], LAS unsigned char* L, int w, int quad, int l15) {
  f32x2 rt[4];
#pragma unroll
  for (int m = 0; m < 4; ++m) rt[m] = *(LAS f32x2*)(L + L_ST + KB + (m * 16 + l15) * 16);
#pragma unroll
  for (int t = 0; t < 3; ++t) {
    const f32x4 cs = *(LAS f32x4*)(L + L_T1 + (48 * w + 16 * t + 4 * quad) * 4), bb = *(LAS f32x4*)(L + L_T1 + 2304 + (48 * w + 16 * t + 4 * quad) * 4);
#pragma unroll
    for (int m = 0; m < 4; ++m) shift4(acc[t][m], cs, bb, rt[m][0], rt[m][1]);
  }
}
template <int SLEEP = 1> __device__ __forceinline__ void wait_flags(const unsigned char* ws, int off, bool active, u64 tag) {
  const GAS u64* f = (const GAS u64*)(ws + off);
  asm volatile("" : "+s"(tag));
#pragma unroll 1
  for (int spins = 0; spins < (1 << 16); ++spins) {
    const u64 v = active ? __hip_atomic_load(f, __ATOMIC_RELAXED, __HIP_MEMORY_SCOPE_AGENT) : tag;
    if (__all(v == tag)) break;
    __builtin_amdgcn_s_sleep(SLEEP);
  }
  asm volatile("" ::: "memory");
}
__device__ __forceinline__ void set_flag(unsigned char* ws, int off, u64 tag) {
  asm volatile("" : "+s"(tag));
  __hip_atomic_store((GAS u64*)(ws + off), tag, __ATOMIC_RELAXED, __HIP_MEMORY_SCOPE_AGENT);
}
constexpr unsigned POISON = 0xffffffffu;
__device__ __forceinline__ unsigned umax4(unsigned m, v4u v) { return max(max(max(m, v.x), v.y), max(v.z, v.w)); }
#define RETRY_BEGIN() _Pragma("unroll 1") for (int spins_ = 0; spins_ < (1 << 15); ++spins_) { unsigned seen_ = 0
#define RETRY_END() if (!__any(seen_ == POISON)) break; __builtin_amdgcn_s_sleep(2); asm volatile("" ::: "memory"); }
template <int LO, int HI> __device__ __forceinline__ void issue_window(v4u (&win)[WIN], rsrc_t rs, int lane16, int base) {
#pragma unroll
  for (int i = LO; i < HI; ++i) win[i] = ldf(rs, lane16, base, i);
  SB();
}

template <int NT, int MT, int SWAP, int NNEXT, int R, int LO, int HI>
__device__ __forceinline__ void gemm18_steps(f32x4 (&acc)[NT][MT], v4u (&win)[WIN], v4u (&act)[R], LAS unsigned char* img, LAS unsigned char* img2, rsrc_t rs,
                                             int lane16, int wbase, int nvo, int nbase) {
#pragma unroll
  for (int n = LO; n < HI; ++n) {
    const int s = n / MT, m = n % MT, nn = n + R - 1;
    if (n < 18 * MT) {
#pragma unroll
      for (int t = 0; t < NT; ++t) {
        if (s == 0) {
          if ((SWAP >> t) & 1) MF0(acc[t][m], act[n % R], win[t]);
          else MF0(acc[t][m], win[t], act[n % R]);
        } else if ((SWAP >> t) & 1) MF(acc[t][m], act[n % R], win[(s * NT + t) % WIN]);
        else MF(acc[t][m], win[(s * NT + t) % WIN], act[n % R]);
      }
      SB();
    }
    if (nn < 18 * MT) act[nn % R] = *(LAS v4u*)(nn % MT < 3 ? img + ((nn % MT) * 18 + nn / MT) * KB : img2 + ((nn % MT - 3) * 18 + nn / MT) * KB);
    if (m == 0 && s > 0) {
#pragma unroll
      for (int t = 0; t < NT; ++t) {
        const int f = (s - 1) * NT + t;
        if (f + WIN < 18 * NT) win[f % WIN] = ldf(rs, lane16, wbase, f + WIN);
        else if (f % WIN < NNEXT) win[f % WIN] = ldf(rs, nvo, nbase, f % WIN);
      }
    }
    SB();
    if (n < 18 * MT) {
      KEEP(act[n % R]);
#pragma unroll
      for (int t = 0; t < NT; ++t) KEEP(win[(s * NT + t) % WIN]);
      SB();
    }
  }
}
template <int NT, int MT, int SWAP, int NNEXT, int R>
__device__ __forceinline__ int gemm18(f32x4 (&acc)[NT][MT], v4u (&win)[WIN], LAS unsigned char* img, rsrc_t rs, int lane16, int wbase, int nbase,
                                      LAS unsigned char* L) {
  v4u act[R];
  LAS unsigned char* img2 = img + 54 * KB;
  if (MT > 3) asm volatile("" : "+v"(img2));
#pragma unroll
  for (int n = 0; n < R - 1; ++n) { act[n] = *(LAS v4u*)(n % MT < 3 ? img + ((n % MT) * 18 + n / MT) * KB : img2 + ((n % MT - 3) * 18 + n / MT) * KB); SB(); }
  gemm18_steps<NT, MT, SWAP, NNEXT, R, 0, 6 * MT>(acc, win, act, img, img2, rs, lane16, wbase, lane16, nbase);
  gemm18_steps<NT, MT, SWAP, NNEXT, R, 6 * MT, 12 * MT>(acc, win, act, img, img2, rs, lane16, wbase, lane16, nbase);
  const int ready = MT > 2 ? cnt_get(L, C_R1) : 0;
  SB();
  gemm18_steps<NT, MT, SWAP, NNEXT, R, 12 * MT, 18 * MT + 1>(acc, win, act, img, img2, rs, lane16, wbase, lane16, nbase);
  ACC_OUT(acc);
  return ready;
}
template <int NT, int MT, int SWAP, int R, int tstep, int NNEXT>
__device__ __forceinline__ void qkv_tiles(v4u (&win)[WIN], LAS unsigned char* L, rsrc_t rs, int lane, int wbase, int nbase, int tile0, int rwait) {
  f32x4 acc[NT][MT];
  const int ready = gemm18<NT, MT, SWAP, NNEXT, R>(acc, win, L + lane * 16, rs, lane * 16, wbase, nbase, L);
  LAS unsigned char *Rq = L + L_RT + (lane & 48), *Rl = L + L_RT + (lane & 15) * 4, *Tq = L + L_TQ + tile0 * 64 + (lane & 48),
                    *Tl = L + L_TQ + tile0 * 64 + (lane & 15) * 4, *X = L + L_QK + lane * 8 + tile0 * 2560;
  asm volatile("" : "+v"(Rq), "+v"(Rl), "+v"(Tq), "+v"(Tl), "+v"(X));
  if (__builtin_amdgcn_readfirstlane(ready) < rwait) cnt_wait(L, C_R1, rwait);
  asm volatile("" ::: "memory");
  v2u u[NT][MT];
#pragma unroll
  for (int t = 0; t < NT; ++t) {
    if ((SWAP >> t) & 1) {
      const f32x4 cs = splat(*(LAS float*)(Tl + t * tstep * 64));
#pragma unroll
      for (int m = 0; m < MT; ++m) u[t][m] = ln_pack(acc[t][m], *(LAS f32x4*)(Rq + m * 64), cs, *(LAS f32x4*)(Rq + 320 + m * 64));
    } else {
      const f32x4 cs = *(LAS f32x4*)(Tq + t * tstep * 64);
#pragma unroll
      for (int m = 0; m < MT; ++m) u[t][m] = ln_pack(acc[t][m], splat(*(LAS float*)(Rl + m * 64)), cs, splat(*(LAS float*)(Rl + 320 + m * 64)));
    }
  }
#pragma unroll
  for (int t = 0; t < NT; ++t)
#pragma unroll
    for (int m = 0; m < MT; ++m) *(LAS v2u*)(X + (t * tstep * 5 + m) * 512) = u[t][m];
}
template <int C>
__device__ __forceinline__ void mlp_chunk(f32x4 (&acc)[3][4], v4u (&win)[WIN], v4u (&act)[8], LAS unsigned char* img, rsrc_t rs, int lane16, int rb,
                                          LAS unsigned char* L) {
  int hv = 4;
#pragma unroll
  for (int s = 0; s < 6; ++s) {
    if (C != 2 && s == 5) {
      if (__builtin_amdgcn_readfirstlane(hv) < 4) cnt_wait(L, C < 0 ? C_XA + 1 : C_H0 + C + 1, 4);
      asm volatile("" ::: "memory");
      SB();
    }
    if (C == 2 && s == 6 - DST) {
      if (lane16 == 0) __hip_atomic_fetch_add((LAS int*)(L + L_HC) + C_LC, 1, __ATOMIC_RELAXED, __HIP_MEMORY_SCOPE_WORKGROUP);
      SB();
    }
    asm volatile("" :: "v"(act[(s & 1) * 4]), "v"(act[(s & 1) * 4 + 1]), "v"(act[(s & 1) * 4 + 2]), "v"(act[(s & 1) * 4 + 3]), "v"(win[s * 3]),
                 "v"(win[s * 3 + 1]), "v"(win[s * 3 + 2]));
    SB();
#pragma unroll
    for (int t = 0; t < 3; ++t)
#pragma unroll
      for (int m = 0; m < 4; ++m) {
        const int mm = t == 1 ? 3 - m : m;
        if (C == 0 && s == 0) MF0(acc[t][mm], win[t], act[mm]);
        else MF(acc[t][mm], win[s * 3 + t], act[(s & 1) * 4 + mm]);
        SB();
        if (t == 0 && !(C == 2 && s == 5)) act[((s + 1) & 1) * 4 + m] = *(LAS v4u*)(img + (m * 18 + s + 1) * KB);
        if (C != 2 && s == 3 && t == 2 && m == 0) hv = cnt_get(L, C < 0 ? C_XA + 1 : C_H0 + C + 1);
        if (t == 1 && m < 3 && !(C == 2 && s + DST >= 6))
          win[((s + DST) % 6) * 3 + m] = ldf(rs, lane16, rb, (s + DST) * 3 + m);
        SB();
      }
  }
}
__device__ __forceinline__ void gemm_up(f32x4 (&acc)[3][4], v4u (&win)[WIN], LAS unsigned char* img, rsrc_t rs, int lane16, int rb, LAS unsigned char* L) {
  v4u act[8];
  cnt_wait(L, C_XA, 4);
#pragma unroll
  for (int n = 0; n < 4; ++n) { act[n] = *(LAS v4u*)(img + n * 18 * KB); SB(); }
  ACC_IN(acc);
#pragma unroll 1
  for (int c = 0; c < 3; ++c) {
    mlp_chunk<-1>(acc, win, act, img, rs, lane16, rb, L);
    img += 6 * KB;
    rb += 18 * KB;
  }
  ACC_OUT(acc);
}
__device__ __forceinline__ void gemm_down(f32x4 (&acc)[3][4], v4u (&win)[WIN], LAS unsigned char* img, rsrc_t rs, int lane16, int rb, LAS unsigned char* L) {
  v4u act[8];
  cnt_wait(L, C_H0, 4);
#pragma unroll
  for (int n = 0; n < 4; ++n) { act[n] = *(LAS v4u*)(img + n * 18 * KB); SB(); }
  mlp_chunk<0>(acc, win, act, img, rs, lane16, rb, L);
  img += 6 * KB;
  asm volatile("" : "+v"(img));
  mlp_chunk<1>(acc, win, act, img, rs, lane16, rb + 18 * KB, L);
  img += 6 * KB;
  asm volatile("" : "+v"(img));
  mlp_chunk<2>(acc, win, act, img, rs, lane16, rb + 36 * KB, L);
  ACC_OUT(acc);
}

__global__ void __launch_bounds__(NTHR, 3) fused_fwd(Params p) {
  extern __shared__ __attribute__((aligned(16))) unsigned char smem[];
  LAS unsigned char* L = (LAS unsigned char*)smem;
  const int w0 = __builtin_amdgcn_readfirstlane(threadIdx.x >> 6);
  const int bid = blockIdx.x;
  u64 pc0;
  int clen;
  asm volatile("s_getpc_b64 %0\n.Lfwd_begin:\n\ts_mov_b32 %1, .Lfwd_end-.Lfwd_begin" : "=s"(pc0), "=s"(clen));
  unsigned cpf = 0;
  const int coff = (((blockIdx.x >> 3) % 11) * 64 + (threadIdx.x & 63)) * 64;
  if (w0 == 11 && coff < clen) cpf = *(const GAS unsigned*)(pc0 + coff);
  constexpr unsigned MPX = 8 / XB;
  const int j0 = ((blockIdx.x & 7) / XB) * (32 / MPX) + (blockIdx.x >> 3) / MPX;
  const int g0 = ((blockIdx.x & 7) % XB) * MPX + (blockIdx.x >> 3) % MPX;
  const rsrc_t rs = __builtin_amdgcn_make_buffer_rsrc((void*)p.ws, 0, WS_BYTES, 0x00020000);
  const int mb0 = WBASE + g0 * WSL + (252 + w0 * 108) * KB;
  const int nb0 = sel(w0 < 9, WOB + ((g0 & 3) * 9 + w0) * 18 * KB, mb0);
  const int qb0 = sel(w0 < 8, WBASE + g0 * WSL + sel(w0 < 6, w0 * 36, 108 + w0 * 18) * KB, nb0);
  const u64 tag0 = __hip_atomic_load((const GAS u64*)p.ws, __ATOMIC_RELAXED, __HIP_MEMORY_SCOPE_AGENT);
  u64 tag = 0;
  v4u win[WIN];
  {
    constexpr int layer = 0;
    LOCALS();
    const rsrc_t rx = __builtin_amdgcn_make_buffer_rsrc((void*)p.x, 0, 2048 * 576 * 2, 0x00020000);
    const int xrow = lane >> 2;
#define LOAD_X() v4u xr[9]; _Pragma("unroll") for (int i = 0; i < 9; ++i) xr[i] = __builtin_amdgcn_raw_buffer_load_b128(rx, xoff + i * 64, 0, 0); SB()
#define STAGE_X() do { float s1 = 0.f, s2 = 0.f; \
      _Pragma("unroll") for (int i = 0; i < 9; ++i) { \
        if (w < 10) *(LAS v4u*)(L + (w * 9 + i) * KB + (lane & 3) * 256 + xrow * 16) = xr[i]; \
        _Pragma("unroll") for (int e = 0; e < 4; ++e) { s1 = dot2(xr[i][e], ONES, s1); s2 = dot2(xr[i][e], xr[i][e], s2); } } \
      s1 += dpp<0xB1>(s1); s2 += dpp<0xB1>(s2); s1 += dpp<0x4E>(s1); s2 += dpp<0x4E>(s2); \
      if (w < 10 && (lane & 3) == 0) *(LAS f32x2*)(L + L_ST + ((w & 1) * 80 + (w >> 1) * 16 + xrow) * 8) = f32x2{s1, s2}; } while (0)
    if (opq_s(w) < 8) {
      LAS unsigned char* T = L + L_QK + w * 3072;
      const int uf = 8 * j + w, uc = sel(uf < 252, uf, 0), us = (uc * 293) >> 12, tp = uc - us * 14;
      const int tile = sel(tp < 4, 10 + tp, sel(tp == 4, 8, sel(tp == 13, 9, tp - 5)));
      const bool vx = tile == 9, ex = (tile & 14) == 8;
      const int c0 = 72 * g + sel(ex, 16, ((tile + sel(tile >= 10, 2, 0)) & 3) * 18) + sel(tile < 4, 576, sel(tile < 8 || vx, 1152, 0));
      const rsrc_t rq = __builtin_amdgcn_make_buffer_rsrc((void*)p.wqkv, 0, 576 * 1728 * 2, 0x00020000);
      const int qo = (lane >> 1) * 3456 + (lane & 1) * sel(ex, sel(vx, 0, 72), 16), qs = us * 32 * 3456 + c0 * 2;
      v4u qv;
      if (ex) {
        const int cA = sel(vx, 36, 1152), cB = sel(vx, 72, 36);
        qv = v4u{__builtin_amdgcn_raw_buffer_load_b32(rq, qo, qs, 0), __builtin_amdgcn_raw_buffer_load_b32(rq, qo, qs + cA, 0),
                 __builtin_amdgcn_raw_buffer_load_b32(rq, qo, qs + cB, 0), __builtin_amdgcn_raw_buffer_load_b32(rq, qo, qs + cA + cB, 0)};
      } else qv = __builtin_amdgcn_raw_buffer_load_b128(rq, qo, qs, 0);
      SB();
      const int of = 3 * j + w, oc = (g >> 2) * 81 + of, os = (oc * 57) >> 9, ot = oc - os * 9;
      const bool hasO = w < 3 && of < 81;
      const bool ox = os >= 16;
      const int orow = quad * sel(ox, 72, 4) + ((lane >> 1) & 1) + ((lane >> 2) & 1) * sel(ox, 18, 2) + ((lane >> 3) & 1) * sel(ox, 36, 18);
      const rsrc_t ro = __builtin_amdgcn_make_buffer_rsrc((void*)p.wo, 0, 576 * 576 * 2, 0x00020000);
      const v4u ov = __builtin_amdgcn_raw_buffer_load_b128(ro, orow * 1152 + (lane & 1) * 16 + sel(hasO, 0, OOR),
                                                           sel(ox, 288 * (os - 16) + 16, 36 * os) * 1152 + 32 * ((g & 3) * 9 + ot), 0);
      SB();
      const int xoff = (64 * j + 32 * ((w >> 1) & 1) + (w >> 2) + 2 * xrow) * 1152 + (w & 1) * 576 + (lane & 3) * 16;
      __builtin_amdgcn_s_barrier();
      LOAD_X();
      *(LAS v4u*)(T + (lane >> 1) * 48 + (lane & 1) * 16) = (vx && (lane & 1)) ? v4u{0u, 0u, 0u, 0u} : qv;
      *(LAS v4u*)(T + 1536 + (lane >> 1) * 48 + (lane & 1) * 16) = ov;
      LAS unsigned char* R = T + (8 * quad + (l15 >> 2)) * 48 + (lane & 3) * 8;
      const v2u a = __builtin_bit_cast(v2u, __builtin_amdgcn_ds_read_tr16_b64_v4i16((LAS s16x4*)(R)));
      const v2u b = __builtin_bit_cast(v2u, __builtin_amdgcn_ds_read_tr16_b64_v4i16((LAS s16x4*)(R + 4 * 48)));
      const v2u c = __builtin_bit_cast(v2u, __builtin_amdgcn_ds_read_tr16_b64_v4i16((LAS s16x4*)(R + 1536)));
      const v2u d = __builtin_bit_cast(v2u, __builtin_amdgcn_ds_read_tr16_b64_v4i16((LAS s16x4*)(R + 1536 + 4 * 48)));
      stg(v4u{a.x, a.y, b.x, b.y}, rs, lane16,
          sel(uf >= 252, OOR, wb + sel(tile < 8, (tile & 3) * 36 + us * 2 + (tile >> 2), sel(tile < 10, 72 + tile * 18 + us, (tile >> 1) * 36 - 36 + us * 2 + (tile & 1))) * KB));
      stg(v4u{c.x, c.y, d.x, d.y}, rs, lane16, sel(hasO, WOB + ((g & 3) * 162 + ot * 18 + os) * KB, OOR));
      const float cq = rows_sum(dot2(a.x, ONES, dot2(a.y, ONES, dot2(b.x, ONES, dot2(b.y, ONES, 0.f)))));
      __builtin_amdgcn_raw_buffer_store_b32(__float_as_uint(cq), rs, sel(lane < 16, lane * 4, OOR) + sel(uf < 252, CSQ + ((g * 18 + us) * 14 + tile) * 64, OOR), 0, SC1);
      SB();
      DRAIN();
      GET_TAG();
      if (lane == 0 && __hip_atomic_fetch_add((LAS int*)(L + L_HC) + C_P0, 1, __ATOMIC_RELAXED, __HIP_MEMORY_SCOPE_WORKGROUP) == 7)
        set_flag(p.ws, SFLAGS + (g * 32 + j) * 8, tag);
      STAGE_X();
      BAR();
      issue_window<0, WIN>(win, rs, lane16, QBASE());
    }
    if (opq_s(w) >= 8) {
      LAS unsigned char* T = L + 125 * KB + (w - 8) * 4608;
      const int item = sel(w < 11, 3 * j + w - 8, sel((j & 7) < 3, 96 + 3 * (j >> 3) + (j & 7), 127)), rbk = (item * 57) >> 9, cb = item - rbk * 9, kind = sel(rbk >= 6, 1, 0), k3 = rbk - kind * 6;
      const int rn = sel(kind, 4, 8), ld = sel(kind, 1152, 9216);
      const rsrc_t rw = __builtin_amdgcn_make_buffer_rsrc((void*)(kind == 0 ? p.w1 : p.w2), 0, 576 * 4608 * 2, 0x00020000);
      const int jj = lane >> 3;
      const int woff = (jj & 3) * ld + (lane & 7) * 16 + sel(item < 108, 96 * k3 * ld + g * sel(kind, 576 * 1152, 1152) + 128 * cb, OOR);
      const int xoff = (sel(j > 0, 64 * j - 16, 0) + 2 * (xrow & 7) + (xrow >> 3)) * 1152 + (lane & 3) * 16 + sel(w < 10, (w & 1) * 576, OOR);
      if (w == 11 && lane < 16) ((volatile LAS int*)(L + L_HC))[lane] = 0;
      BAR();
      LOAD_X();
      v4u wv[3][4];
#pragma unroll
      for (int n = 0; n < 4; ++n)
#pragma unroll
        for (int c = 0; c < 3; ++c)
          wv[c][n] = __builtin_amdgcn_raw_buffer_load_b128(rw, woff + (jj >> 2) * (sel(kind, c < 2 ? 16 : 48, 4) * ld),
                                                           (n * rn + sel(kind, c < 2 ? 48 * c : 32, 32 * c)) * ld, NTW ? 2 : 0);
      SB();
      GET_TAG();
      STAGE_X();
      float cs[4] = {0.f, 0.f, 0.f, 0.f};
      LAS unsigned char* R = T + (8 * quad + (l15 >> 2)) * 144 + (lane & 3) * 8;
#pragma unroll
      for (int c = 0; c < 3; ++c) {
#pragma unroll
        for (int n = 0; n < 4; ++n) *(LAS v4u*)(T + (n * 8 + (lane >> 3)) * 144 + (lane & 7) * 16) = wv[c][n];
#pragma unroll
        for (int tt = 0; tt < 4; ++tt) {
          const v2u a = __builtin_bit_cast(v2u, __builtin_amdgcn_ds_read_tr16_b64_v4i16((LAS s16x4*)(R + tt * 32)));
          const v2u b = __builtin_bit_cast(v2u, __builtin_amdgcn_ds_read_tr16_b64_v4i16((LAS s16x4*)(R + tt * 32 + 4 * 144)));
          wv[c][tt] = v4u{a.x, a.y, b.x, b.y};
          cs[tt] = dot2(a.x, ONES, dot2(a.y, ONES, dot2(b.x, ONES, dot2(b.y, ONES, cs[tt]))));
        }
      }
#pragma unroll
      for (int tt = 0; tt < 4; ++tt) { cs[tt] = rows_sum(cs[tt]); OPAQUE(cs[tt]); }
      float csum = quad == 0 ? cs[0] : quad == 1 ? cs[1] : quad == 2 ? cs[2] : cs[3];
      asm volatile("" : "+v"(csum));
      if (opq_s(w) == 11) {
        cnt_wait(L, C_P0, 8);
        wait_flags<4>(p.ws, SFLAGS + ((g ^ ((lane >> 3) & 4)) * 32 + (lane & 31)) * 8, true, tag);
#pragma unroll
        for (int i = 0; i < 18; ++i) win[i] = ldg(rs, lane16, CSQ + (g * 18 + i) * 896);
      }
      BAR();
      row_table1<2>(L, w - 8, lane);
      if (opq_s(w) == 11) {
        f32x4 cs = __builtin_bit_cast(f32x4, win[0]);
#pragma unroll
        for (int i = 1; i < 18; ++i) add4(cs, win[i]);
        if (lane < 56) *(LAS f32x4*)(L + L_TQ + lane16) = cs;
        row_table1<2>(L, 4, lane);
      }
#pragma unroll
      for (int i = 0; i < 5; ++i) {
        const int n = (w - 8) * 5 + i;
        stg(v4u{POISON, POISON, POISON, POISON}, rs, lane16, sel(n < 18, ab + sel(n < 9, A_O, A_X1 - 9 * KB) + (g * 9 + n) * KB, OOR));
      }
      __builtin_amdgcn_s_sleep(BDELAY);
      DRAIN();
      if (lane == 0 && __hip_atomic_fetch_add((LAS int*)(L + L_HC) + C_P0, 1, __ATOMIC_RELAXED, __HIP_MEMORY_SCOPE_WORKGROUP) == 11)
        set_flag(p.ws, FLAGS + j * 128 + g * 8, tag);
      const int dst0 = sel(item < 108, wb + (252 + 54 * kind + 9 * k3) * KB, OOR);
#pragma unroll
      for (int tt = 0; tt < 4; ++tt)
#pragma unroll
        for (int c = 0; c < 3; ++c) stg(wv[c][tt], rs, lane16 + c * 3 * KB, dst0 + ((((4 * cb + tt) * 43) >> 7) * 105 + 4 * cb + tt) * KB);
      __builtin_amdgcn_raw_buffer_store_b32(__float_as_uint(csum), rs,
                                            lane * 4 + sel(kind == 0, CS1 + (k3 * 4608 + 576 * g + 64 * cb) * 4, OOR), 0, SC1);
      SB();
      if (opq_s(w) == 8) issue_window<0, WIN>(win, rs, lane16, QBASE());
    }
  }
  int os0[3];
  const int wq = opq_s(w0);
  {
    const int T = 2 * (g0 >> 2) + sel(wq >= 6, 1, 0), s0 = 3 * sel(wq >= 6, wq - 6, wq);
#pragma unroll
    for (int i = 0; i < 3; ++i) {
      const int s = s0 + i;
      os0[i] = ABASE + j0 * ASL + A_O + sel(s < 16, ((s >> 1) * 9 + T * 2 + (s & 1)) * KB, ((s - 16) * 36 + 8) * KB + T * 256);
    }
  }
  const int oq0 = sel(wq == 5 || wq == 11, 9 * KB, 256);
  const int ft0 = (g0 & 3) * 9 + sel(wq < 9, wq, 0), xs0 = ((g0 >> 2) * 36 + (ft0 >> 1)) * KB + (ft0 & 1) * 512;

#pragma unroll 1
  for (int layer = 0; layer < 2; ++layer) {
    v4u xres;
    {
      LOCALS();
      {
        const int qbase = QBASE(), nbase = NBASE(), rw = 5 + 5 * layer;
        if (opq_s(w) < 4) qkv_tiles<2, 5, 2, 7, 4, WIN>(win, L, rs, lane, qbase, nbase, w, rw);
        if ((opq_s(w) & 14) == 4) qkv_tiles<2, 4, 0, 7, 1, 0>(win, L, rs, lane, qbase, nbase, 2 * w + 2, rw);
        if (opq_s(w) == 6) qkv_tiles<1, 5, 0, 10, 0, WIN>(win, L, rs, lane, qbase, nbase, 8, rw);
        if (opq_s(w) == 7) qkv_tiles<1, 5, 1, 10, 0, WIN>(win, L, rs, lane, qbase, nbase, 9, rw);
      }
      if (layer == 1 && bid == 0 && w == 10) {
#pragma unroll 1
        for (int i = 0; i < 4; ++i) wait_flags(p.ws, SFLAGS + (256 + i * 64 + lane) * 8, true, tag);
        if (lane == 0) set_flag(p.ws, 0, tag);
      }
      xres = *(LAS v4u*)(L + opq_s(xs0) + (quad & 1) * 18 * KB + (quad >> 1) * 256 + l15 * 16);
      if (layer == 0 && w >= 8) DRAIN();
      if ((opq_s(w) & 12) == 4) {
        BAR();
        if (layer == 0 && w == 6 && lane == 0) set_flag(p.ws, SFLAGS + (256 + g * 32 + j) * 8, tag);
        if (w == 7 && layer == 0) {
          wait_flags(p.ws, FLAGS + j * 128 + (lane & 7) * 8, lane < 8, tag);
        }
        if (w < 6) issue_window<0, WIN>(win, rs, lane16, NBASE());
      }
      if ((opq_s(w) & 12) != 4) {
        const int T = w & 3, hp = w >> 3, Tp = (T & 1) ? T - 1 : 4;
        f32x4 mp, ms;
        const float neg = __uint_as_float(vconst<0xf149f2cau>());
        const int lo = l15 + ((T & 1) ? 9 : 1 + 4 * T), hi = (T & 1) ? 16 : j > 0 ? 8 + 4 * T : 0;
#pragma unroll
        for (int r = 0; r < 4; ++r) {
          const int jj = 4 * quad + r;
          mp[r] = ((jj >= lo) & (jj < hi)) ? 0.f : neg;
          ms[r] = (unsigned)(l15 - jj) <= 7u ? 0.f : neg;
        }
        int xs = L_QK + lane * 8 + T * 512, xp = L_QK + lane * 8 + Tp * 512, hs = xs + hp * 5120, hq = xp + hp * 5120;
        int om = ab + A_O + (g * 9 + T * 2 + hp) * KB + lane16;
        int oe = quad == hp ? ab + A_O + (g * 9 + 8) * KB + T * 256 + hp * 8 + l15 * 16 : OOR;
        unsigned qm0 = quad == 2 * hp ? ~0u : 0u, qm1 = quad == 2 * hp + 1 ? ~0u : 0u, zr = vconst<0>();
        v4u one4 = {vconst<ONES>(), vconst<ONES>(), vconst<ONES>(), vconst<ONES>()};
        asm volatile("" : "+v"(mp), "+v"(ms), "+v"(xs), "+v"(xp), "+v"(hs), "+v"(hq), "+v"(om), "+v"(oe), "+v"(qm0), "+v"(qm1), "+v"(zr), "+v"(one4));
        BAR();
        const v2u kxs = *(LAS v2u*)(L + xs + 40 * 512);
        const unsigned kxp = *(LAS unsigned*)(L + xp + 40 * 512 + 4);
        v2u q[2], ks[2], kp[2], vs[2], vp[2];
#pragma unroll
        for (int hh = 0; hh < 2; ++hh) {
          q[hh] = *(LAS v2u*)(L + hs + (50 + 5 * hh) * 512);
          kp[hh] = *(LAS v2u*)(L + hq + 5 * hh * 512);
          ks[hh] = *(LAS v2u*)(L + hs + 5 * hh * 512);
        }
        SB();
#pragma unroll
        for (int hh = 0; hh < 2; ++hh) {
          vp[hh] = *(LAS v2u*)(L + hq + (20 + 5 * hh) * 512);
          vs[hh] = *(LAS v2u*)(L + hs + (20 + 5 * hh) * 512);
        }
        const v2u vxp = *(LAS v2u*)(L + xp + 45 * 512), vxs = *(LAS v2u*)(L + xs + 45 * 512);
        SB();
        const f32x4 z = {0.f, 0.f, 0.f, 0.f};
        f32x4 sp[2], ss[2], o[2], ox[2], sm[2];
        float mx[2];
#pragma unroll
        for (int hh = 0; hh < 2; ++hh) {
          const v4u qf = {q[hh].x, q[hh].y, kxs.x & (hh ? qm1 : qm0), zr};
          sp[hh] = mfma(v4u{kp[hh].x, kp[hh].y, kxp, zr}, qf, mp);
          ss[hh] = mfma(v4u{ks[hh].x, ks[hh].y, kxs.y, zr}, qf, ms);
        }
#pragma unroll
        for (int hh = 0; hh < 2; ++hh) {
          const float m0 = __builtin_fmaxf(__builtin_fmaxf(sp[hh][0], sp[hh][1]), sp[hh][2]), m1 = __builtin_fmaxf(__builtin_fmaxf(ss[hh][0], ss[hh][1]), ss[hh][2]);
          mx[hh] = __builtin_fmaxf(__builtin_fmaxf(__builtin_fmaxf(__builtin_fmaxf(m0, sp[hh][3]), ss[hh][3]), m1), m1);
        }
        rows_max2(mx[0], mx[1]);
#pragma unroll
        for (int hh = 0; hh < 2; ++hh) {
          const float c = 0.23570226039551584f * 1.4426950408889634f, mc = -c * mx[hh];
          float e[8];
#pragma unroll
          for (int r = 0; r < 4; ++r) {
            e[r] = __builtin_amdgcn_exp2f(__builtin_fmaf(sp[hh][r], c, mc));
            e[4 + r] = __builtin_amdgcn_exp2f(__builtin_fmaf(ss[hh][r], c, mc));
          }
          const v4u pf = {pk(e[0], e[1]), pk(e[2], e[3]), pk(e[4], e[5]), pk(e[6], e[7])};
          sm[hh] = mfma(one4, pf, z);
          o[hh] = mfma(v4u{vp[hh].x, vp[hh].y, vs[hh].x, vs[hh].y}, pf, z);
          ox[hh] = mfma(v4u{vxp.x, vxp.y, vxs.x, vxs.y}, pf, z);
        }
        const float i0 = __builtin_amdgcn_rcpf(sm[0][0]), i1 = __builtin_amdgcn_rcpf(sm[1][0]);
        stg(v4u{pk(o[0][0] * i0, o[0][1] * i0), pk(o[0][2] * i0, o[0][3] * i0), pk(o[1][0] * i1, o[1][1] * i1), pk(o[1][2] * i1, o[1][3] * i1)}, rs, om, 0);
        __builtin_amdgcn_raw_buffer_store_b64(v2u{pk(ox[0][0] * i0, ox[0][1] * i0), pk(ox[1][2] * i1, ox[1][3] * i1)}, rs, oe, 0, SC1);
        SETTLE();
      }
      BAR();
    }
    {
      LOCALS();
      const int th = g >> 2, ft = (g & 3) * 9 + w;
      {
        const int ov = quad * opq_s(oq0) + l15 * 16;
        RETRY_BEGIN();
        v4u orr[3];
#pragma unroll
        for (int i = 0; i < 3; ++i) orr[i] = ldg(rs, i ? ov : lane16, os0[i] + layer * ALY);
#pragma unroll
        for (int i = 0; i < 3; ++i) { *(LAS v4u*)(L + (w * 3 + i) * KB + lane16) = orr[i]; seen_ = umax4(seen_, orr[i]); }
        RETRY_END();
      }
      float xf[8] = {blo(xres.x), bhi(xres.x), blo(xres.y), bhi(xres.y), blo(xres.z), bhi(xres.z), blo(xres.w), bhi(xres.w)};
      int xo = lane16 + ab + A_X1 + (th * 36 + ft) * KB;
      asm volatile("" : "+v"(xo), "+v"(xf[0]), "+v"(xf[1]), "+v"(xf[2]), "+v"(xf[3]), "+v"(xf[4]), "+v"(xf[5]), "+v"(xf[6]), "+v"(xf[7]));
      if (layer == 0 && w == 0) wait_flags<2>(p.ws, SFLAGS + (256 + g * 32 + (lane & 31)) * 8, lane < 32, tag);
      BAR();
      if (w < 9) {
        f32x4 acc[1][2];
        gemm18<1, 2, 0, P2N, 10>(acc, win, L + lane16, rs, lane16, 0, MBASE(), L);
        float a[8];
#pragma unroll
        for (int r = 0; r < 4; ++r) {
          auto s = __builtin_amdgcn_permlane16_swap(__float_as_uint(acc[0][0][r]), __float_as_uint(acc[0][1][r]), false, false);
          a[r] = __uint_as_float(s[0]);
          a[4 + r] = __uint_as_float(s[1]);
        }
        stg(v4u{pk(a[0] + xf[0], a[1] + xf[1]), pk(a[2] + xf[2], a[3] + xf[3]), pk(a[4] + xf[4], a[5] + xf[5]), pk(a[6] + xf[6], a[7] + xf[7])}, rs, xo, 0);
        SB();
        if (P2N < 3 * DST) issue_window<P2N, 3 * DST>(win, rs, lane16, MBASE());
        else SETTLE();
      }
      if (layer == 0 && opq_s(w) >= 9) {
        const int f4 = (w - 9) * 48 + (lane < 48 ? lane : 0);
        v4u cp[6];
#pragma unroll
        for (int i = 0; i < 6; ++i) cp[i] = ldg(rs, f4 * 16, CS1 + (i * 4608 + 576 * g) * 4);
        const v2u b = *(const GAS v2u*)(p.b1 + 576 * g + 4 * f4);
        *(LAS v2u*)(L + L_B2 + f4 * 8) = *(const GAS v2u*)(p.b2 + 4 * f4);
        f32x4 cs = __builtin_bit_cast(f32x4, cp[0]);
#pragma unroll
        for (int i = 1; i < 6; ++i) add4(cs, cp[i]);
        *(LAS f32x4*)(L + L_T1 + f4 * 16) = cs;
        *(LAS f32x4*)(L + L_T1 + 2304 + f4 * 16) = f32x4{blo(b.x), bhi(b.x), blo(b.y), bhi(b.y)};
      }
      if (w == 10 && lane < 10) ((volatile LAS int*)(L + L_HC))[lane] = 0;
      BAR();
    }
    {
      LOCALS();
      const int mbase = MBASE();
      const int T = w & 3, cls = w >> 2;
      if (opq_s(cls) == 0) {
        if (layer == 0) {
          __builtin_amdgcn_s_sleep(PZD);
          const v4u pz = {vconst<POISON>(), vconst<POISON>(), vconst<POISON>(), vconst<POISON>()};
#pragma unroll
          for (int i = 0; i < 8; ++i) {
            const int n = w * 8 + i;
            stg(pz, rs, lane16,
                sel(n < 30, ab + ALY + n * KB + sel(n < 12, A_X2 + g * 12 * KB, sel(n < 21, A_O, A_X1 - 9 * KB) + (g * 9 - 12) * KB), OOR));
          }
        }
      }
      if (opq_s(cls) > 0) {
        v4u xr[9];
        const int xo = (quad >> 1) * KB + ((quad & 1) * 2 + (T & 1)) * 256 + l15 * 16;
        RETRY_BEGIN();
#pragma unroll
        for (int i = 0; i < 9; ++i) xr[i] = ldg(rs, xo + (i & 1) * 2 * KB, ab + A_X1 + ((T >> 1) * 36 + 18 * (cls - 1) + (i >> 1) * 4) * KB);
#pragma unroll
        for (int i = 0; i < 9; ++i) { *(LAS v4u*)(L + (T * 18 + 9 * (cls - 1) + i) * KB + lane16) = xr[i]; seen_ = umax4(seen_, xr[i]); }
        RETRY_END();
        cnt_add(L, C_XA + cls - 1, lane);
        __builtin_amdgcn_s_setprio(3);
        const v4u one4 = {vconst<ONES>(), vconst<ONES>(), vconst<ONES>(), vconst<ONES>()};
        f32x4 sa = {0.f, 0.f, 0.f, 0.f}, sq = {0.f, 0.f, 0.f, 0.f};
        asm volatile("s_nop 1");
#pragma unroll
        for (int i = 0; i < 9; ++i) { sa = mfma(one4, xr[i], sa); sq = mfma(xr[i], xr[i], sq); }
        __builtin_amdgcn_s_setprio(0);
        if (layer == 0 && opq_s(w) >= 9) issue_window<0, 3 * DST>(win, rs, lane16, mbase);
        const float dg = (lane & 2) ? ((lane & 1) ? sq[3] : sq[2]) : ((lane & 1) ? sq[1] : sq[0]);
        if (quad == (l15 >> 2)) *(LAS f32x2*)(L + L_ST + ((T * 16 + l15) * 2 + cls - 1) * 8) = f32x2{sa[0], dg};
        cnt_add(L, C_ST, lane);
      }
      f32x4 acc[3][4];
#pragma unroll
      for (int t = 0; t < 3; ++t)
#pragma unroll
        for (int m = 0; m < 4; ++m) acc[t][m] = zero4();
      if (opq_s(cls) > 0) { row_table(L, lane); shift_acc(acc, L, w, quad, l15); }
      __builtin_amdgcn_s_setprio(3);
      gemm_up(acc, win, L + lane16, rs, lane16, mbase, L);
      __builtin_amdgcn_s_setprio(0);
      {
        LAS unsigned char* H = L + L_H + lane16 + 3 * (w >> 1) * KB;
        if (opq_s(cls) == 0) {
          cnt_wait(L, C_RT, 1);
          shift_acc(acc, L, w, quad, l15);
        }
        f32x4 rt[4];
#pragma unroll
        for (int m = 0; m < 4; ++m) rt[m] = *(LAS f32x4*)(L + L_ST + KB + (m * 16 + l15) * 16);
#pragma unroll
        for (int m = 0; m < 4; ++m) {
          const v2u u0 = gelu4(acc[0][m], rt[m][2], rt[m][3], rt[m][1]), u1 = gelu4(acc[1][m], rt[m][2], rt[m][3], rt[m][1]);
          *(LAS v4u*)(H + (m * 18 + (w & 1)) * KB) = v4u{u0.x, u0.y, u1.x, u1.y};
          *(LAS v2u*)(H + (m * 18 + 2) * KB + (w & 1) * 8) = gelu4(acc[2][m], rt[m][2], rt[m][3], rt[m][1]);
        }
        cnt_add(L, C_H0 + cls, lane);
      }
      __builtin_amdgcn_s_setprio(2);
      gemm_down(acc, win, L + L_H + lane16, rs, lane16, mbase + 54 * KB, L);
      __builtin_amdgcn_s_setprio(0);
      const int pw = ab + A_PT + (g * 12 + w) * 6144;
      const int poff = (quad & 1) * 1536 + (quad >> 1) * 256 + l15 * 16;
#pragma unroll
      for (int t = 0; t < 3; ++t)
#pragma unroll
        for (int m = 0; m < 4; m += 2) {
          const v2u a = {pk(acc[t][m][0], acc[t][m][1]), pk(acc[t][m][2], acc[t][m][3])};
          const v2u b = {pk(acc[t][m + 1][0], acc[t][m + 1][1]), pk(acc[t][m + 1][2], acc[t][m + 1][3])};
          const v4u o = widen(a, b);
          const bool own = m + (quad & 1) == (g >> 1) && (l15 >> 3) == (g & 1);
          stg(o, rs, own ? OOR : poff + m * 1536 + t * 512, pw);
          if (own) *(LAS v4u*)(L + (((g >> 1) ^ 1) * 18 + w) * KB + ((2 * t + (quad >> 1)) * 8 + (lane & 7)) * 16) = o;
        }
    }
    {
      LOCALS();
      OWNROWS(6);
      const bool act = lane < 48;
      float sum[8];
      {
        const v4u bb = *(LAS v4u*)(L + L_B2 + (act ? c8 : 0) * 16);
        const v4u xr = *(LAS v4u*)(L + (act ? piece : 0)), ow = *(LAS v4u*)(L + (((g >> 1) ^ 1) * 18 + w) * KB + lane16);
#pragma unroll
        for (int e = 0; e < 4; ++e) {
          sum[2 * e] = blo(xr[e]) + blo(ow[e]); OPAQUE(sum[2 * e]); sum[2 * e] += blo(bb[e]); OPAQUE(sum[2 * e]);
          sum[2 * e + 1] = bhi(xr[e]) + bhi(ow[e]) + bhi(bb[e]);
        }
      }
      int roff = (g >> 1) * 1536 + (lane >> 3) * 256 + (8 * (g & 1) + (lane & 7)) * 16 + (act ? 0 : OOR);
      int foff = WFLAGS + ((layer * 32 + j - (lane >> 3)) * 12 + w) * 128 + (lane < 8 ? lane : 3 + 4 * (lane & 1)) * 8;
      asm volatile("" : "+v"(roff), "+v"(foff), "+v"(sum[0]), "+v"(sum[1]), "+v"(sum[2]), "+v"(sum[3]), "+v"(sum[4]), "+v"(sum[5]), "+v"(sum[6]), "+v"(sum[7]));
      if (w >= 8) __builtin_amdgcn_s_barrier();
      DRAIN();
      if (lane == 0) set_flag(p.ws, WFLAGS + ((layer * 32 + j) * 12 + w) * 128 + g * 8, tag);
      if (layer == 0 && opq_s(w) < 8) {
        if (QA) cnt_wait(L, C_LC, 12);
        issue_window<0, QA>(win, rs, lane16, QBASE());
      }
      __builtin_amdgcn_s_barrier();
      wait_flags(p.ws, foff, lane < (layer == 0 && j > 0 ? 10 : 8), tag);
      if (w < 8) __builtin_amdgcn_s_barrier();
      asm volatile("" ::: "memory");
      v4u xb;
      {
        v4u pr[7];
#pragma unroll
        for (int i = 0; i < 7; ++i) pr[i] = ldg(rs, roff, ab + A_PT + (((i + 1 + g) & 7) * 12 + w) * 6144);
        SB();
        const unsigned sel_lo = opq_s(0x00003f80), sel_hi = opq_s(0x3f800000);
#pragma unroll
        for (int i = 0; i < 7; ++i)
#pragma unroll
          for (int e = 0; e < 4; ++e) { sum[2 * e] = dot2(pr[i][e], sel_lo, sum[2 * e]); sum[2 * e + 1] = dot2(pr[i][e], sel_hi, sum[2 * e + 1]); }
#pragma unroll
        for (int e = 0; e < 4; ++e) xb[e] = pk(sum[2 * e], sum[2 * e + 1]);
      }
      if (layer == 0) {
        float s1 = dot2(xb.x, ONES, dot2(xb.y, ONES, dot2(xb.z, ONES, dot2(xb.w, ONES, 0.f))));
        float s2 = dot2(xb.x, xb.x, dot2(xb.y, xb.y, dot2(xb.z, xb.z, dot2(xb.w, xb.w, 0.f))));
        if (!act) { s1 = 0.f; s2 = 0.f; }
        s1 += ror8(s1); OPAQUE(s1); s2 += ror8(s2);
        s1 = rows_sum(s1); OPAQUE(s1); s2 = rows_sum(s2);
        if (!act) xb = v4u{__float_as_uint(s1), __float_as_uint(s2), 0u, 0u};
        int vo = lane16 + ab + ALY + A_X2 + w * KB + (lane < 56 ? 0 : OOR);
        stg(xb, rs, vo, g * 12 * KB);
        SB();
        LAS unsigned char *Iw = L + (c8 >> 2) * KB + ((c8 & 3) * 16 + (lane & 7)) * 16, *Iw2 = Iw + 54 * KB, *Sw = L + L_ST + (w * 80 + (lane & 7)) * 8;
        int vh = vo - (j > 0 ? ASL : 0);
        asm volatile("" : "+v"(vo), "+v"(vh), "+v"(Iw), "+v"(Iw2), "+v"(Sw));
        if (QB > QA && opq_s(w) < 8) issue_window<QA, QB>(win, rs, lane16, QBASE());
        else SETTLE();
        BAR();
        v4u xv[10];
        RETRY_BEGIN();
#pragma unroll
        for (int i = 0; i < 10; ++i) xv[i] = ldg(rs, i < 8 ? vo : vh, (i < 8 ? i : 3 + 4 * (i - 8)) * 12 * KB);
        SB();
        if (act) {
#pragma unroll
          for (int i = 0; i < 10; ++i) *(LAS v4u*)((i < 6 ? Iw + (i >> 1) * 18 * KB : Iw2 + ((i >> 1) - 3) * 18 * KB) + (i & 1) * 128) = xv[i];
        }
#pragma unroll
        for (int i = 0; i < 10; ++i) seen_ = umax4(seen_, xv[i]);
        RETRY_END();
        if ((lane >> 3) == 6) {
#pragma unroll
          for (int i = 0; i < 10; ++i) *(LAS v2u*)(Sw + i * 64) = v2u{xv[i].x, xv[i].y};
        }
        BAR();
        if (QB < WIN && opq_s(w) < 8) issue_window<QB, WIN>(win, rs, lane16, QBASE());
        if (w == 11) row_table1<12>(L, 4, lane);
        if (w >= 8) row_table1<12>(L, w - 8, lane);
        if (opq_s(w) == 8) issue_window<3 * DST, WIN>(win, rs, lane16, QBASE());
        if (opq_s(w) >= 8) issue_window<0, 3 * DST>(win, rs, lane16, QBASE());
      } else {
        const rsrc_t ro = __builtin_amdgcn_make_buffer_rsrc((void*)p.out, 0, 2048 * 576 * 2, 0x00020000);
        __builtin_amdgcn_raw_buffer_store_b128(xb, ro, act ? mytok * 1152 + c8 * 16 : OOR, 0, 0);
      }
    }
  }
  asm volatile(".Lfwd_end:");
}
}

extern "C" void kernel_launch(void* const* d_in, const int* in_sizes, int n_in, void* d_out, int out_size, void* d_ws, size_t ws_size,
                              hipStream_t stream) {
  (void)in_sizes; (void)n_in; (void)out_size; (void)ws_size;
  Params p;
  p.x = (const unsigned short*)d_in[0];
  p.wqkv = (const unsigned short*)d_in[1];
  p.wo = (const unsigned short*)d_in[2];
  p.w1 = (const unsigned short*)d_in[4];
  p.b1 = (const unsigned short*)d_in[5];
  p.w2 = (const unsigned short*)d_in[6];
  p.b2 = (const unsigned short*)d_in[7];
  p.out = (unsigned short*)d_out;
  p.ws = (unsigned char*)d_ws;
  (void)hipFuncSetAttribute((const void*)fused_fwd, hipFuncAttributeMaxDynamicSharedMemorySize, LDS_BYTES);
  hipLaunchKernelGGL(fused_fwd, dim3(256), dim3(NTHR), LDS_BYTES, stream, p);
}
```

```cpp
#include <hip/hip_runtime.h>
#include <hip/hip_bf16.h>
#include <stdint.h>

namespace {
#define LAS __attribute__((address_space(3)))
#define GAS __attribute__((address_space(1)))
typedef unsigned v4u __attribute__((ext_vector_type(4)));
typedef unsigned v2u __attribute__((ext_vector_type(2)));
typedef float f32x4 __attribute__((ext_vector_type(4)));
typedef float f32x2 __attribute__((ext_vector_type(2)));
typedef __bf16 bf16x8 __attribute__((ext_vector_type(8)));
typedef __bf16 bf16x2 __attribute__((ext_vector_type(2)));
typedef short s16x4 __attribute__((ext_vector_type(4)));
typedef unsigned long long u64;
typedef __amdgpu_buffer_rsrc_t rsrc_t;

constexpr int KB = 1024;
constexpr int NTHR = 768;
constexpr int WIN = 18;
#define XB 2
#define P2N 9
#define QA 6
#define QB 12
#define PZD 20
#define BDELAY 10
#define CODEPF 1
#define NTW 1
constexpr int DST = 5;
constexpr float EPS = 1e-5f;
constexpr int SC1 = 16;
constexpr float GC0 = -2.0f * 0.7978845608028654f * 1.4426950408889634f;
constexpr int FLAGS = 4096;
constexpr int SFLAGS = 32 * KB;
constexpr int WFLAGS = 48 * KB;
constexpr int CS1 = 192 * KB;
constexpr int CSQ = 520 * KB;
constexpr int WBASE = 704 * KB;
constexpr int WSL = (252 + 1296) * KB;
constexpr int WOB = WBASE + 8 * WSL;
constexpr int ABASE = 14 * 1024 * KB;
constexpr int IMG = 72 * KB;
constexpr int A_X2 = 0, A_O = 96 * KB, A_X1 = A_O + IMG, A_PT = A_X1 + IMG, ALY = 1024 * KB, ASL = 2 * ALY;
constexpr int WS_BYTES = ABASE + 32 * ASL;
static_assert(WOB + 648 * KB <= ABASE && A_PT + 8 * IMG <= ALY && CSQ + 8 * 14 * 1152 + 256 <= WBASE, "workspace map");
constexpr int L_H = IMG;
constexpr int L_QK = 90 * KB;
constexpr int L_ST = 145 * KB;
constexpr int L_T1 = L_ST + 7680;
constexpr int L_TQ = L_T1 + 4608;
constexpr int L_RT = L_TQ + 896;
constexpr int L_HC = L_RT + 640;
constexpr int L_B2 = L_HC + 64;
constexpr int LDS_BYTES = L_B2 + 1152;
static_assert(LDS_BYTES <= 160 * KB, "LDS");
enum { C_H0 = 0, C_ST = 3, C_RT = 4, C_LC = 5, C_XA = 6, C_R1 = 10, C_P0 = 11 };

struct Params {
  const unsigned short *x, *wqkv, *wo, *w1, *b1, *w2, *b2;
  unsigned short *out;
  unsigned char *ws;
};

#define STAMP(i)
#define STAMP_INIT()
#define RETRY_COUNT()
#define STAMP_ARG
#define STAMP_PASS
#define STAMP_DUMP()
#define SB() __builtin_amdgcn_sched_barrier(0)
#define BAR() do { asm volatile("s_waitcnt lgkmcnt(0)" ::: "memory"); __builtin_amdgcn_s_barrier(); asm volatile("" ::: "memory"); } while (0)
#define DRAIN() asm volatile("s_waitcnt vmcnt(0)" ::: "memory")
#define SETTLE() DRAIN()
#define OPAQUE(x) asm("" : "+v"(x))
#define KEEP(x) asm volatile("" :: "v"(x))

__device__ __forceinline__ f32x4 mfma(v4u a, v4u b, f32x4 c) {
  return __builtin_amdgcn_mfma_f32_16x16x32_bf16(__builtin_bit_cast(bf16x8, a), __builtin_bit_cast(bf16x8, b), c, 0, 0, 0);
}
#define MF(acc, a, b) asm volatile("v_mfma_f32_16x16x32_bf16 %0, %1, %2, %0" : "+v"(acc) : "v"(a), "v"(b))
#define MF0(acc, a, b) asm volatile("v_mfma_f32_16x16x32_bf16 %0, %1, %2, 0" : "=&v"(acc) : "v"(a), "v"(b))
template <int NT, int MT> __device__ __forceinline__ void acc_fence(f32x4 (&acc)[NT][MT]) {
#pragma unroll
  for (int t = 0; t < NT; ++t)
#pragma unroll
    for (int m = 0; m < MT; ++m) asm volatile("" : "+v"(acc[t][m]));
}
#define ACC_IN(acc) do { acc_fence(acc); asm volatile("s_nop 1"); SB(); } while (0)
#define ACC_OUT(acc) do { SB(); asm volatile("s_nop 7\n\ts_nop 1"); acc_fence(acc); } while (0)
__device__ __forceinline__ unsigned pk(float a, float b) {
  f32x2 v = {a, b};
  return __builtin_bit_cast(unsigned, __builtin_convertvector(v, bf16x2));
}
__device__ __forceinline__ float dot2(unsigned a, unsigned b, float c) {
  return __builtin_amdgcn_fdot2_f32_bf16(__builtin_bit_cast(bf16x2, a), __builtin_bit_cast(bf16x2, b), c, false);
}
constexpr unsigned ONES = 0x3f803f80u;
__device__ __forceinline__ float blo(unsigned u) { return __uint_as_float(u << 16); }
__device__ __forceinline__ float bhi(unsigned u) { return __uint_as_float(u & 0xffff0000u); }
__device__ __forceinline__ v4u ldg(rsrc_t rs, int voff, int soff) { return __builtin_amdgcn_raw_buffer_load_b128(rs, voff, soff, SC1); }
__device__ __forceinline__ void stg(v4u v, rsrc_t rs, int voff, int soff) { __builtin_amdgcn_raw_buffer_store_b128(v, rs, voff + soff, 0, SC1); }
__device__ __forceinline__ v4u ldf(rsrc_t rs, int lane16, int base, int f) { return ldg(rs, lane16 + (f & 3) * KB, base + (f >> 2) * 4 * KB); }
__device__ __forceinline__ int opq_v(int x) { asm volatile("" : "+v"(x)); return x; }
__device__ __forceinline__ int lane_id() { int x; asm volatile("v_mbcnt_lo_u32_b32 %0, -1, 0\n\tv_mbcnt_hi_u32_b32 %0, -1, %0" : "=v"(x)); return x; }
template <unsigned C> __device__ __forceinline__ unsigned vconst() { unsigned x; asm volatile("v_mov_b32 %0, %1" : "=v"(x) : "i"(C)); return x; }
__device__ __forceinline__ int sel(bool c, int a, int b) { return c ? a : b; }
constexpr int OOR = 0x10000000;
__device__ __forceinline__ int opq_s(int x) { asm volatile("" : "+s"(x)); return x; }
#define LOCALS() const int lane = lane_id(), w = opq_s(w0), j = opq_s(j0), g = opq_s(g0); \
  const int lane16 = lane * 16, quad = lane >> 4, l15 = lane & 15, ab = ABASE + j * ASL + layer * ALY, wb = WBASE + g * WSL; \
  (void)lane16; (void)quad; (void)l15; (void)ab; (void)wb
#define OWNROWS(CPW) const int c8 = CPW * w + (lane >> 3); \
  const int piece = ((g >> 1) * 18 + (c8 >> 2)) * KB + ((c8 & 3) * 16 + 8 * (g & 1) + (lane & 7)) * 16; \
  const int mytok = 64 * j + 2 * (16 * ((g >> 1) & 1) + 8 * (g & 1) + (lane & 7)) + (g >> 2)
#define CPF_DONE() KEEP(cpf)
#define GET_TAG() CPF_DONE(); tag = 1 + ((u64)(unsigned)__builtin_amdgcn_readfirstlane((int)(unsigned)tag0) | \
                             ((u64)(unsigned)__builtin_amdgcn_readfirstlane((int)(unsigned)(tag0 >> 32)) << 32))
#define NBASE() opq_s(nb0)
#define QBASE() opq_s(qb0)
#define MBASE() opq_s(mb0)
__device__ __forceinline__ float rows_sum(float x) {
  auto s = __builtin_amdgcn_permlane16_swap(__float_as_uint(x), __float_as_uint(x), false, false);
  x = __uint_as_float(s[0]) + __uint_as_float(s[1]);
  auto t = __builtin_amdgcn_permlane32_swap(__float_as_uint(x), __float_as_uint(x), false, false);
  return __uint_as_float(t[0]) + __uint_as_float(t[1]);
}
__device__ __forceinline__ float vmax(unsigned a, unsigned b) { float r; asm("v_max_f32 %0, %1, %2\n\ts_nop 1" : "=v"(r) : "v"(a), "v"(b)); return r; }
__device__ __forceinline__ void rows_max2(float& a, float& b) {
  auto s = __builtin_amdgcn_permlane16_swap(__float_as_uint(a), __float_as_uint(b), false, false);
  float x = vmax(s[0], s[1]);
  auto t = __builtin_amdgcn_permlane32_swap(__float_as_uint(x), __float_as_uint(x), false, false);
  x = vmax(t[0], t[1]);
  auto u = __builtin_amdgcn_permlane16_swap(__float_as_uint(x), __float_as_uint(x), false, false);
  a = __uint_as_float(u[0]);
  b = __uint_as_float(u[1]);
}
template <int CTRL> __device__ __forceinline__ float dpp(float x) {
  return __int_as_float(__builtin_amdgcn_mov_dpp(__float_as_int(x), CTRL, 0xf, 0xf, true));
}
__device__ __forceinline__ float ror8(float x) { return dpp<0x128>(x); }
__device__ __forceinline__ void add4(f32x4& a, v4u b) {
  float a0 = a[0], a1 = a[1], a2 = a[2], a3 = a[3];
  asm("v_add_f32 %0, %0, %4\n\tv_add_f32 %1, %1, %5\n\tv_add_f32 %2, %2, %6\n\tv_add_f32 %3, %3, %7"
      : "+v"(a0), "+v"(a1), "+v"(a2), "+v"(a3) : "v"(b.x), "v"(b.y), "v"(b.z), "v"(b.w));
  a = f32x4{a0, a1, a2, a3};
}
__device__ __forceinline__ v4u widen(v2u a, v2u b) {
  auto s0 = __builtin_amdgcn_permlane16_swap(a.x, b.x, false, false);
  auto s1 = __builtin_amdgcn_permlane16_swap(a.y, b.y, false, false);
  return v4u{s0[0], s1[0], s0[1], s1[1]};
}
__device__ __forceinline__ v2u gelu4(f32x4 a, float k0, float k1, float is) {
  float s0, s1, s2, s3;
  unsigned u0, u1;
  asm("v_mul_f32 %2, %6, %6\n\tv_mul_f32 %3, %7, %7\n\tv_mul_f32 %4, %8, %8\n\tv_mul_f32 %5, %9, %9\n\t"
      "v_fma_f32 %2, %2, %11, %10\n\tv_fma_f32 %3, %3, %11, %10\n\tv_fma_f32 %4, %4, %11, %10\n\tv_fma_f32 %5, %5, %11, %10\n\t"
      "v_mul_f32 %2, %2, %6\n\tv_mul_f32 %3, %3, %7\n\tv_mul_f32 %4, %4, %8\n\tv_mul_f32 %5, %5, %9\n\t"
      "v_exp_f32 %2, %2\n\tv_exp_f32 %3, %3\n\tv_exp_f32 %4, %4\n\tv_exp_f32 %5, %5\n\t"
      "v_fma_f32 %2, %2, %12, %12\n\tv_fma_f32 %3, %3, %12, %12\n\tv_fma_f32 %4, %4, %12, %12\n\tv_fma_f32 %5, %5, %12, %12\n\t"
      "v_rcp_f32 %2, %2\n\tv_rcp_f32 %3, %3\n\tv_rcp_f32 %4, %4\n\tv_rcp_f32 %5, %5\n\t"
      "v_mul_f32 %2, %2, %6\n\tv_mul_f32 %3, %3, %7\n\tv_mul_f32 %4, %4, %8\n\tv_mul_f32 %5, %5, %9\n\t"
      "v_cvt_pk_bf16_f32 %0, %2, %3\n\tv_cvt_pk_bf16_f32 %1, %4, %5"
      : "=&v"(u0), "=&v"(u1), "=&v"(s0), "=&v"(s1), "=&v"(s2), "=&v"(s3)
      : "v"(a[0]), "v"(a[1]), "v"(a[2]), "v"(a[3]), "v"(k0), "v"(k1), "v"(is));
  return v2u{u0, u1};
}
__device__ __forceinline__ void shift4(f32x4& a, f32x4 cs, f32x4 b, float nmean, float is) {
  float a0 = a[0], a1 = a[1], a2 = a[2], a3 = a[3];
  asm("v_fmac_f32 %0, %12, %4\n\tv_fmac_f32 %1, %12, %5\n\tv_fmac_f32 %2, %12, %6\n\tv_fmac_f32 %3, %12, %7\n\t"
      "v_fmac_f32 %0, %13, %8\n\tv_fmac_f32 %1, %13, %9\n\tv_fmac_f32 %2, %13, %10\n\tv_fmac_f32 %3, %13, %11"
      : "+v"(a0), "+v"(a1), "+v"(a2), "+v"(a3)
      : "v"(cs[0]), "v"(cs[1]), "v"(cs[2]), "v"(cs[3]), "v"(b[0]), "v"(b[1]), "v"(b[2]), "v"(b[3]), "v"(nmean), "v"(is));
  a = f32x4{a0, a1, a2, a3};
}
__device__ __forceinline__ int cnt_get(LAS unsigned char* L, int i) { return ((volatile LAS int*)(L + L_HC))[i]; }
__device__ __forceinline__ void cnt_wait(LAS unsigned char* L, int i, int n) {
  while (__builtin_amdgcn_readfirstlane(cnt_get(L, i)) < n) __builtin_amdgcn_s_sleep(1);
  asm volatile("" ::: "memory");
}
__device__ __forceinline__ void cnt_add(LAS unsigned char* L, int i, int lane) {
  asm volatile("s_waitcnt lgkmcnt(0)" ::: "memory");
  if (lane == 0) __hip_atomic_fetch_add((LAS int*)(L + L_HC) + i, 1, __ATOMIC_RELAXED, __HIP_MEMORY_SCOPE_WORKGROUP);
}
template <int NP> __device__ __forceinline__ void row_table1(LAS unsigned char* L, int t5, int lane) {
  const int r = t5 * 16 + (lane & 15), q = lane >> 4;
  float t1 = 0.f, t2 = 0.f;
#pragma unroll
  for (int i = 0; i < (NP + 3) / 4; ++i)
    if (NP % 4 == 0 || q < NP) { const f32x2 v = *(LAS f32x2*)(L + L_ST + ((i * 4 + q) * 80 + r) * 8); t1 += v.x; OPAQUE(t1); t2 += v.y; }
  t1 = rows_sum(t1); OPAQUE(t1); t2 = rows_sum(t2);
  const float nmean = t1 * (-1.0f / 576.0f);
  const float var = fmaxf(__builtin_fmaf(-nmean, nmean, t2 * (1.0f / 576.0f)), 0.f);
  const float rstd = __builtin_amdgcn_rsqf(var + EPS);
  if (q < 2) *(LAS float*)(L + L_RT + q * 320 + r * 4) = q ? nmean * rstd : rstd;
  cnt_add(L, C_R1, lane);
}
__device__ __forceinline__ v2u ln_pack(f32x4 a, f32x4 r, f32x4 c, f32x4 nm) {
  float t0, t1, t2, t3;
  unsigned u0, u1;
  asm("v_mul_f32 %2, %14, %18\n\tv_mul_f32 %3, %15, %19\n\tv_mul_f32 %4, %16, %20\n\tv_mul_f32 %5, %17, %21\n\t"
      "v_fmac_f32 %2, %6, %10\n\tv_fmac_f32 %3, %7, %11\n\tv_fmac_f32 %4, %8, %12\n\tv_fmac_f32 %5, %9, %13\n\t"
      "v_cvt_pk_bf16_f32 %0, %2, %3\n\tv_cvt_pk_bf16_f32 %1, %4, %5"
      : "=&v"(u0), "=&v"(u1), "=&v"(t0), "=&v"(t1), "=&v"(t2), "=&v"(t3)
      : "v"(a[0]), "v"(a[1]), "v"(a[2]), "v"(a[3]), "v"(r[0]), "v"(r[1]), "v"(r[2]), "v"(r[3]), "v"(c[0]), "v"(c[1]), "v"(c[2]), "v"(c[3]),
        "v"(nm[0]), "v"(nm[1]), "v"(nm[2]), "v"(nm[3]));
  return v2u{u0, u1};
}
__device__ __forceinline__ f32x4 zero4() {
  float a, b, c, d;
  asm volatile("v_mov_b32 %0, 0\n\tv_mov_b32 %1, 0\n\tv_mov_b32 %2, 0\n\tv_mov_b32 %3, 0" : "=v"(a), "=v"(b), "=v"(c), "=v"(d));
  return f32x4{a, b, c, d};
}
__device__ __forceinline__ f32x4 splat(float x) { return f32x4{x, x, x, x}; }
__device__ __forceinline__ void row_table(LAS unsigned char* L, int lane) {
  cnt_wait(L, C_ST, 8);
  const f32x4 q = *(LAS f32x4*)(L + L_ST + lane * 16);
  float t1 = q[0] + q[2];
  OPAQUE(t1);
  float nmean = t1 * (-1.0f / 576.0f);
  const float var = fmaxf(__builtin_fmaf(-nmean, nmean, (q[1] + q[3]) * (1.0f / 576.0f)), 0.f);
  const float r1 = __builtin_amdgcn_rsqf(var + EPS);
  const float sc = r1 * __builtin_amdgcn_rsqf(var * r1 * r1 + EPS);
  float k0 = GC0 * sc;
  OPAQUE(k0);
  *(LAS f32x4*)(L + L_ST + KB + lane * 16) = f32x4{nmean, __builtin_amdgcn_rcpf(sc), k0, k0 * (0.044715f * sc * sc)};
  cnt_add(L, C_RT, lane);
}
__device__ __forceinline__ void shift_acc(f32x4 (&acc)[3][4], LAS unsigned char* L, int w, int quad, int l15) {
  f32x2 rt[4];
#pragma unroll
  for (int m = 0; m < 4; ++m) rt[m] = *(LAS f32x2*)(L + L_ST + KB + (m * 16 + l15) * 16);
#pragma unroll
  for (int t = 0; t < 3; ++t) {
    const f32x4 cs = *(LAS f32x4*)(L + L_T1 + (48 * w + 16 * t + 4 * quad) * 4), bb = *(LAS f32x4*)(L + L_T1 + 2304 + (48 * w + 16 * t + 4 * quad) * 4);
#pragma unroll
    for (int m = 0; m < 4; ++m) shift4(acc[t][m], cs, bb, rt[m][0], rt[m][1]);
  }
}
template <int SLEEP = 1> __device__ __forceinline__ void wait_flags(const unsigned char* ws, int off, bool active, u64 tag) {
  const GAS u64* f = (const GAS u64*)(ws + off);
  asm volatile("" : "+s"(tag));
#pragma unroll 1
  for (int spins = 0; spins < (1 << 16); ++spins) {
    const u64 v = active ? __hip_atomic_load(f, __ATOMIC_RELAXED, __HIP_MEMORY_SCOPE_AGENT) : tag;
    if (__all(v == tag)) break;
    __builtin_amdgcn_s_sleep(SLEEP);
  }
  asm volatile("" ::: "memory");
}
__device__ __forceinline__ void set_flag(unsigned char* ws, int off, u64 tag) {
  asm volatile("" : "+s"(tag));
  __hip_atomic_store((GAS u64*)(ws + off), tag, __ATOMIC_RELAXED, __HIP_MEMORY_SCOPE_AGENT);
}
constexpr unsigned POISON = 0xffffffffu;
__device__ __forceinline__ unsigned umax4(unsigned m, v4u v) { return max(max(max(m, v.x), v.y), max(v.z, v.w)); }
#define RETRY_BEGIN() _Pragma("unroll 1") for (int spins_ = 0; spins_ < (1 << 15); ++spins_) { unsigned seen_ = 0
#define RETRY_END() if (!__any(seen_ == POISON)) break; RETRY_COUNT(); __builtin_amdgcn_s_sleep(2); asm volatile("" ::: "memory"); }
template <int LO, int HI> __device__ __forceinline__ void issue_window(v4u (&win)[WIN], rsrc_t rs, int lane16, int base) {
#pragma unroll
  for (int i = LO; i < HI; ++i) win[i] = ldf(rs, lane16, base, i);
  SB();
}

template <int NT, int MT, int SWAP, int NNEXT, int R, int LO, int HI>
__device__ __forceinline__ void gemm18_steps(f32x4 (&acc)[NT][MT], v4u (&win)[WIN], v4u (&act)[R], LAS unsigned char* img, LAS unsigned char* img2, rsrc_t rs,
                                             int lane16, int wbase, int nvo, int nbase) {
#pragma unroll
  for (int n = LO; n < HI; ++n) {
    const int s = n / MT, m = n % MT, nn = n + R - 1;
    if (n < 18 * MT) {
#pragma unroll
      for (int t = 0; t < NT; ++t) {
        if (s == 0) {
          if ((SWAP >> t) & 1) MF0(acc[t][m], act[n % R], win[t]);
          else MF0(acc[t][m], win[t], act[n % R]);
        } else if ((SWAP >> t) & 1) MF(acc[t][m], act[n % R], win[(s * NT + t) % WIN]);
        else MF(acc[t][m], win[(s * NT + t) % WIN], act[n % R]);
      }
      SB();
    }
    if (nn < 18 * MT) act[nn % R] = *(LAS v4u*)(nn % MT < 3 ? img + ((nn % MT) * 18 + nn / MT) * KB : img2 + ((nn % MT - 3) * 18 + nn / MT) * KB);
    if (m == 0 && s > 0) {
#pragma unroll
      for (int t = 0; t < NT; ++t) {
        const int f = (s - 1) * NT + t;
        if (f + WIN < 18 * NT) win[f % WIN] = ldf(rs, lane16, wbase, f + WIN);
        else if (f % WIN < NNEXT) win[f % WIN] = ldf(rs, nvo, nbase, f % WIN);
      }
    }
    SB();
    if (n < 18 * MT) {
      KEEP(act[n % R]);
#pragma unroll
      for (int t = 0; t < NT; ++t) KEEP(win[(s * NT + t) % WIN]);
      SB();
    }
  }
}
template <int NT, int MT, int SWAP, int NNEXT, int R>
__device__ __forceinline__ void gemm18(f32x4 (&acc)[NT][MT], v4u (&win)[WIN], LAS unsigned char* img, rsrc_t rs, int lane16, int wbase, int nbase) {
  v4u act[R];
  LAS unsigned char* img2 = img + 54 * KB;
  if (MT > 3) asm volatile("" : "+v"(img2));
#pragma unroll
  for (int n = 0; n < R - 1; ++n) { act[n] = *(LAS v4u*)(n % MT < 3 ? img + ((n % MT) * 18 + n / MT) * KB : img2 + ((n % MT - 3) * 18 + n / MT) * KB); SB(); }
  gemm18_steps<NT, MT, SWAP, NNEXT, R, 0, 6 * MT>(acc, win, act, img, img2, rs, lane16, wbase, lane16, nbase);
  gemm18_steps<NT, MT, SWAP, NNEXT, R, 6 * MT, 12 * MT>(acc, win, act, img, img2, rs, lane16, wbase, lane16, nbase);
  gemm18_steps<NT, MT, SWAP, NNEXT, R, 12 * MT, 18 * MT + 1>(acc, win, act, img, img2, rs, lane16, wbase, lane16, nbase);
  ACC_OUT(acc);
}
template <int NT, int MT, int SWAP, int R, int tstep, int NNEXT>
__device__ __forceinline__ void qkv_tiles(v4u (&win)[WIN], LAS unsigned char* L, rsrc_t rs, int lane, int wbase, int nbase, int tile0, int rwait STAMP_ARG) {
  f32x4 acc[NT][MT];
  gemm18<NT, MT, SWAP, NNEXT, R>(acc, win, L + lane * 16, rs, lane * 16, wbase, nbase);
  STAMP(38);
  LAS unsigned char *Rq = L + L_RT + (lane & 48), *Rl = L + L_RT + (lane & 15) * 4, *Tq = L + L_TQ + tile0 * 64 + (lane & 48),
                    *Tl = L + L_TQ + tile0 * 64 + (lane & 15) * 4, *X = L + L_QK + lane * 8 + tile0 * 2560;
  asm volatile("" : "+v"(Rq), "+v"(Rl), "+v"(Tq), "+v"(Tl), "+v"(X));
  cnt_wait(L, C_R1, rwait);
  v2u u[NT][MT];
#pragma unroll
  for (int t = 0; t < NT; ++t) {
    if ((SWAP >> t) & 1) {
      const f32x4 cs = splat(*(LAS float*)(Tl + t * tstep * 64));
#pragma unroll
      for (int m = 0; m < MT; ++m) u[t][m] = ln_pack(acc[t][m], *(LAS f32x4*)(Rq + m * 64), cs, *(LAS f32x4*)(Rq + 320 + m * 64));
    } else {
      const f32x4 cs = *(LAS f32x4*)(Tq + t * tstep * 64);
#pragma unroll
      for (int m = 0; m < MT; ++m) u[t][m] = ln_pack(acc[t][m], splat(*(LAS float*)(Rl + m * 64)), cs, splat(*(LAS float*)(Rl + 320 + m * 64)));
    }
  }
#pragma unroll
  for (int t = 0; t < NT; ++t)
#pragma unroll
    for (int m = 0; m < MT; ++m) *(LAS v2u*)(X + (t * tstep * 5 + m) * 512) = u[t][m];
}
template <int C>
__device__ __forceinline__ void mlp_chunk(f32x4 (&acc)[3][4], v4u (&win)[WIN], v4u (&act)[8], LAS unsigned char* img, rsrc_t rs, int lane16, int rb,
                                          LAS unsigned char* L) {
  int hv = 4;
#pragma unroll
  for (int s = 0; s < 6; ++s) {
    if (C != 2 && s == 5) {
      if (__builtin_amdgcn_readfirstlane(hv) < 4) cnt_wait(L, C < 0 ? C_XA + 1 : C_H0 + C + 1, 4);
      asm volatile("" ::: "memory");
      SB();
    }
    if (C == 2 && s == 6 - DST) {
      if (lane16 == 0) __hip_atomic_fetch_add((LAS int*)(L + L_HC) + C_LC, 1, __ATOMIC_RELAXED, __HIP_MEMORY_SCOPE_WORKGROUP);
      SB();
    }
    asm volatile("" :: "v"(act[(s & 1) * 4]), "v"(act[(s & 1) * 4 + 1]), "v"(act[(s & 1) * 4 + 2]), "v"(act[(s & 1) * 4 + 3]), "v"(win[s * 3]),
                 "v"(win[s * 3 + 1]), "v"(win[s * 3 + 2]));
    SB();
#pragma unroll
    for (int t = 0; t < 3; ++t)
#pragma unroll
      for (int m = 0; m < 4; ++m) {
        const int mm = t == 1 ? 3 - m : m;
        if (C == 0 && s == 0) MF0(acc[t][mm], win[t], act[mm]);
        else MF(acc[t][mm], win[s * 3 + t], act[(s & 1) * 4 + mm]);
        SB();
        if (t == 0 && !(C == 2 && s == 5)) act[((s + 1) & 1) * 4 + m] = *(LAS v4u*)(img + (m * 18 + s + 1) * KB);
        if (C != 2 && s == 3 && t == 2 && m == 0) hv = cnt_get(L, C < 0 ? C_XA + 1 : C_H0 + C + 1);
        if (t == 1 && m < 3 && !(C == 2 && s + DST >= 6))
          win[((s + DST) % 6) * 3 + m] = ldf(rs, lane16, rb, (s + DST) * 3 + m);
        SB();
      }
  }
}
__device__ __forceinline__ void gemm_up(f32x4 (&acc)[3][4], v4u (&win)[WIN], LAS unsigned char* img, rsrc_t rs, int lane16, int rb, LAS unsigned char* L) {
  v4u act[8];
  cnt_wait(L, C_XA, 4);
#pragma unroll
  for (int n = 0; n < 4; ++n) { act[n] = *(LAS v4u*)(img + n * 18 * KB); SB(); }
  ACC_IN(acc);
#pragma unroll 1
  for (int c = 0; c < 3; ++c) {
    mlp_chunk<-1>(acc, win, act, img, rs, lane16, rb, L);
    img += 6 * KB;
    rb += 18 * KB;
  }
  ACC_OUT(acc);
}
__device__ __forceinline__ void gemm_down(f32x4 (&acc)[3][4], v4u (&win)[WIN], LAS unsigned char* img, rsrc_t rs, int lane16, int rb, LAS unsigned char* L STAMP_ARG) {
  v4u act[8];
  cnt_wait(L, C_H0, 4);
  STAMP(46);
#pragma unroll
  for (int n = 0; n < 4; ++n) { act[n] = *(LAS v4u*)(img + n * 18 * KB); SB(); }
  mlp_chunk<0>(acc, win, act, img, rs, lane16, rb, L);
  img += 6 * KB;
  asm volatile("" : "+v"(img));
  STAMP(47);
  mlp_chunk<1>(acc, win, act, img, rs, lane16, rb + 18 * KB, L);
  img += 6 * KB;
  asm volatile("" : "+v"(img));
  STAMP(48);
  mlp_chunk<2>(acc, win, act, img, rs, lane16, rb + 36 * KB, L);
  ACC_OUT(acc);
}

__global__ void __launch_bounds__(NTHR, 3) fused_fwd(Params p) {
  extern __shared__ __attribute__((aligned(16))) unsigned char smem[];
  LAS unsigned char* L = (LAS unsigned char*)smem;
  const int w0 = __builtin_amdgcn_readfirstlane(threadIdx.x >> 6);
  const int bid = blockIdx.x;
  u64 pc0;
  int clen;
  asm volatile("s_getpc_b64 %0\n.Lfwd_begin:\n\ts_mov_b32 %1, .Lfwd_end-.Lfwd_begin" : "=s"(pc0), "=s"(clen));
  unsigned cpf = 0;
  const int coff = (((blockIdx.x >> 3) % 11) * 64 + (threadIdx.x & 63)) * 64;
  if (w0 == 11 && coff < clen) cpf = *(const GAS unsigned*)(pc0 + coff);
  constexpr unsigned MPX = 8 / XB;
  const int j0 = ((blockIdx.x & 7) / XB) * (32 / MPX) + (blockIdx.x >> 3) / MPX;
  const int g0 = ((blockIdx.x & 7) % XB) * MPX + (blockIdx.x >> 3) % MPX;
  const rsrc_t rs = __builtin_amdgcn_make_buffer_rsrc((void*)p.ws, 0, WS_BYTES, 0x00020000);
  const int mb0 = WBASE + g0 * WSL + (252 + w0 * 108) * KB;
  const int nb0 = sel(w0 < 9, WOB + ((g0 & 3) * 9 + w0) * 18 * KB, mb0);
  const int qb0 = sel(w0 < 8, WBASE + g0 * WSL + sel(w0 < 6, w0 * 36, 108 + w0 * 18) * KB, nb0);
  STAMP_INIT();
  STAMP(0);
  const u64 tag0 = __hip_atomic_load((const GAS u64*)p.ws, __ATOMIC_RELAXED, __HIP_MEMORY_SCOPE_AGENT);
  u64 tag = 0;
  v4u win[WIN];
  {
    constexpr int layer = 0;
    LOCALS();
    const rsrc_t rx = __builtin_amdgcn_make_buffer_rsrc((void*)p.x, 0, 2048 * 576 * 2, 0x00020000);
    const int xrow = lane >> 2;
#define LOAD_X() v4u xr[9]; _Pragma("unroll") for (int i = 0; i < 9; ++i) xr[i] = __builtin_amdgcn_raw_buffer_load_b128(rx, xoff + i * 64, 0, 0); SB()
#define STAGE_X() do { float s1 = 0.f, s2 = 0.f; \
      _Pragma("unroll") for (int i = 0; i < 9; ++i) { \
        if (w < 10) *(LAS v4u*)(L + (w * 9 + i) * KB + (lane & 3) * 256 + xrow * 16) = xr[i]; \
        _Pragma("unroll") for (int e = 0; e < 4; ++e) { s1 = dot2(xr[i][e], ONES, s1); s2 = dot2(xr[i][e], xr[i][e], s2); } } \
      s1 += dpp<0xB1>(s1); s2 += dpp<0xB1>(s2); s1 += dpp<0x4E>(s1); s2 += dpp<0x4E>(s2); \
      if (w < 10 && (lane & 3) == 0) *(LAS f32x2*)(L + L_ST + ((w & 1) * 80 + (w >> 1) * 16 + xrow) * 8) = f32x2{s1, s2}; } while (0)
    if (opq_s(w) < 8) {
      LAS unsigned char* T = L + L_QK + w * 3072;
      const int uf = 8 * j + w, uc = sel(uf < 252, uf, 0), us = (uc * 293) >> 12, tp = uc - us * 14;
      const int tile = sel(tp < 4, 10 + tp, sel(tp == 4, 8, sel(tp == 13, 9, tp - 5)));
      const bool vx = tile == 9, ex = (tile & 14) == 8;
      const int c0 = 72 * g + sel(ex, 16, ((tile + sel(tile >= 10, 2, 0)) & 3) * 18) + sel(tile < 4, 576, sel(tile < 8 || vx, 1152, 0));
      const rsrc_t rq = __builtin_amdgcn_make_buffer_rsrc((void*)p.wqkv, 0, 576 * 1728 * 2, 0x00020000);
      const int qo = (lane >> 1) * 3456 + (lane & 1) * sel(ex, sel(vx, 0, 72), 16), qs = us * 32 * 3456 + c0 * 2;
      v4u qv;
      if (ex) {
        const int cA = sel(vx, 36, 1152), cB = sel(vx, 72, 36);
        qv = v4u{__builtin_amdgcn_raw_buffer_load_b32(rq, qo, qs, 0), __builtin_amdgcn_raw_buffer_load_b32(rq, qo, qs + cA, 0),
                 __builtin_amdgcn_raw_buffer_load_b32(rq, qo, qs + cB, 0), __builtin_amdgcn_raw_buffer_load_b32(rq, qo, qs + cA + cB, 0)};
      } else qv = __builtin_amdgcn_raw_buffer_load_b128(rq, qo, qs, 0);
      SB();
      const int of = 3 * j + w, oc = (g >> 2) * 81 + of, os = (oc * 57) >> 9, ot = oc - os * 9;
      const bool hasO = w < 3 && of < 81;
      const bool ox = os >= 16;
      const int orow = quad * sel(ox, 72, 4) + ((lane >> 1) & 1) + ((lane >> 2) & 1) * sel(ox, 18, 2) + ((lane >> 3) & 1) * sel(ox, 36, 18);
      const rsrc_t ro = __builtin_amdgcn_make_buffer_rsrc((void*)p.wo, 0, 576 * 576 * 2, 0x00020000);
      const v4u ov = __builtin_amdgcn_raw_buffer_load_b128(ro, orow * 1152 + (lane & 1) * 16 + sel(hasO, 0, OOR),
                                                           sel(ox, 288 * (os - 16) + 16, 36 * os) * 1152 + 32 * ((g & 3) * 9 + ot), 0);
      SB();
      const int xoff = (64 * j + 32 * ((w >> 1) & 1) + (w >> 2) + 2 * xrow) * 1152 + (w & 1) * 576 + (lane & 3) * 16;
      __builtin_amdgcn_s_barrier();
      LOAD_X();
      STAMP(41);
      *(LAS v4u*)(T + (lane >> 1) * 48 + (lane & 1) * 16) = (vx && (lane & 1)) ? v4u{0u, 0u, 0u, 0u} : qv;
      *(LAS v4u*)(T + 1536 + (lane >> 1) * 48 + (lane & 1) * 16) = ov;
      LAS unsigned char* R = T + (8 * quad + (l15 >> 2)) * 48 + (lane & 3) * 8;
      const v2u a = __builtin_bit_cast(v2u, __builtin_amdgcn_ds_read_tr16_b64_v4i16((LAS s16x4*)(R)));
      const v2u b = __builtin_bit_cast(v2u, __builtin_amdgcn_ds_read_tr16_b64_v4i16((LAS s16x4*)(R + 4 * 48)));
      const v2u c = __builtin_bit_cast(v2u, __builtin_amdgcn_ds_read_tr16_b64_v4i16((LAS s16x4*)(R + 1536)));
      const v2u d = __builtin_bit_cast(v2u, __builtin_amdgcn_ds_read_tr16_b64_v4i16((LAS s16x4*)(R + 1536 + 4 * 48)));
      stg(v4u{a.x, a.y, b.x, b.y}, rs, lane16,
          sel(uf >= 252, OOR, wb + sel(tile < 8, (tile & 3) * 36 + us * 2 + (tile >> 2), sel(tile < 10, 72 + tile * 18 + us, (tile >> 1) * 36 - 36 + us * 2 + (tile & 1))) * KB));
      stg(v4u{c.x, c.y, d.x, d.y}, rs, lane16, sel(hasO, WOB + ((g & 3) * 162 + ot * 18 + os) * KB, OOR));
      const float cq = rows_sum(dot2(a.x, ONES, dot2(a.y, ONES, dot2(b.x, ONES, dot2(b.y, ONES, 0.f)))));
      __builtin_amdgcn_raw_buffer_store_b32(__float_as_uint(cq), rs, sel(lane < 16, lane * 4, OOR) + sel(uf < 252, CSQ + ((g * 18 + us) * 14 + tile) * 64, OOR), 0, SC1);
      SB();
      STAMP(43);
      DRAIN();
      STAMP(44);
      GET_TAG();
      if (lane == 0 && __hip_atomic_fetch_add((LAS int*)(L + L_HC) + C_P0, 1, __ATOMIC_RELAXED, __HIP_MEMORY_SCOPE_WORKGROUP) == 7)
        set_flag(p.ws, SFLAGS + (g * 32 + j) * 8, tag);
      STAGE_X();
      STAMP(2);
      BAR();
      STAMP(3);
      issue_window<0, WIN>(win, rs, lane16, QBASE());
    }
    if (opq_s(w) >= 8) {
      LAS unsigned char* T = L + 125 * KB + (w - 8) * 4608;
      const int item = sel(w < 11, 3 * j + w - 8, sel((j & 7) < 3, 96 + 3 * (j >> 3) + (j & 7), 127)), rbk = (item * 57) >> 9, cb = item - rbk * 9, kind = sel(rbk >= 6, 1, 0), k3 = rbk - kind * 6;
      const int rn = sel(kind, 4, 8), ld = sel(kind, 1152, 9216);
      const rsrc_t rw = __builtin_amdgcn_make_buffer_rsrc((void*)(kind == 0 ? p.w1 : p.w2), 0, 576 * 4608 * 2, 0x00020000);
      const int jj = lane >> 3;
      const int woff = (jj & 3) * ld + (lane & 7) * 16 + sel(item < 108, 96 * k3 * ld + g * sel(kind, 576 * 1152, 1152) + 128 * cb, OOR);
      const int xoff = (sel(j > 0, 64 * j - 16, 0) + 2 * (xrow & 7) + (xrow >> 3)) * 1152 + (lane & 3) * 16 + sel(w < 10, (w & 1) * 576, OOR);
      if (w == 11 && lane < 16) ((volatile LAS int*)(L + L_HC))[lane] = 0;
      BAR();
      LOAD_X();
      v4u wv[3][4];
#pragma unroll
      for (int n = 0; n < 4; ++n)
#pragma unroll
        for (int c = 0; c < 3; ++c)
          wv[c][n] = __builtin_amdgcn_raw_buffer_load_b128(rw, woff + (jj >> 2) * (sel(kind, c < 2 ? 16 : 48, 4) * ld),
                                                           (n * rn + sel(kind, c < 2 ? 48 * c : 32, 32 * c)) * ld, NTW ? 2 : 0);
      SB();
      STAMP(41);
      GET_TAG();
      STAGE_X();
      float cs[4] = {0.f, 0.f, 0.f, 0.f};
      LAS unsigned char* R = T + (8 * quad + (l15 >> 2)) * 144 + (lane & 3) * 8;
#pragma unroll
      for (int c = 0; c < 3; ++c) {
#pragma unroll
        for (int n = 0; n < 4; ++n) *(LAS v4u*)(T + (n * 8 + (lane >> 3)) * 144 + (lane & 7) * 16) = wv[c][n];
#pragma unroll
        for (int tt = 0; tt < 4; ++tt) {
          const v2u a = __builtin_bit_cast(v2u, __builtin_amdgcn_ds_read_tr16_b64_v4i16((LAS s16x4*)(R + tt * 32)));
          const v2u b = __builtin_bit_cast(v2u, __builtin_amdgcn_ds_read_tr16_b64_v4i16((LAS s16x4*)(R + tt * 32 + 4 * 144)));
          wv[c][tt] = v4u{a.x, a.y, b.x, b.y};
          cs[tt] = dot2(a.x, ONES, dot2(a.y, ONES, dot2(b.x, ONES, dot2(b.y, ONES, cs[tt]))));
        }
      }
#pragma unroll
      for (int tt = 0; tt < 4; ++tt) { cs[tt] = rows_sum(cs[tt]); OPAQUE(cs[tt]); }
      float csum = quad == 0 ? cs[0] : quad == 1 ? cs[1] : quad == 2 ? cs[2] : cs[3];
      asm volatile("" : "+v"(csum));
      if (opq_s(w) == 11) {
        cnt_wait(L, C_P0, 8);
        wait_flags<4>(p.ws, SFLAGS + ((g ^ ((lane >> 3) & 4)) * 32 + (lane & 31)) * 8, true, tag);
#pragma unroll
        for (int i = 0; i < 18; ++i) win[i] = ldg(rs, lane16, CSQ + (g * 18 + i) * 896);
      }
      STAMP(2);
      BAR();
      STAMP(3);
      row_table1<2>(L, w - 8, lane);
      if (opq_s(w) == 11) {
        f32x4 cs = __builtin_bit_cast(f32x4, win[0]);
#pragma unroll
        for (int i = 1; i < 18; ++i) add4(cs, win[i]);
        if (lane < 56) *(LAS f32x4*)(L + L_TQ + lane16) = cs;
        row_table1<2>(L, 4, lane);
      }
#pragma unroll
      for (int i = 0; i < 5; ++i) {
        const int n = (w - 8) * 5 + i;
        stg(v4u{POISON, POISON, POISON, POISON}, rs, lane16, sel(n < 18, ab + sel(n < 9, A_O, A_X1 - 9 * KB) + (g * 9 + n) * KB, OOR));
      }
      __builtin_amdgcn_s_sleep(BDELAY);
      DRAIN();
      if (lane == 0 && __hip_atomic_fetch_add((LAS int*)(L + L_HC) + C_P0, 1, __ATOMIC_RELAXED, __HIP_MEMORY_SCOPE_WORKGROUP) == 11)
        set_flag(p.ws, FLAGS + j * 128 + g * 8, tag);
      STAMP(45);
      const int dst0 = sel(item < 108, wb + (252 + 54 * kind + 9 * k3) * KB, OOR);
#pragma unroll
      for (int tt = 0; tt < 4; ++tt)
#pragma unroll
        for (int c = 0; c < 3; ++c) stg(wv[c][tt], rs, lane16 + c * 3 * KB, dst0 + ((((4 * cb + tt) * 43) >> 7) * 105 + 4 * cb + tt) * KB);
      __builtin_amdgcn_raw_buffer_store_b32(__float_as_uint(csum), rs,
                                            lane * 4 + sel(kind == 0, CS1 + (k3 * 4608 + 576 * g + 64 * cb) * 4, OOR), 0, SC1);
      SB();
      if (opq_s(w) == 8) issue_window<0, WIN>(win, rs, lane16, QBASE());
    }
  }
  int os0[3];
  const int wq = opq_s(w0);
  {
    const int T = 2 * (g0 >> 2) + sel(wq >= 6, 1, 0), s0 = 3 * sel(wq >= 6, wq - 6, wq);
#pragma unroll
    for (int i = 0; i < 3; ++i) {
      const int s = s0 + i;
      os0[i] = ABASE + j0 * ASL + A_O + sel(s < 16, ((s >> 1) * 9 + T * 2 + (s & 1)) * KB, ((s - 16) * 36 + 8) * KB + T * 256);
    }
  }
  const int oq0 = sel(wq == 5 || wq == 11, 9 * KB, 256);
  const int ft0 = (g0 & 3) * 9 + sel(wq < 9, wq, 0), xs0 = ((g0 >> 2) * 36 + (ft0 >> 1)) * KB + (ft0 & 1) * 512;

#pragma unroll 1
  for (int layer = 0; layer < 2; ++layer) {
    v4u xres;
    {
      LOCALS();
      STAMP(4 + layer * 16);
      {
        const int qbase = QBASE(), nbase = NBASE(), rw = 5 + 5 * layer;
        if (opq_s(w) < 4) qkv_tiles<2, 5, 2, 7, 4, WIN>(win, L, rs, lane, qbase, nbase, w, rw STAMP_PASS);
        if ((opq_s(w) & 14) == 4) qkv_tiles<2, 4, 0, 7, 1, 0>(win, L, rs, lane, qbase, nbase, 2 * w + 2, rw STAMP_PASS);
        if (opq_s(w) == 6) qkv_tiles<1, 5, 0, 10, 0, WIN>(win, L, rs, lane, qbase, nbase, 8, rw STAMP_PASS);
        if (opq_s(w) == 7) qkv_tiles<1, 5, 1, 10, 0, WIN>(win, L, rs, lane, qbase, nbase, 9, rw STAMP_PASS);
      }
      if (layer == 1 && bid == 0 && w == 10) {
#pragma unroll 1
        for (int i = 0; i < 4; ++i) wait_flags(p.ws, SFLAGS + (256 + i * 64 + lane) * 8, true, tag);
        if (lane == 0) set_flag(p.ws, 0, tag);
      }
      STAMP(5 + layer * 16);
      xres = *(LAS v4u*)(L + opq_s(xs0) + (quad & 1) * 18 * KB + (quad >> 1) * 256 + l15 * 16);
      if (layer == 0 && w >= 8) DRAIN();
      if ((opq_s(w) & 12) == 4) {
        BAR();
        if (layer == 0 && w == 6 && lane == 0) set_flag(p.ws, SFLAGS + (256 + g * 32 + j) * 8, tag);
        if (w == 7 && layer == 0) {
          wait_flags(p.ws, FLAGS + j * 128 + (lane & 7) * 8, lane < 8, tag);
        }
        if (w < 6) issue_window<0, WIN>(win, rs, lane16, NBASE());
      }
      if ((opq_s(w) & 12) != 4) {
        const int T = w & 3, hp = w >> 3, Tp = (T & 1) ? T - 1 : 4;
        f32x4 mp, ms;
        const float neg = __uint_as_float(vconst<0xf149f2cau>());
        const int lo = l15 + ((T & 1) ? 9 : 1 + 4 * T), hi = (T & 1) ? 16 : j > 0 ? 8 + 4 * T : 0;
#pragma unroll
        for (int r = 0; r < 4; ++r) {
          const int jj = 4 * quad + r;
          mp[r] = ((jj >= lo) & (jj < hi)) ? 0.f : neg;
          ms[r] = (unsigned)(l15 - jj) <= 7u ? 0.f : neg;
        }
        int xs = L_QK + lane * 8 + T * 512, xp = L_QK + lane * 8 + Tp * 512, hs = xs + hp * 5120, hq = xp + hp * 5120;
        int om = ab + A_O + (g * 9 + T * 2 + hp) * KB + lane16;
        int oe = quad == hp ? ab + A_O + (g * 9 + 8) * KB + T * 256 + hp * 8 + l15 * 16 : WS_BYTES;
        unsigned qm0 = quad == 2 * hp ? ~0u : 0u, qm1 = quad == 2 * hp + 1 ? ~0u : 0u, zr = vconst<0>();
        v4u one4 = {vconst<ONES>(), vconst<ONES>(), vconst<ONES>(), vconst<ONES>()};
        asm volatile("" : "+v"(mp), "+v"(ms), "+v"(xs), "+v"(xp), "+v"(hs), "+v"(hq), "+v"(om), "+v"(oe), "+v"(qm0), "+v"(qm1), "+v"(zr), "+v"(one4));
        BAR();
        const v2u kxs = *(LAS v2u*)(L + xs + 40 * 512);
        const unsigned kxp = *(LAS unsigned*)(L + xp + 40 * 512 + 4);
        v2u q[2], ks[2], kp[2], vs[2], vp[2];
#pragma unroll
        for (int hh = 0; hh < 2; ++hh) {
          q[hh] = *(LAS v2u*)(L + hs + (50 + 5 * hh) * 512);
          kp[hh] = *(LAS v2u*)(L + hq + 5 * hh * 512);
          ks[hh] = *(LAS v2u*)(L + hs + 5 * hh * 512);
        }
        SB();
#pragma unroll
        for (int hh = 0; hh < 2; ++hh) {
          vp[hh] = *(LAS v2u*)(L + hq + (20 + 5 * hh) * 512);
          vs[hh] = *(LAS v2u*)(L + hs + (20 + 5 * hh) * 512);
        }
        const v2u vxp = *(LAS v2u*)(L + xp + 45 * 512), vxs = *(LAS v2u*)(L + xs + 45 * 512);
        SB();
        const f32x4 z = {0.f, 0.f, 0.f, 0.f};
        f32x4 sp[2], ss[2], o[2], ox[2], sm[2];
        float mx[2];
#pragma unroll
        for (int hh = 0; hh < 2; ++hh) {
          const v4u qf = {q[hh].x, q[hh].y, kxs.x & (hh ? qm1 : qm0), zr};
          sp[hh] = mfma(v4u{kp[hh].x, kp[hh].y, kxp, zr}, qf, mp);
          ss[hh] = mfma(v4u{ks[hh].x, ks[hh].y, kxs.y, zr}, qf, ms);
        }
#pragma unroll
        for (int hh = 0; hh < 2; ++hh) {
          const float m0 = __builtin_fmaxf(__builtin_fmaxf(sp[hh][0], sp[hh][1]), sp[hh][2]), m1 = __builtin_fmaxf(__builtin_fmaxf(ss[hh][0], ss[hh][1]), ss[hh][2]);
          mx[hh] = __builtin_fmaxf(__builtin_fmaxf(__builtin_fmaxf(__builtin_fmaxf(m0, sp[hh][3]), ss[hh][3]), m1), m1);
        }
        rows_max2(mx[0], mx[1]);
#pragma unroll
        for (int hh = 0; hh < 2; ++hh) {
          const float c = 0.23570226039551584f * 1.4426950408889634f, mc = -c * mx[hh];
          float e[8];
#pragma unroll
          for (int r = 0; r < 4; ++r) {
            e[r] = __builtin_amdgcn_exp2f(__builtin_fmaf(sp[hh][r], c, mc));
            e[4 + r] = __builtin_amdgcn_exp2f(__builtin_fmaf(ss[hh][r], c, mc));
          }
          const v4u pf = {pk(e[0], e[1]), pk(e[2], e[3]), pk(e[4], e[5]), pk(e[6], e[7])};
          sm[hh] = mfma(one4, pf, z);
          o[hh] = mfma(v4u{vp[hh].x, vp[hh].y, vs[hh].x, vs[hh].y}, pf, z);
          ox[hh] = mfma(v4u{vxp.x, vxp.y, vxs.x, vxs.y}, pf, z);
        }
        const float i0 = __builtin_amdgcn_rcpf(sm[0][0]), i1 = __builtin_amdgcn_rcpf(sm[1][0]);
        stg(v4u{pk(o[0][0] * i0, o[0][1] * i0), pk(o[0][2] * i0, o[0][3] * i0), pk(o[1][0] * i1, o[1][1] * i1), pk(o[1][2] * i1, o[1][3] * i1)}, rs, om, 0);
        __builtin_amdgcn_raw_buffer_store_b64(v2u{pk(ox[0][0] * i0, ox[0][1] * i0), pk(ox[1][2] * i1, ox[1][3] * i1)}, rs, oe, 0, SC1);
        STAMP(34);
        SETTLE();
      }
      STAMP(6 + layer * 16);
      BAR();
      STAMP(7 + layer * 16);
    }
    {
      LOCALS();
      const int th = g >> 2, ft = (g & 3) * 9 + w;
      {
        const int ov = quad * opq_s(oq0) + l15 * 16;
        RETRY_BEGIN();
        v4u orr[3];
#pragma unroll
        for (int i = 0; i < 3; ++i) orr[i] = ldg(rs, i ? ov : lane16, os0[i] + layer * ALY);
#pragma unroll
        for (int i = 0; i < 3; ++i) { *(LAS v4u*)(L + (w * 3 + i) * KB + lane16) = orr[i]; seen_ = umax4(seen_, orr[i]); }
        RETRY_END();
      }
      const v4u xr = xres;
      if (layer == 0 && w == 0) wait_flags<2>(p.ws, SFLAGS + (256 + g * 32 + (lane & 31)) * 8, lane < 32, tag);
      BAR();
      STAMP(8 + layer * 16);
      if (w < 9) {
        f32x4 acc[1][2];
        gemm18<1, 2, 0, P2N, 10>(acc, win, L + lane16, rs, lane16, 0, MBASE());
        float a[8];
#pragma unroll
        for (int r = 0; r < 4; ++r) {
          auto s = __builtin_amdgcn_permlane16_swap(__float_as_uint(acc[0][0][r]), __float_as_uint(acc[0][1][r]), false, false);
          a[r] = __uint_as_float(s[0]);
          a[4 + r] = __uint_as_float(s[1]);
        }
        const v4u o = {pk(a[0] + blo(xr.x), a[1] + bhi(xr.x)), pk(a[2] + blo(xr.y), a[3] + bhi(xr.y)),
                       pk(a[4] + blo(xr.z), a[5] + bhi(xr.z)), pk(a[6] + blo(xr.w), a[7] + bhi(xr.w))};
        stg(o, rs, lane16, ab + A_X1 + (th * 36 + ft) * KB);
        STAMP(35);
        SB();
        if (P2N < 3 * DST) issue_window<P2N, 3 * DST>(win, rs, lane16, MBASE());
        else SETTLE();
      }
      if (layer == 0 && opq_s(w) >= 9) {
        const int f4 = (w - 9) * 48 + (lane < 48 ? lane : 0);
        v4u cp[6];
#pragma unroll
        for (int i = 0; i < 6; ++i) cp[i] = ldg(rs, f4 * 16, CS1 + (i * 4608 + 576 * g) * 4);
        const v2u b = *(const GAS v2u*)(p.b1 + 576 * g + 4 * f4);
        *(LAS v2u*)(L + L_B2 + f4 * 8) = *(const GAS v2u*)(p.b2 + 4 * f4);
        f32x4 cs = __builtin_bit_cast(f32x4, cp[0]);
#pragma unroll
        for (int i = 1; i < 6; ++i) add4(cs, cp[i]);
        *(LAS f32x4*)(L + L_T1 + f4 * 16) = cs;
        *(LAS f32x4*)(L + L_T1 + 2304 + f4 * 16) = f32x4{blo(b.x), bhi(b.x), blo(b.y), bhi(b.y)};
      }
      if (w == 10 && lane < 10) ((volatile LAS int*)(L + L_HC))[lane] = 0;
      STAMP(9 + layer * 16);
      BAR();
      STAMP(10 + layer * 16);
    }
    {
      LOCALS();
      const int mbase = MBASE();
      const int T = w & 3, cls = w >> 2;
      if (opq_s(cls) == 0) {
        if (layer == 0) {
          __builtin_amdgcn_s_sleep(PZD);
          const v4u pz = {vconst<POISON>(), vconst<POISON>(), vconst<POISON>(), vconst<POISON>()};
#pragma unroll
          for (int i = 0; i < 8; ++i) {
            const int n = w * 8 + i;
            stg(pz, rs, lane16,
                sel(n < 30, ab + ALY + n * KB + sel(n < 12, A_X2 + g * 12 * KB, sel(n < 21, A_O, A_X1 - 9 * KB) + (g * 9 - 12) * KB), OOR));
          }
        }
      }
      if (opq_s(cls) > 0) {
        v4u xr[9];
        const int xo = (quad >> 1) * KB + ((quad & 1) * 2 + (T & 1)) * 256 + l15 * 16;
        RETRY_BEGIN();
#pragma unroll
        for (int i = 0; i < 9; ++i) xr[i] = ldg(rs, xo + (i & 1) * 2 * KB, ab + A_X1 + ((T >> 1) * 36 + 18 * (cls - 1) + (i >> 1) * 4) * KB);
#pragma unroll
        for (int i = 0; i < 9; ++i) { *(LAS v4u*)(L + (T * 18 + 9 * (cls - 1) + i) * KB + lane16) = xr[i]; seen_ = umax4(seen_, xr[i]); }
        RETRY_END();
        cnt_add(L, C_XA + cls - 1, lane);
        __builtin_amdgcn_s_setprio(3);
        const v4u one4 = {vconst<ONES>(), vconst<ONES>(), vconst<ONES>(), vconst<ONES>()};
        f32x4 sa = {0.f, 0.f, 0.f, 0.f}, sq = {0.f, 0.f, 0.f, 0.f};
        asm volatile("s_nop 1");
#pragma unroll
        for (int i = 0; i < 9; ++i) { sa = mfma(one4, xr[i], sa); sq = mfma(xr[i], xr[i], sq); }
        __builtin_amdgcn_s_setprio(0);
        if (layer == 0 && opq_s(w) >= 9) issue_window<0, 3 * DST>(win, rs, lane16, mbase);
        const float dg = (lane & 2) ? ((lane & 1) ? sq[3] : sq[2]) : ((lane & 1) ? sq[1] : sq[0]);
        if (quad == (l15 >> 2)) *(LAS f32x2*)(L + L_ST + ((T * 16 + l15) * 2 + cls - 1) * 8) = f32x2{sa[0], dg};
        cnt_add(L, C_ST, lane);
      }
      STAMP(11 + layer * 16);
      f32x4 acc[3][4];
#pragma unroll
      for (int t = 0; t < 3; ++t)
#pragma unroll
        for (int m = 0; m < 4; ++m) acc[t][m] = zero4();
      if (opq_s(cls) > 0) { row_table(L, lane); shift_acc(acc, L, w, quad, l15); }
      STAMP(49);
      __builtin_amdgcn_s_setprio(3);
      gemm_up(acc, win, L + lane16, rs, lane16, mbase, L);
      __builtin_amdgcn_s_setprio(0);
      STAMP(12 + layer * 16);
      {
        LAS unsigned char* H = L + L_H + lane16 + 3 * (w >> 1) * KB;
        if (opq_s(cls) == 0) {
          cnt_wait(L, C_RT, 1);
          shift_acc(acc, L, w, quad, l15);
        }
        f32x4 rt[4];
#pragma unroll
        for (int m = 0; m < 4; ++m) rt[m] = *(LAS f32x4*)(L + L_ST + KB + (m * 16 + l15) * 16);
#pragma unroll
        for (int m = 0; m < 4; ++m) {
          const v2u u0 = gelu4(acc[0][m], rt[m][2], rt[m][3], rt[m][1]), u1 = gelu4(acc[1][m], rt[m][2], rt[m][3], rt[m][1]);
          *(LAS v4u*)(H + (m * 18 + (w & 1)) * KB) = v4u{u0.x, u0.y, u1.x, u1.y};
          *(LAS v2u*)(H + (m * 18 + 2) * KB + (w & 1) * 8) = gelu4(acc[2][m], rt[m][2], rt[m][3], rt[m][1]);
        }
        cnt_add(L, C_H0 + cls, lane);
      }
      STAMP(13 + layer * 16);
      __builtin_amdgcn_s_setprio(2);
      gemm_down(acc, win, L + L_H + lane16, rs, lane16, mbase + 54 * KB, L STAMP_PASS);
      __builtin_amdgcn_s_setprio(0);
      STAMP(14 + layer * 16);
      const int pw = ab + A_PT + (g * 12 + w) * 6144;
      const int poff = (quad & 1) * 1536 + (quad >> 1) * 256 + l15 * 16;
#pragma unroll
      for (int t = 0; t < 3; ++t)
#pragma unroll
        for (int m = 0; m < 4; m += 2) {
          const v2u a = {pk(acc[t][m][0], acc[t][m][1]), pk(acc[t][m][2], acc[t][m][3])};
          const v2u b = {pk(acc[t][m + 1][0], acc[t][m + 1][1]), pk(acc[t][m + 1][2], acc[t][m + 1][3])};
          const v4u o = widen(a, b);
          const bool own = m + (quad & 1) == (g >> 1) && (l15 >> 3) == (g & 1);
          stg(o, rs, own ? WS_BYTES : poff + m * 1536 + t * 512, pw);
          if (own) *(LAS v4u*)(L + (((g >> 1) ^ 1) * 18 + w) * KB + ((2 * t + (quad >> 1)) * 8 + (lane & 7)) * 16) = o;
        }
      __builtin_amdgcn_s_setprio(0);
    }
    {
      LOCALS();
      OWNROWS(6);
      const bool act = lane < 48;
      float sum[8];
      {
        const v4u bb = *(LAS v4u*)(L + L_B2 + (act ? c8 : 0) * 16);
        const v4u xr = *(LAS v4u*)(L + (act ? piece : 0)), ow = *(LAS v4u*)(L + (((g >> 1) ^ 1) * 18 + w) * KB + lane16);
#pragma unroll
        for (int e = 0; e < 4; ++e) {
          sum[2 * e] = blo(xr[e]) + blo(ow[e]); OPAQUE(sum[2 * e]); sum[2 * e] += blo(bb[e]); OPAQUE(sum[2 * e]);
          sum[2 * e + 1] = bhi(xr[e]) + bhi(ow[e]) + bhi(bb[e]);
        }
      }
      int roff = (g >> 1) * 1536 + (lane >> 3) * 256 + (8 * (g & 1) + (lane & 7)) * 16 + (act ? 0 : OOR);
      int foff = WFLAGS + ((layer * 32 + j - (lane >> 3)) * 12 + w) * 128 + (lane < 8 ? lane : 3 + 4 * (lane & 1)) * 8;
      asm volatile("" : "+v"(roff), "+v"(foff), "+v"(sum[0]), "+v"(sum[1]), "+v"(sum[2]), "+v"(sum[3]), "+v"(sum[4]), "+v"(sum[5]), "+v"(sum[6]), "+v"(sum[7]));
      if (w >= 8) __builtin_amdgcn_s_barrier();
      DRAIN();
      if (lane == 0) set_flag(p.ws, WFLAGS + ((layer * 32 + j) * 12 + w) * 128 + g * 8, tag);
      STAMP(15 + layer * 16);
      if (layer == 0 && opq_s(w) < 8) {
        if (QA) cnt_wait(L, C_LC, 12);
        issue_window<0, QA>(win, rs, lane16, QBASE());
      }
      __builtin_amdgcn_s_barrier();
      wait_flags(p.ws, foff, lane < (layer == 0 && j > 0 ? 10 : 8), tag);
      if (w < 8) __builtin_amdgcn_s_barrier();
      asm volatile("" ::: "memory");
      STAMP(16 + layer * 16);
      v4u xb;
      {
        v4u pr[7];
#pragma unroll
        for (int i = 0; i < 7; ++i) pr[i] = ldg(rs, roff, ab + A_PT + (((i + 1 + g) & 7) * 12 + w) * 6144);
        SB();
        const unsigned sel_lo = opq_s(0x00003f80), sel_hi = opq_s(0x3f800000);
#pragma unroll
        for (int i = 0; i < 7; ++i)
#pragma unroll
          for (int e = 0; e < 4; ++e) { sum[2 * e] = dot2(pr[i][e], sel_lo, sum[2 * e]); sum[2 * e + 1] = dot2(pr[i][e], sel_hi, sum[2 * e + 1]); }
#pragma unroll
        for (int e = 0; e < 4; ++e) xb[e] = pk(sum[2 * e], sum[2 * e + 1]);
      }
      STAMP(17 + layer * 16);
      if (layer == 0) {
        float s1 = dot2(xb.x, ONES, dot2(xb.y, ONES, dot2(xb.z, ONES, dot2(xb.w, ONES, 0.f))));
        float s2 = dot2(xb.x, xb.x, dot2(xb.y, xb.y, dot2(xb.z, xb.z, dot2(xb.w, xb.w, 0.f))));
        if (!act) { s1 = 0.f; s2 = 0.f; }
        s1 += ror8(s1); OPAQUE(s1); s2 += ror8(s2);
        s1 = rows_sum(s1); OPAQUE(s1); s2 = rows_sum(s2);
        if (!act) xb = v4u{__float_as_uint(s1), __float_as_uint(s2), 0u, 0u};
        if (lane < 56) stg(xb, rs, lane16, ab + ALY + A_X2 + (g * 12 + w) * KB);
        SB();
        if (QB > QA && opq_s(w) < 8) issue_window<QA, QB>(win, rs, lane16, QBASE());
        else SETTLE();
        STAMP(36);
        const bool stl = (lane >> 3) == 6;
        LAS unsigned char* Iw = act ? L + (c8 >> 2) * KB + ((c8 & 3) * 16 + (lane & 7)) * 16 : L + 126 * KB + lane16;
        LAS unsigned char* Sw = L + L_ST + (w * 80 + (lane & 7)) * 8;
        BAR();
        STAMP(37);
        v4u xv[10];
        const int vo = lane < 56 ? lane16 : lane16 + WS_BYTES;
        xv[0] = xb;
        RETRY_BEGIN();
#pragma unroll
        for (int i = 1; i < 10; ++i)
          xv[i] = ldg(rs, vo, ab + ALY + A_X2 + w * KB + (i < 8 ? (i ^ g) * 12 * KB : (3 + 4 * (i - 8)) * 12 * KB - (j > 0 ? ASL : 0)));
        SB();
#pragma unroll
        for (int i = 0; i < 10; ++i) {
          const int mi = i < 8 ? i ^ g : i;
          *(LAS v4u*)(Iw + (act ? (mi >> 1) * 18 * KB + (mi & 1) * 128 : 0)) = xv[i];
          if (i) seen_ = umax4(seen_, xv[i]);
        }
        RETRY_END();
        if (stl) {
#pragma unroll
          for (int i = 0; i < 10; ++i) *(LAS v2u*)(Sw + (i < 8 ? i ^ g : i) * 64) = v2u{xv[i].x, xv[i].y};
        }
        BAR();
        STAMP(18);
        if (QB < WIN && opq_s(w) < 8) issue_window<QB, WIN>(win, rs, lane16, QBASE());
        if (w == 11) row_table1<12>(L, 4, lane);
        if (w >= 8) row_table1<12>(L, w - 8, lane);
        if (opq_s(w) == 8) issue_window<3 * DST, WIN>(win, rs, lane16, QBASE());
        if (opq_s(w) >= 8) issue_window<0, 3 * DST>(win, rs, lane16, QBASE());
      } else {
        const rsrc_t ro = __builtin_amdgcn_make_buffer_rsrc((void*)p.out, 0, 2048 * 576 * 2, 0x00020000);
        __builtin_amdgcn_raw_buffer_store_b128(xb, ro, act ? mytok * 1152 + c8 * 16 : OOR, 0, 0);
      }
    }
  }
  asm volatile(".Lfwd_end:");
  STAMP(40);
  STAMP_DUMP();
}
}

extern "C" void kernel_launch(void* const* d_in, const int* in_sizes, int n_in, void* d_out, int out_size, void* d_ws, size_t ws_size,
                              hipStream_t stream) {
  (void)in_sizes; (void)n_in; (void)out_size; (void)ws_size;
  Params p;
  p.x = (const unsigned short*)d_in[0];
  p.wqkv = (const unsigned short*)d_in[1];
  p.wo = (const unsigned short*)d_in[2];
  p.w1 = (const unsigned short*)d_in[4];
  p.b1 = (const unsigned short*)d_in[5];
  p.w2 = (const unsigned short*)d_in[6];
  p.b2 = (const unsigned short*)d_in[7];
  p.out = (unsigned short*)d_out;
  p.ws = (unsigned char*)d_ws;
  (void)hipFuncSetAttribute((const void*)fused_fwd, hipFuncAttributeMaxDynamicSharedMemorySize, LDS_BYTES);
  hipLaunchKernelGGL(fused_fwd, dim3(256), dim3(NTHR), LDS_BYTES, stream, p);
}
```

```cpp
#include <hip/hip_runtime.h>
#include <hip/hip_bf16.h>
#include <stdint.h>

namespace {
#define LAS __attribute__((address_space(3)))
#define GAS __attribute__((address_space(1)))
typedef unsigned v4u __attribute__((ext_vector_type(4)));
typedef unsigned v2u __attribute__((ext_vector_type(2)));
typedef float f32x4 __attribute__((ext_vector_type(4)));
typedef float f32x2 __attribute__((ext_vector_type(2)));
typedef __bf16 bf16x8 __attribute__((ext_vector_type(8)));
typedef __bf16 bf16x2 __attribute__((ext_vector_type(2)));
typedef short s16x4 __attribute__((ext_vector_type(4)));
typedef unsigned long long u64;
typedef __amdgpu_buffer_rsrc_t rsrc_t;

constexpr int KB = 1024;
constexpr int NTHR = 768;
constexpr int WIN = 18;
constexpr int XB = 2;
constexpr int P2N = 9;
constexpr int QA = 6;
constexpr int QB = 12;
constexpr int PZD = 20;
constexpr int BDELAY = 10;
constexpr int NTW = 1;
constexpr int DST = 5;
constexpr float EPS = 1e-5f;
constexpr int SC1 = 16;
constexpr float GC0 = -2.0f * 0.7978845608028654f * 1.4426950408889634f;
constexpr int FLAGS = 4096;
constexpr int SFLAGS = 32 * KB;
constexpr int WFLAGS = 48 * KB;
constexpr int CS1 = 192 * KB;
constexpr int CSQ = 520 * KB;
constexpr int WBASE = 704 * KB;
constexpr int WSL = (252 + 1296) * KB;
constexpr int WOB = WBASE + 8 * WSL;
constexpr int ABASE = 14 * 1024 * KB;
constexpr int IMG = 72 * KB;
constexpr int A_X2 = 0, A_O = 96 * KB, A_X1 = A_O + IMG, A_PT = A_X1 + IMG, ALY = 1024 * KB, ASL = 2 * ALY;
constexpr int WS_BYTES = ABASE + 32 * ASL;
static_assert(WOB + 648 * KB <= ABASE && A_PT + 8 * IMG <= ALY && CSQ + 8 * 14 * 1152 + 256 <= WBASE, "workspace map");
constexpr int L_H = IMG;
constexpr int L_QK = 90 * KB;
constexpr int L_ST = 145 * KB;
constexpr int L_T1 = L_ST + 7680;
constexpr int L_TQ = L_T1 + 4608;
constexpr int L_RT = L_TQ + 896;
constexpr int L_HC = L_RT + 640;
constexpr int L_B2 = L_HC + 64;
constexpr int LDS_BYTES = L_B2 + 1152;
static_assert(LDS_BYTES <= 160 * KB, "LDS");
enum { C_H0 = 0, C_ST = 3, C_RT = 4, C_LC = 5, C_XA = 6, C_R1 = 10, C_P0 = 11 };

struct Params {
  const unsigned short *x, *wqkv, *wo, *w1, *b1, *w2, *b2;
  unsigned short *out;
  unsigned char *ws;
};

#define SB() __builtin_amdgcn_sched_barrier(0)
#define BAR() do { asm volatile("s_waitcnt lgkmcnt(0)" ::: "memory"); __builtin_amdgcn_s_barrier(); asm volatile("" ::: "memory"); } while (0)
#define DRAIN() asm volatile("s_waitcnt vmcnt(0)" ::: "memory")
#define SETTLE() DRAIN()
#define OPAQUE(x) asm("" : "+v"(x))
#define KEEP(x) asm volatile("" :: "v"(x))

__device__ __forceinline__ f32x4 mfma(v4u a, v4u b, f32x4 c) {
  return __builtin_amdgcn_mfma_f32_16x16x32_bf16(__builtin_bit_cast(bf16x8, a), __builtin_bit_cast(bf16x8, b), c, 0, 0, 0);
}
#define MF(acc, a, b) asm volatile("v_mfma_f32_16x16x32_bf16 %0, %1, %2, %0" : "+v"(acc) : "v"(a), "v"(b))
#define MF0(acc, a, b) asm volatile("v_mfma_f32_16x16x32_bf16 %0, %1, %2, 0" : "=&v"(acc) : "v"(a), "v"(b))
template <int NT, int MT> __device__ __forceinline__ void acc_fence(f32x4 (&acc)[NT][MT]) {
#pragma unroll
  for (int t = 0; t < NT; ++t)
#pragma unroll
    for (int m = 0; m < MT; ++m) asm volatile("" : "+v"(acc[t][m]));
}
#define ACC_IN(acc) do { acc_fence(acc); asm volatile("s_nop 1"); SB(); } while (0)
#define ACC_OUT(acc) do { SB(); asm volatile("s_nop 7\n\ts_nop 1"); acc_fence(acc); } while (0)
__device__ __forceinline__ unsigned pk(float a, float b) {
  f32x2 v = {a, b};
  return __builtin_bit_cast(unsigned, __builtin_convertvector(v, bf16x2));
}
__device__ __forceinline__ float dot2(unsigned a, unsigned b, float c) {
  return __builtin_amdgcn_fdot2_f32_bf16(__builtin_bit_cast(bf16x2, a), __builtin_bit_cast(bf16x2, b), c, false);
}
constexpr unsigned ONES = 0x3f803f80u;
__device__ __forceinline__ float blo(unsigned u) { return __uint_as_float(u << 16); }
__device__ __forceinline__ float bhi(unsigned u) { return __uint_as_float(u & 0xffff0000u); }
__device__ __forceinline__ v4u ldg(rsrc_t rs, int voff, int soff) { return __builtin_amdgcn_raw_buffer_load_b128(rs, voff, soff, SC1); }
__device__ __forceinline__ void stg(v4u v, rsrc_t rs, int voff, int soff) { __builtin_amdgcn_raw_buffer_store_b128(v, rs, voff + soff, 0, SC1); }
__device__ __forceinline__ v4u ldf(rsrc_t rs, int lane16, int base, int f) { return ldg(rs, lane16 + (f & 3) * KB, base + (f >> 2) * 4 * KB); }
__device__ __forceinline__ int lane_id() { int x; asm volatile("v_mbcnt_lo_u32_b32 %0, -1, 0\n\tv_mbcnt_hi_u32_b32 %0, -1, %0" : "=v"(x)); return x; }
template <unsigned C> __device__ __forceinline__ unsigned vconst() { unsigned x; asm volatile("v_mov_b32 %0, %1" : "=v"(x) : "i"(C)); return x; }
__device__ __forceinline__ int sel(bool c, int a, int b) { return c ? a : b; }
constexpr int OOR = 0x10000000;
__device__ __forceinline__ int opq_s(int x) { asm volatile("" : "+s"(x)); return x; }
#define LOCALS() const int lane = lane_id(), w = opq_s(w0), j = opq_s(j0), g = opq_s(g0); \
  const int lane16 = lane * 16, quad = lane >> 4, l15 = lane & 15, ab = ABASE + j * ASL + layer * ALY, wb = WBASE + g * WSL; \
  (void)lane16; (void)quad; (void)l15; (void)ab; (void)wb
#define OWNROWS(CPW) const int c8 = CPW * w + (lane >> 3); \
  const int piece = ((g >> 1) * 18 + (c8 >> 2)) * KB + ((c8 & 3) * 16 + 8 * (g & 1) + (lane & 7)) * 16; \
  const int mytok = 64 * j + 2 * (16 * ((g >> 1) & 1) + 8 * (g & 1) + (lane & 7)) + (g >> 2)
#define GET_TAG() KEEP(cpf); tag = 1 + ((u64)(unsigned)__builtin_amdgcn_readfirstlane((int)(unsigned)tag0) | \
                             ((u64)(unsigned)__builtin_amdgcn_readfirstlane((int)(unsigned)(tag0 >> 32)) << 32))
#define NBASE() opq_s(nb0)
#define QBASE() opq_s(qb0)
#define MBASE() opq_s(mb0)
__device__ __forceinline__ float rows_sum(float x) {
  auto s = __builtin_amdgcn_permlane16_swap(__float_as_uint(x), __float_as_uint(x), false, false);
  x = __uint_as_float(s[0]) + __uint_as_float(s[1]);
  auto t = __builtin_amdgcn_permlane32_swap(__float_as_uint(x), __float_as_uint(x), false, false);
  return __uint_as_float(t[0]) + __uint_as_float(t[1]);
}
__device__ __forceinline__ float vmax(unsigned a, unsigned b) { float r; asm("v_max_f32 %0, %1, %2\n\ts_nop 1" : "=v"(r) : "v"(a), "v"(b)); return r; }
__device__ __forceinline__ void rows_max2(float& a, float& b) {
  auto s = __builtin_amdgcn_permlane16_swap(__float_as_uint(a), __float_as_uint(b), false, false);
  float x = vmax(s[0], s[1]);
  auto t = __builtin_amdgcn_permlane32_swap(__float_as_uint(x), __float_as_uint(x), false, false);
  x = vmax(t[0], t[1]);
  auto u = __builtin_amdgcn_permlane16_swap(__float_as_uint(x), __float_as_uint(x), false, false);
  a = __uint_as_float(u[0]);
  b = __uint_as_float(u[1]);
}
template <int CTRL> __device__ __forceinline__ float dpp(float x) {
  return __int_as_float(__builtin_amdgcn_mov_dpp(__float_as_int(x), CTRL, 0xf, 0xf, true));
}
__device__ __forceinline__ float ror8(float x) { return dpp<0x128>(x); }
__device__ __forceinline__ void add4(f32x4& a, v4u b) {
  float a0 = a[0], a1 = a[1], a2 = a[2], a3 = a[3];
  asm("v_add_f32 %0, %0, %4\n\tv_add_f32 %1, %1, %5\n\tv_add_f32 %2, %2, %6\n\tv_add_f32 %3, %3, %7"
      : "+v"(a0), "+v"(a1), "+v"(a2), "+v"(a3) : "v"(b.x), "v"(b.y), "v"(b.z), "v"(b.w));
  a = f32x4{a0, a1, a2, a3};
}
__device__ __forceinline__ v4u widen(v2u a, v2u b) {
  auto s0 = __builtin_amdgcn_permlane16_swap(a.x, b.x, false, false);
  auto s1 = __builtin_amdgcn_permlane16_swap(a.y, b.y, false, false);
  return v4u{s0[0], s1[0], s0[1], s1[1]};
}
__device__ __forceinline__ v2u gelu4(f32x4 a, float k0, float k1, float is) {
  float s0, s1, s2, s3;
  unsigned u0, u1;
  asm("v_mul_f32 %2, %6, %6\n\tv_mul_f32 %3, %7, %7\n\tv_mul_f32 %4, %8, %8\n\tv_mul_f32 %5, %9, %9\n\t"
      "v_fma_f32 %2, %2, %11, %10\n\tv_fma_f32 %3, %3, %11, %10\n\tv_fma_f32 %4, %4, %11, %10\n\tv_fma_f32 %5, %5, %11, %10\n\t"
      "v_mul_f32 %2, %2, %6\n\tv_mul_f32 %3, %3, %7\n\tv_mul_f32 %4, %4, %8\n\tv_mul_f32 %5, %5, %9\n\t"
      "v_exp_f32 %2, %2\n\tv_exp_f32 %3, %3\n\tv_exp_f32 %4, %4\n\tv_exp_f32 %5, %5\n\t"
      "v_fma_f32 %2, %2, %12, %12\n\tv_fma_f32 %3, %3, %12, %12\n\tv_fma_f32 %4, %4, %12, %12\n\tv_fma_f32 %5, %5, %12, %12\n\t"
      "v_rcp_f32 %2, %2\n\tv_rcp_f32 %3, %3\n\tv_rcp_f32 %4, %4\n\tv_rcp_f32 %5, %5\n\t"
      "v_mul_f32 %2, %2, %6\n\tv_mul_f32 %3, %3, %7\n\tv_mul_f32 %4, %4, %8\n\tv_mul_f32 %5, %5, %9\n\t"
      "v_cvt_pk_bf16_f32 %0, %2, %3\n\tv_cvt_pk_bf16_f32 %1, %4, %5"
      : "=&v"(u0), "=&v"(u1), "=&v"(s0), "=&v"(s1), "=&v"(s2), "=&v"(s3)
      : "v"(a[0]), "v"(a[1]), "v"(a[2]), "v"(a[3]), "v"(k0), "v"(k1), "v"(is));
  return v2u{u0, u1};
}
__device__ __forceinline__ void shift4(f32x4& a, f32x4 cs, f32x4 b, float nmean, float is) {
  float a0 = a[0], a1 = a[1], a2 = a[2], a3 = a[3];
  asm("v_fmac_f32 %0, %12, %4\n\tv_fmac_f32 %1, %12, %5\n\tv_fmac_f32 %2, %12, %6\n\tv_fmac_f32 %3, %12, %7\n\t"
      "v_fmac_f32 %0, %13, %8\n\tv_fmac_f32 %1, %13, %9\n\tv_fmac_f32 %2, %13, %10\n\tv_fmac_f32 %3, %13, %11"
      : "+v"(a0), "+v"(a1), "+v"(a2), "+v"(a3)
      : "v"(cs[0]), "v"(cs[1]), "v"(cs[2]), "v"(cs[3]), "v"(b[0]), "v"(b[1]), "v"(b[2]), "v"(b[3]), "v"(nmean), "v"(is));
  a = f32x4{a0, a1, a2, a3};
}
__device__ __forceinline__ int cnt_get(LAS unsigned char* L, int i) { return ((volatile LAS int*)(L + L_HC))[i]; }
__device__ __forceinline__ void cnt_wait(LAS unsigned char* L, int i, int n) {
  while (__builtin_amdgcn_readfirstlane(cnt_get(L, i)) < n) __builtin_amdgcn_s_sleep(1);
  asm volatile("" ::: "memory");
}
__device__ __forceinline__ void cnt_add(LAS unsigned char* L, int i, int lane) {
  asm volatile("s_waitcnt lgkmcnt(0)" ::: "memory");
  if (lane == 0) __hip_atomic_fetch_add((LAS int*)(L + L_HC) + i, 1, __ATOMIC_RELAXED, __HIP_MEMORY_SCOPE_WORKGROUP);
}
template <int NP> __device__ __forceinline__ void row_table1(LAS unsigned char* L, int t5, int lane) {
  const int r = t5 * 16 + (lane & 15), q = lane >> 4;
  float t1 = 0.f, t2 = 0.f;
#pragma unroll
  for (int i = 0; i < (NP + 3) / 4; ++i)
    if (NP % 4 == 0 || q < NP) { const f32x2 v = *(LAS f32x2*)(L + L_ST + ((i * 4 + q) * 80 + r) * 8); t1 += v.x; OPAQUE(t1); t2 += v.y; }
  t1 = rows_sum(t1); OPAQUE(t1); t2 = rows_sum(t2);
  const float nmean = t1 * (-1.0f / 576.0f);
  const float var = fmaxf(__builtin_fmaf(-nmean, nmean, t2 * (1.0f / 576.0f)), 0.f);
  const float rstd = __builtin_amdgcn_rsqf(var + EPS);
  if (q < 2) *(LAS float*)(L + L_RT + q * 320 + r * 4) = q ? nmean * rstd : rstd;
  cnt_add(L, C_R1, lane);
}
__device__ __forceinline__ v2u ln_pack(f32x4 a, f32x4 r, f32x4 c, f32x4 nm) {
  float t0, t1, t2, t3;
  unsigned u0, u1;
  asm("v_mul_f32 %2, %14, %18\n\tv_mul_f32 %3, %15, %19\n\tv_mul_f32 %4, %16, %20\n\tv_mul_f32 %5, %17, %21\n\t"
      "v_fmac_f32 %2, %6, %10\n\tv_fmac_f32 %3, %7, %11\n\tv_fmac_f32 %4, %8, %12\n\tv_fmac_f32 %5, %9, %13\n\t"
      "v_cvt_pk_bf16_f32 %0, %2, %3\n\tv_cvt_pk_bf16_f32 %1, %4, %5"
      : "=&v"(u0), "=&v"(u1), "=&v"(t0), "=&v"(t1), "=&v"(t2), "=&v"(t3)
      : "v"(a[0]), "v"(a[1]), "v"(a[2]), "v"(a[3]), "v"(r[0]), "v"(r[1]), "v"(r[2]), "v"(r[3]), "v"(c[0]), "v"(c[1]), "v"(c[2]), "v"(c[3]),
        "v"(nm[0]), "v"(nm[1]), "v"(nm[2]), "v"(nm[3]));
  return v2u{u0, u1};
}
__device__ __forceinline__ f32x4 zero4() {
  float a, b, c, d;
  asm volatile("v_mov_b32 %0, 0\n\tv_mov_b32 %1, 0\n\tv_mov_b32 %2, 0\n\tv_mov_b32 %3, 0" : "=v"(a), "=v"(b), "=v"(c), "=v"(d));
  return f32x4{a, b, c, d};
}
__device__ __forceinline__ f32x4 splat(float x) { return f32x4{x, x, x, x}; }
__device__ __forceinline__ void row_table(LAS unsigned char* L, int lane) {
  cnt_wait(L, C_ST, 8);
  const f32x4 q = *(LAS f32x4*)(L + L_ST + lane * 16);
  float t1 = q[0] + q[2];
  OPAQUE(t1);
  float nmean = t1 * (-1.0f / 576.0f);
  const float var = fmaxf(__builtin_fmaf(-nmean, nmean, (q[1] + q[3]) * (1.0f / 576.0f)), 0.f);
  const float r1 = __builtin_amdgcn_rsqf(var + EPS);
  const float sc = r1 * __builtin_amdgcn_rsqf(var * r1 * r1 + EPS);
  float k0 = GC0 * sc;
  OPAQUE(k0);
  *(LAS f32x4*)(L + L_ST + KB + lane * 16) = f32x4{nmean, __builtin_amdgcn_rcpf(sc), k0, k0 * (0.044715f * sc * sc)};
  cnt_add(L, C_RT, lane);
}
__device__ __forceinline__ void shift_acc(f32x4 (&acc)[3][4], LAS unsigned char* L, int w, int quad, int l15) {
  f32x2 rt[4];
#pragma unroll
  for (int m = 0; m < 4; ++m) rt[m] = *(LAS f32x2*)(L + L_ST + KB + (m * 16 + l15) * 16);
#pragma unroll
  for (int t = 0; t < 3; ++t) {
    const f32x4 cs = *(LAS f32x4*)(L + L_T1 + (48 * w + 16 * t + 4 * quad) * 4), bb = *(LAS f32x4*)(L + L_T1 + 2304 + (48 * w + 16 * t + 4 * quad) * 4);
#pragma unroll
    for (int m = 0; m < 4; ++m) shift4(acc[t][m], cs, bb, rt[m][0], rt[m][1]);
  }
}
template <int SLEEP = 1> __device__ __forceinline__ void wait_flags(const unsigned char* ws, int off, bool active, u64 tag) {
  const GAS u64* f = (const GAS u64*)(ws + off);
  asm volatile("" : "+s"(tag));
#pragma unroll 1
  for (int spins = 0; spins < (1 << 16); ++spins) {
    const u64 v = active ? __hip_atomic_load(f, __ATOMIC_RELAXED, __HIP_MEMORY_SCOPE_AGENT) : tag;
    if (__all(v == tag)) break;
    __builtin_amdgcn_s_sleep(SLEEP);
  }
  asm volatile("" ::: "memory");
}
__device__ __forceinline__ void set_flag(unsigned char* ws, int off, u64 tag) {
  asm volatile("" : "+s"(tag));
  __hip_atomic_store((GAS u64*)(ws + off), tag, __ATOMIC_RELAXED, __HIP_MEMORY_SCOPE_AGENT);
}
constexpr unsigned POISON = 0xffffffffu;
__device__ __forceinline__ unsigned umax4(unsigned m, v4u v) { return max(max(max(m, v.x), v.y), max(v.z, v.w)); }
#define RETRY_BEGIN() _Pragma("unroll 1") for (int spins_ = 0; spins_ < (1 << 15); ++spins_) { unsigned seen_ = 0
#define RETRY_END() if (!__any(seen_ == POISON)) break; __builtin_amdgcn_s_sleep(2); asm volatile("" ::: "memory"); }
template <int LO, int HI> __device__ __forceinline__ void issue_window(v4u (&win)[WIN], rsrc_t rs, int lane16, int base) {
#pragma unroll
  for (int i = LO; i < HI; ++i) win[i] = ldf(rs, lane16, base, i);
  SB();
}

template <int NT, int MT, int SWAP, int NNEXT, int R, int LO, int HI>
__device__ __forceinline__ void gemm18_steps(f32x4 (&acc)[NT][MT], v4u (&win)[WIN], v4u (&act)[R], LAS unsigned char* img, LAS unsigned char* img2, rsrc_t rs,
                                             int lane16, int wbase, int nvo, int nbase) {
#pragma unroll
  for (int n = LO; n < HI; ++n) {
    const int s = n / MT, m = n % MT, nn = n + R - 1;
    if (n < 18 * MT) {
#pragma unroll
      for (int t = 0; t < NT; ++t) {
        if (s == 0) {
          if ((SWAP >> t) & 1) MF0(acc[t][m], act[n % R], win[t]);
          else MF0(acc[t][m], win[t], act[n % R]);
        } else if ((SWAP >> t) & 1) MF(acc[t][m], act[n % R], win[(s * NT + t) % WIN]);
        else MF(acc[t][m], win[(s * NT + t) % WIN], act[n % R]);
      }
      SB();
    }
    if (nn < 18 * MT) act[nn % R] = *(LAS v4u*)(nn % MT < 3 ? img + ((nn % MT) * 18 + nn / MT) * KB : img2 + ((nn % MT - 3) * 18 + nn / MT) * KB);
    if (m == 0 && s > 0) {
#pragma unroll
      for (int t = 0; t < NT; ++t) {
        const int f = (s - 1) * NT + t;
        if (f + WIN < 18 * NT) win[f % WIN] = ldf(rs, lane16, wbase, f + WIN);
        else if (f % WIN < NNEXT) win[f % WIN] = ldf(rs, nvo, nbase, f % WIN);
      }
    }
    SB();
    if (n < 18 * MT) {
      KEEP(act[n % R]);
#pragma unroll
      for (int t = 0; t < NT; ++t) KEEP(win[(s * NT + t) % WIN]);
      SB();
    }
  }
}
template <int NT, int MT, int SWAP, int NNEXT, int R>
__device__ __forceinline__ int gemm18(f32x4 (&acc)[NT][MT], v4u (&win)[WIN], LAS unsigned char* img, rsrc_t rs, int lane16, int wbase, int nbase,
                                      LAS unsigned char* L) {
  v4u act[R];
  LAS unsigned char* img2 = img + 54 * KB;
  if (MT > 3) asm volatile("" : "+v"(img2));
#pragma unroll
  for (int n = 0; n < R - 1; ++n) { act[n] = *(LAS v4u*)(n % MT < 3 ? img + ((n % MT) * 18 + n / MT) * KB : img2 + ((n % MT - 3) * 18 + n / MT) * KB); SB(); }
  gemm18_steps<NT, MT, SWAP, NNEXT, R, 0, 6 * MT>(acc, win, act, img, img2, rs, lane16, wbase, lane16, nbase);
  gemm18_steps<NT, MT, SWAP, NNEXT, R, 6 * MT, 12 * MT>(acc, win, act, img, img2, rs, lane16, wbase, lane16, nbase);
  const int ready = MT > 2 ? cnt_get(L, C_R1) : 0;
  SB();
  gemm18_steps<NT, MT, SWAP, NNEXT, R, 12 * MT, 18 * MT + 1>(acc, win, act, img, img2, rs, lane16, wbase, lane16, nbase);
  ACC_OUT(acc);
  return ready;
}
template <int NT, int MT, int SWAP, int R, int tstep, int NNEXT>
__device__ __forceinline__ void qkv_tiles(v4u (&win)[WIN], LAS unsigned char* L, rsrc_t rs, int lane, int wbase, int nbase, int tile0, int rwait) {
  f32x4 acc[NT][MT];
  const int ready = gemm18<NT, MT, SWAP, NNEXT, R>(acc, win, L + lane * 16, rs, lane * 16, wbase, nbase, L);
  LAS unsigned char *Rq = L + L_RT + (lane & 48), *Rl = L + L_RT + (lane & 15) * 4, *Tq = L + L_TQ + tile0 * 64 + (lane & 48),
                    *Tl = L + L_TQ + tile0 * 64 + (lane & 15) * 4, *X = L + L_QK + lane * 8 + tile0 * 2560;
  asm volatile("" : "+v"(Rq), "+v"(Rl), "+v"(Tq), "+v"(Tl), "+v"(X));
  if (__builtin_amdgcn_readfirstlane(ready) < rwait) cnt_wait(L, C_R1, rwait);
  asm volatile("" ::: "memory");
  v2u u[NT][MT];
#pragma unroll
  for (int t = 0; t < NT; ++t) {
    if ((SWAP >> t) & 1) {
      const f32x4 cs = splat(*(LAS float*)(Tl + t * tstep * 64));
#pragma unroll
      for (int m = 0; m < MT; ++m) u[t][m] = ln_pack(acc[t][m], *(LAS f32x4*)(Rq + m * 64), cs, *(LAS f32x4*)(Rq + 320 + m * 64));
    } else {
      const f32x4 cs = *(LAS f32x4*)(Tq + t * tstep * 64);
#pragma unroll
      for (int m = 0; m < MT; ++m) u[t][m] = ln_pack(acc[t][m], splat(*(LAS float*)(Rl + m * 64)), cs, splat(*(LAS float*)(Rl + 320 + m * 64)));
    }
  }
#pragma unroll
  for (int t = 0; t < NT; ++t)
#pragma unroll
    for (int m = 0; m < MT; ++m) *(LAS v2u*)(X + (t * tstep * 5 + m) * 512) = u[t][m];
}
template <int C>
__device__ __forceinline__ void mlp_chunk(f32x4 (&acc)[3][4], v4u (&win)[WIN], v4u (&act)[8], LAS unsigned char* img, rsrc_t rs, int lane16, int rb,
                                          LAS unsigned char* L) {
  int hv = 4;
#pragma unroll
  for (int s = 0; s < 6; ++s) {
    if (C != 2 && s == 5) {
      if (__builtin_amdgcn_readfirstlane(hv) < 4) cnt_wait(L, C < 0 ? C_XA + 1 : C_H0 + C + 1, 4);
      asm volatile("" ::: "memory");
      SB();
    }
    if (C == 2 && s == 6 - DST) {
      if (lane16 == 0) __hip_atomic_fetch_add((LAS int*)(L + L_HC) + C_LC, 1, __ATOMIC_RELAXED, __HIP_MEMORY_SCOPE_WORKGROUP);
      SB();
    }
    asm volatile("" :: "v"(act[(s & 1) * 4]), "v"(act[(s & 1) * 4 + 1]), "v"(act[(s & 1) * 4 + 2]), "v"(act[(s & 1) * 4 + 3]), "v"(win[s * 3]),
                 "v"(win[s * 3 + 1]), "v"(win[s * 3 + 2]));
    SB();
#pragma unroll
    for (int t = 0; t < 3; ++t)
#pragma unroll
      for (int m = 0; m < 4; ++m) {
        const int mm = t == 1 ? 3 - m : m;
        if (C == 0 && s == 0) MF0(acc[t][mm], win[t], act[mm]);
        else MF(acc[t][mm], win[s * 3 + t], act[(s & 1) * 4 + mm]);
        SB();
        if (t == 0 && !(C == 2 && s == 5)) act[((s + 1) & 1) * 4 + m] = *(LAS v4u*)(img + (m * 18 + s + 1) * KB);
        if (C != 2 && s == 3 && t == 2 && m == 0) hv = cnt_get(L, C < 0 ? C_XA + 1 : C_H0 + C + 1);
        if (t == 1 && m < 3 && !(C == 2 && s + DST >= 6))
          win[((s + DST) % 6) * 3 + m] = ldf(rs, lane16, rb, (s + DST) * 3 + m);
        SB();
      }
  }
}
__device__ __forceinline__ void gemm_up(f32x4 (&acc)[3][4], v4u (&win)[WIN], LAS unsigned char* img, rsrc_t rs, int lane16, int rb, LAS unsigned char* L) {
  v4u act[8];
  cnt_wait(L, C_XA, 4);
#pragma unroll
  for (int n = 0; n < 4; ++n) { act[n] = *(LAS v4u*)(img + n * 18 * KB); SB(); }
  ACC_IN(acc);
#pragma unroll 1
  for (int c = 0; c < 3; ++c) {
    mlp_chunk<-1>(acc, win, act, img, rs, lane16, rb, L);
    img += 6 * KB;
    rb += 18 * KB;
  }
  ACC_OUT(acc);
}
__device__ __forceinline__ void gemm_down(f32x4 (&acc)[3][4], v4u (&win)[WIN], LAS unsigned char* img, rsrc_t rs, int lane16, int rb, LAS unsigned char* L) {
  v4u act[8];
  cnt_wait(L, C_H0, 4);
#pragma unroll
  for (int n = 0; n < 4; ++n) { act[n] = *(LAS v4u*)(img + n * 18 * KB); SB(); }
  mlp_chunk<0>(acc, win, act, img, rs, lane16, rb, L);
  img += 6 * KB;
  asm volatile("" : "+v"(img));
  mlp_chunk<1>(acc, win, act, img, rs, lane16, rb + 18 * KB, L);
  img += 6 * KB;
  asm volatile("" : "+v"(img));
  mlp_chunk<2>(acc, win, act, img, rs, lane16, rb + 36 * KB, L);
  ACC_OUT(acc);
}

__global__ void __launch_bounds__(NTHR, 3) fused_fwd(Params p) {
  extern __shared__ __attribute__((aligned(16))) unsigned char smem[];
  LAS unsigned char* L = (LAS unsigned char*)smem;
  const int w0 = __builtin_amdgcn_readfirstlane(threadIdx.x >> 6);
  const int bid = blockIdx.x;
  u64 pc0;
  int clen;
  asm volatile("s_getpc_b64 %0\n.Lfwd_begin:\n\ts_mov_b32 %1, .Lfwd_end-.Lfwd_begin" : "=s"(pc0), "=s"(clen));
  unsigned cpf = 0;
  const int coff = (((blockIdx.x >> 3) % 11) * 64 + (threadIdx.x & 63)) * 64;
  if (w0 == 11 && coff < clen) cpf = *(const GAS unsigned*)(pc0 + coff);
  constexpr unsigned MPX = 8 / XB;
  const int j0 = ((blockIdx.x & 7) / XB) * (32 / MPX) + (blockIdx.x >> 3) / MPX;
  const int g0 = ((blockIdx.x & 7) % XB) * MPX + (blockIdx.x >> 3) % MPX;
  const rsrc_t rs = __builtin_amdgcn_make_buffer_rsrc((void*)p.ws, 0, WS_BYTES, 0x00020000);
  const int mb0 = WBASE + g0 * WSL + (252 + w0 * 108) * KB;
  const int nb0 = sel(w0 < 9, WOB + ((g0 & 3) * 9 + w0) * 18 * KB, mb0);
  const int qb0 = sel(w0 < 8, WBASE + g0 * WSL + sel(w0 < 6, w0 * 36, 108 + w0 * 18) * KB, nb0);
  const u64 tag0 = __hip_atomic_load((const GAS u64*)p.ws, __ATOMIC_RELAXED, __HIP_MEMORY_SCOPE_AGENT);
  u64 tag = 0;
  v4u win[WIN];
  {
    constexpr int layer = 0;
    LOCALS();
    const rsrc_t rx = __builtin_amdgcn_make_buffer_rsrc((void*)p.x, 0, 2048 * 576 * 2, 0x00020000);
    const int xrow = (lane >> 2) & 7, xcol = (lane >> 5) * 64 + (lane & 3) * 16;
#define LOAD_X() v4u xr[9]; _Pragma("unroll") for (int i = 0; i < 9; ++i) xr[i] = __builtin_amdgcn_raw_buffer_load_b128(rx, xoff + i * 128, 0, 0); SB()
#define STAGE_X() do { float s1 = 0.f, s2 = 0.f; \
      _Pragma("unroll") for (int i = 0; i < 9; ++i) { \
        if (w < 10) *(LAS v4u*)(L + ((w >> 1) * 18 + 2 * i + (lane >> 5)) * KB + (lane & 3) * 256 + ((w & 1) * 8 + xrow) * 16) = xr[i]; \
        _Pragma("unroll") for (int e = 0; e < 4; ++e) { s1 = dot2(xr[i][e], ONES, s1); s2 = dot2(xr[i][e], xr[i][e], s2); } } \
      s1 += dpp<0xB1>(s1); s2 += dpp<0xB1>(s2); s1 += dpp<0x4E>(s1); s2 += dpp<0x4E>(s2); \
      if (w < 10 && (lane & 3) == 0) *(LAS f32x2*)(L + L_ST + ((lane >> 5) * 80 + w * 8 + xrow) * 8) = f32x2{s1, s2}; } while (0)
    if (opq_s(w) < 8) {
      LAS unsigned char* T = L + L_QK + w * 3072;
      const int uf = 8 * j + w, uc = sel(uf < 252, uf, 0), us = (uc * 293) >> 12, tp = uc - us * 14;
      const int tile = sel(tp < 4, 10 + tp, sel(tp == 4, 8, sel(tp == 13, 9, tp - 5)));
      const bool vx = tile == 9, ex = (tile & 14) == 8;
      const int c0 = 72 * g + sel(ex, 16, ((tile + sel(tile >= 10, 2, 0)) & 3) * 18) + sel(tile < 4, 576, sel(tile < 8 || vx, 1152, 0));
      const rsrc_t rq = __builtin_amdgcn_make_buffer_rsrc((void*)p.wqkv, 0, 576 * 1728 * 2, 0x00020000);
      const int qo = (lane >> 1) * 3456 + (lane & 1) * sel(ex, sel(vx, 0, 72), 16), qs = us * 32 * 3456 + c0 * 2;
      v4u qv;
      if (ex) {
        const int cA = sel(vx, 36, 1152), cB = sel(vx, 72, 36);
        qv = v4u{__builtin_amdgcn_raw_buffer_load_b32(rq, qo, qs, 0), __builtin_amdgcn_raw_buffer_load_b32(rq, qo, qs + cA, 0),
                 __builtin_amdgcn_raw_buffer_load_b32(rq, qo, qs + cB, 0), __builtin_amdgcn_raw_buffer_load_b32(rq, qo, qs + cA + cB, 0)};
      } else qv = __builtin_amdgcn_raw_buffer_load_b128(rq, qo, qs, 0);
      SB();
      const int of = 3 * j + w, oc = (g >> 2) * 81 + of, os = (oc * 57) >> 9, ot = oc - os * 9;
      const bool hasO = w < 3 && of < 81;
      const bool ox = os >= 16;
      const int orow = quad * sel(ox, 72, 4) + ((lane >> 1) & 1) + ((lane >> 2) & 1) * sel(ox, 18, 2) + ((lane >> 3) & 1) * sel(ox, 36, 18);
      const rsrc_t ro = __builtin_amdgcn_make_buffer_rsrc((void*)p.wo, 0, 576 * 576 * 2, 0x00020000);
      const v4u ov = __builtin_amdgcn_raw_buffer_load_b128(ro, orow * 1152 + (lane & 1) * 16 + sel(hasO, 0, OOR),
                                                           sel(ox, 288 * (os - 16) + 16, 36 * os) * 1152 + 32 * ((g & 3) * 9 + ot), 0);
      SB();
      const int xoff = (64 * j + 16 * (w & 3) + (w >> 2) + 2 * xrow) * 1152 + xcol;
      __builtin_amdgcn_s_barrier();
      LOAD_X();
      *(LAS v4u*)(T + (lane >> 1) * 48 + (lane & 1) * 16) = (vx && (lane & 1)) ? v4u{0u, 0u, 0u, 0u} : qv;
      *(LAS v4u*)(T + 1536 + (lane >> 1) * 48 + (lane & 1) * 16) = ov;
      LAS unsigned char* R = T + (8 * quad + (l15 >> 2)) * 48 + (lane & 3) * 8;
      const v2u a = __builtin_bit_cast(v2u, __builtin_amdgcn_ds_read_tr16_b64_v4i16((LAS s16x4*)(R)));
      const v2u b = __builtin_bit_cast(v2u, __builtin_amdgcn_ds_read_tr16_b64_v4i16((LAS s16x4*)(R + 4 * 48)));
      const v2u c = __builtin_bit_cast(v2u, __builtin_amdgcn_ds_read_tr16_b64_v4i16((LAS s16x4*)(R + 1536)));
      const v2u d = __builtin_bit_cast(v2u, __builtin_amdgcn_ds_read_tr16_b64_v4i16((LAS s16x4*)(R + 1536 + 4 * 48)));
      stg(v4u{a.x, a.y, b.x, b.y}, rs, lane16,
          sel(uf >= 252, OOR, wb + sel(tile < 8, (tile & 3) * 36 + us * 2 + (tile >> 2), sel(tile < 10, 72 + tile * 18 + us, (tile >> 1) * 36 - 36 + us * 2 + (tile & 1))) * KB));
      stg(v4u{c.x, c.y, d.x, d.y}, rs, lane16, sel(hasO, WOB + ((g & 3) * 162 + ot * 18 + os) * KB, OOR));
      const float cq = rows_sum(dot2(a.x, ONES, dot2(a.y, ONES, dot2(b.x, ONES, dot2(b.y, ONES, 0.f)))));
      __builtin_amdgcn_raw_buffer_store_b32(__float_as_uint(cq), rs, sel(lane < 16, lane * 4, OOR) + sel(uf < 252, CSQ + ((g * 18 + us) * 14 + tile) * 64, OOR), 0, SC1);
      SB();
      DRAIN();
      GET_TAG();
      if (lane == 0 && __hip_atomic_fetch_add((LAS int*)(L + L_HC) + C_P0, 1, __ATOMIC_RELAXED, __HIP_MEMORY_SCOPE_WORKGROUP) == 7)
        set_flag(p.ws, SFLAGS + (g * 32 + j) * 8, tag);
      STAGE_X();
      BAR();
      issue_window<0, WIN>(win, rs, lane16, QBASE());
    }
    if (opq_s(w) >= 8) {
      LAS unsigned char* T = L + 125 * KB + (w - 8) * 4608;
      const int item = sel(w < 11, 3 * j + w - 8, sel((j & 7) < 3, 96 + 3 * (j >> 3) + (j & 7), 127)), rbk = (item * 57) >> 9, cb = item - rbk * 9, kind = sel(rbk >= 6, 1, 0), k3 = rbk - kind * 6;
      const int rn = sel(kind, 4, 8), ld = sel(kind, 1152, 9216);
      const rsrc_t rw = __builtin_amdgcn_make_buffer_rsrc((void*)(kind == 0 ? p.w1 : p.w2), 0, 576 * 4608 * 2, 0x00020000);
      const int jj = lane >> 3;
      const int woff = (jj & 3) * ld + (lane & 7) * 16 + sel(item < 108, 96 * k3 * ld + g * sel(kind, 576 * 1152, 1152) + 128 * cb, OOR);
      const int xoff = (sel(j > 0, 64 * j - 16, 0) + (w & 1) + 2 * xrow) * 1152 + xcol + sel(w < 10, 0, OOR);
      if (w == 11 && lane < 16) ((volatile LAS int*)(L + L_HC))[lane] = 0;
      BAR();
      LOAD_X();
      v4u wv[3][4];
#pragma unroll
      for (int n = 0; n < 4; ++n)
#pragma unroll
        for (int c = 0; c < 3; ++c)
          wv[c][n] = __builtin_amdgcn_raw_buffer_load_b128(rw, woff + (jj >> 2) * (sel(kind, c < 2 ? 16 : 48, 4) * ld),
                                                           (n * rn + sel(kind, c < 2 ? 48 * c : 32, 32 * c)) * ld, NTW ? 2 : 0);
      SB();
      GET_TAG();
      STAGE_X();
      float cs[4] = {0.f, 0.f, 0.f, 0.f};
      LAS unsigned char* R = T + (8 * quad + (l15 >> 2)) * 144 + (lane & 3) * 8;
#pragma unroll
      for (int c = 0; c < 3; ++c) {
#pragma unroll
        for (int n = 0; n < 4; ++n) *(LAS v4u*)(T + (n * 8 + (lane >> 3)) * 144 + (lane & 7) * 16) = wv[c][n];
#pragma unroll
        for (int tt = 0; tt < 4; ++tt) {
          const v2u a = __builtin_bit_cast(v2u, __builtin_amdgcn_ds_read_tr16_b64_v4i16((LAS s16x4*)(R + tt * 32)));
          const v2u b = __builtin_bit_cast(v2u, __builtin_amdgcn_ds_read_tr16_b64_v4i16((LAS s16x4*)(R + tt * 32 + 4 * 144)));
          wv[c][tt] = v4u{a.x, a.y, b.x, b.y};
          cs[tt] = dot2(a.x, ONES, dot2(a.y, ONES, dot2(b.x, ONES, dot2(b.y, ONES, cs[tt]))));
        }
      }
#pragma unroll
      for (int tt = 0; tt < 4; ++tt) { cs[tt] = rows_sum(cs[tt]); OPAQUE(cs[tt]); }
      float csum = quad == 0 ? cs[0] : quad == 1 ? cs[1] : quad == 2 ? cs[2] : cs[3];
      asm volatile("" : "+v"(csum));
      if (opq_s(w) == 11) {
        cnt_wait(L, C_P0, 8);
        wait_flags<4>(p.ws, SFLAGS + ((g ^ ((lane >> 3) & 4)) * 32 + (lane & 31)) * 8, true, tag);
#pragma unroll
        for (int i = 0; i < 18; ++i) win[i] = ldg(rs, lane16, CSQ + (g * 18 + i) * 896);
      }
      BAR();
      row_table1<2>(L, w - 8, lane);
      if (opq_s(w) == 11) {
        f32x4 cs = __builtin_bit_cast(f32x4, win[0]);
#pragma unroll
        for (int i = 1; i < 18; ++i) add4(cs, win[i]);
        if (lane < 56) *(LAS f32x4*)(L + L_TQ + lane16) = cs;
        row_table1<2>(L, 4, lane);
      }
#pragma unroll
      for (int i = 0; i < 5; ++i) {
        const int n = (w - 8) * 5 + i;
        stg(v4u{POISON, POISON, POISON, POISON}, rs, lane16, sel(n < 18, ab + sel(n < 9, A_O, A_X1 - 9 * KB) + (g * 9 + n) * KB, OOR));
      }
      __builtin_amdgcn_s_sleep(BDELAY);
      DRAIN();
      if (lane == 0 && __hip_atomic_fetch_add((LAS int*)(L + L_HC) + C_P0, 1, __ATOMIC_RELAXED, __HIP_MEMORY_SCOPE_WORKGROUP) == 11)
        set_flag(p.ws, FLAGS + j * 128 + g * 8, tag);
      const int dst0 = sel(item < 108, wb + (252 + 54 * kind + 9 * k3) * KB, OOR);
#pragma unroll
      for (int tt = 0; tt < 4; ++tt)
#pragma unroll
        for (int c = 0; c < 3; ++c) stg(wv[c][tt], rs, lane16 + c * 3 * KB, dst0 + ((((4 * cb + tt) * 43) >> 7) * 105 + 4 * cb + tt) * KB);
      __builtin_amdgcn_raw_buffer_store_b32(__float_as_uint(csum), rs,
                                            lane * 4 + sel(kind == 0, CS1 + (k3 * 4608 + 576 * g + 64 * cb) * 4, OOR), 0, SC1);
      SB();
      if (opq_s(w) == 8) issue_window<0, WIN>(win, rs, lane16, QBASE());
    }
  }
  int os0[3];
  const int wq = opq_s(w0);
  {
    const int T = 2 * (g0 >> 2) + sel(wq >= 6, 1, 0), s0 = 3 * sel(wq >= 6, wq - 6, wq);
#pragma unroll
    for (int i = 0; i < 3; ++i) {
      const int s = s0 + i;
      os0[i] = ABASE + j0 * ASL + A_O + sel(s < 16, ((s >> 1) * 9 + T * 2 + (s & 1)) * KB, ((s - 16) * 36 + 8) * KB + T * 256);
    }
  }
  const int oq0 = sel(wq == 5 || wq == 11, 9 * KB, 256);
  const int ft0 = (g0 & 3) * 9 + sel(wq < 9, wq, 0), xs0 = ((g0 >> 2) * 36 + (ft0 >> 1)) * KB + (ft0 & 1) * 512;

#pragma unroll 1
  for (int layer = 0; layer < 2; ++layer) {
    v4u xres;
    {
      LOCALS();
      {
        const int qbase = QBASE(), nbase = NBASE(), rw = 5 + 5 * layer;
        if (opq_s(w) < 4) qkv_tiles<2, 5, 2, 7, 4, WIN>(win, L, rs, lane, qbase, nbase, w, rw);
        if ((opq_s(w) & 14) == 4) qkv_tiles<2, 4, 0, 7, 1, 0>(win, L, rs, lane, qbase, nbase, 2 * w + 2, rw);
        if (opq_s(w) == 6) qkv_tiles<1, 5, 0, 10, 0, WIN>(win, L, rs, lane, qbase, nbase, 8, rw);
        if (opq_s(w) == 7) qkv_tiles<1, 5, 1, 10, 0, WIN>(win, L, rs, lane, qbase, nbase, 9, rw);
      }
      if (layer == 1 && bid == 0 && w == 10) {
#pragma unroll 1
        for (int i = 0; i < 4; ++i) wait_flags(p.ws, SFLAGS + (256 + i * 64 + lane) * 8, true, tag);
        if (lane == 0) set_flag(p.ws, 0, tag);
      }
      xres = *(LAS v4u*)(L + opq_s(xs0) + (quad & 1) * 18 * KB + (quad >> 1) * 256 + l15 * 16);
      if (layer == 0 && w >= 8) DRAIN();
      if ((opq_s(w) & 12) == 4) {
        BAR();
        if (layer == 0 && w == 6 && lane == 0) set_flag(p.ws, SFLAGS + (256 + g * 32 + j) * 8, tag);
        if (w == 7 && layer == 0) {
          wait_flags(p.ws, FLAGS + j * 128 + (lane & 7) * 8, lane < 8, tag);
        }
        if (w < 6) issue_window<0, WIN>(win, rs, lane16, NBASE());
      }
      if ((opq_s(w) & 12) != 4) {
        const int T = w & 3, hp = w >> 3, Tp = (T & 1) ? T - 1 : 4;
        f32x4 mp, ms;
        const float neg = __uint_as_float(vconst<0xf149f2cau>());
        const int lo = l15 + ((T & 1) ? 9 : 1 + 4 * T), hi = (T & 1) ? 16 : j > 0 ? 8 + 4 * T : 0;
#pragma unroll
        for (int r = 0; r < 4; ++r) {
          const int jj = 4 * quad + r;
          mp[r] = ((jj >= lo) & (jj < hi)) ? 0.f : neg;
          ms[r] = (unsigned)(l15 - jj) <= 7u ? 0.f : neg;
        }
        int xs = L_QK + lane * 8 + T * 512, xp = L_QK + lane * 8 + Tp * 512, hs = xs + hp * 5120, hq = xp + hp * 5120;
        int om = ab + A_O + (g * 9 + T * 2 + hp) * KB + lane16;
        int oe = quad == hp ? ab + A_O + (g * 9 + 8) * KB + T * 256 + hp * 8 + l15 * 16 : OOR;
        unsigned qm0 = quad == 2 * hp ? ~0u : 0u, qm1 = quad == 2 * hp + 1 ? ~0u : 0u, zr = vconst<0>();
        v4u one4 = {vconst<ONES>(), vconst<ONES>(), vconst<ONES>(), vconst<ONES>()};
        asm volatile("" : "+v"(mp), "+v"(ms), "+v"(xs), "+v"(xp), "+v"(hs), "+v"(hq), "+v"(om), "+v"(oe), "+v"(qm0), "+v"(qm1), "+v"(zr), "+v"(one4));
        BAR();
        const v2u kxs = *(LAS v2u*)(L + xs + 40 * 512);
        const unsigned kxp = *(LAS unsigned*)(L + xp + 40 * 512 + 4);
        v2u q[2], ks[2], kp[2], vs[2], vp[2];
#pragma unroll
        for (int hh = 0; hh < 2; ++hh) {
          q[hh] = *(LAS v2u*)(L + hs + (50 + 5 * hh) * 512);
          kp[hh] = *(LAS v2u*)(L + hq + 5 * hh * 512);
          ks[hh] = *(LAS v2u*)(L + hs + 5 * hh * 512);
        }
        SB();
#pragma unroll
        for (int hh = 0; hh < 2; ++hh) {
          vp[hh] = *(LAS v2u*)(L + hq + (20 + 5 * hh) * 512);
          vs[hh] = *(LAS v2u*)(L + hs + (20 + 5 * hh) * 512);
        }
        const v2u vxp = *(LAS v2u*)(L + xp + 45 * 512), vxs = *(LAS v2u*)(L + xs + 45 * 512);
        SB();
        const f32x4 z = {0.f, 0.f, 0.f, 0.f};
        f32x4 sp[2], ss[2], o[2], ox[2], sm[2];
        float mx[2];
#pragma unroll
        for (int hh = 0; hh < 2; ++hh) {
          const v4u qf = {q[hh].x, q[hh].y, kxs.x & (hh ? qm1 : qm0), zr};
          sp[hh] = mfma(v4u{kp[hh].x, kp[hh].y, kxp, zr}, qf, mp);
          ss[hh] = mfma(v4u{ks[hh].x, ks[hh].y, kxs.y, zr}, qf, ms);
        }
#pragma unroll
        for (int hh = 0; hh < 2; ++hh) {
          const float m0 = __builtin_fmaxf(__builtin_fmaxf(sp[hh][0], sp[hh][1]), sp[hh][2]), m1 = __builtin_fmaxf(__builtin_fmaxf(ss[hh][0], ss[hh][1]), ss[hh][2]);
          mx[hh] = __builtin_fmaxf(__builtin_fmaxf(__builtin_fmaxf(__builtin_fmaxf(m0, sp[hh][3]), ss[hh][3]), m1), m1);
        }
        rows_max2(mx[0], mx[1]);
#pragma unroll
        for (int hh = 0; hh < 2; ++hh) {
          const float c = 0.23570226039551584f * 1.4426950408889634f, mc = -c * mx[hh];
          float e[8];
#pragma unroll
          for (int r = 0; r < 4; ++r) {
            e[r] = __builtin_amdgcn_exp2f(__builtin_fmaf(sp[hh][r], c, mc));
            e[4 + r] = __builtin_amdgcn_exp2f(__builtin_fmaf(ss[hh][r], c, mc));
          }
          const v4u pf = {pk(e[0], e[1]), pk(e[2], e[3]), pk(e[4], e[5]), pk(e[6], e[7])};
          sm[hh] = mfma(one4, pf, z);
          o[hh] = mfma(v4u{vp[hh].x, vp[hh].y, vs[hh].x, vs[hh].y}, pf, z);
          ox[hh] = mfma(v4u{vxp.x, vxp.y, vxs.x, vxs.y}, pf, z);
        }
        const float i0 = __builtin_amdgcn_rcpf(sm[0][0]), i1 = __builtin_amdgcn_rcpf(sm[1][0]);
        stg(v4u{pk(o[0][0] * i0, o[0][1] * i0), pk(o[0][2] * i0, o[0][3] * i0), pk(o[1][0] * i1, o[1][1] * i1), pk(o[1][2] * i1, o[1][3] * i1)}, rs, om, 0);
        __builtin_amdgcn_raw_buffer_store_b64(v2u{pk(ox[0][0] * i0, ox[0][1] * i0), pk(ox[1][2] * i1, ox[1][3] * i1)}, rs, oe, 0, SC1);
        SETTLE();
      }
      BAR();
    }
    {
      LOCALS();
      const int th = g >> 2, ft = (g & 3) * 9 + w;
      {
        const int ov = quad * opq_s(oq0) + l15 * 16;
        RETRY_BEGIN();
        v4u orr[3];
#pragma unroll
        for (int i = 0; i < 3; ++i) orr[i] = ldg(rs, i ? ov : lane16, os0[i] + layer * ALY);
#pragma unroll
        for (int i = 0; i < 3; ++i) { *(LAS v4u*)(L + (w * 3 + i) * KB + lane16) = orr[i]; seen_ = umax4(seen_, orr[i]); }
        RETRY_END();
      }
      float xf[8] = {blo(xres.x), bhi(xres.x), blo(xres.y), bhi(xres.y), blo(xres.z), bhi(xres.z), blo(xres.w), bhi(xres.w)};
      int xo = lane16 + ab + A_X1 + (th * 36 + ft) * KB;
      asm volatile("" : "+v"(xo), "+v"(xf[0]), "+v"(xf[1]), "+v"(xf[2]), "+v"(xf[3]), "+v"(xf[4]), "+v"(xf[5]), "+v"(xf[6]), "+v"(xf[7]));
      if (layer == 0 && w == 0) wait_flags<2>(p.ws, SFLAGS + (256 + g * 32 + (lane & 31)) * 8, lane < 32, tag);
      BAR();
      if (w < 9) {
        f32x4 acc[1][2];
        gemm18<1, 2, 0, P2N, 10>(acc, win, L + lane16, rs, lane16, 0, MBASE(), L);
        float a[8];
#pragma unroll
        for (int r = 0; r < 4; ++r) {
          auto s = __builtin_amdgcn_permlane16_swap(__float_as_uint(acc[0][0][r]), __float_as_uint(acc[0][1][r]), false, false);
          a[r] = __uint_as_float(s[0]);
          a[4 + r] = __uint_as_float(s[1]);
        }
        stg(v4u{pk(a[0] + xf[0], a[1] + xf[1]), pk(a[2] + xf[2], a[3] + xf[3]), pk(a[4] + xf[4], a[5] + xf[5]), pk(a[6] + xf[6], a[7] + xf[7])}, rs, xo, 0);
        SB();
        if (P2N < 3 * DST) issue_window<P2N, 3 * DST>(win, rs, lane16, MBASE());
        else SETTLE();
      }
      if (layer == 0 && opq_s(w) >= 9) {
        const int f4 = (w - 9) * 48 + (lane < 48 ? lane : 0);
        v4u cp[6];
#pragma unroll
        for (int i = 0; i < 6; ++i) cp[i] = ldg(rs, f4 * 16, CS1 + (i * 4608 + 576 * g) * 4);
        const v2u b = *(const GAS v2u*)(p.b1 + 576 * g + 4 * f4);
        *(LAS v2u*)(L + L_B2 + f4 * 8) = *(const GAS v2u*)(p.b2 + 4 * f4);
        f32x4 cs = __builtin_bit_cast(f32x4, cp[0]);
#pragma unroll
        for (int i = 1; i < 6; ++i) add4(cs, cp[i]);
        *(LAS f32x4*)(L + L_T1 + f4 * 16) = cs;
        *(LAS f32x4*)(L + L_T1 + 2304 + f4 * 16) = f32x4{blo(b.x), bhi(b.x), blo(b.y), bhi(b.y)};
      }
      if (w == 10 && lane < 10) ((volatile LAS int*)(L + L_HC))[lane] = 0;
      BAR();
    }
    {
      LOCALS();
      const int mbase = MBASE();
      const int T = w & 3, cls = w >> 2;
      if (opq_s(cls) == 0) {
        if (layer == 0) {
          __builtin_amdgcn_s_sleep(PZD);
          const v4u pz = {vconst<POISON>(), vconst<POISON>(), vconst<POISON>(), vconst<POISON>()};
#pragma unroll
          for (int i = 0; i < 8; ++i) {
            const int n = w * 8 + i;
            stg(pz, rs, lane16,
                sel(n < 30, ab + ALY + n * KB + sel(n < 12, A_X2 + g * 12 * KB, sel(n < 21, A_O, A_X1 - 9 * KB) + (g * 9 - 12) * KB), OOR));
          }
        }
      }
      if (opq_s(cls) > 0) {
        v4u xr[9];
        const int xo = (quad >> 1) * KB + ((quad & 1) * 2 + (T & 1)) * 256 + l15 * 16;
        RETRY_BEGIN();
#pragma unroll
        for (int i = 0; i < 9; ++i) xr[i] = ldg(rs, xo + (i & 1) * 2 * KB, ab + A_X1 + ((T >> 1) * 36 + 18 * (cls - 1) + (i >> 1) * 4) * KB);
#pragma unroll
        for (int i = 0; i < 9; ++i) { *(LAS v4u*)(L + (T * 18 + 9 * (cls - 1) + i) * KB + lane16) = xr[i]; seen_ = umax4(seen_, xr[i]); }
        RETRY_END();
        cnt_add(L, C_XA + cls - 1, lane);
        __builtin_amdgcn_s_setprio(3);
        const v4u one4 = {vconst<ONES>(), vconst<ONES>(), vconst<ONES>(), vconst<ONES>()};
        f32x4 sa = {0.f, 0.f, 0.f, 0.f}, sq = {0.f, 0.f, 0.f, 0.f};
        asm volatile("s_nop 1");
#pragma unroll
        for (int i = 0; i < 9; ++i) { sa = mfma(one4, xr[i], sa); sq = mfma(xr[i], xr[i], sq); }
        __builtin_amdgcn_s_setprio(0);
        if (layer == 0 && opq_s(w) >= 9) issue_window<0, 3 * DST>(win, rs, lane16, mbase);
        const float dg = (lane & 2) ? ((lane & 1) ? sq[3] : sq[2]) : ((lane & 1) ? sq[1] : sq[0]);
        if (quad == (l15 >> 2)) *(LAS f32x2*)(L + L_ST + ((T * 16 + l15) * 2 + cls - 1) * 8) = f32x2{sa[0], dg};
        cnt_add(L, C_ST, lane);
      }
      f32x4 acc[3][4];
#pragma unroll
      for (int t = 0; t < 3; ++t)
#pragma unroll
        for (int m = 0; m < 4; ++m) acc[t][m] = zero4();
      if (opq_s(cls) > 0) { row_table(L, lane); shift_acc(acc, L, w, quad, l15); }
      __builtin_amdgcn_s_setprio(3);
      gemm_up(acc, win, L + lane16, rs, lane16, mbase, L);
      __builtin_amdgcn_s_setprio(0);
      {
        LAS unsigned char* H = L + L_H + lane16 + 3 * (w >> 1) * KB;
        if (opq_s(cls) == 0) {
          cnt_wait(L, C_RT, 1);
          shift_acc(acc, L, w, quad, l15);
        }
        f32x4 rt[4];
#pragma unroll
        for (int m = 0; m < 4; ++m) rt[m] = *(LAS f32x4*)(L + L_ST + KB + (m * 16 + l15) * 16);
#pragma unroll
        for (int m = 0; m < 4; ++m) {
          const v2u u0 = gelu4(acc[0][m], rt[m][2], rt[m][3], rt[m][1]), u1 = gelu4(acc[1][m], rt[m][2], rt[m][3], rt[m][1]);
          *(LAS v4u*)(H + (m * 18 + (w & 1)) * KB) = v4u{u0.x, u0.y, u1.x, u1.y};
          *(LAS v2u*)(H + (m * 18 + 2) * KB + (w & 1) * 8) = gelu4(acc[2][m], rt[m][2], rt[m][3], rt[m][1]);
        }
        cnt_add(L, C_H0 + cls, lane);
      }
      __builtin_amdgcn_s_setprio(2);
      gemm_down(acc, win, L + L_H + lane16, rs, lane16, mbase + 54 * KB, L);
      __builtin_amdgcn_s_setprio(0);
      const int pw = ab + A_PT + (g * 12 + w) * 6144;
      const int poff = (quad & 1) * 1536 + (quad >> 1) * 256 + l15 * 16;
#pragma unroll
      for (int t = 0; t < 3; ++t)
#pragma unroll
        for (int m = 0; m < 4; m += 2) {
          const v2u a = {pk(acc[t][m][0], acc[t][m][1]), pk(acc[t][m][2], acc[t][m][3])};
          const v2u b = {pk(acc[t][m + 1][0], acc[t][m + 1][1]), pk(acc[t][m + 1][2], acc[t][m + 1][3])};
          const v4u o = widen(a, b);
          const bool own = m + (quad & 1) == (g >> 1) && (l15 >> 3) == (g & 1);
          stg(o, rs, own ? OOR : poff + m * 1536 + t * 512, pw);
          if (own) *(LAS v4u*)(L + (((g >> 1) ^ 1) * 18 + w) * KB + ((2 * t + (quad >> 1)) * 8 + (lane & 7)) * 16) = o;
        }
    }
    {
      LOCALS();
      OWNROWS(6);
      const bool act = lane < 48;
      float sum[8];
      {
        const v4u bb = *(LAS v4u*)(L + L_B2 + (act ? c8 : 0) * 16);
        const v4u xr = *(LAS v4u*)(L + (act ? piece : 0)), ow = *(LAS v4u*)(L + (((g >> 1) ^ 1) * 18 + w) * KB + lane16);
#pragma unroll
        for (int e = 0; e < 4; ++e) {
          sum[2 * e] = blo(xr[e]) + blo(ow[e]); OPAQUE(sum[2 * e]); sum[2 * e] += blo(bb[e]); OPAQUE(sum[2 * e]);
          sum[2 * e + 1] = bhi(xr[e]) + bhi(ow[e]) + bhi(bb[e]);
        }
      }
      int roff = (g >> 1) * 1536 + (lane >> 3) * 256 + (8 * (g & 1) + (lane & 7)) * 16 + (act ? 0 : OOR);
      int foff = WFLAGS + ((layer * 32 + j - (lane >> 3)) * 12 + w) * 128 + (lane < 8 ? lane : 3 + 4 * (lane & 1)) * 8;
      asm volatile("" : "+v"(roff), "+v"(foff), "+v"(sum[0]), "+v"(sum[1]), "+v"(sum[2]), "+v"(sum[3]), "+v"(sum[4]), "+v"(sum[5]), "+v"(sum[6]), "+v"(sum[7]));
      if (w >= 8) __builtin_amdgcn_s_barrier();
      DRAIN();
      if (lane == 0) set_flag(p.ws, WFLAGS + ((layer * 32 + j) * 12 + w) * 128 + g * 8, tag);
      if (layer == 0 && opq_s(w) < 8) {
        if (QA) cnt_wait(L, C_LC, 12);
        issue_window<0, QA>(win, rs, lane16, QBASE());
      }
      __builtin_amdgcn_s_barrier();
      wait_flags(p.ws, foff, lane < (layer == 0 && j > 0 ? 10 : 8), tag);
      if (w < 8) __builtin_amdgcn_s_barrier();
      asm volatile("" ::: "memory");
      v4u xb;
      {
        v4u pr[7];
#pragma unroll
        for (int i = 0; i < 7; ++i) pr[i] = ldg(rs, roff, ab + A_PT + (((i + 1 + g) & 7) * 12 + w) * 6144);
        SB();
        const unsigned sel_lo = opq_s(0x00003f80), sel_hi = opq_s(0x3f800000);
#pragma unroll
        for (int i = 0; i < 7; ++i)
#pragma unroll
          for (int e = 0; e < 4; ++e) { sum[2 * e] = dot2(pr[i][e], sel_lo, sum[2 * e]); sum[2 * e + 1] = dot2(pr[i][e], sel_hi, sum[2 * e + 1]); }
#pragma unroll
        for (int e = 0; e < 4; ++e) xb[e] = pk(sum[2 * e], sum[2 * e + 1]);
      }
      if (layer == 0) {
        float s1 = dot2(xb.x, ONES, dot2(xb.y, ONES, dot2(xb.z, ONES, dot2(xb.w, ONES, 0.f))));
        float s2 = dot2(xb.x, xb.x, dot2(xb.y, xb.y, dot2(xb.z, xb.z, dot2(xb.w, xb.w, 0.f))));
        if (!act) { s1 = 0.f; s2 = 0.f; }
        s1 += ror8(s1); OPAQUE(s1); s2 += ror8(s2);
        s1 = rows_sum(s1); OPAQUE(s1); s2 = rows_sum(s2);
        if (!act) xb = v4u{__float_as_uint(s1), __float_as_uint(s2), 0u, 0u};
        int vo = lane16 + ab + ALY + A_X2 + w * KB + (lane < 56 ? 0 : OOR);
        stg(xb, rs, vo, g * 12 * KB);
        SB();
        LAS unsigned char *Iw = L + (c8 >> 2) * KB + ((c8 & 3) * 16 + (lane & 7)) * 16, *Iw2 = Iw + 54 * KB, *Sw = L + L_ST + (w * 80 + (lane & 7)) * 8;
        int vh = vo - (j > 0 ? ASL : 0);
        asm volatile("" : "+v"(vo), "+v"(vh), "+v"(Iw), "+v"(Iw2), "+v"(Sw));
        if (QB > QA && opq_s(w) < 8) issue_window<QA, QB>(win, rs, lane16, QBASE());
        else SETTLE();
        BAR();
        v4u xv[10];
        RETRY_BEGIN();
#pragma unroll
        for (int i = 0; i < 10; ++i) xv[i] = ldg(rs, i < 8 ? vo : vh, (i < 8 ? i : 3 + 4 * (i - 8)) * 12 * KB);
        SB();
        if (act) {
#pragma unroll
          for (int i = 0; i < 10; ++i) *(LAS v4u*)((i < 6 ? Iw + (i >> 1) * 18 * KB : Iw2 + ((i >> 1) - 3) * 18 * KB) + (i & 1) * 128) = xv[i];
        }
#pragma unroll
        for (int i = 0; i < 10; ++i) seen_ = umax4(seen_, xv[i]);
        RETRY_END();
        if ((lane >> 3) == 6) {
#pragma unroll
          for (int i = 0; i < 10; ++i) *(LAS v2u*)(Sw + i * 64) = v2u{xv[i].x, xv[i].y};
        }
        BAR();
        if (QB < WIN && opq_s(w) < 8) issue_window<QB, WIN>(win, rs, lane16, QBASE());
        if (w == 11) row_table1<12>(L, 4, lane);
        if (w >= 8) row_table1<12>(L, w - 8, lane);
        if (opq_s(w) == 8) issue_window<3 * DST, WIN>(win, rs, lane16, QBASE());
        if (opq_s(w) >= 8) issue_window<0, 3 * DST>(win, rs, lane16, QBASE());
      } else {
        const rsrc_t ro = __builtin_amdgcn_make_buffer_rsrc((void*)p.out, 0, 2048 * 576 * 2, 0x00020000);
        __builtin_amdgcn_raw_buffer_store_b128(xb, ro, act ? mytok * 1152 + c8 * 16 : OOR, 0, 0);
      }
    }
  }
  asm volatile(".Lfwd_end:");
}
}

extern "C" void kernel_launch(void* const* d_in, const int* in_sizes, int n_in, void* d_out, int out_size, void* d_ws, size_t ws_size,
                              hipStream_t stream) {
  (void)in_sizes; (void)n_in; (void)out_size; (void)ws_size;
  Params p;
  p.x = (const unsigned short*)d_in[0];
  p.wqkv = (const unsigned short*)d_in[1];
  p.wo = (const unsigned short*)d_in[2];
  p.w1 = (const unsigned short*)d_in[4];
  p.b1 = (const unsigned short*)d_in[5];
  p.w2 = (const unsigned short*)d_in[6];
  p.b2 = (const unsigned short*)d_in[7];
  p.out = (unsigned short*)d_out;
  p.ws = (unsigned char*)d_ws;
  (void)hipFuncSetAttribute((const void*)fused_fwd, hipFuncAttributeMaxDynamicSharedMemorySize, LDS_BYTES);
  hipLaunchKernelGGL(fused_fwd, dim3(256), dim3(NTHR), LDS_BYTES, stream, p);
}
```
